```python
import torch

def _softmax(x, axis=-1):
    return torch.softmax(x, dim=axis)
def _gelu(x):
    return torch.nn.functional.gelu(x, approximate="tanh")
def _silu(x):
    return torch.nn.functional.silu(x)
def _sigmoid(x):
    return torch.sigmoid(x)
def _layernorm(x, axis=-1, eps=1e-5):
    m = x.mean(dim=axis, keepdim=True)
    v = x.var(dim=axis, keepdim=True, unbiased=False)
    return (x - m) / torch.sqrt(v + eps)
def _scatter_add(tgt, idx, src, axis=0):
    return tgt.clone().index_add_(axis, idx, src)
def _segment_sum(src, seg, n):
    out = torch.zeros((n,) + src.shape[1:], dtype=src.dtype)
    return out.index_add_(0, seg, src)
def _pad(x, pw):
    flat = tuple(v for pair in reversed(pw) for v in pair)
    return torch.nn.functional.pad(x, flat)

kernel_name = 'concat_heads__gqa_block_cached__gqa_block_cached__near_ridge'

def setup_inputs():

    _w = (lambda a: (lambda t: t.to(torch.bfloat16) if t.is_floating_point() else t)(torch.as_tensor(a)))
    def _A_setup():
        B, Sq, D = 16, 128, 640
        Hq, Hkv, Sk, Dh = 32, 32, 512, 192
        assert Hq % Hkv == 0, (Hq, Hkv)
        return {
            "x": _w((lambda *s: torch.randn(*s, dtype=torch.bfloat16) * 0.8)(B, Sq, D)),
            "k_cache": _w((lambda *s: torch.randn(*s, dtype=torch.bfloat16) * 0.8)(B, Hkv, Sk, Dh)),
            "v_cache": _w((lambda *s: torch.randn(*s, dtype=torch.bfloat16) * 0.05)(B, Hkv, Sk, Dh)),
            "wq": _w((lambda *s: torch.randn(*s, dtype=torch.bfloat16) * 0.8)(D, Hq * Dh) * (D ** -0.5)),
            "wo": _w((lambda *s: torch.randn(*s, dtype=torch.bfloat16) * 0.05)(Hq * Dh, D) * ((Hq * Dh) ** -0.5)),
        }
    _A_in = _A_setup()

    def _B_setup():
        B, Sq, D = 16, 128, 640
        Hq, Hkv, Sk, Dh = 32, 32, 512, 192
        assert Hq % Hkv == 0, (Hq, Hkv)
        return {
            "x": _w((lambda *s: torch.randn(*s, dtype=torch.bfloat16) * 0.8)(B, Sq, D)),
            "k_cache": _w((lambda *s: torch.randn(*s, dtype=torch.bfloat16) * 0.8)(B, Hkv, Sk, Dh)),
            "v_cache": _w((lambda *s: torch.randn(*s, dtype=torch.bfloat16) * 0.05)(B, Hkv, Sk, Dh)),
            "wq": _w((lambda *s: torch.randn(*s, dtype=torch.bfloat16) * 0.8)(D, Hq * Dh) * (D ** -0.5)),
            "wo": _w((lambda *s: torch.randn(*s, dtype=torch.bfloat16) * 0.05)(Hq * Dh, D) * ((Hq * Dh) ** -0.5)),
        }
    _B_in = _B_setup()

    return {
        "A_x": _A_in["x"],
        "A_k_cache": _A_in["k_cache"],
        "A_v_cache": _A_in["v_cache"],
        "A_wq": _A_in["wq"],
        "A_wo": _A_in["wo"],
        "B_x": _B_in["x"],
        "B_k_cache": _B_in["k_cache"],
        "B_v_cache": _B_in["v_cache"],
        "B_wq": _B_in["wq"],
        "B_wo": _B_in["wo"],
    }

def reference(A_x, A_k_cache, A_v_cache, A_wq, A_wo, B_x, B_k_cache, B_v_cache, B_wq, B_wo):
    def _A_ref(x, k_cache, v_cache, wq, wo):
        B, Sq, D = x.shape
        Hq, Dh = 32, 192
        Hkv = k_cache.shape[1]
        rep = Hq // Hkv
        q = (lambda x, *d: x.permute(*d))((x @ wq).reshape(B, Sq, Hq, Dh), 0, 2, 1, 3)
        k = (lambda x, n, axis: torch.repeat_interleave(x, n, dim=axis))(k_cache, rep, axis=1)
        v = (lambda x, n, axis: torch.repeat_interleave(x, n, dim=axis))(v_cache, rep, axis=1)
        scores = torch.einsum("bhqd,bhkd->bhqk", q, k) * (Dh ** -0.5)
        p = _softmax(scores, axis=-1)
        o = torch.einsum("bhqk,bhkd->bhqd", p, v)
        return (lambda x, *d: x.permute(*d))(o, 0, 2, 1, 3).reshape(B, Sq, Hq * Dh) @ wo

    def _B_ref(x, k_cache, v_cache, wq, wo):
        B, Sq, D = x.shape
        Hq, Dh = 32, 192
        Hkv = k_cache.shape[1]
        rep = Hq // Hkv
        q = (lambda x, *d: x.permute(*d))((x @ wq).reshape(B, Sq, Hq, Dh), 0, 2, 1, 3)
        k = (lambda x, n, axis: torch.repeat_interleave(x, n, dim=axis))(k_cache, rep, axis=1)
        v = (lambda x, n, axis: torch.repeat_interleave(x, n, dim=axis))(v_cache, rep, axis=1)
        scores = torch.einsum("bhqd,bhkd->bhqk", q, k) * (Dh ** -0.5)
        p = _softmax(scores, axis=-1)
        o = torch.einsum("bhqk,bhkd->bhqd", p, v)
        return (lambda x, *d: x.permute(*d))(o, 0, 2, 1, 3).reshape(B, Sq, Hq * Dh) @ wo

    a = _A_ref(A_x, A_k_cache, A_v_cache, A_wq, A_wo)
    b = _B_ref(B_x, B_k_cache, B_v_cache, B_wq, B_wo)
    return torch.concatenate([a, b], axis=-1)
```

```cpp
#include <hip/hip_runtime.h>
#include <hip/hip_bf16.h>
#include <stdint.h>

typedef __bf16 bf16x4 __attribute__((ext_vector_type(4)));
typedef __bf16 bf16x8 __attribute__((ext_vector_type(8)));
typedef __bf16 bf16x2 __attribute__((ext_vector_type(2)));
typedef float f32x2 __attribute__((ext_vector_type(2)));
typedef float f32x16 __attribute__((ext_vector_type(16)));
typedef unsigned u32x4 __attribute__((ext_vector_type(4)));
#define LDSP __attribute__((address_space(3)))
#define DI __device__ __forceinline__
typedef __amdgpu_buffer_rsrc_t rsrc_t;

constexpr int NB = 16, SQ = 128, DM = 640, NH = 32, SK = 512, DH = 192, HD = NH * DH;

DI unsigned cvtpk(float lo, float hi) { f32x2 v = {lo, hi}; bf16x2 b = __builtin_convertvector(v, bf16x2); return __builtin_bit_cast(unsigned, b); }
DI bf16x8 pack8(const f32x16& a, int s, float sc) {
  u32x4 w = {cvtpk(a[8*s]*sc, a[8*s+1]*sc), cvtpk(a[8*s+2]*sc, a[8*s+3]*sc), cvtpk(a[8*s+4]*sc, a[8*s+5]*sc), cvtpk(a[8*s+6]*sc, a[8*s+7]*sc)};
  return __builtin_bit_cast(bf16x8, w);
}
DI rsrc_t mkrsrc(const void* p, unsigned bytes) { return __builtin_amdgcn_make_buffer_rsrc((void*)p, (short)0, (int)bytes, 0x00020000); }
DI void dma16(rsrc_t r, char* l, int voff, int soff) { __builtin_amdgcn_raw_ptr_buffer_load_lds(r, (LDSP void*)l, 16, voff, soff, 0, 0); }
DI bf16x8 ldsr128(const char* p) { return *(const bf16x8*)p; }
DI bf16x8 trread2(const char* p0, const char* p1) {
  bf16x4 a = __builtin_amdgcn_ds_read_tr16_b64_v4bf16((LDSP bf16x4*)p0);
  bf16x4 b = __builtin_amdgcn_ds_read_tr16_b64_v4bf16((LDSP bf16x4*)p1);
  return __builtin_shufflevector(a, b, 0, 1, 2, 3, 4, 5, 6, 7);
}
#define MFMA(a, b, c) __builtin_amdgcn_mfma_f32_32x32x16_bf16((a), (b), (c), 0, 0, 0)

DI void stage_tile(rsrc_t r, int voff, int soff, int rs, char* lds, int wid) {
#pragma unroll
  for (int i = 0; i < 2; ++i)
#pragma unroll
    for (int cb = 0; cb < 3; ++cb) dma16(r, lds + ((wid + 4 * i) * 3 + cb) * 1024, voff, soff + (wid + 4 * i) * 8 * rs + cb * 128);
}

__global__ __launch_bounds__(256, 2) void attn_kernel(const __bf16* xA, const __bf16* kA, const __bf16* vA, const __bf16* wqA,
                                                      const __bf16* xB, const __bf16* kB, const __bf16* vB, const __bf16* wqB, __bf16* Ows) {
  extern __shared__ __attribute__((aligned(16))) char lds[];
  const int tid = threadIdx.x, lane = tid & 63, wid = __builtin_amdgcn_readfirstlane(tid >> 6), r32 = lane & 31, hi = lane >> 5;
  const int id = blockIdx.x, xcd = id & 7, j = id >> 3;
  const int br = xcd >> 2, h = (xcd & 3) * 8 + (j >> 4), b = j & 15;
  const rsrc_t rx = mkrsrc((const char*)(br ? xB : xA) + (size_t)b * SQ * DM * 2, SQ * DM * 2);
  const rsrc_t rw = mkrsrc((const char*)(br ? wqB : wqA), DM * HD * 2);
  const rsrc_t rk = mkrsrc((const char*)(br ? kB : kA) + ((size_t)b * NH + h) * SK * DH * 2, SK * DH * 2);
  const rsrc_t rv = mkrsrc((const char*)(br ? vB : vA) + ((size_t)b * NH + h) * SK * DH * 2, SK * DH * 2);
  const int g16 = (lane >> 4) & 1, q4 = (lane & 15) >> 2, p4 = lane & 3, pperm = ((p4 & 1) << 1) | (p4 >> 1);
  const int l8 = lane >> 3, c8 = lane & 7;
  f32x16 acc[6] = {};
  {
    const int vw = l8 * (HD * 2) + 16 * (c8 ^ (((l8 >> 1) & 1) << 2));
    const int vx = l8 * (DM * 2) + 16 * (c8 ^ (l8 >> 1));
    auto stage = [&](int c, int buf) {
      stage_tile(rw, vw, c * 64 * HD * 2 + h * DH * 2, HD * 2, lds + buf * 40960, wid);
#pragma unroll
      for (int i = 0; i < 4; ++i) dma16(rx, lds + buf * 40960 + 24576 + wid * 4096 + i * 1024, vx ^ ((i & 1) * 64), (wid * 32 + i * 8) * (DM * 2) + c * 128);
    };
    stage(0, 0);
    for (int c = 0; c < 10; ++c) {
      __syncthreads();
      if (c + 1 < 10) stage(c + 1, (c + 1) & 1);
      const char* W = lds + (c & 1) * 40960; const char* X = W + 24576 + wid * 4096;
#pragma unroll
      for (int ks = 0; ks < 4; ++ks) {
        bf16x8 xb = ldsr128(X + r32 * 128 + 16 * ((2 * ks + hi) ^ ((r32 >> 1) & 7)));
#pragma unroll
        for (int t = 0; t < 6; ++t) {
          const char* a0 = W + (2 * ks + hi) * 3072 + (t >> 1) * 1024 + q4 * 128 + 64 * ((t & 1) ^ (q4 >> 1)) + 32 * g16 + 8 * pperm;
          bf16x8 a = trread2(a0, a0 + 512);
          acc[t] = MFMA(a, xb, acc[t]);
        }
      }
    }
  }
  bf16x8 qf[12];
#pragma unroll
  for (int t = 0; t < 6; ++t) { qf[2 * t] = pack8(acc[t], 0, 1.f); qf[2 * t + 1] = pack8(acc[t], 1, 1.f); }
  const float C = 0.07216878364870322f * 1.4426950408889634f;
  float m = -1e30f, l = 0.f;
#pragma unroll
  for (int t = 0; t < 6; ++t) acc[t] = f32x16{};
  const int vk = l8 * 384 + 16 * (c8 ^ (4 * (wid & 1) + (l8 >> 1)));
  const int vv = l8 * 384 + 16 * (c8 ^ (((l8 >> 1) & 1) << 2));
  __syncthreads();
  stage_tile(rk, vk, 0, 384, lds, wid);
  for (int t8 = 0; t8 < 8; ++t8) {
    __syncthreads();
    stage_tile(rv, vv, t8 * 24576, 384, lds + 2 * 24576, wid);
    if (t8 + 1 < 8) stage_tile(rk, vk, (t8 + 1) * 24576, 384, lds + ((t8 + 1) & 1) * 24576, wid);
    const char* K = lds + (t8 & 1) * 24576;
    f32x16 s0 = {}, s1 = {};
#pragma unroll
    for (int ks = 0; ks < 12; ++ks) {
      const int off = (r32 >> 3) * 3072 + (ks >> 2) * 1024 + (r32 & 7) * 128 + 16 * (((2 * ks + hi) & 7) ^ ((r32 >> 1) & 7));
      s0 = MFMA(ldsr128(K + off), qf[ks], s0);
      s1 = MFMA(ldsr128(K + off + 4 * 3072), qf[ks], s1);
    }
    float mx = s0[0];
#pragma unroll
    for (int i = 1; i < 16; ++i) mx = fmaxf(mx, s0[i]);
#pragma unroll
    for (int i = 0; i < 16; ++i) mx = fmaxf(mx, s1[i]);
    mx = fmaxf(mx, __shfl_xor(mx, 32));
    const float mn = fmaxf(m, mx), alpha = __builtin_amdgcn_exp2f((m - mn) * C); m = mn;
    const float nm = -mn * C; float ps = 0.f;
#pragma unroll
    for (int i = 0; i < 16; ++i) { s0[i] = __builtin_amdgcn_exp2f(fmaf(s0[i], C, nm)); s1[i] = __builtin_amdgcn_exp2f(fmaf(s1[i], C, nm)); ps += s0[i] + s1[i]; }
    l = l * alpha + ps;
#pragma unroll
    for (int t = 0; t < 6; ++t) acc[t] *= alpha;
    bf16x8 pf[4] = {pack8(s0, 0, 1.f), pack8(s0, 1, 1.f), pack8(s1, 0, 1.f), pack8(s1, 1, 1.f)};
    __syncthreads();
    const char* V = lds + 2 * 24576;
#pragma unroll
    for (int ks = 0; ks < 4; ++ks) {
#pragma unroll
      for (int t = 0; t < 6; ++t) {
        const char* a0 = V + (2 * ks) * 3072 + (t >> 1) * 1024 + (4 * hi + q4) * 128 + 64 * ((t & 1) ^ (q4 >> 1)) + 32 * g16 + 8 * pperm;
        bf16x8 a = trread2(a0, a0 + 3072);
        acc[t] = MFMA(a, pf[ks], acc[t]);
      }
    }
  }
  l += __shfl_xor(l, 32);
  const float il = 1.f / l;
  char* o = (char*)Ows + (((size_t)br * NB + b) * SQ + wid * 32 + r32) * HD * 2 + h * DH * 2 + hi * 16;
#pragma unroll
  for (int t = 0; t < 6; ++t) { *(bf16x8*)(o + t * 64) = pack8(acc[t], 0, il); *(bf16x8*)(o + t * 64 + 32) = pack8(acc[t], 1, il); }
}

__global__ __launch_bounds__(256, 2) void oproj_kernel(const __bf16* Ows, const __bf16* woA, const __bf16* woB, __bf16* out) {
  extern __shared__ __attribute__((aligned(16))) char lds[];
  const int tid = threadIdx.x, lane = tid & 63, wid = __builtin_amdgcn_readfirstlane(tid >> 6), r32 = lane & 31, hi = lane >> 5;
  const int nc = blockIdx.x % 5, mb = blockIdx.x / 5, br = mb >> 4, b = mb & 15;
  const rsrc_t ro = mkrsrc((const char*)Ows + (size_t)mb * SQ * HD * 2, SQ * HD * 2);
  const rsrc_t rw = mkrsrc((const char*)(br ? woB : woA), HD * DM * 2);
  const int g16 = (lane >> 4) & 1, q4 = (lane & 15) >> 2, p4 = lane & 3, pperm = ((p4 & 1) << 1) | (p4 >> 1);
  const int l8 = lane >> 3, c8 = lane & 7;
  const int vw = l8 * (DM * 2) + 16 * (c8 ^ (((l8 >> 1) & 1) << 2));
  const int vx = l8 * (HD * 2) + 16 * (c8 ^ (l8 >> 1));
  f32x16 acc[4] = {};
  auto stage = [&](int c, int buf) {
    char* L = lds + buf * 32768;
#pragma unroll
    for (int i = 0; i < 2; ++i)
#pragma unroll
      for (int cb = 0; cb < 2; ++cb) dma16(rw, L + ((wid + 4 * i) * 2 + cb) * 1024, vw, (c * 64 + (wid + 4 * i) * 8) * (DM * 2) + nc * 256 + cb * 128);
#pragma unroll
    for (int i = 0; i < 4; ++i) dma16(ro, L + 16384 + wid * 4096 + i * 1024, vx ^ ((i & 1) * 64), (wid * 32 + i * 8) * (HD * 2) + c * 128);
  };
  stage(0, 0);
  for (int c = 0; c < 96; ++c) {
    __syncthreads();
    if (c + 1 < 96) stage(c + 1, (c + 1) & 1);
    const char* W = lds + (c & 1) * 32768; const char* X = W + 16384 + wid * 4096;
#pragma unroll
    for (int ks = 0; ks < 4; ++ks) {
      bf16x8 xb = ldsr128(X + r32 * 128 + 16 * ((2 * ks + hi) ^ ((r32 >> 1) & 7)));
#pragma unroll
      for (int t = 0; t < 4; ++t) {
        const char* a0 = W + (2 * ks + hi) * 2048 + (t >> 1) * 1024 + q4 * 128 + 64 * ((t & 1) ^ (q4 >> 1)) + 32 * g16 + 8 * pperm;
        bf16x8 a = trread2(a0, a0 + 512);
        acc[t] = MFMA(a, xb, acc[t]);
      }
    }
  }
  char* op = (char*)out + (((size_t)b * SQ + wid * 32 + r32) * (2 * DM) + br * DM + nc * 128) * 2 + hi * 16;
#pragma unroll
  for (int t = 0; t < 4; ++t) { *(bf16x8*)(op + t * 64) = pack8(acc[t], 0, 1.f); *(bf16x8*)(op + t * 64 + 32) = pack8(acc[t], 1, 1.f); }
}

extern "C" void kernel_launch(void* const* d_in, const int* in_sizes, int n_in, void* d_out, int out_size, void* d_ws, size_t ws_size, hipStream_t stream) {
  (void)hipFuncSetAttribute((const void*)attn_kernel, hipFuncAttributeMaxDynamicSharedMemorySize, 81920);
  (void)hipFuncSetAttribute((const void*)oproj_kernel, hipFuncAttributeMaxDynamicSharedMemorySize, 65536);
  attn_kernel<<<1024, 256, 81920, stream>>>((const __bf16*)d_in[0], (const __bf16*)d_in[1], (const __bf16*)d_in[2], (const __bf16*)d_in[3],
                                            (const __bf16*)d_in[5], (const __bf16*)d_in[6], (const __bf16*)d_in[7], (const __bf16*)d_in[8], (__bf16*)d_ws);
  oproj_kernel<<<160, 256, 65536, stream>>>((const __bf16*)d_ws, (const __bf16*)d_in[4], (const __bf16*)d_in[9], (__bf16*)d_out);
}
```

```cpp
#include <hip/hip_runtime.h>
#include <hip/hip_bf16.h>
#include <stdint.h>

typedef __amdgpu_buffer_rsrc_t rsrc_t;
#define DI __device__ __forceinline__
constexpr int NB = 16, SQ = 128, DM = 640, NH = 32, SK = 512, DH = 192, HD = NH * DH;
constexpr int LDS_BYTES = 163840;
constexpr unsigned CNT_BYTES = 32 * 8192, GRP_BYTES = 8 * 114688 + 64 * 4096;

extern "C" __device__ unsigned long long dispatch_id_() __asm("llvm.amdgcn.dispatch.id");
DI rsrc_t mkrsrc(const void* p, unsigned bytes) { return __builtin_amdgcn_make_buffer_rsrc((void*)p, (short)0, (int)bytes, 0x00020000); }

__global__ __launch_bounds__(256, 1) void fused_kernel(const char* xA, const char* kA, const char* vA, const char* wqA, const char* woA,
                                                       const char* xB, const char* kB, const char* vB, const char* wqB, const char* woB, char* ws, char* out) {
  char* part = ws + CNT_BYTES;
  const unsigned l = threadIdx.x & 63, wid = __builtin_amdgcn_readfirstlane(threadIdx.x >> 6);
  const unsigned id = blockIdx.x, xcd = id & 7, j = id >> 3;
  const unsigned br = xcd >> 2, hg = (xcd & 3) * 2 + (j >> 4), b = j & 15;
  const rsrc_t srdX = mkrsrc((br ? xB : xA) + (size_t)b * SQ * DM * 2, SQ * DM * 2);
  const rsrc_t srdW = mkrsrc((br ? wqB : wqA) + hg * 4 * DH * 2, DM * HD * 2 - hg * 4 * DH * 2);
  const rsrc_t srdK = mkrsrc((br ? kB : kA) + ((size_t)b * NH + hg * 4) * SK * DH * 2, 4 * SK * DH * 2);
  const rsrc_t srdV = mkrsrc((br ? vB : vA) + ((size_t)b * NH + hg * 4) * SK * DH * 2, 4 * SK * DH * 2);
  const rsrc_t srdWo = mkrsrc((br ? woB : woA) + (size_t)hg * 4 * DH * DM * 2, 4 * DH * DM * 2);
  const rsrc_t srdG = mkrsrc(part + (size_t)(br * NB + b) * GRP_BYTES, GRP_BYTES);
  const rsrc_t srdC = mkrsrc(ws + (br * NB + b) * 8192 + wid * 256, 8192 - wid * 256);
  const rsrc_t srdO = mkrsrc(out + (size_t)b * SQ * 2 * DM * 2 + br * DM * 2, SQ * 2 * DM * 2 - br * DM * 2);
  const unsigned bq = b * 4 + wid, cq = j * 4 + wid;
  const unsigned u48 = bq < 48 ? bq : 47, pw2 = (64 + u48 / 3) * 8 * 12288 + (u48 % 3) * 128, po2 = (64 + (bq >> 1)) * 8 * 1280 + (bq & 1) * 128;
  const unsigned tag = (unsigned)dispatch_id_() * 2654435761u + (unsigned)((size_t)__builtin_amdgcn_dispatch_ptr() >> 6);
  const unsigned c16 = l & 15, g = l >> 4, q4 = c16 >> 2, p4 = l & 3, l8 = l >> 3, c8 = l & 7, w1 = wid & 1;
  auto sK = [](unsigned r) { unsigned r1 = (r >> 1) & 1, r2 = (r >> 2) & 1, r3 = (r >> 3) & 1; return (r2 ^ r3) | (r1 << 1) | (r3 << 2); };
  const unsigned pi = ((g & 1) << 1) | (g >> 1);
  unsigned pK = (c16 >> 3) * 3072 + (c16 & 7) * 128 + 16 * (pi ^ sK(c16));
  const unsigned r8v = 4 * (g & 1) + q4;
  unsigned pV = (g >> 1) * 3072 + r8v * 128 + 32 * ((r8v >> 1) & 3) + 8 * p4;
  unsigned pWo = (g >> 1) * 1024 + r8v * 128 + 32 * ((r8v >> 1) & 3) + 8 * p4;
  unsigned pW = g * 3072 + q4 * 128 + 32 * ((q4 >> 1) + 2 * (g & 1)) + 8 * p4;
  unsigned pX0 = 122880 + wid * 4096 + (c16 >> 3) * 1024 + (c16 & 7) * 128 + 16 * (g ^ ((c16 >> 1) & 7));
  unsigned pX1 = pX0 ^ 64;
  unsigned vK = l8 * 384 + 16 * (c8 ^ sK(8 * w1 + l8));
  unsigned vV = l8 * 384 + 16 * (c8 ^ (2 * ((l8 >> 1) & 3)));
  unsigned vWo = l8 * 1280 + 16 * (c8 ^ (2 * ((l8 >> 1) & 3)));
  unsigned vW = l8 * 12288 + 16 * (c8 ^ (2 * (((l8 >> 1) & 1) + 2 * w1)));
  unsigned vX0 = l8 * 1280 + 16 * (c8 ^ (l8 >> 1));
  unsigned vX1 = vX0 ^ 64;
  unsigned vP = wid * 1024 + l * 16;
  asm volatile(
R"(v_bfe_u32 v6, %[vP], 4, 4
v_lshlrev_b32 v7, 3, v6
v_lshlrev_b32 v6, 2, v6
s_lshl_b32 s79, %[hgi], 10
buffer_load_dword v4, v6, %[srdC], s79 offen sc1
s_mov_b32 s48, 0
s_mov_b32 s49, 24576
s_mov_b32 s50, 49152
s_mov_b32 s51, 73728
s_mov_b32 s52, 98304
s_mov_b32 s53, 122880
s_mul_i32 s75, %[wid], 3072
s_mul_i32 s76, %[wid], 6144
s_add_u32 s54, s48, s75
s_add_u32 s60, s48, s76
s_add_u32 s55, s49, s75
s_add_u32 s61, s49, s76
s_add_u32 s56, s50, s75
s_add_u32 s62, s50, s76
s_add_u32 s57, s51, s75
s_add_u32 s63, s51, s76
s_add_u32 s58, s52, s75
s_add_u32 s64, s52, s76
s_add_u32 s59, s53, s75
)"
R"(s_add_u32 s65, s53, s76
s_mov_b32 s67, s75
s_mov_b32 s68, s76
s_mul_i32 s66, %[wid], 98304
s_mul_i32 s69, %[wid], 40960
s_add_u32 s70, s69, 10240
s_add_u32 s71, s69, 20480
s_add_u32 s72, s69, 30720
s_mul_i32 s73, %[wid], 4096
s_add_u32 s73, s73, 122880
s_mul_i32 s78, %[wid], 61440
s_mov_b32 s74, 0
s_mov_b32 s77, 0x41000000
s_mov_b32 s80, 0x3dd53b95
s_mov_b32 s81, 0x3dd53b95
s_mul_i32 s82, %[bq], 98304
s_mul_i32 s83, %[bq], 10240
s_mul_i32 s45, %[hgi], 114688
s_lshl_b32 s84, %[wid], 11
s_add_u32 s84, s84, 155648
s_mul_i32 s85, %[bq], 122880
s_mov_b32 m0, s84
s_nop 0
v_bfe_u32 v0, %[vP], 4, 6
)"
R"(v_min_u32 v0, 29, v0
v_mul_u32_u24 v1, 43, v0
v_lshrrev_b32 v1, 7, v1
v_lshlrev_b32 v0, 7, v0
v_mul_u32_u24 v1, 11904, v1
v_add_u32 v0, v0, v1
buffer_load_dword v0, %[srdW], s85 offen offset:0 lds
s_bfe_u32 s79, %[bq], 131074
STG_%=:
s_cmp_eq_u32 s79, 0
s_cbranch_scc1 STE_%=
s_sleep 20
s_sub_u32 s79, s79, 1
s_branch STG_%=
STE_%=:
s_add_u32 s75, s66, 0
s_add_u32 m0, s54, 0
s_nop 0
buffer_load_dwordx4 %[vW], %[srdW], s75 offen offset:0 lds
s_add_u32 m0, s54, 896
s_nop 0
buffer_load_dwordx4 %[vW], %[srdW], s75 offen offset:128 lds
s_add_u32 m0, s54, 1792
s_nop 0
)"
R"(buffer_load_dwordx4 %[vW], %[srdW], s75 offen offset:256 lds
s_add_u32 s75, s66, 393216
s_add_u32 m0, s54, 12288
s_nop 0
buffer_load_dwordx4 %[vW], %[srdW], s75 offen offset:0 lds
s_add_u32 m0, s54, 13184
s_nop 0
buffer_load_dwordx4 %[vW], %[srdW], s75 offen offset:128 lds
s_add_u32 m0, s54, 14080
s_nop 0
buffer_load_dwordx4 %[vW], %[srdW], s75 offen offset:256 lds
s_add_u32 m0, s73, 0
s_nop 0
buffer_load_dwordx4 %[vX0], %[srdX], s69 offen offset:0 lds
s_add_u32 m0, s73, 1024
s_nop 0
buffer_load_dwordx4 %[vX1], %[srdX], s70 offen offset:0 lds
s_add_u32 m0, s73, 2048
s_nop 0
buffer_load_dwordx4 %[vX0], %[srdX], s71 offen offset:0 lds
s_add_u32 m0, s73, 3072
s_nop 0
buffer_load_dwordx4 %[vX1], %[srdX], s72 offen offset:0 lds
s_add_u32 s75, s66, 786432
)"
R"(s_add_u32 m0, s55, 0
s_nop 0
buffer_load_dwordx4 %[vW], %[srdW], s75 offen offset:0 lds
s_add_u32 m0, s55, 896
s_nop 0
buffer_load_dwordx4 %[vW], %[srdW], s75 offen offset:128 lds
s_add_u32 m0, s55, 1792
s_nop 0
buffer_load_dwordx4 %[vW], %[srdW], s75 offen offset:256 lds
s_add_u32 s75, s66, 1179648
s_add_u32 m0, s55, 12288
s_nop 0
buffer_load_dwordx4 %[vW], %[srdW], s75 offen offset:0 lds
s_add_u32 m0, s55, 13184
s_nop 0
buffer_load_dwordx4 %[vW], %[srdW], s75 offen offset:128 lds
s_add_u32 m0, s55, 14080
s_nop 0
buffer_load_dwordx4 %[vW], %[srdW], s75 offen offset:256 lds
s_add_u32 m0, s73, 16256
s_nop 0
buffer_load_dwordx4 %[vX0], %[srdX], s69 offen offset:128 lds
s_add_u32 m0, s73, 17280
s_nop 0
)"
R"(buffer_load_dwordx4 %[vX1], %[srdX], s70 offen offset:128 lds
s_add_u32 m0, s73, 18304
s_nop 0
buffer_load_dwordx4 %[vX0], %[srdX], s71 offen offset:128 lds
s_add_u32 m0, s73, 19328
s_nop 0
buffer_load_dwordx4 %[vX1], %[srdX], s72 offen offset:128 lds
s_mov_b32 m0, s84
s_nop 0
buffer_load_dword v0, %[srdW], s85 offen offset:384 lds
s_getpc_b64 s[92:93]
PCREF_%=:
s_and_b32 s93, s93, 0xffff
s_mov_b32 s94, END_%=-PCREF_%=
s_mov_b32 s95, 0x20000
v_bfe_u32 v237, %[vP], 4, 3
v_lshlrev_b32 v237, 7, v237
s_lshl_b32 s79, %[cq], 10
buffer_load_dword v237, s[92:95], s79 offen offset:0 lds
s_add_u32 s75, s66, 1572864
s_add_u32 m0, s56, 0
s_nop 0
buffer_load_dwordx4 %[vW], %[srdW], s75 offen offset:0 lds
s_add_u32 m0, s56, 896
)"
R"(s_nop 0
buffer_load_dwordx4 %[vW], %[srdW], s75 offen offset:128 lds
s_add_u32 m0, s56, 1792
s_nop 0
buffer_load_dwordx4 %[vW], %[srdW], s75 offen offset:256 lds
s_add_u32 s75, s66, 1966080
s_add_u32 m0, s56, 12288
s_nop 0
buffer_load_dwordx4 %[vW], %[srdW], s75 offen offset:0 lds
s_add_u32 m0, s56, 13184
s_nop 0
buffer_load_dwordx4 %[vW], %[srdW], s75 offen offset:128 lds
s_add_u32 m0, s56, 14080
s_nop 0
buffer_load_dwordx4 %[vW], %[srdW], s75 offen offset:256 lds
s_add_u32 s75, s66, 2359296
s_add_u32 m0, s57, 0
s_nop 0
buffer_load_dwordx4 %[vW], %[srdW], s75 offen offset:0 lds
s_add_u32 m0, s57, 896
s_nop 0
buffer_load_dwordx4 %[vW], %[srdW], s75 offen offset:128 lds
s_add_u32 m0, s57, 1792
s_nop 0
)"
R"(buffer_load_dwordx4 %[vW], %[srdW], s75 offen offset:256 lds
s_add_u32 s75, s66, 2752512
s_add_u32 m0, s57, 12288
s_nop 0
buffer_load_dwordx4 %[vW], %[srdW], s75 offen offset:0 lds
s_add_u32 m0, s57, 13184
s_nop 0
buffer_load_dwordx4 %[vW], %[srdW], s75 offen offset:128 lds
s_add_u32 m0, s57, 14080
s_nop 0
buffer_load_dwordx4 %[vW], %[srdW], s75 offen offset:256 lds
v_mov_b32 v236, 0x3f803f80
v_accvgpr_write_b32 a128, v236
v_accvgpr_write_b32 a129, v236
v_accvgpr_write_b32 a130, v236
v_accvgpr_write_b32 a131, v236
v_add_u32 v232, s48, %[pW]
v_xor_b32 v233, 32, v232
v_xor_b32 v234, 64, v232
v_xor_b32 v235, 96, v232
s_waitcnt vmcnt(24)
s_barrier
v_add_u32 v4, 1, v4
v_mov_b32 v5, %[tag]
)"
R"(s_lshl_b32 s79, %[hgi], 10
buffer_store_dwordx2 v[4:5], v7, %[srdC], s79 offen offset:128 sc1
ds_read_b128 v[96:99], %[pX0] offset:0
ds_read_b128 v[100:103], %[pX0] offset:2048
ds_read_b128 v[104:107], %[pX1] offset:0
ds_read_b128 v[108:111], %[pX1] offset:2048
ds_read_b64_tr_b16 a[96:97], v232 offset:0
ds_read_b64_tr_b16 a[98:99], v232 offset:512
ds_read_b64_tr_b16 a[100:101], v232 offset:12288
ds_read_b64_tr_b16 a[102:103], v232 offset:12800
ds_read_b64_tr_b16 a[104:105], v233 offset:0
ds_read_b64_tr_b16 a[106:107], v233 offset:512
ds_read_b64_tr_b16 a[108:109], v233 offset:12288
ds_read_b64_tr_b16 a[110:111], v233 offset:12800
ds_read_b64_tr_b16 a[112:113], v234 offset:0
ds_read_b64_tr_b16 a[114:115], v234 offset:512
ds_read_b64_tr_b16 a[116:117], v234 offset:12288
ds_read_b64_tr_b16 a[118:119], v234 offset:12800
s_waitcnt lgkmcnt(4)
v_mfma_f32_16x16x32_bf16 v[0:3], a[96:99], v[96:99], 0
ds_read_b64_tr_b16 a[120:121], v235 offset:0
ds_read_b64_tr_b16 a[122:123], v235 offset:512
v_mfma_f32_16x16x32_bf16 v[0:3], a[100:103], v[104:107], v[0:3]
v_mfma_f32_16x16x32_bf16 v[4:7], a[96:99], v[100:103], 0
)"
R"(ds_read_b64_tr_b16 a[124:125], v235 offset:12288
ds_read_b64_tr_b16 a[126:127], v235 offset:12800
v_mfma_f32_16x16x32_bf16 v[4:7], a[100:103], v[108:111], v[4:7]
v_mfma_f32_16x16x32_bf16 v[8:11], a[104:107], v[96:99], 0
ds_read_b64_tr_b16 a[96:97], v232 offset:1024
ds_read_b64_tr_b16 a[98:99], v232 offset:1536
v_mfma_f32_16x16x32_bf16 v[8:11], a[108:111], v[104:107], v[8:11]
v_mfma_f32_16x16x32_bf16 v[12:15], a[104:107], v[100:103], 0
s_add_u32 m0, s73, -256
ds_read_b64_tr_b16 a[100:101], v232 offset:13312
ds_read_b64_tr_b16 a[102:103], v232 offset:13824
v_mfma_f32_16x16x32_bf16 v[12:15], a[108:111], v[108:111], v[12:15]
s_waitcnt lgkmcnt(4)
buffer_load_dwordx4 %[vX0], %[srdX], s69 offen offset:256 lds
v_mfma_f32_16x16x32_bf16 v[16:19], a[112:115], v[96:99], 0
ds_read_b64_tr_b16 a[104:105], v233 offset:1024
ds_read_b64_tr_b16 a[106:107], v233 offset:1536
v_mfma_f32_16x16x32_bf16 v[16:19], a[116:119], v[104:107], v[16:19]
v_mfma_f32_16x16x32_bf16 v[20:23], a[112:115], v[100:103], 0
s_add_u32 m0, s73, 768
ds_read_b64_tr_b16 a[108:109], v233 offset:13312
ds_read_b64_tr_b16 a[110:111], v233 offset:13824
v_mfma_f32_16x16x32_bf16 v[20:23], a[116:119], v[108:111], v[20:23]
buffer_load_dwordx4 %[vX1], %[srdX], s70 offen offset:256 lds
)"
R"(v_mfma_f32_16x16x32_bf16 v[24:27], a[120:123], v[96:99], 0
ds_read_b64_tr_b16 a[112:113], v234 offset:1024
ds_read_b64_tr_b16 a[114:115], v234 offset:1536
v_mfma_f32_16x16x32_bf16 v[24:27], a[124:127], v[104:107], v[24:27]
v_mfma_f32_16x16x32_bf16 v[28:31], a[120:123], v[100:103], 0
s_add_u32 m0, s73, 1792
ds_read_b64_tr_b16 a[116:117], v234 offset:13312
ds_read_b64_tr_b16 a[118:119], v234 offset:13824
v_mfma_f32_16x16x32_bf16 v[28:31], a[124:127], v[108:111], v[28:31]
s_waitcnt lgkmcnt(4)
buffer_load_dwordx4 %[vX0], %[srdX], s71 offen offset:256 lds
v_mfma_f32_16x16x32_bf16 v[32:35], a[96:99], v[96:99], 0
ds_read_b64_tr_b16 a[120:121], v235 offset:1024
ds_read_b64_tr_b16 a[122:123], v235 offset:1536
v_mfma_f32_16x16x32_bf16 v[32:35], a[100:103], v[104:107], v[32:35]
v_mfma_f32_16x16x32_bf16 v[36:39], a[96:99], v[100:103], 0
s_add_u32 m0, s73, 2816
ds_read_b64_tr_b16 a[124:125], v235 offset:13312
ds_read_b64_tr_b16 a[126:127], v235 offset:13824
v_mfma_f32_16x16x32_bf16 v[36:39], a[100:103], v[108:111], v[36:39]
buffer_load_dwordx4 %[vX1], %[srdX], s72 offen offset:256 lds
v_mfma_f32_16x16x32_bf16 v[40:43], a[104:107], v[96:99], 0
ds_read_b64_tr_b16 a[96:97], v232 offset:2048
ds_read_b64_tr_b16 a[98:99], v232 offset:2560
)"
R"(v_mfma_f32_16x16x32_bf16 v[40:43], a[108:111], v[104:107], v[40:43]
v_mfma_f32_16x16x32_bf16 v[44:47], a[104:107], v[100:103], 0
ds_read_b64_tr_b16 a[100:101], v232 offset:14336
ds_read_b64_tr_b16 a[102:103], v232 offset:14848
v_mfma_f32_16x16x32_bf16 v[44:47], a[108:111], v[108:111], v[44:47]
s_waitcnt lgkmcnt(4)
v_mfma_f32_16x16x32_bf16 v[48:51], a[112:115], v[96:99], 0
ds_read_b64_tr_b16 a[104:105], v233 offset:2048
ds_read_b64_tr_b16 a[106:107], v233 offset:2560
v_mfma_f32_16x16x32_bf16 v[48:51], a[116:119], v[104:107], v[48:51]
v_mfma_f32_16x16x32_bf16 v[52:55], a[112:115], v[100:103], 0
ds_read_b64_tr_b16 a[108:109], v233 offset:14336
ds_read_b64_tr_b16 a[110:111], v233 offset:14848
v_mfma_f32_16x16x32_bf16 v[52:55], a[116:119], v[108:111], v[52:55]
v_mfma_f32_16x16x32_bf16 v[56:59], a[120:123], v[96:99], 0
ds_read_b64_tr_b16 a[112:113], v234 offset:2048
ds_read_b64_tr_b16 a[114:115], v234 offset:2560
v_mfma_f32_16x16x32_bf16 v[56:59], a[124:127], v[104:107], v[56:59]
v_mfma_f32_16x16x32_bf16 v[60:63], a[120:123], v[100:103], 0
ds_read_b64_tr_b16 a[116:117], v234 offset:14336
ds_read_b64_tr_b16 a[118:119], v234 offset:14848
v_mfma_f32_16x16x32_bf16 v[60:63], a[124:127], v[108:111], v[60:63]
s_waitcnt lgkmcnt(4)
s_waitcnt vmcnt(19)
)"
R"(s_barrier
v_mfma_f32_16x16x32_bf16 v[64:67], a[96:99], v[96:99], 0
ds_read_b64_tr_b16 a[120:121], v235 offset:2048
ds_read_b64_tr_b16 a[122:123], v235 offset:2560
v_mfma_f32_16x16x32_bf16 v[64:67], a[100:103], v[104:107], v[64:67]
ds_read_b128 v[128:131], %[pX0] offset:16384
ds_read_b128 v[132:135], %[pX0] offset:18432
v_mfma_f32_16x16x32_bf16 v[68:71], a[96:99], v[100:103], 0
ds_read_b64_tr_b16 a[124:125], v235 offset:14336
ds_read_b64_tr_b16 a[126:127], v235 offset:14848
v_mfma_f32_16x16x32_bf16 v[68:71], a[100:103], v[108:111], v[68:71]
ds_read_b128 v[136:139], %[pX1] offset:16384
ds_read_b128 v[140:143], %[pX1] offset:18432
v_add_u32 v232, s49, %[pW]
v_xor_b32 v233, 32, v232
v_mfma_f32_16x16x32_bf16 v[72:75], a[104:107], v[96:99], 0
ds_read_b64_tr_b16 a[96:97], v232 offset:0
ds_read_b64_tr_b16 a[98:99], v232 offset:512
v_mfma_f32_16x16x32_bf16 v[72:75], a[108:111], v[104:107], v[72:75]
s_add_u32 s75, s66, 3145728
v_xor_b32 v234, 64, v232
v_xor_b32 v235, 96, v232
v_mfma_f32_16x16x32_bf16 v[76:79], a[104:107], v[100:103], 0
s_add_u32 m0, s58, 0
)"
R"(ds_read_b64_tr_b16 a[100:101], v232 offset:12288
ds_read_b64_tr_b16 a[102:103], v232 offset:12800
v_mfma_f32_16x16x32_bf16 v[76:79], a[108:111], v[108:111], v[76:79]
s_waitcnt lgkmcnt(6)
buffer_load_dwordx4 %[vW], %[srdW], s75 offen offset:0 lds
v_mfma_f32_16x16x32_bf16 v[80:83], a[112:115], v[96:99], 0
ds_read_b64_tr_b16 a[104:105], v233 offset:0
ds_read_b64_tr_b16 a[106:107], v233 offset:512
v_mfma_f32_16x16x32_bf16 v[80:83], a[116:119], v[104:107], v[80:83]
v_mfma_f32_16x16x32_bf16 v[84:87], a[112:115], v[100:103], 0
s_add_u32 m0, s58, 896
ds_read_b64_tr_b16 a[108:109], v233 offset:12288
ds_read_b64_tr_b16 a[110:111], v233 offset:12800
v_mfma_f32_16x16x32_bf16 v[84:87], a[116:119], v[108:111], v[84:87]
buffer_load_dwordx4 %[vW], %[srdW], s75 offen offset:128 lds
v_mfma_f32_16x16x32_bf16 v[88:91], a[120:123], v[96:99], 0
ds_read_b64_tr_b16 a[112:113], v234 offset:0
ds_read_b64_tr_b16 a[114:115], v234 offset:512
v_mfma_f32_16x16x32_bf16 v[88:91], a[124:127], v[104:107], v[88:91]
v_mfma_f32_16x16x32_bf16 v[92:95], a[120:123], v[100:103], 0
s_add_u32 m0, s58, 1792
ds_read_b64_tr_b16 a[116:117], v234 offset:12288
ds_read_b64_tr_b16 a[118:119], v234 offset:12800
v_mfma_f32_16x16x32_bf16 v[92:95], a[124:127], v[108:111], v[92:95]
)"
R"(s_waitcnt lgkmcnt(4)
buffer_load_dwordx4 %[vW], %[srdW], s75 offen offset:256 lds
v_mfma_f32_16x16x32_bf16 v[0:3], a[96:99], v[128:131], v[0:3]
ds_read_b64_tr_b16 a[120:121], v235 offset:0
ds_read_b64_tr_b16 a[122:123], v235 offset:512
v_mfma_f32_16x16x32_bf16 v[0:3], a[100:103], v[136:139], v[0:3]
s_add_u32 s75, s66, 3538944
v_mfma_f32_16x16x32_bf16 v[4:7], a[96:99], v[132:135], v[4:7]
s_add_u32 m0, s58, 12288
ds_read_b64_tr_b16 a[124:125], v235 offset:12288
ds_read_b64_tr_b16 a[126:127], v235 offset:12800
v_mfma_f32_16x16x32_bf16 v[4:7], a[100:103], v[140:143], v[4:7]
buffer_load_dwordx4 %[vW], %[srdW], s75 offen offset:0 lds
v_mfma_f32_16x16x32_bf16 v[8:11], a[104:107], v[128:131], v[8:11]
ds_read_b64_tr_b16 a[96:97], v232 offset:1024
ds_read_b64_tr_b16 a[98:99], v232 offset:1536
v_mfma_f32_16x16x32_bf16 v[8:11], a[108:111], v[136:139], v[8:11]
v_mfma_f32_16x16x32_bf16 v[12:15], a[104:107], v[132:135], v[12:15]
s_add_u32 m0, s58, 13184
ds_read_b64_tr_b16 a[100:101], v232 offset:13312
ds_read_b64_tr_b16 a[102:103], v232 offset:13824
v_mfma_f32_16x16x32_bf16 v[12:15], a[108:111], v[140:143], v[12:15]
s_waitcnt lgkmcnt(4)
buffer_load_dwordx4 %[vW], %[srdW], s75 offen offset:128 lds
)"
R"(v_mfma_f32_16x16x32_bf16 v[16:19], a[112:115], v[128:131], v[16:19]
ds_read_b64_tr_b16 a[104:105], v233 offset:1024
ds_read_b64_tr_b16 a[106:107], v233 offset:1536
v_mfma_f32_16x16x32_bf16 v[16:19], a[116:119], v[136:139], v[16:19]
v_mfma_f32_16x16x32_bf16 v[20:23], a[112:115], v[132:135], v[20:23]
s_add_u32 m0, s58, 14080
ds_read_b64_tr_b16 a[108:109], v233 offset:13312
ds_read_b64_tr_b16 a[110:111], v233 offset:13824
v_mfma_f32_16x16x32_bf16 v[20:23], a[116:119], v[140:143], v[20:23]
buffer_load_dwordx4 %[vW], %[srdW], s75 offen offset:256 lds
v_mfma_f32_16x16x32_bf16 v[24:27], a[120:123], v[128:131], v[24:27]
ds_read_b64_tr_b16 a[112:113], v234 offset:1024
ds_read_b64_tr_b16 a[114:115], v234 offset:1536
v_mfma_f32_16x16x32_bf16 v[24:27], a[124:127], v[136:139], v[24:27]
v_mfma_f32_16x16x32_bf16 v[28:31], a[120:123], v[132:135], v[28:31]
s_add_u32 m0, s73, 16000
ds_read_b64_tr_b16 a[116:117], v234 offset:13312
ds_read_b64_tr_b16 a[118:119], v234 offset:13824
v_mfma_f32_16x16x32_bf16 v[28:31], a[124:127], v[140:143], v[28:31]
s_waitcnt lgkmcnt(4)
buffer_load_dwordx4 %[vX0], %[srdX], s69 offen offset:384 lds
v_mfma_f32_16x16x32_bf16 v[32:35], a[96:99], v[128:131], v[32:35]
ds_read_b64_tr_b16 a[120:121], v235 offset:1024
ds_read_b64_tr_b16 a[122:123], v235 offset:1536
)"
R"(v_mfma_f32_16x16x32_bf16 v[32:35], a[100:103], v[136:139], v[32:35]
v_mfma_f32_16x16x32_bf16 v[36:39], a[96:99], v[132:135], v[36:39]
s_add_u32 m0, s73, 17024
ds_read_b64_tr_b16 a[124:125], v235 offset:13312
ds_read_b64_tr_b16 a[126:127], v235 offset:13824
v_mfma_f32_16x16x32_bf16 v[36:39], a[100:103], v[140:143], v[36:39]
buffer_load_dwordx4 %[vX1], %[srdX], s70 offen offset:384 lds
v_mfma_f32_16x16x32_bf16 v[40:43], a[104:107], v[128:131], v[40:43]
ds_read_b64_tr_b16 a[96:97], v232 offset:2048
ds_read_b64_tr_b16 a[98:99], v232 offset:2560
v_mfma_f32_16x16x32_bf16 v[40:43], a[108:111], v[136:139], v[40:43]
v_mfma_f32_16x16x32_bf16 v[44:47], a[104:107], v[132:135], v[44:47]
s_add_u32 m0, s73, 18048
ds_read_b64_tr_b16 a[100:101], v232 offset:14336
ds_read_b64_tr_b16 a[102:103], v232 offset:14848
v_mfma_f32_16x16x32_bf16 v[44:47], a[108:111], v[140:143], v[44:47]
s_waitcnt lgkmcnt(4)
buffer_load_dwordx4 %[vX0], %[srdX], s71 offen offset:384 lds
v_mfma_f32_16x16x32_bf16 v[48:51], a[112:115], v[128:131], v[48:51]
ds_read_b64_tr_b16 a[104:105], v233 offset:2048
ds_read_b64_tr_b16 a[106:107], v233 offset:2560
v_mfma_f32_16x16x32_bf16 v[48:51], a[116:119], v[136:139], v[48:51]
v_mfma_f32_16x16x32_bf16 v[52:55], a[112:115], v[132:135], v[52:55]
s_add_u32 m0, s73, 19072
)"
R"(ds_read_b64_tr_b16 a[108:109], v233 offset:14336
ds_read_b64_tr_b16 a[110:111], v233 offset:14848
v_mfma_f32_16x16x32_bf16 v[52:55], a[116:119], v[140:143], v[52:55]
buffer_load_dwordx4 %[vX1], %[srdX], s72 offen offset:384 lds
v_mfma_f32_16x16x32_bf16 v[56:59], a[120:123], v[128:131], v[56:59]
ds_read_b64_tr_b16 a[112:113], v234 offset:2048
ds_read_b64_tr_b16 a[114:115], v234 offset:2560
v_mfma_f32_16x16x32_bf16 v[56:59], a[124:127], v[136:139], v[56:59]
v_mfma_f32_16x16x32_bf16 v[60:63], a[120:123], v[132:135], v[60:63]
ds_read_b64_tr_b16 a[116:117], v234 offset:14336
ds_read_b64_tr_b16 a[118:119], v234 offset:14848
v_mfma_f32_16x16x32_bf16 v[60:63], a[124:127], v[140:143], v[60:63]
s_waitcnt lgkmcnt(4)
s_waitcnt vmcnt(10)
s_barrier
v_mfma_f32_16x16x32_bf16 v[64:67], a[96:99], v[128:131], v[64:67]
ds_read_b64_tr_b16 a[120:121], v235 offset:2048
ds_read_b64_tr_b16 a[122:123], v235 offset:2560
v_mfma_f32_16x16x32_bf16 v[64:67], a[100:103], v[136:139], v[64:67]
ds_read_b128 v[96:99], %[pX0] offset:0
ds_read_b128 v[100:103], %[pX0] offset:2048
v_mfma_f32_16x16x32_bf16 v[68:71], a[96:99], v[132:135], v[68:71]
ds_read_b64_tr_b16 a[124:125], v235 offset:14336
ds_read_b64_tr_b16 a[126:127], v235 offset:14848
)"
R"(v_mfma_f32_16x16x32_bf16 v[68:71], a[100:103], v[140:143], v[68:71]
ds_read_b128 v[104:107], %[pX1] offset:0
ds_read_b128 v[108:111], %[pX1] offset:2048
v_add_u32 v232, s50, %[pW]
v_xor_b32 v233, 32, v232
v_mfma_f32_16x16x32_bf16 v[72:75], a[104:107], v[128:131], v[72:75]
ds_read_b64_tr_b16 a[96:97], v232 offset:0
ds_read_b64_tr_b16 a[98:99], v232 offset:512
v_mfma_f32_16x16x32_bf16 v[72:75], a[108:111], v[136:139], v[72:75]
s_add_u32 s75, s66, 3932160
v_xor_b32 v234, 64, v232
v_xor_b32 v235, 96, v232
v_mfma_f32_16x16x32_bf16 v[76:79], a[104:107], v[132:135], v[76:79]
s_add_u32 m0, s54, 0
ds_read_b64_tr_b16 a[100:101], v232 offset:12288
ds_read_b64_tr_b16 a[102:103], v232 offset:12800
v_mfma_f32_16x16x32_bf16 v[76:79], a[108:111], v[140:143], v[76:79]
s_waitcnt lgkmcnt(6)
buffer_load_dwordx4 %[vW], %[srdW], s75 offen offset:0 lds
v_mfma_f32_16x16x32_bf16 v[80:83], a[112:115], v[128:131], v[80:83]
ds_read_b64_tr_b16 a[104:105], v233 offset:0
ds_read_b64_tr_b16 a[106:107], v233 offset:512
v_mfma_f32_16x16x32_bf16 v[80:83], a[116:119], v[136:139], v[80:83]
v_mfma_f32_16x16x32_bf16 v[84:87], a[112:115], v[132:135], v[84:87]
)"
R"(s_add_u32 m0, s54, 896
ds_read_b64_tr_b16 a[108:109], v233 offset:12288
ds_read_b64_tr_b16 a[110:111], v233 offset:12800
v_mfma_f32_16x16x32_bf16 v[84:87], a[116:119], v[140:143], v[84:87]
buffer_load_dwordx4 %[vW], %[srdW], s75 offen offset:128 lds
v_mfma_f32_16x16x32_bf16 v[88:91], a[120:123], v[128:131], v[88:91]
ds_read_b64_tr_b16 a[112:113], v234 offset:0
ds_read_b64_tr_b16 a[114:115], v234 offset:512
v_mfma_f32_16x16x32_bf16 v[88:91], a[124:127], v[136:139], v[88:91]
v_mfma_f32_16x16x32_bf16 v[92:95], a[120:123], v[132:135], v[92:95]
s_add_u32 m0, s54, 1792
ds_read_b64_tr_b16 a[116:117], v234 offset:12288
ds_read_b64_tr_b16 a[118:119], v234 offset:12800
v_mfma_f32_16x16x32_bf16 v[92:95], a[124:127], v[140:143], v[92:95]
s_waitcnt lgkmcnt(4)
buffer_load_dwordx4 %[vW], %[srdW], s75 offen offset:256 lds
v_mfma_f32_16x16x32_bf16 v[0:3], a[96:99], v[96:99], v[0:3]
ds_read_b64_tr_b16 a[120:121], v235 offset:0
ds_read_b64_tr_b16 a[122:123], v235 offset:512
v_mfma_f32_16x16x32_bf16 v[0:3], a[100:103], v[104:107], v[0:3]
s_add_u32 s75, s66, 4325376
v_mfma_f32_16x16x32_bf16 v[4:7], a[96:99], v[100:103], v[4:7]
s_add_u32 m0, s54, 12288
ds_read_b64_tr_b16 a[124:125], v235 offset:12288
)"
R"(ds_read_b64_tr_b16 a[126:127], v235 offset:12800
v_mfma_f32_16x16x32_bf16 v[4:7], a[100:103], v[108:111], v[4:7]
buffer_load_dwordx4 %[vW], %[srdW], s75 offen offset:0 lds
v_mfma_f32_16x16x32_bf16 v[8:11], a[104:107], v[96:99], v[8:11]
ds_read_b64_tr_b16 a[96:97], v232 offset:1024
ds_read_b64_tr_b16 a[98:99], v232 offset:1536
v_mfma_f32_16x16x32_bf16 v[8:11], a[108:111], v[104:107], v[8:11]
v_mfma_f32_16x16x32_bf16 v[12:15], a[104:107], v[100:103], v[12:15]
s_add_u32 m0, s54, 13184
ds_read_b64_tr_b16 a[100:101], v232 offset:13312
ds_read_b64_tr_b16 a[102:103], v232 offset:13824
v_mfma_f32_16x16x32_bf16 v[12:15], a[108:111], v[108:111], v[12:15]
s_waitcnt lgkmcnt(4)
buffer_load_dwordx4 %[vW], %[srdW], s75 offen offset:128 lds
v_mfma_f32_16x16x32_bf16 v[16:19], a[112:115], v[96:99], v[16:19]
ds_read_b64_tr_b16 a[104:105], v233 offset:1024
ds_read_b64_tr_b16 a[106:107], v233 offset:1536
v_mfma_f32_16x16x32_bf16 v[16:19], a[116:119], v[104:107], v[16:19]
v_mfma_f32_16x16x32_bf16 v[20:23], a[112:115], v[100:103], v[20:23]
s_add_u32 m0, s54, 14080
ds_read_b64_tr_b16 a[108:109], v233 offset:13312
ds_read_b64_tr_b16 a[110:111], v233 offset:13824
v_mfma_f32_16x16x32_bf16 v[20:23], a[116:119], v[108:111], v[20:23]
buffer_load_dwordx4 %[vW], %[srdW], s75 offen offset:256 lds
)"
R"(v_mfma_f32_16x16x32_bf16 v[24:27], a[120:123], v[96:99], v[24:27]
ds_read_b64_tr_b16 a[112:113], v234 offset:1024
ds_read_b64_tr_b16 a[114:115], v234 offset:1536
v_mfma_f32_16x16x32_bf16 v[24:27], a[124:127], v[104:107], v[24:27]
v_mfma_f32_16x16x32_bf16 v[28:31], a[120:123], v[100:103], v[28:31]
s_add_u32 m0, s73, -512
ds_read_b64_tr_b16 a[116:117], v234 offset:13312
ds_read_b64_tr_b16 a[118:119], v234 offset:13824
v_mfma_f32_16x16x32_bf16 v[28:31], a[124:127], v[108:111], v[28:31]
s_waitcnt lgkmcnt(4)
buffer_load_dwordx4 %[vX0], %[srdX], s69 offen offset:512 lds
v_mfma_f32_16x16x32_bf16 v[32:35], a[96:99], v[96:99], v[32:35]
ds_read_b64_tr_b16 a[120:121], v235 offset:1024
ds_read_b64_tr_b16 a[122:123], v235 offset:1536
v_mfma_f32_16x16x32_bf16 v[32:35], a[100:103], v[104:107], v[32:35]
v_mfma_f32_16x16x32_bf16 v[36:39], a[96:99], v[100:103], v[36:39]
s_add_u32 m0, s73, 512
ds_read_b64_tr_b16 a[124:125], v235 offset:13312
ds_read_b64_tr_b16 a[126:127], v235 offset:13824
v_mfma_f32_16x16x32_bf16 v[36:39], a[100:103], v[108:111], v[36:39]
buffer_load_dwordx4 %[vX1], %[srdX], s70 offen offset:512 lds
v_mfma_f32_16x16x32_bf16 v[40:43], a[104:107], v[96:99], v[40:43]
ds_read_b64_tr_b16 a[96:97], v232 offset:2048
ds_read_b64_tr_b16 a[98:99], v232 offset:2560
)"
R"(v_mfma_f32_16x16x32_bf16 v[40:43], a[108:111], v[104:107], v[40:43]
v_mfma_f32_16x16x32_bf16 v[44:47], a[104:107], v[100:103], v[44:47]
s_add_u32 m0, s73, 1536
ds_read_b64_tr_b16 a[100:101], v232 offset:14336
ds_read_b64_tr_b16 a[102:103], v232 offset:14848
v_mfma_f32_16x16x32_bf16 v[44:47], a[108:111], v[108:111], v[44:47]
s_waitcnt lgkmcnt(4)
buffer_load_dwordx4 %[vX0], %[srdX], s71 offen offset:512 lds
v_mfma_f32_16x16x32_bf16 v[48:51], a[112:115], v[96:99], v[48:51]
ds_read_b64_tr_b16 a[104:105], v233 offset:2048
ds_read_b64_tr_b16 a[106:107], v233 offset:2560
v_mfma_f32_16x16x32_bf16 v[48:51], a[116:119], v[104:107], v[48:51]
v_mfma_f32_16x16x32_bf16 v[52:55], a[112:115], v[100:103], v[52:55]
s_add_u32 m0, s73, 2560
ds_read_b64_tr_b16 a[108:109], v233 offset:14336
ds_read_b64_tr_b16 a[110:111], v233 offset:14848
v_mfma_f32_16x16x32_bf16 v[52:55], a[116:119], v[108:111], v[52:55]
buffer_load_dwordx4 %[vX1], %[srdX], s72 offen offset:512 lds
v_mfma_f32_16x16x32_bf16 v[56:59], a[120:123], v[96:99], v[56:59]
ds_read_b64_tr_b16 a[112:113], v234 offset:2048
ds_read_b64_tr_b16 a[114:115], v234 offset:2560
v_mfma_f32_16x16x32_bf16 v[56:59], a[124:127], v[104:107], v[56:59]
v_mfma_f32_16x16x32_bf16 v[60:63], a[120:123], v[100:103], v[60:63]
ds_read_b64_tr_b16 a[116:117], v234 offset:14336
)"
R"(ds_read_b64_tr_b16 a[118:119], v234 offset:14848
v_mfma_f32_16x16x32_bf16 v[60:63], a[124:127], v[108:111], v[60:63]
s_waitcnt lgkmcnt(4)
s_waitcnt vmcnt(10)
s_barrier
v_mfma_f32_16x16x32_bf16 v[64:67], a[96:99], v[96:99], v[64:67]
ds_read_b64_tr_b16 a[120:121], v235 offset:2048
ds_read_b64_tr_b16 a[122:123], v235 offset:2560
v_mfma_f32_16x16x32_bf16 v[64:67], a[100:103], v[104:107], v[64:67]
ds_read_b128 v[128:131], %[pX0] offset:16384
ds_read_b128 v[132:135], %[pX0] offset:18432
v_mfma_f32_16x16x32_bf16 v[68:71], a[96:99], v[100:103], v[68:71]
ds_read_b64_tr_b16 a[124:125], v235 offset:14336
ds_read_b64_tr_b16 a[126:127], v235 offset:14848
v_mfma_f32_16x16x32_bf16 v[68:71], a[100:103], v[108:111], v[68:71]
ds_read_b128 v[136:139], %[pX1] offset:16384
ds_read_b128 v[140:143], %[pX1] offset:18432
v_add_u32 v232, s51, %[pW]
v_xor_b32 v233, 32, v232
v_mfma_f32_16x16x32_bf16 v[72:75], a[104:107], v[96:99], v[72:75]
ds_read_b64_tr_b16 a[96:97], v232 offset:0
ds_read_b64_tr_b16 a[98:99], v232 offset:512
v_mfma_f32_16x16x32_bf16 v[72:75], a[108:111], v[104:107], v[72:75]
s_add_u32 s75, s66, 4718592
)"
R"(v_xor_b32 v234, 64, v232
v_xor_b32 v235, 96, v232
v_mfma_f32_16x16x32_bf16 v[76:79], a[104:107], v[100:103], v[76:79]
s_add_u32 m0, s55, 0
ds_read_b64_tr_b16 a[100:101], v232 offset:12288
ds_read_b64_tr_b16 a[102:103], v232 offset:12800
v_mfma_f32_16x16x32_bf16 v[76:79], a[108:111], v[108:111], v[76:79]
s_waitcnt lgkmcnt(6)
buffer_load_dwordx4 %[vW], %[srdW], s75 offen offset:0 lds
v_mfma_f32_16x16x32_bf16 v[80:83], a[112:115], v[96:99], v[80:83]
ds_read_b64_tr_b16 a[104:105], v233 offset:0
ds_read_b64_tr_b16 a[106:107], v233 offset:512
v_mfma_f32_16x16x32_bf16 v[80:83], a[116:119], v[104:107], v[80:83]
v_mfma_f32_16x16x32_bf16 v[84:87], a[112:115], v[100:103], v[84:87]
s_add_u32 m0, s55, 896
ds_read_b64_tr_b16 a[108:109], v233 offset:12288
ds_read_b64_tr_b16 a[110:111], v233 offset:12800
v_mfma_f32_16x16x32_bf16 v[84:87], a[116:119], v[108:111], v[84:87]
buffer_load_dwordx4 %[vW], %[srdW], s75 offen offset:128 lds
v_mfma_f32_16x16x32_bf16 v[88:91], a[120:123], v[96:99], v[88:91]
ds_read_b64_tr_b16 a[112:113], v234 offset:0
ds_read_b64_tr_b16 a[114:115], v234 offset:512
v_mfma_f32_16x16x32_bf16 v[88:91], a[124:127], v[104:107], v[88:91]
v_mfma_f32_16x16x32_bf16 v[92:95], a[120:123], v[100:103], v[92:95]
)"
R"(s_add_u32 m0, s55, 1792
ds_read_b64_tr_b16 a[116:117], v234 offset:12288
ds_read_b64_tr_b16 a[118:119], v234 offset:12800
v_mfma_f32_16x16x32_bf16 v[92:95], a[124:127], v[108:111], v[92:95]
s_waitcnt lgkmcnt(4)
buffer_load_dwordx4 %[vW], %[srdW], s75 offen offset:256 lds
v_mfma_f32_16x16x32_bf16 v[0:3], a[96:99], v[128:131], v[0:3]
ds_read_b64_tr_b16 a[120:121], v235 offset:0
ds_read_b64_tr_b16 a[122:123], v235 offset:512
v_mfma_f32_16x16x32_bf16 v[0:3], a[100:103], v[136:139], v[0:3]
s_add_u32 s75, s66, 5111808
v_mfma_f32_16x16x32_bf16 v[4:7], a[96:99], v[132:135], v[4:7]
s_add_u32 m0, s55, 12288
ds_read_b64_tr_b16 a[124:125], v235 offset:12288
ds_read_b64_tr_b16 a[126:127], v235 offset:12800
v_mfma_f32_16x16x32_bf16 v[4:7], a[100:103], v[140:143], v[4:7]
buffer_load_dwordx4 %[vW], %[srdW], s75 offen offset:0 lds
v_mfma_f32_16x16x32_bf16 v[8:11], a[104:107], v[128:131], v[8:11]
ds_read_b64_tr_b16 a[96:97], v232 offset:1024
ds_read_b64_tr_b16 a[98:99], v232 offset:1536
v_mfma_f32_16x16x32_bf16 v[8:11], a[108:111], v[136:139], v[8:11]
v_mfma_f32_16x16x32_bf16 v[12:15], a[104:107], v[132:135], v[12:15]
s_add_u32 m0, s55, 13184
ds_read_b64_tr_b16 a[100:101], v232 offset:13312
)"
R"(ds_read_b64_tr_b16 a[102:103], v232 offset:13824
v_mfma_f32_16x16x32_bf16 v[12:15], a[108:111], v[140:143], v[12:15]
s_waitcnt lgkmcnt(4)
buffer_load_dwordx4 %[vW], %[srdW], s75 offen offset:128 lds
v_mfma_f32_16x16x32_bf16 v[16:19], a[112:115], v[128:131], v[16:19]
ds_read_b64_tr_b16 a[104:105], v233 offset:1024
ds_read_b64_tr_b16 a[106:107], v233 offset:1536
v_mfma_f32_16x16x32_bf16 v[16:19], a[116:119], v[136:139], v[16:19]
v_mfma_f32_16x16x32_bf16 v[20:23], a[112:115], v[132:135], v[20:23]
s_add_u32 m0, s55, 14080
ds_read_b64_tr_b16 a[108:109], v233 offset:13312
ds_read_b64_tr_b16 a[110:111], v233 offset:13824
v_mfma_f32_16x16x32_bf16 v[20:23], a[116:119], v[140:143], v[20:23]
buffer_load_dwordx4 %[vW], %[srdW], s75 offen offset:256 lds
v_mfma_f32_16x16x32_bf16 v[24:27], a[120:123], v[128:131], v[24:27]
ds_read_b64_tr_b16 a[112:113], v234 offset:1024
ds_read_b64_tr_b16 a[114:115], v234 offset:1536
v_mfma_f32_16x16x32_bf16 v[24:27], a[124:127], v[136:139], v[24:27]
v_mfma_f32_16x16x32_bf16 v[28:31], a[120:123], v[132:135], v[28:31]
s_add_u32 m0, s73, 15744
ds_read_b64_tr_b16 a[116:117], v234 offset:13312
ds_read_b64_tr_b16 a[118:119], v234 offset:13824
v_mfma_f32_16x16x32_bf16 v[28:31], a[124:127], v[140:143], v[28:31]
s_waitcnt lgkmcnt(4)
)"
R"(buffer_load_dwordx4 %[vX0], %[srdX], s69 offen offset:640 lds
v_mfma_f32_16x16x32_bf16 v[32:35], a[96:99], v[128:131], v[32:35]
ds_read_b64_tr_b16 a[120:121], v235 offset:1024
ds_read_b64_tr_b16 a[122:123], v235 offset:1536
v_mfma_f32_16x16x32_bf16 v[32:35], a[100:103], v[136:139], v[32:35]
v_mfma_f32_16x16x32_bf16 v[36:39], a[96:99], v[132:135], v[36:39]
s_add_u32 m0, s73, 16768
ds_read_b64_tr_b16 a[124:125], v235 offset:13312
ds_read_b64_tr_b16 a[126:127], v235 offset:13824
v_mfma_f32_16x16x32_bf16 v[36:39], a[100:103], v[140:143], v[36:39]
buffer_load_dwordx4 %[vX1], %[srdX], s70 offen offset:640 lds
v_mfma_f32_16x16x32_bf16 v[40:43], a[104:107], v[128:131], v[40:43]
ds_read_b64_tr_b16 a[96:97], v232 offset:2048
ds_read_b64_tr_b16 a[98:99], v232 offset:2560
v_mfma_f32_16x16x32_bf16 v[40:43], a[108:111], v[136:139], v[40:43]
v_mfma_f32_16x16x32_bf16 v[44:47], a[104:107], v[132:135], v[44:47]
s_add_u32 m0, s73, 17792
ds_read_b64_tr_b16 a[100:101], v232 offset:14336
ds_read_b64_tr_b16 a[102:103], v232 offset:14848
v_mfma_f32_16x16x32_bf16 v[44:47], a[108:111], v[140:143], v[44:47]
s_waitcnt lgkmcnt(4)
buffer_load_dwordx4 %[vX0], %[srdX], s71 offen offset:640 lds
v_mfma_f32_16x16x32_bf16 v[48:51], a[112:115], v[128:131], v[48:51]
ds_read_b64_tr_b16 a[104:105], v233 offset:2048
)"
R"(ds_read_b64_tr_b16 a[106:107], v233 offset:2560
v_mfma_f32_16x16x32_bf16 v[48:51], a[116:119], v[136:139], v[48:51]
v_mfma_f32_16x16x32_bf16 v[52:55], a[112:115], v[132:135], v[52:55]
s_add_u32 m0, s73, 18816
ds_read_b64_tr_b16 a[108:109], v233 offset:14336
ds_read_b64_tr_b16 a[110:111], v233 offset:14848
v_mfma_f32_16x16x32_bf16 v[52:55], a[116:119], v[140:143], v[52:55]
buffer_load_dwordx4 %[vX1], %[srdX], s72 offen offset:640 lds
v_mfma_f32_16x16x32_bf16 v[56:59], a[120:123], v[128:131], v[56:59]
ds_read_b64_tr_b16 a[112:113], v234 offset:2048
ds_read_b64_tr_b16 a[114:115], v234 offset:2560
v_mfma_f32_16x16x32_bf16 v[56:59], a[124:127], v[136:139], v[56:59]
v_mfma_f32_16x16x32_bf16 v[60:63], a[120:123], v[132:135], v[60:63]
ds_read_b64_tr_b16 a[116:117], v234 offset:14336
ds_read_b64_tr_b16 a[118:119], v234 offset:14848
v_mfma_f32_16x16x32_bf16 v[60:63], a[124:127], v[140:143], v[60:63]
s_waitcnt lgkmcnt(4)
s_waitcnt vmcnt(10)
s_barrier
v_mfma_f32_16x16x32_bf16 v[64:67], a[96:99], v[128:131], v[64:67]
ds_read_b64_tr_b16 a[120:121], v235 offset:2048
ds_read_b64_tr_b16 a[122:123], v235 offset:2560
v_mfma_f32_16x16x32_bf16 v[64:67], a[100:103], v[136:139], v[64:67]
ds_read_b128 v[96:99], %[pX0] offset:0
)"
R"(ds_read_b128 v[100:103], %[pX0] offset:2048
v_mfma_f32_16x16x32_bf16 v[68:71], a[96:99], v[132:135], v[68:71]
ds_read_b64_tr_b16 a[124:125], v235 offset:14336
ds_read_b64_tr_b16 a[126:127], v235 offset:14848
v_mfma_f32_16x16x32_bf16 v[68:71], a[100:103], v[140:143], v[68:71]
ds_read_b128 v[104:107], %[pX1] offset:0
ds_read_b128 v[108:111], %[pX1] offset:2048
v_add_u32 v232, s52, %[pW]
v_xor_b32 v233, 32, v232
v_mfma_f32_16x16x32_bf16 v[72:75], a[104:107], v[128:131], v[72:75]
ds_read_b64_tr_b16 a[96:97], v232 offset:0
ds_read_b64_tr_b16 a[98:99], v232 offset:512
v_mfma_f32_16x16x32_bf16 v[72:75], a[108:111], v[136:139], v[72:75]
s_add_u32 s75, s66, 5505024
v_xor_b32 v234, 64, v232
v_xor_b32 v235, 96, v232
v_mfma_f32_16x16x32_bf16 v[76:79], a[104:107], v[132:135], v[76:79]
s_add_u32 m0, s56, 0
ds_read_b64_tr_b16 a[100:101], v232 offset:12288
ds_read_b64_tr_b16 a[102:103], v232 offset:12800
v_mfma_f32_16x16x32_bf16 v[76:79], a[108:111], v[140:143], v[76:79]
s_waitcnt lgkmcnt(6)
buffer_load_dwordx4 %[vW], %[srdW], s75 offen offset:0 lds
v_mfma_f32_16x16x32_bf16 v[80:83], a[112:115], v[128:131], v[80:83]
)"
R"(ds_read_b64_tr_b16 a[104:105], v233 offset:0
ds_read_b64_tr_b16 a[106:107], v233 offset:512
v_mfma_f32_16x16x32_bf16 v[80:83], a[116:119], v[136:139], v[80:83]
v_mfma_f32_16x16x32_bf16 v[84:87], a[112:115], v[132:135], v[84:87]
s_add_u32 m0, s56, 896
ds_read_b64_tr_b16 a[108:109], v233 offset:12288
ds_read_b64_tr_b16 a[110:111], v233 offset:12800
v_mfma_f32_16x16x32_bf16 v[84:87], a[116:119], v[140:143], v[84:87]
buffer_load_dwordx4 %[vW], %[srdW], s75 offen offset:128 lds
v_mfma_f32_16x16x32_bf16 v[88:91], a[120:123], v[128:131], v[88:91]
ds_read_b64_tr_b16 a[112:113], v234 offset:0
ds_read_b64_tr_b16 a[114:115], v234 offset:512
v_mfma_f32_16x16x32_bf16 v[88:91], a[124:127], v[136:139], v[88:91]
v_mfma_f32_16x16x32_bf16 v[92:95], a[120:123], v[132:135], v[92:95]
s_add_u32 m0, s56, 1792
ds_read_b64_tr_b16 a[116:117], v234 offset:12288
ds_read_b64_tr_b16 a[118:119], v234 offset:12800
v_mfma_f32_16x16x32_bf16 v[92:95], a[124:127], v[140:143], v[92:95]
s_waitcnt lgkmcnt(4)
buffer_load_dwordx4 %[vW], %[srdW], s75 offen offset:256 lds
v_mfma_f32_16x16x32_bf16 v[0:3], a[96:99], v[96:99], v[0:3]
ds_read_b64_tr_b16 a[120:121], v235 offset:0
ds_read_b64_tr_b16 a[122:123], v235 offset:512
v_mfma_f32_16x16x32_bf16 v[0:3], a[100:103], v[104:107], v[0:3]
)"
R"(s_add_u32 s75, s66, 5898240
v_mfma_f32_16x16x32_bf16 v[4:7], a[96:99], v[100:103], v[4:7]
s_add_u32 m0, s56, 12288
ds_read_b64_tr_b16 a[124:125], v235 offset:12288
ds_read_b64_tr_b16 a[126:127], v235 offset:12800
v_mfma_f32_16x16x32_bf16 v[4:7], a[100:103], v[108:111], v[4:7]
buffer_load_dwordx4 %[vW], %[srdW], s75 offen offset:0 lds
v_mfma_f32_16x16x32_bf16 v[8:11], a[104:107], v[96:99], v[8:11]
ds_read_b64_tr_b16 a[96:97], v232 offset:1024
ds_read_b64_tr_b16 a[98:99], v232 offset:1536
v_mfma_f32_16x16x32_bf16 v[8:11], a[108:111], v[104:107], v[8:11]
v_mfma_f32_16x16x32_bf16 v[12:15], a[104:107], v[100:103], v[12:15]
s_add_u32 m0, s56, 13184
ds_read_b64_tr_b16 a[100:101], v232 offset:13312
ds_read_b64_tr_b16 a[102:103], v232 offset:13824
v_mfma_f32_16x16x32_bf16 v[12:15], a[108:111], v[108:111], v[12:15]
s_waitcnt lgkmcnt(4)
buffer_load_dwordx4 %[vW], %[srdW], s75 offen offset:128 lds
v_mfma_f32_16x16x32_bf16 v[16:19], a[112:115], v[96:99], v[16:19]
ds_read_b64_tr_b16 a[104:105], v233 offset:1024
ds_read_b64_tr_b16 a[106:107], v233 offset:1536
v_mfma_f32_16x16x32_bf16 v[16:19], a[116:119], v[104:107], v[16:19]
v_mfma_f32_16x16x32_bf16 v[20:23], a[112:115], v[100:103], v[20:23]
s_add_u32 m0, s56, 14080
)"
R"(ds_read_b64_tr_b16 a[108:109], v233 offset:13312
ds_read_b64_tr_b16 a[110:111], v233 offset:13824
v_mfma_f32_16x16x32_bf16 v[20:23], a[116:119], v[108:111], v[20:23]
buffer_load_dwordx4 %[vW], %[srdW], s75 offen offset:256 lds
v_mfma_f32_16x16x32_bf16 v[24:27], a[120:123], v[96:99], v[24:27]
ds_read_b64_tr_b16 a[112:113], v234 offset:1024
ds_read_b64_tr_b16 a[114:115], v234 offset:1536
v_mfma_f32_16x16x32_bf16 v[24:27], a[124:127], v[104:107], v[24:27]
v_mfma_f32_16x16x32_bf16 v[28:31], a[120:123], v[100:103], v[28:31]
s_add_u32 m0, s73, -768
ds_read_b64_tr_b16 a[116:117], v234 offset:13312
ds_read_b64_tr_b16 a[118:119], v234 offset:13824
v_mfma_f32_16x16x32_bf16 v[28:31], a[124:127], v[108:111], v[28:31]
s_waitcnt lgkmcnt(4)
buffer_load_dwordx4 %[vX0], %[srdX], s69 offen offset:768 lds
v_mfma_f32_16x16x32_bf16 v[32:35], a[96:99], v[96:99], v[32:35]
ds_read_b64_tr_b16 a[120:121], v235 offset:1024
ds_read_b64_tr_b16 a[122:123], v235 offset:1536
v_mfma_f32_16x16x32_bf16 v[32:35], a[100:103], v[104:107], v[32:35]
v_mfma_f32_16x16x32_bf16 v[36:39], a[96:99], v[100:103], v[36:39]
s_add_u32 m0, s73, 256
ds_read_b64_tr_b16 a[124:125], v235 offset:13312
ds_read_b64_tr_b16 a[126:127], v235 offset:13824
v_mfma_f32_16x16x32_bf16 v[36:39], a[100:103], v[108:111], v[36:39]
)"
R"(buffer_load_dwordx4 %[vX1], %[srdX], s70 offen offset:768 lds
v_mfma_f32_16x16x32_bf16 v[40:43], a[104:107], v[96:99], v[40:43]
ds_read_b64_tr_b16 a[96:97], v232 offset:2048
ds_read_b64_tr_b16 a[98:99], v232 offset:2560
v_mfma_f32_16x16x32_bf16 v[40:43], a[108:111], v[104:107], v[40:43]
v_mfma_f32_16x16x32_bf16 v[44:47], a[104:107], v[100:103], v[44:47]
s_add_u32 m0, s73, 1280
ds_read_b64_tr_b16 a[100:101], v232 offset:14336
ds_read_b64_tr_b16 a[102:103], v232 offset:14848
v_mfma_f32_16x16x32_bf16 v[44:47], a[108:111], v[108:111], v[44:47]
s_waitcnt lgkmcnt(4)
buffer_load_dwordx4 %[vX0], %[srdX], s71 offen offset:768 lds
v_mfma_f32_16x16x32_bf16 v[48:51], a[112:115], v[96:99], v[48:51]
ds_read_b64_tr_b16 a[104:105], v233 offset:2048
ds_read_b64_tr_b16 a[106:107], v233 offset:2560
v_mfma_f32_16x16x32_bf16 v[48:51], a[116:119], v[104:107], v[48:51]
v_mfma_f32_16x16x32_bf16 v[52:55], a[112:115], v[100:103], v[52:55]
s_add_u32 m0, s73, 2304
ds_read_b64_tr_b16 a[108:109], v233 offset:14336
ds_read_b64_tr_b16 a[110:111], v233 offset:14848
v_mfma_f32_16x16x32_bf16 v[52:55], a[116:119], v[108:111], v[52:55]
buffer_load_dwordx4 %[vX1], %[srdX], s72 offen offset:768 lds
v_mfma_f32_16x16x32_bf16 v[56:59], a[120:123], v[96:99], v[56:59]
ds_read_b64_tr_b16 a[112:113], v234 offset:2048
)"
R"(ds_read_b64_tr_b16 a[114:115], v234 offset:2560
v_mfma_f32_16x16x32_bf16 v[56:59], a[124:127], v[104:107], v[56:59]
v_mfma_f32_16x16x32_bf16 v[60:63], a[120:123], v[100:103], v[60:63]
ds_read_b64_tr_b16 a[116:117], v234 offset:14336
ds_read_b64_tr_b16 a[118:119], v234 offset:14848
v_mfma_f32_16x16x32_bf16 v[60:63], a[124:127], v[108:111], v[60:63]
s_waitcnt lgkmcnt(4)
s_waitcnt vmcnt(10)
s_barrier
v_mfma_f32_16x16x32_bf16 v[64:67], a[96:99], v[96:99], v[64:67]
ds_read_b64_tr_b16 a[120:121], v235 offset:2048
ds_read_b64_tr_b16 a[122:123], v235 offset:2560
v_mfma_f32_16x16x32_bf16 v[64:67], a[100:103], v[104:107], v[64:67]
ds_read_b128 v[128:131], %[pX0] offset:16384
ds_read_b128 v[132:135], %[pX0] offset:18432
v_mfma_f32_16x16x32_bf16 v[68:71], a[96:99], v[100:103], v[68:71]
ds_read_b64_tr_b16 a[124:125], v235 offset:14336
ds_read_b64_tr_b16 a[126:127], v235 offset:14848
v_mfma_f32_16x16x32_bf16 v[68:71], a[100:103], v[108:111], v[68:71]
ds_read_b128 v[136:139], %[pX1] offset:16384
ds_read_b128 v[140:143], %[pX1] offset:18432
v_add_u32 v232, s48, %[pW]
v_xor_b32 v233, 32, v232
v_mfma_f32_16x16x32_bf16 v[72:75], a[104:107], v[96:99], v[72:75]
)"
R"(ds_read_b64_tr_b16 a[96:97], v232 offset:0
ds_read_b64_tr_b16 a[98:99], v232 offset:512
v_mfma_f32_16x16x32_bf16 v[72:75], a[108:111], v[104:107], v[72:75]
s_add_u32 s75, s66, 6291456
v_xor_b32 v234, 64, v232
v_xor_b32 v235, 96, v232
v_mfma_f32_16x16x32_bf16 v[76:79], a[104:107], v[100:103], v[76:79]
s_add_u32 m0, s57, 0
ds_read_b64_tr_b16 a[100:101], v232 offset:12288
ds_read_b64_tr_b16 a[102:103], v232 offset:12800
v_mfma_f32_16x16x32_bf16 v[76:79], a[108:111], v[108:111], v[76:79]
s_waitcnt lgkmcnt(6)
buffer_load_dwordx4 %[vW], %[srdW], s75 offen offset:0 lds
v_mfma_f32_16x16x32_bf16 v[80:83], a[112:115], v[96:99], v[80:83]
ds_read_b64_tr_b16 a[104:105], v233 offset:0
ds_read_b64_tr_b16 a[106:107], v233 offset:512
v_mfma_f32_16x16x32_bf16 v[80:83], a[116:119], v[104:107], v[80:83]
v_mfma_f32_16x16x32_bf16 v[84:87], a[112:115], v[100:103], v[84:87]
s_add_u32 m0, s57, 896
ds_read_b64_tr_b16 a[108:109], v233 offset:12288
ds_read_b64_tr_b16 a[110:111], v233 offset:12800
v_mfma_f32_16x16x32_bf16 v[84:87], a[116:119], v[108:111], v[84:87]
buffer_load_dwordx4 %[vW], %[srdW], s75 offen offset:128 lds
v_mfma_f32_16x16x32_bf16 v[88:91], a[120:123], v[96:99], v[88:91]
)"
R"(ds_read_b64_tr_b16 a[112:113], v234 offset:0
ds_read_b64_tr_b16 a[114:115], v234 offset:512
v_mfma_f32_16x16x32_bf16 v[88:91], a[124:127], v[104:107], v[88:91]
v_mfma_f32_16x16x32_bf16 v[92:95], a[120:123], v[100:103], v[92:95]
s_add_u32 m0, s57, 1792
ds_read_b64_tr_b16 a[116:117], v234 offset:12288
ds_read_b64_tr_b16 a[118:119], v234 offset:12800
v_mfma_f32_16x16x32_bf16 v[92:95], a[124:127], v[108:111], v[92:95]
s_waitcnt lgkmcnt(4)
buffer_load_dwordx4 %[vW], %[srdW], s75 offen offset:256 lds
v_mfma_f32_16x16x32_bf16 v[0:3], a[96:99], v[128:131], v[0:3]
ds_read_b64_tr_b16 a[120:121], v235 offset:0
ds_read_b64_tr_b16 a[122:123], v235 offset:512
v_mfma_f32_16x16x32_bf16 v[0:3], a[100:103], v[136:139], v[0:3]
s_add_u32 s75, s66, 6684672
v_mfma_f32_16x16x32_bf16 v[4:7], a[96:99], v[132:135], v[4:7]
s_add_u32 m0, s57, 12288
ds_read_b64_tr_b16 a[124:125], v235 offset:12288
ds_read_b64_tr_b16 a[126:127], v235 offset:12800
v_mfma_f32_16x16x32_bf16 v[4:7], a[100:103], v[140:143], v[4:7]
buffer_load_dwordx4 %[vW], %[srdW], s75 offen offset:0 lds
v_mfma_f32_16x16x32_bf16 v[8:11], a[104:107], v[128:131], v[8:11]
ds_read_b64_tr_b16 a[96:97], v232 offset:1024
ds_read_b64_tr_b16 a[98:99], v232 offset:1536
)"
R"(v_mfma_f32_16x16x32_bf16 v[8:11], a[108:111], v[136:139], v[8:11]
v_mfma_f32_16x16x32_bf16 v[12:15], a[104:107], v[132:135], v[12:15]
s_add_u32 m0, s57, 13184
ds_read_b64_tr_b16 a[100:101], v232 offset:13312
ds_read_b64_tr_b16 a[102:103], v232 offset:13824
v_mfma_f32_16x16x32_bf16 v[12:15], a[108:111], v[140:143], v[12:15]
s_waitcnt lgkmcnt(4)
buffer_load_dwordx4 %[vW], %[srdW], s75 offen offset:128 lds
v_mfma_f32_16x16x32_bf16 v[16:19], a[112:115], v[128:131], v[16:19]
ds_read_b64_tr_b16 a[104:105], v233 offset:1024
ds_read_b64_tr_b16 a[106:107], v233 offset:1536
v_mfma_f32_16x16x32_bf16 v[16:19], a[116:119], v[136:139], v[16:19]
v_mfma_f32_16x16x32_bf16 v[20:23], a[112:115], v[132:135], v[20:23]
s_add_u32 m0, s57, 14080
ds_read_b64_tr_b16 a[108:109], v233 offset:13312
ds_read_b64_tr_b16 a[110:111], v233 offset:13824
v_mfma_f32_16x16x32_bf16 v[20:23], a[116:119], v[140:143], v[20:23]
buffer_load_dwordx4 %[vW], %[srdW], s75 offen offset:256 lds
v_mfma_f32_16x16x32_bf16 v[24:27], a[120:123], v[128:131], v[24:27]
ds_read_b64_tr_b16 a[112:113], v234 offset:1024
ds_read_b64_tr_b16 a[114:115], v234 offset:1536
v_mfma_f32_16x16x32_bf16 v[24:27], a[124:127], v[136:139], v[24:27]
v_mfma_f32_16x16x32_bf16 v[28:31], a[120:123], v[132:135], v[28:31]
s_add_u32 m0, s73, 15488
)"
R"(ds_read_b64_tr_b16 a[116:117], v234 offset:13312
ds_read_b64_tr_b16 a[118:119], v234 offset:13824
v_mfma_f32_16x16x32_bf16 v[28:31], a[124:127], v[140:143], v[28:31]
s_waitcnt lgkmcnt(4)
buffer_load_dwordx4 %[vX0], %[srdX], s69 offen offset:896 lds
v_mfma_f32_16x16x32_bf16 v[32:35], a[96:99], v[128:131], v[32:35]
ds_read_b64_tr_b16 a[120:121], v235 offset:1024
ds_read_b64_tr_b16 a[122:123], v235 offset:1536
v_mfma_f32_16x16x32_bf16 v[32:35], a[100:103], v[136:139], v[32:35]
v_mfma_f32_16x16x32_bf16 v[36:39], a[96:99], v[132:135], v[36:39]
s_add_u32 m0, s73, 16512
ds_read_b64_tr_b16 a[124:125], v235 offset:13312
ds_read_b64_tr_b16 a[126:127], v235 offset:13824
v_mfma_f32_16x16x32_bf16 v[36:39], a[100:103], v[140:143], v[36:39]
buffer_load_dwordx4 %[vX1], %[srdX], s70 offen offset:896 lds
v_mfma_f32_16x16x32_bf16 v[40:43], a[104:107], v[128:131], v[40:43]
ds_read_b64_tr_b16 a[96:97], v232 offset:2048
ds_read_b64_tr_b16 a[98:99], v232 offset:2560
v_mfma_f32_16x16x32_bf16 v[40:43], a[108:111], v[136:139], v[40:43]
v_mfma_f32_16x16x32_bf16 v[44:47], a[104:107], v[132:135], v[44:47]
s_add_u32 m0, s73, 17536
ds_read_b64_tr_b16 a[100:101], v232 offset:14336
ds_read_b64_tr_b16 a[102:103], v232 offset:14848
v_mfma_f32_16x16x32_bf16 v[44:47], a[108:111], v[140:143], v[44:47]
)"
R"(s_waitcnt lgkmcnt(4)
buffer_load_dwordx4 %[vX0], %[srdX], s71 offen offset:896 lds
v_mfma_f32_16x16x32_bf16 v[48:51], a[112:115], v[128:131], v[48:51]
ds_read_b64_tr_b16 a[104:105], v233 offset:2048
ds_read_b64_tr_b16 a[106:107], v233 offset:2560
v_mfma_f32_16x16x32_bf16 v[48:51], a[116:119], v[136:139], v[48:51]
v_mfma_f32_16x16x32_bf16 v[52:55], a[112:115], v[132:135], v[52:55]
s_add_u32 m0, s73, 18560
ds_read_b64_tr_b16 a[108:109], v233 offset:14336
ds_read_b64_tr_b16 a[110:111], v233 offset:14848
v_mfma_f32_16x16x32_bf16 v[52:55], a[116:119], v[140:143], v[52:55]
buffer_load_dwordx4 %[vX1], %[srdX], s72 offen offset:896 lds
v_mfma_f32_16x16x32_bf16 v[56:59], a[120:123], v[128:131], v[56:59]
ds_read_b64_tr_b16 a[112:113], v234 offset:2048
ds_read_b64_tr_b16 a[114:115], v234 offset:2560
v_mfma_f32_16x16x32_bf16 v[56:59], a[124:127], v[136:139], v[56:59]
v_mfma_f32_16x16x32_bf16 v[60:63], a[120:123], v[132:135], v[60:63]
ds_read_b64_tr_b16 a[116:117], v234 offset:14336
ds_read_b64_tr_b16 a[118:119], v234 offset:14848
v_mfma_f32_16x16x32_bf16 v[60:63], a[124:127], v[140:143], v[60:63]
s_waitcnt lgkmcnt(4)
s_waitcnt vmcnt(10)
s_barrier
v_mfma_f32_16x16x32_bf16 v[64:67], a[96:99], v[128:131], v[64:67]
)"
R"(ds_read_b64_tr_b16 a[120:121], v235 offset:2048
ds_read_b64_tr_b16 a[122:123], v235 offset:2560
v_mfma_f32_16x16x32_bf16 v[64:67], a[100:103], v[136:139], v[64:67]
ds_read_b128 v[96:99], %[pX0] offset:0
ds_read_b128 v[100:103], %[pX0] offset:2048
v_mfma_f32_16x16x32_bf16 v[68:71], a[96:99], v[132:135], v[68:71]
ds_read_b64_tr_b16 a[124:125], v235 offset:14336
ds_read_b64_tr_b16 a[126:127], v235 offset:14848
v_mfma_f32_16x16x32_bf16 v[68:71], a[100:103], v[140:143], v[68:71]
ds_read_b128 v[104:107], %[pX1] offset:0
ds_read_b128 v[108:111], %[pX1] offset:2048
v_add_u32 v232, s49, %[pW]
v_xor_b32 v233, 32, v232
v_mfma_f32_16x16x32_bf16 v[72:75], a[104:107], v[128:131], v[72:75]
ds_read_b64_tr_b16 a[96:97], v232 offset:0
ds_read_b64_tr_b16 a[98:99], v232 offset:512
v_mfma_f32_16x16x32_bf16 v[72:75], a[108:111], v[136:139], v[72:75]
s_add_u32 s75, s66, 7077888
v_xor_b32 v234, 64, v232
v_xor_b32 v235, 96, v232
v_mfma_f32_16x16x32_bf16 v[76:79], a[104:107], v[132:135], v[76:79]
s_add_u32 m0, s58, 0
ds_read_b64_tr_b16 a[100:101], v232 offset:12288
ds_read_b64_tr_b16 a[102:103], v232 offset:12800
)"
R"(v_mfma_f32_16x16x32_bf16 v[76:79], a[108:111], v[140:143], v[76:79]
s_waitcnt lgkmcnt(6)
buffer_load_dwordx4 %[vW], %[srdW], s75 offen offset:0 lds
v_mfma_f32_16x16x32_bf16 v[80:83], a[112:115], v[128:131], v[80:83]
ds_read_b64_tr_b16 a[104:105], v233 offset:0
ds_read_b64_tr_b16 a[106:107], v233 offset:512
v_mfma_f32_16x16x32_bf16 v[80:83], a[116:119], v[136:139], v[80:83]
v_mfma_f32_16x16x32_bf16 v[84:87], a[112:115], v[132:135], v[84:87]
s_add_u32 m0, s58, 896
ds_read_b64_tr_b16 a[108:109], v233 offset:12288
ds_read_b64_tr_b16 a[110:111], v233 offset:12800
v_mfma_f32_16x16x32_bf16 v[84:87], a[116:119], v[140:143], v[84:87]
buffer_load_dwordx4 %[vW], %[srdW], s75 offen offset:128 lds
v_mfma_f32_16x16x32_bf16 v[88:91], a[120:123], v[128:131], v[88:91]
ds_read_b64_tr_b16 a[112:113], v234 offset:0
ds_read_b64_tr_b16 a[114:115], v234 offset:512
v_mfma_f32_16x16x32_bf16 v[88:91], a[124:127], v[136:139], v[88:91]
v_mfma_f32_16x16x32_bf16 v[92:95], a[120:123], v[132:135], v[92:95]
s_add_u32 m0, s58, 1792
ds_read_b64_tr_b16 a[116:117], v234 offset:12288
ds_read_b64_tr_b16 a[118:119], v234 offset:12800
v_mfma_f32_16x16x32_bf16 v[92:95], a[124:127], v[140:143], v[92:95]
s_waitcnt lgkmcnt(4)
buffer_load_dwordx4 %[vW], %[srdW], s75 offen offset:256 lds
)"
R"(v_mfma_f32_16x16x32_bf16 v[0:3], a[96:99], v[96:99], v[0:3]
ds_read_b64_tr_b16 a[120:121], v235 offset:0
ds_read_b64_tr_b16 a[122:123], v235 offset:512
v_mfma_f32_16x16x32_bf16 v[0:3], a[100:103], v[104:107], v[0:3]
s_add_u32 s75, s66, 7471104
v_mfma_f32_16x16x32_bf16 v[4:7], a[96:99], v[100:103], v[4:7]
s_add_u32 m0, s58, 12288
ds_read_b64_tr_b16 a[124:125], v235 offset:12288
ds_read_b64_tr_b16 a[126:127], v235 offset:12800
v_mfma_f32_16x16x32_bf16 v[4:7], a[100:103], v[108:111], v[4:7]
buffer_load_dwordx4 %[vW], %[srdW], s75 offen offset:0 lds
v_mfma_f32_16x16x32_bf16 v[8:11], a[104:107], v[96:99], v[8:11]
ds_read_b64_tr_b16 a[96:97], v232 offset:1024
ds_read_b64_tr_b16 a[98:99], v232 offset:1536
v_mfma_f32_16x16x32_bf16 v[8:11], a[108:111], v[104:107], v[8:11]
v_mfma_f32_16x16x32_bf16 v[12:15], a[104:107], v[100:103], v[12:15]
s_add_u32 m0, s58, 13184
ds_read_b64_tr_b16 a[100:101], v232 offset:13312
ds_read_b64_tr_b16 a[102:103], v232 offset:13824
v_mfma_f32_16x16x32_bf16 v[12:15], a[108:111], v[108:111], v[12:15]
s_waitcnt lgkmcnt(4)
buffer_load_dwordx4 %[vW], %[srdW], s75 offen offset:128 lds
v_mfma_f32_16x16x32_bf16 v[16:19], a[112:115], v[96:99], v[16:19]
ds_read_b64_tr_b16 a[104:105], v233 offset:1024
)"
R"(ds_read_b64_tr_b16 a[106:107], v233 offset:1536
v_mfma_f32_16x16x32_bf16 v[16:19], a[116:119], v[104:107], v[16:19]
v_mfma_f32_16x16x32_bf16 v[20:23], a[112:115], v[100:103], v[20:23]
s_add_u32 m0, s58, 14080
ds_read_b64_tr_b16 a[108:109], v233 offset:13312
ds_read_b64_tr_b16 a[110:111], v233 offset:13824
v_mfma_f32_16x16x32_bf16 v[20:23], a[116:119], v[108:111], v[20:23]
buffer_load_dwordx4 %[vW], %[srdW], s75 offen offset:256 lds
v_mfma_f32_16x16x32_bf16 v[24:27], a[120:123], v[96:99], v[24:27]
ds_read_b64_tr_b16 a[112:113], v234 offset:1024
ds_read_b64_tr_b16 a[114:115], v234 offset:1536
v_mfma_f32_16x16x32_bf16 v[24:27], a[124:127], v[104:107], v[24:27]
v_mfma_f32_16x16x32_bf16 v[28:31], a[120:123], v[100:103], v[28:31]
s_add_u32 m0, s73, -1024
ds_read_b64_tr_b16 a[116:117], v234 offset:13312
ds_read_b64_tr_b16 a[118:119], v234 offset:13824
v_mfma_f32_16x16x32_bf16 v[28:31], a[124:127], v[108:111], v[28:31]
s_waitcnt lgkmcnt(4)
buffer_load_dwordx4 %[vX0], %[srdX], s69 offen offset:1024 lds
v_mfma_f32_16x16x32_bf16 v[32:35], a[96:99], v[96:99], v[32:35]
ds_read_b64_tr_b16 a[120:121], v235 offset:1024
ds_read_b64_tr_b16 a[122:123], v235 offset:1536
v_mfma_f32_16x16x32_bf16 v[32:35], a[100:103], v[104:107], v[32:35]
v_mfma_f32_16x16x32_bf16 v[36:39], a[96:99], v[100:103], v[36:39]
)"
R"(s_add_u32 m0, s73, 0
ds_read_b64_tr_b16 a[124:125], v235 offset:13312
ds_read_b64_tr_b16 a[126:127], v235 offset:13824
v_mfma_f32_16x16x32_bf16 v[36:39], a[100:103], v[108:111], v[36:39]
buffer_load_dwordx4 %[vX1], %[srdX], s70 offen offset:1024 lds
v_mfma_f32_16x16x32_bf16 v[40:43], a[104:107], v[96:99], v[40:43]
ds_read_b64_tr_b16 a[96:97], v232 offset:2048
ds_read_b64_tr_b16 a[98:99], v232 offset:2560
v_mfma_f32_16x16x32_bf16 v[40:43], a[108:111], v[104:107], v[40:43]
v_mfma_f32_16x16x32_bf16 v[44:47], a[104:107], v[100:103], v[44:47]
s_add_u32 m0, s73, 1024
ds_read_b64_tr_b16 a[100:101], v232 offset:14336
ds_read_b64_tr_b16 a[102:103], v232 offset:14848
v_mfma_f32_16x16x32_bf16 v[44:47], a[108:111], v[108:111], v[44:47]
s_waitcnt lgkmcnt(4)
buffer_load_dwordx4 %[vX0], %[srdX], s71 offen offset:1024 lds
v_mfma_f32_16x16x32_bf16 v[48:51], a[112:115], v[96:99], v[48:51]
ds_read_b64_tr_b16 a[104:105], v233 offset:2048
ds_read_b64_tr_b16 a[106:107], v233 offset:2560
v_mfma_f32_16x16x32_bf16 v[48:51], a[116:119], v[104:107], v[48:51]
v_mfma_f32_16x16x32_bf16 v[52:55], a[112:115], v[100:103], v[52:55]
s_add_u32 m0, s73, 2048
ds_read_b64_tr_b16 a[108:109], v233 offset:14336
ds_read_b64_tr_b16 a[110:111], v233 offset:14848
)"
R"(v_mfma_f32_16x16x32_bf16 v[52:55], a[116:119], v[108:111], v[52:55]
buffer_load_dwordx4 %[vX1], %[srdX], s72 offen offset:1024 lds
v_mfma_f32_16x16x32_bf16 v[56:59], a[120:123], v[96:99], v[56:59]
ds_read_b64_tr_b16 a[112:113], v234 offset:2048
ds_read_b64_tr_b16 a[114:115], v234 offset:2560
v_mfma_f32_16x16x32_bf16 v[56:59], a[124:127], v[104:107], v[56:59]
v_mfma_f32_16x16x32_bf16 v[60:63], a[120:123], v[100:103], v[60:63]
ds_read_b64_tr_b16 a[116:117], v234 offset:14336
ds_read_b64_tr_b16 a[118:119], v234 offset:14848
v_mfma_f32_16x16x32_bf16 v[60:63], a[124:127], v[108:111], v[60:63]
s_waitcnt lgkmcnt(4)
s_waitcnt vmcnt(10)
s_barrier
v_mfma_f32_16x16x32_bf16 v[64:67], a[96:99], v[96:99], v[64:67]
ds_read_b64_tr_b16 a[120:121], v235 offset:2048
ds_read_b64_tr_b16 a[122:123], v235 offset:2560
v_mfma_f32_16x16x32_bf16 v[64:67], a[100:103], v[104:107], v[64:67]
ds_read_b128 v[128:131], %[pX0] offset:16384
ds_read_b128 v[132:135], %[pX0] offset:18432
v_mfma_f32_16x16x32_bf16 v[68:71], a[96:99], v[100:103], v[68:71]
ds_read_b64_tr_b16 a[124:125], v235 offset:14336
ds_read_b64_tr_b16 a[126:127], v235 offset:14848
v_mfma_f32_16x16x32_bf16 v[68:71], a[100:103], v[108:111], v[68:71]
ds_read_b128 v[136:139], %[pX1] offset:16384
)"
R"(ds_read_b128 v[140:143], %[pX1] offset:18432
v_add_u32 v232, s50, %[pW]
v_xor_b32 v233, 32, v232
v_mfma_f32_16x16x32_bf16 v[72:75], a[104:107], v[96:99], v[72:75]
ds_read_b64_tr_b16 a[96:97], v232 offset:0
ds_read_b64_tr_b16 a[98:99], v232 offset:512
v_mfma_f32_16x16x32_bf16 v[72:75], a[108:111], v[104:107], v[72:75]
s_add_u32 s75, s67, 0
v_xor_b32 v234, 64, v232
v_xor_b32 v235, 96, v232
v_mfma_f32_16x16x32_bf16 v[76:79], a[104:107], v[100:103], v[76:79]
s_add_u32 m0, s54, 0
ds_read_b64_tr_b16 a[100:101], v232 offset:12288
ds_read_b64_tr_b16 a[102:103], v232 offset:12800
v_mfma_f32_16x16x32_bf16 v[76:79], a[108:111], v[108:111], v[76:79]
s_waitcnt lgkmcnt(6)
buffer_load_dwordx4 %[vK], %[srdK], s75 offen offset:0 nt lds
v_mfma_f32_16x16x32_bf16 v[80:83], a[112:115], v[96:99], v[80:83]
ds_read_b64_tr_b16 a[104:105], v233 offset:0
ds_read_b64_tr_b16 a[106:107], v233 offset:512
v_mfma_f32_16x16x32_bf16 v[80:83], a[116:119], v[104:107], v[80:83]
v_mfma_f32_16x16x32_bf16 v[84:87], a[112:115], v[100:103], v[84:87]
s_add_u32 m0, s54, 896
ds_read_b64_tr_b16 a[108:109], v233 offset:12288
)"
R"(ds_read_b64_tr_b16 a[110:111], v233 offset:12800
v_mfma_f32_16x16x32_bf16 v[84:87], a[116:119], v[108:111], v[84:87]
buffer_load_dwordx4 %[vK], %[srdK], s75 offen offset:128 nt lds
v_mfma_f32_16x16x32_bf16 v[88:91], a[120:123], v[96:99], v[88:91]
ds_read_b64_tr_b16 a[112:113], v234 offset:0
ds_read_b64_tr_b16 a[114:115], v234 offset:512
v_mfma_f32_16x16x32_bf16 v[88:91], a[124:127], v[104:107], v[88:91]
v_mfma_f32_16x16x32_bf16 v[92:95], a[120:123], v[100:103], v[92:95]
s_add_u32 m0, s54, 1792
ds_read_b64_tr_b16 a[116:117], v234 offset:12288
ds_read_b64_tr_b16 a[118:119], v234 offset:12800
v_mfma_f32_16x16x32_bf16 v[92:95], a[124:127], v[108:111], v[92:95]
s_waitcnt lgkmcnt(4)
buffer_load_dwordx4 %[vK], %[srdK], s75 offen offset:256 nt lds
v_mfma_f32_16x16x32_bf16 v[0:3], a[96:99], v[128:131], v[0:3]
ds_read_b64_tr_b16 a[120:121], v235 offset:0
ds_read_b64_tr_b16 a[122:123], v235 offset:512
v_mfma_f32_16x16x32_bf16 v[0:3], a[100:103], v[136:139], v[0:3]
s_add_u32 s75, s67, 12288
v_mfma_f32_16x16x32_bf16 v[4:7], a[96:99], v[132:135], v[4:7]
s_add_u32 m0, s54, 12288
ds_read_b64_tr_b16 a[124:125], v235 offset:12288
ds_read_b64_tr_b16 a[126:127], v235 offset:12800
v_mfma_f32_16x16x32_bf16 v[4:7], a[100:103], v[140:143], v[4:7]
)"
R"(buffer_load_dwordx4 %[vK], %[srdK], s75 offen offset:0 nt lds
v_mfma_f32_16x16x32_bf16 v[8:11], a[104:107], v[128:131], v[8:11]
ds_read_b64_tr_b16 a[96:97], v232 offset:1024
ds_read_b64_tr_b16 a[98:99], v232 offset:1536
v_mfma_f32_16x16x32_bf16 v[8:11], a[108:111], v[136:139], v[8:11]
v_mfma_f32_16x16x32_bf16 v[12:15], a[104:107], v[132:135], v[12:15]
s_add_u32 m0, s54, 13184
ds_read_b64_tr_b16 a[100:101], v232 offset:13312
ds_read_b64_tr_b16 a[102:103], v232 offset:13824
v_mfma_f32_16x16x32_bf16 v[12:15], a[108:111], v[140:143], v[12:15]
s_waitcnt lgkmcnt(4)
buffer_load_dwordx4 %[vK], %[srdK], s75 offen offset:128 nt lds
v_mfma_f32_16x16x32_bf16 v[16:19], a[112:115], v[128:131], v[16:19]
ds_read_b64_tr_b16 a[104:105], v233 offset:1024
ds_read_b64_tr_b16 a[106:107], v233 offset:1536
v_mfma_f32_16x16x32_bf16 v[16:19], a[116:119], v[136:139], v[16:19]
v_mfma_f32_16x16x32_bf16 v[20:23], a[112:115], v[132:135], v[20:23]
s_add_u32 m0, s54, 14080
ds_read_b64_tr_b16 a[108:109], v233 offset:13312
ds_read_b64_tr_b16 a[110:111], v233 offset:13824
v_mfma_f32_16x16x32_bf16 v[20:23], a[116:119], v[140:143], v[20:23]
buffer_load_dwordx4 %[vK], %[srdK], s75 offen offset:256 nt lds
v_mfma_f32_16x16x32_bf16 v[24:27], a[120:123], v[128:131], v[24:27]
ds_read_b64_tr_b16 a[112:113], v234 offset:1024
)"
R"(ds_read_b64_tr_b16 a[114:115], v234 offset:1536
v_mfma_f32_16x16x32_bf16 v[24:27], a[124:127], v[136:139], v[24:27]
v_mfma_f32_16x16x32_bf16 v[28:31], a[120:123], v[132:135], v[28:31]
s_add_u32 m0, s73, 15232
ds_read_b64_tr_b16 a[116:117], v234 offset:13312
ds_read_b64_tr_b16 a[118:119], v234 offset:13824
v_mfma_f32_16x16x32_bf16 v[28:31], a[124:127], v[140:143], v[28:31]
s_waitcnt lgkmcnt(4)
buffer_load_dwordx4 %[vX0], %[srdX], s69 offen offset:1152 lds
v_mfma_f32_16x16x32_bf16 v[32:35], a[96:99], v[128:131], v[32:35]
ds_read_b64_tr_b16 a[120:121], v235 offset:1024
ds_read_b64_tr_b16 a[122:123], v235 offset:1536
v_mfma_f32_16x16x32_bf16 v[32:35], a[100:103], v[136:139], v[32:35]
v_mfma_f32_16x16x32_bf16 v[36:39], a[96:99], v[132:135], v[36:39]
s_add_u32 m0, s73, 16256
ds_read_b64_tr_b16 a[124:125], v235 offset:13312
ds_read_b64_tr_b16 a[126:127], v235 offset:13824
v_mfma_f32_16x16x32_bf16 v[36:39], a[100:103], v[140:143], v[36:39]
buffer_load_dwordx4 %[vX1], %[srdX], s70 offen offset:1152 lds
v_mfma_f32_16x16x32_bf16 v[40:43], a[104:107], v[128:131], v[40:43]
ds_read_b64_tr_b16 a[96:97], v232 offset:2048
ds_read_b64_tr_b16 a[98:99], v232 offset:2560
v_mfma_f32_16x16x32_bf16 v[40:43], a[108:111], v[136:139], v[40:43]
v_mfma_f32_16x16x32_bf16 v[44:47], a[104:107], v[132:135], v[44:47]
)"
R"(s_add_u32 m0, s73, 17280
ds_read_b64_tr_b16 a[100:101], v232 offset:14336
ds_read_b64_tr_b16 a[102:103], v232 offset:14848
v_mfma_f32_16x16x32_bf16 v[44:47], a[108:111], v[140:143], v[44:47]
s_waitcnt lgkmcnt(4)
buffer_load_dwordx4 %[vX0], %[srdX], s71 offen offset:1152 lds
v_mfma_f32_16x16x32_bf16 v[48:51], a[112:115], v[128:131], v[48:51]
ds_read_b64_tr_b16 a[104:105], v233 offset:2048
ds_read_b64_tr_b16 a[106:107], v233 offset:2560
v_mfma_f32_16x16x32_bf16 v[48:51], a[116:119], v[136:139], v[48:51]
v_mfma_f32_16x16x32_bf16 v[52:55], a[112:115], v[132:135], v[52:55]
s_add_u32 m0, s73, 18304
ds_read_b64_tr_b16 a[108:109], v233 offset:14336
ds_read_b64_tr_b16 a[110:111], v233 offset:14848
v_mfma_f32_16x16x32_bf16 v[52:55], a[116:119], v[140:143], v[52:55]
buffer_load_dwordx4 %[vX1], %[srdX], s72 offen offset:1152 lds
v_mfma_f32_16x16x32_bf16 v[56:59], a[120:123], v[128:131], v[56:59]
ds_read_b64_tr_b16 a[112:113], v234 offset:2048
ds_read_b64_tr_b16 a[114:115], v234 offset:2560
v_mfma_f32_16x16x32_bf16 v[56:59], a[124:127], v[136:139], v[56:59]
v_mfma_f32_16x16x32_bf16 v[60:63], a[120:123], v[132:135], v[60:63]
ds_read_b64_tr_b16 a[116:117], v234 offset:14336
ds_read_b64_tr_b16 a[118:119], v234 offset:14848
v_mfma_f32_16x16x32_bf16 v[60:63], a[124:127], v[140:143], v[60:63]
)"
R"(s_waitcnt lgkmcnt(4)
s_waitcnt vmcnt(10)
s_barrier
v_mfma_f32_16x16x32_bf16 v[64:67], a[96:99], v[128:131], v[64:67]
ds_read_b64_tr_b16 a[120:121], v235 offset:2048
ds_read_b64_tr_b16 a[122:123], v235 offset:2560
v_mfma_f32_16x16x32_bf16 v[64:67], a[100:103], v[136:139], v[64:67]
ds_read_b128 v[96:99], %[pX0] offset:0
ds_read_b128 v[100:103], %[pX0] offset:2048
v_mfma_f32_16x16x32_bf16 v[68:71], a[96:99], v[132:135], v[68:71]
ds_read_b64_tr_b16 a[124:125], v235 offset:14336
ds_read_b64_tr_b16 a[126:127], v235 offset:14848
v_mfma_f32_16x16x32_bf16 v[68:71], a[100:103], v[140:143], v[68:71]
ds_read_b128 v[104:107], %[pX1] offset:0
ds_read_b128 v[108:111], %[pX1] offset:2048
v_add_u32 v232, s51, %[pW]
v_xor_b32 v233, 32, v232
v_mfma_f32_16x16x32_bf16 v[72:75], a[104:107], v[128:131], v[72:75]
ds_read_b64_tr_b16 a[96:97], v232 offset:0
ds_read_b64_tr_b16 a[98:99], v232 offset:512
v_mfma_f32_16x16x32_bf16 v[72:75], a[108:111], v[136:139], v[72:75]
s_add_u32 s75, s66, 384
v_xor_b32 v234, 64, v232
v_xor_b32 v235, 96, v232
)"
R"(v_mfma_f32_16x16x32_bf16 v[76:79], a[104:107], v[132:135], v[76:79]
s_add_u32 m0, s55, 0
ds_read_b64_tr_b16 a[100:101], v232 offset:12288
ds_read_b64_tr_b16 a[102:103], v232 offset:12800
v_mfma_f32_16x16x32_bf16 v[76:79], a[108:111], v[140:143], v[76:79]
s_waitcnt lgkmcnt(6)
buffer_load_dwordx4 %[vW], %[srdW], s75 offen offset:0 lds
v_mfma_f32_16x16x32_bf16 v[80:83], a[112:115], v[128:131], v[80:83]
ds_read_b64_tr_b16 a[104:105], v233 offset:0
ds_read_b64_tr_b16 a[106:107], v233 offset:512
v_mfma_f32_16x16x32_bf16 v[80:83], a[116:119], v[136:139], v[80:83]
v_mfma_f32_16x16x32_bf16 v[84:87], a[112:115], v[132:135], v[84:87]
s_add_u32 m0, s55, 896
ds_read_b64_tr_b16 a[108:109], v233 offset:12288
ds_read_b64_tr_b16 a[110:111], v233 offset:12800
v_mfma_f32_16x16x32_bf16 v[84:87], a[116:119], v[140:143], v[84:87]
buffer_load_dwordx4 %[vW], %[srdW], s75 offen offset:128 lds
v_mfma_f32_16x16x32_bf16 v[88:91], a[120:123], v[128:131], v[88:91]
ds_read_b64_tr_b16 a[112:113], v234 offset:0
ds_read_b64_tr_b16 a[114:115], v234 offset:512
v_mfma_f32_16x16x32_bf16 v[88:91], a[124:127], v[136:139], v[88:91]
v_mfma_f32_16x16x32_bf16 v[92:95], a[120:123], v[132:135], v[92:95]
s_add_u32 m0, s55, 1792
ds_read_b64_tr_b16 a[116:117], v234 offset:12288
)"
R"(ds_read_b64_tr_b16 a[118:119], v234 offset:12800
v_mfma_f32_16x16x32_bf16 v[92:95], a[124:127], v[140:143], v[92:95]
s_waitcnt lgkmcnt(4)
buffer_load_dwordx4 %[vW], %[srdW], s75 offen offset:256 lds
v_mfma_f32_16x16x32_bf16 v[0:3], a[96:99], v[96:99], v[0:3]
ds_read_b64_tr_b16 a[120:121], v235 offset:0
ds_read_b64_tr_b16 a[122:123], v235 offset:512
v_mfma_f32_16x16x32_bf16 v[0:3], a[100:103], v[104:107], v[0:3]
s_add_u32 s75, s66, 393600
v_mfma_f32_16x16x32_bf16 v[4:7], a[96:99], v[100:103], v[4:7]
s_add_u32 m0, s55, 12288
ds_read_b64_tr_b16 a[124:125], v235 offset:12288
ds_read_b64_tr_b16 a[126:127], v235 offset:12800
v_mfma_f32_16x16x32_bf16 v[4:7], a[100:103], v[108:111], v[4:7]
buffer_load_dwordx4 %[vW], %[srdW], s75 offen offset:0 lds
v_mfma_f32_16x16x32_bf16 v[8:11], a[104:107], v[96:99], v[8:11]
ds_read_b64_tr_b16 a[96:97], v232 offset:1024
ds_read_b64_tr_b16 a[98:99], v232 offset:1536
v_mfma_f32_16x16x32_bf16 v[8:11], a[108:111], v[104:107], v[8:11]
v_mfma_f32_16x16x32_bf16 v[12:15], a[104:107], v[100:103], v[12:15]
s_add_u32 m0, s55, 13184
ds_read_b64_tr_b16 a[100:101], v232 offset:13312
ds_read_b64_tr_b16 a[102:103], v232 offset:13824
v_mfma_f32_16x16x32_bf16 v[12:15], a[108:111], v[108:111], v[12:15]
)"
R"(s_waitcnt lgkmcnt(4)
buffer_load_dwordx4 %[vW], %[srdW], s75 offen offset:128 lds
v_mfma_f32_16x16x32_bf16 v[16:19], a[112:115], v[96:99], v[16:19]
ds_read_b64_tr_b16 a[104:105], v233 offset:1024
ds_read_b64_tr_b16 a[106:107], v233 offset:1536
v_mfma_f32_16x16x32_bf16 v[16:19], a[116:119], v[104:107], v[16:19]
v_mfma_f32_16x16x32_bf16 v[20:23], a[112:115], v[100:103], v[20:23]
s_add_u32 m0, s55, 14080
ds_read_b64_tr_b16 a[108:109], v233 offset:13312
ds_read_b64_tr_b16 a[110:111], v233 offset:13824
v_mfma_f32_16x16x32_bf16 v[20:23], a[116:119], v[108:111], v[20:23]
buffer_load_dwordx4 %[vW], %[srdW], s75 offen offset:256 lds
v_mfma_f32_16x16x32_bf16 v[24:27], a[120:123], v[96:99], v[24:27]
ds_read_b64_tr_b16 a[112:113], v234 offset:1024
ds_read_b64_tr_b16 a[114:115], v234 offset:1536
v_mfma_f32_16x16x32_bf16 v[24:27], a[124:127], v[104:107], v[24:27]
v_mfma_f32_16x16x32_bf16 v[28:31], a[120:123], v[100:103], v[28:31]
s_add_u32 m0, s73, 0
ds_read_b64_tr_b16 a[116:117], v234 offset:13312
ds_read_b64_tr_b16 a[118:119], v234 offset:13824
v_mfma_f32_16x16x32_bf16 v[28:31], a[124:127], v[108:111], v[28:31]
s_waitcnt lgkmcnt(4)
buffer_load_dwordx4 %[vX0], %[srdX], s69 offen offset:0 lds
v_mfma_f32_16x16x32_bf16 v[32:35], a[96:99], v[96:99], v[32:35]
)"
R"(ds_read_b64_tr_b16 a[120:121], v235 offset:1024
ds_read_b64_tr_b16 a[122:123], v235 offset:1536
v_mfma_f32_16x16x32_bf16 v[32:35], a[100:103], v[104:107], v[32:35]
v_mfma_f32_16x16x32_bf16 v[36:39], a[96:99], v[100:103], v[36:39]
s_add_u32 m0, s73, 1024
ds_read_b64_tr_b16 a[124:125], v235 offset:13312
ds_read_b64_tr_b16 a[126:127], v235 offset:13824
v_mfma_f32_16x16x32_bf16 v[36:39], a[100:103], v[108:111], v[36:39]
buffer_load_dwordx4 %[vX1], %[srdX], s70 offen offset:0 lds
v_mfma_f32_16x16x32_bf16 v[40:43], a[104:107], v[96:99], v[40:43]
ds_read_b64_tr_b16 a[96:97], v232 offset:2048
ds_read_b64_tr_b16 a[98:99], v232 offset:2560
v_mfma_f32_16x16x32_bf16 v[40:43], a[108:111], v[104:107], v[40:43]
v_mfma_f32_16x16x32_bf16 v[44:47], a[104:107], v[100:103], v[44:47]
s_add_u32 m0, s73, 2048
ds_read_b64_tr_b16 a[100:101], v232 offset:14336
ds_read_b64_tr_b16 a[102:103], v232 offset:14848
v_mfma_f32_16x16x32_bf16 v[44:47], a[108:111], v[108:111], v[44:47]
s_waitcnt lgkmcnt(4)
buffer_load_dwordx4 %[vX0], %[srdX], s71 offen offset:0 lds
v_mfma_f32_16x16x32_bf16 v[48:51], a[112:115], v[96:99], v[48:51]
ds_read_b64_tr_b16 a[104:105], v233 offset:2048
ds_read_b64_tr_b16 a[106:107], v233 offset:2560
v_mfma_f32_16x16x32_bf16 v[48:51], a[116:119], v[104:107], v[48:51]
)"
R"(v_mfma_f32_16x16x32_bf16 v[52:55], a[112:115], v[100:103], v[52:55]
s_add_u32 m0, s73, 3072
ds_read_b64_tr_b16 a[108:109], v233 offset:14336
ds_read_b64_tr_b16 a[110:111], v233 offset:14848
v_mfma_f32_16x16x32_bf16 v[52:55], a[116:119], v[108:111], v[52:55]
buffer_load_dwordx4 %[vX1], %[srdX], s72 offen offset:0 lds
v_mfma_f32_16x16x32_bf16 v[56:59], a[120:123], v[96:99], v[56:59]
ds_read_b64_tr_b16 a[112:113], v234 offset:2048
ds_read_b64_tr_b16 a[114:115], v234 offset:2560
v_mfma_f32_16x16x32_bf16 v[56:59], a[124:127], v[104:107], v[56:59]
v_mfma_f32_16x16x32_bf16 v[60:63], a[120:123], v[100:103], v[60:63]
ds_read_b64_tr_b16 a[116:117], v234 offset:14336
ds_read_b64_tr_b16 a[118:119], v234 offset:14848
v_mfma_f32_16x16x32_bf16 v[60:63], a[124:127], v[108:111], v[60:63]
s_waitcnt lgkmcnt(4)
s_waitcnt vmcnt(10)
s_barrier
v_mfma_f32_16x16x32_bf16 v[64:67], a[96:99], v[96:99], v[64:67]
ds_read_b64_tr_b16 a[120:121], v235 offset:2048
ds_read_b64_tr_b16 a[122:123], v235 offset:2560
v_mfma_f32_16x16x32_bf16 v[64:67], a[100:103], v[104:107], v[64:67]
ds_read_b128 v[128:131], %[pX0] offset:16384
ds_read_b128 v[132:135], %[pX0] offset:18432
v_mfma_f32_16x16x32_bf16 v[68:71], a[96:99], v[100:103], v[68:71]
)"
R"(ds_read_b64_tr_b16 a[124:125], v235 offset:14336
ds_read_b64_tr_b16 a[126:127], v235 offset:14848
v_mfma_f32_16x16x32_bf16 v[68:71], a[100:103], v[108:111], v[68:71]
ds_read_b128 v[136:139], %[pX1] offset:16384
ds_read_b128 v[140:143], %[pX1] offset:18432
v_add_u32 v232, s52, %[pW]
v_xor_b32 v233, 32, v232
v_mfma_f32_16x16x32_bf16 v[72:75], a[104:107], v[96:99], v[72:75]
ds_read_b64_tr_b16 a[96:97], v232 offset:0
ds_read_b64_tr_b16 a[98:99], v232 offset:512
v_mfma_f32_16x16x32_bf16 v[72:75], a[108:111], v[104:107], v[72:75]
s_add_u32 s75, s67, 24576
v_xor_b32 v234, 64, v232
v_xor_b32 v235, 96, v232
v_mfma_f32_16x16x32_bf16 v[76:79], a[104:107], v[100:103], v[76:79]
s_add_u32 m0, s56, 0
ds_read_b64_tr_b16 a[100:101], v232 offset:12288
ds_read_b64_tr_b16 a[102:103], v232 offset:12800
v_mfma_f32_16x16x32_bf16 v[76:79], a[108:111], v[108:111], v[76:79]
s_waitcnt lgkmcnt(6)
buffer_load_dwordx4 %[vK], %[srdK], s75 offen offset:0 nt lds
v_mfma_f32_16x16x32_bf16 v[80:83], a[112:115], v[96:99], v[80:83]
ds_read_b64_tr_b16 a[104:105], v233 offset:0
ds_read_b64_tr_b16 a[106:107], v233 offset:512
)"
R"(v_mfma_f32_16x16x32_bf16 v[80:83], a[116:119], v[104:107], v[80:83]
v_mfma_f32_16x16x32_bf16 v[84:87], a[112:115], v[100:103], v[84:87]
s_add_u32 m0, s56, 896
ds_read_b64_tr_b16 a[108:109], v233 offset:12288
ds_read_b64_tr_b16 a[110:111], v233 offset:12800
v_mfma_f32_16x16x32_bf16 v[84:87], a[116:119], v[108:111], v[84:87]
buffer_load_dwordx4 %[vK], %[srdK], s75 offen offset:128 nt lds
v_mfma_f32_16x16x32_bf16 v[88:91], a[120:123], v[96:99], v[88:91]
ds_read_b64_tr_b16 a[112:113], v234 offset:0
ds_read_b64_tr_b16 a[114:115], v234 offset:512
v_mfma_f32_16x16x32_bf16 v[88:91], a[124:127], v[104:107], v[88:91]
v_mfma_f32_16x16x32_bf16 v[92:95], a[120:123], v[100:103], v[92:95]
s_add_u32 m0, s56, 1792
ds_read_b64_tr_b16 a[116:117], v234 offset:12288
ds_read_b64_tr_b16 a[118:119], v234 offset:12800
v_mfma_f32_16x16x32_bf16 v[92:95], a[124:127], v[108:111], v[92:95]
s_waitcnt lgkmcnt(4)
buffer_load_dwordx4 %[vK], %[srdK], s75 offen offset:256 nt lds
v_mfma_f32_16x16x32_bf16 v[0:3], a[96:99], v[128:131], v[0:3]
ds_read_b64_tr_b16 a[120:121], v235 offset:0
ds_read_b64_tr_b16 a[122:123], v235 offset:512
v_mfma_f32_16x16x32_bf16 v[0:3], a[100:103], v[136:139], v[0:3]
s_add_u32 s75, s67, 36864
v_mfma_f32_16x16x32_bf16 v[4:7], a[96:99], v[132:135], v[4:7]
)"
R"(s_add_u32 m0, s56, 12288
ds_read_b64_tr_b16 a[124:125], v235 offset:12288
ds_read_b64_tr_b16 a[126:127], v235 offset:12800
v_mfma_f32_16x16x32_bf16 v[4:7], a[100:103], v[140:143], v[4:7]
buffer_load_dwordx4 %[vK], %[srdK], s75 offen offset:0 nt lds
v_mfma_f32_16x16x32_bf16 v[8:11], a[104:107], v[128:131], v[8:11]
ds_read_b64_tr_b16 a[96:97], v232 offset:1024
ds_read_b64_tr_b16 a[98:99], v232 offset:1536
v_mfma_f32_16x16x32_bf16 v[8:11], a[108:111], v[136:139], v[8:11]
v_mfma_f32_16x16x32_bf16 v[12:15], a[104:107], v[132:135], v[12:15]
s_add_u32 m0, s56, 13184
ds_read_b64_tr_b16 a[100:101], v232 offset:13312
ds_read_b64_tr_b16 a[102:103], v232 offset:13824
v_mfma_f32_16x16x32_bf16 v[12:15], a[108:111], v[140:143], v[12:15]
s_waitcnt lgkmcnt(4)
buffer_load_dwordx4 %[vK], %[srdK], s75 offen offset:128 nt lds
v_mfma_f32_16x16x32_bf16 v[16:19], a[112:115], v[128:131], v[16:19]
ds_read_b64_tr_b16 a[104:105], v233 offset:1024
ds_read_b64_tr_b16 a[106:107], v233 offset:1536
v_mfma_f32_16x16x32_bf16 v[16:19], a[116:119], v[136:139], v[16:19]
v_mfma_f32_16x16x32_bf16 v[20:23], a[112:115], v[132:135], v[20:23]
s_add_u32 m0, s56, 14080
ds_read_b64_tr_b16 a[108:109], v233 offset:13312
ds_read_b64_tr_b16 a[110:111], v233 offset:13824
)"
R"(v_mfma_f32_16x16x32_bf16 v[20:23], a[116:119], v[140:143], v[20:23]
buffer_load_dwordx4 %[vK], %[srdK], s75 offen offset:256 nt lds
v_mfma_f32_16x16x32_bf16 v[24:27], a[120:123], v[128:131], v[24:27]
ds_read_b64_tr_b16 a[112:113], v234 offset:1024
ds_read_b64_tr_b16 a[114:115], v234 offset:1536
v_mfma_f32_16x16x32_bf16 v[24:27], a[124:127], v[136:139], v[24:27]
v_mfma_f32_16x16x32_bf16 v[28:31], a[120:123], v[132:135], v[28:31]
s_add_u32 m0, s73, 16256
ds_read_b64_tr_b16 a[116:117], v234 offset:13312
ds_read_b64_tr_b16 a[118:119], v234 offset:13824
v_mfma_f32_16x16x32_bf16 v[28:31], a[124:127], v[140:143], v[28:31]
s_waitcnt lgkmcnt(4)
buffer_load_dwordx4 %[vX0], %[srdX], s69 offen offset:128 lds
v_mfma_f32_16x16x32_bf16 v[32:35], a[96:99], v[128:131], v[32:35]
ds_read_b64_tr_b16 a[120:121], v235 offset:1024
ds_read_b64_tr_b16 a[122:123], v235 offset:1536
v_mfma_f32_16x16x32_bf16 v[32:35], a[100:103], v[136:139], v[32:35]
v_mfma_f32_16x16x32_bf16 v[36:39], a[96:99], v[132:135], v[36:39]
s_add_u32 m0, s73, 17280
ds_read_b64_tr_b16 a[124:125], v235 offset:13312
ds_read_b64_tr_b16 a[126:127], v235 offset:13824
v_mfma_f32_16x16x32_bf16 v[36:39], a[100:103], v[140:143], v[36:39]
buffer_load_dwordx4 %[vX1], %[srdX], s70 offen offset:128 lds
v_mfma_f32_16x16x32_bf16 v[40:43], a[104:107], v[128:131], v[40:43]
)"
R"(ds_read_b64_tr_b16 a[96:97], v232 offset:2048
ds_read_b64_tr_b16 a[98:99], v232 offset:2560
v_mfma_f32_16x16x32_bf16 v[40:43], a[108:111], v[136:139], v[40:43]
v_mfma_f32_16x16x32_bf16 v[44:47], a[104:107], v[132:135], v[44:47]
s_add_u32 m0, s73, 18304
ds_read_b64_tr_b16 a[100:101], v232 offset:14336
ds_read_b64_tr_b16 a[102:103], v232 offset:14848
v_mfma_f32_16x16x32_bf16 v[44:47], a[108:111], v[140:143], v[44:47]
s_waitcnt lgkmcnt(4)
buffer_load_dwordx4 %[vX0], %[srdX], s71 offen offset:128 lds
v_mfma_f32_16x16x32_bf16 v[48:51], a[112:115], v[128:131], v[48:51]
ds_read_b64_tr_b16 a[104:105], v233 offset:2048
ds_read_b64_tr_b16 a[106:107], v233 offset:2560
v_mfma_f32_16x16x32_bf16 v[48:51], a[116:119], v[136:139], v[48:51]
v_mfma_f32_16x16x32_bf16 v[52:55], a[112:115], v[132:135], v[52:55]
s_add_u32 m0, s73, 19328
ds_read_b64_tr_b16 a[108:109], v233 offset:14336
ds_read_b64_tr_b16 a[110:111], v233 offset:14848
v_mfma_f32_16x16x32_bf16 v[52:55], a[116:119], v[140:143], v[52:55]
buffer_load_dwordx4 %[vX1], %[srdX], s72 offen offset:128 lds
v_mfma_f32_16x16x32_bf16 v[56:59], a[120:123], v[128:131], v[56:59]
ds_read_b64_tr_b16 a[112:113], v234 offset:2048
ds_read_b64_tr_b16 a[114:115], v234 offset:2560
v_mfma_f32_16x16x32_bf16 v[56:59], a[124:127], v[136:139], v[56:59]
)"
R"(v_mfma_f32_16x16x32_bf16 v[60:63], a[120:123], v[132:135], v[60:63]
s_add_u32 s76, s68, 0
ds_read_b64_tr_b16 a[116:117], v234 offset:14336
ds_read_b64_tr_b16 a[118:119], v234 offset:14848
v_mfma_f32_16x16x32_bf16 v[60:63], a[124:127], v[140:143], v[60:63]
s_waitcnt lgkmcnt(4)
s_waitcnt vmcnt(24)
s_barrier
v_mfma_f32_16x16x32_bf16 v[64:67], a[96:99], v[128:131], v[64:67]
s_add_u32 m0, s63, 0
ds_read_b64_tr_b16 a[120:121], v235 offset:2048
ds_read_b64_tr_b16 a[122:123], v235 offset:2560
v_mfma_f32_16x16x32_bf16 v[64:67], a[100:103], v[136:139], v[64:67]
buffer_load_dwordx4 %[vV], %[srdV], s76 offen offset:0 nt lds
v_mfma_f32_16x16x32_bf16 v[68:71], a[96:99], v[132:135], v[68:71]
ds_read_b64_tr_b16 a[124:125], v235 offset:14336
ds_read_b64_tr_b16 a[126:127], v235 offset:14848
v_mfma_f32_16x16x32_bf16 v[68:71], a[100:103], v[140:143], v[68:71]
v_add_u32 v232, s48, %[pK]
v_xor_b32 v233, 64, v232
v_mfma_f32_16x16x32_bf16 v[72:75], a[104:107], v[128:131], v[72:75]
s_add_u32 m0, s63, 896
ds_read_b128 a[96:99], v232 offset:0
ds_read_b128 a[100:103], v233 offset:0
)"
R"(v_mfma_f32_16x16x32_bf16 v[72:75], a[108:111], v[136:139], v[72:75]
buffer_load_dwordx4 %[vV], %[srdV], s76 offen offset:128 nt lds
v_mfma_f32_16x16x32_bf16 v[76:79], a[104:107], v[132:135], v[76:79]
v_mfma_f32_16x16x32_bf16 v[76:79], a[108:111], v[140:143], v[76:79]
s_waitcnt lgkmcnt(2)
v_mfma_f32_16x16x32_bf16 v[80:83], a[112:115], v[128:131], v[80:83]
ds_read_b128 a[104:107], v232 offset:1024
ds_read_b128 a[108:111], v233 offset:1024
v_mfma_f32_16x16x32_bf16 v[80:83], a[116:119], v[136:139], v[80:83]
v_mfma_f32_16x16x32_bf16 v[84:87], a[112:115], v[132:135], v[84:87]
v_mfma_f32_16x16x32_bf16 v[84:87], a[116:119], v[140:143], v[84:87]
v_mfma_f32_16x16x32_bf16 v[88:91], a[120:123], v[128:131], v[88:91]
ds_read_b128 a[112:115], v232 offset:2048
ds_read_b128 a[116:119], v233 offset:2048
v_mfma_f32_16x16x32_bf16 v[88:91], a[124:127], v[136:139], v[88:91]
v_mfma_f32_16x16x32_bf16 v[92:95], a[120:123], v[132:135], v[92:95]
v_mfma_f32_16x16x32_bf16 v[92:95], a[124:127], v[140:143], v[92:95]
v_bfe_u32 v236, %[vP], 4, 6
v_min_u32 v236, 29, v236
v_mul_u32_u24 v237, 43, v236
v_lshrrev_b32 v237, 7, v237
v_lshlrev_b32 v236, 7, v236
v_mul_u32_u24 v237, 11904, v237
v_add_u32 v236, v236, v237
)"
R"(s_mov_b32 m0, s84
s_nop 0
buffer_load_dword v236, %[srdW], s85 offen offset:768 lds
v_pk_mul_f32 v[0:1], v[0:1], s[80:81]
v_pk_mul_f32 v[2:3], v[2:3], s[80:81]
v_pk_mul_f32 v[4:5], v[4:5], s[80:81]
v_pk_mul_f32 v[6:7], v[6:7], s[80:81]
v_pk_mul_f32 v[8:9], v[8:9], s[80:81]
v_pk_mul_f32 v[10:11], v[10:11], s[80:81]
v_pk_mul_f32 v[12:13], v[12:13], s[80:81]
v_pk_mul_f32 v[14:15], v[14:15], s[80:81]
v_pk_mul_f32 v[16:17], v[16:17], s[80:81]
v_pk_mul_f32 v[18:19], v[18:19], s[80:81]
v_pk_mul_f32 v[20:21], v[20:21], s[80:81]
v_pk_mul_f32 v[22:23], v[22:23], s[80:81]
v_pk_mul_f32 v[24:25], v[24:25], s[80:81]
v_pk_mul_f32 v[26:27], v[26:27], s[80:81]
v_pk_mul_f32 v[28:29], v[28:29], s[80:81]
v_pk_mul_f32 v[30:31], v[30:31], s[80:81]
v_pk_mul_f32 v[32:33], v[32:33], s[80:81]
v_pk_mul_f32 v[34:35], v[34:35], s[80:81]
v_pk_mul_f32 v[36:37], v[36:37], s[80:81]
v_pk_mul_f32 v[38:39], v[38:39], s[80:81]
v_pk_mul_f32 v[40:41], v[40:41], s[80:81]
)"
R"(v_pk_mul_f32 v[42:43], v[42:43], s[80:81]
v_pk_mul_f32 v[44:45], v[44:45], s[80:81]
v_pk_mul_f32 v[46:47], v[46:47], s[80:81]
v_pk_mul_f32 v[48:49], v[48:49], s[80:81]
v_pk_mul_f32 v[50:51], v[50:51], s[80:81]
v_pk_mul_f32 v[52:53], v[52:53], s[80:81]
v_pk_mul_f32 v[54:55], v[54:55], s[80:81]
v_pk_mul_f32 v[56:57], v[56:57], s[80:81]
v_pk_mul_f32 v[58:59], v[58:59], s[80:81]
v_pk_mul_f32 v[60:61], v[60:61], s[80:81]
v_pk_mul_f32 v[62:63], v[62:63], s[80:81]
v_pk_mul_f32 v[64:65], v[64:65], s[80:81]
v_pk_mul_f32 v[66:67], v[66:67], s[80:81]
v_pk_mul_f32 v[68:69], v[68:69], s[80:81]
v_pk_mul_f32 v[70:71], v[70:71], s[80:81]
v_pk_mul_f32 v[72:73], v[72:73], s[80:81]
v_pk_mul_f32 v[74:75], v[74:75], s[80:81]
v_pk_mul_f32 v[76:77], v[76:77], s[80:81]
v_pk_mul_f32 v[78:79], v[78:79], s[80:81]
v_pk_mul_f32 v[80:81], v[80:81], s[80:81]
v_pk_mul_f32 v[82:83], v[82:83], s[80:81]
v_pk_mul_f32 v[84:85], v[84:85], s[80:81]
v_pk_mul_f32 v[86:87], v[86:87], s[80:81]
v_pk_mul_f32 v[88:89], v[88:89], s[80:81]
)"
R"(v_pk_mul_f32 v[90:91], v[90:91], s[80:81]
v_pk_mul_f32 v[92:93], v[92:93], s[80:81]
v_pk_mul_f32 v[94:95], v[94:95], s[80:81]
v_cvt_pk_bf16_f32 v184, v0, v1
v_cvt_pk_bf16_f32 v185, v2, v3
v_cvt_pk_bf16_f32 v186, v8, v9
v_cvt_pk_bf16_f32 v187, v10, v11
v_cvt_pk_bf16_f32 v188, v4, v5
v_cvt_pk_bf16_f32 v189, v6, v7
v_cvt_pk_bf16_f32 v190, v12, v13
v_cvt_pk_bf16_f32 v191, v14, v15
v_permlane16_swap_b32 v184, v186
v_permlane16_swap_b32 v185, v187
v_cvt_pk_bf16_f32 v192, v16, v17
v_cvt_pk_bf16_f32 v193, v18, v19
v_cvt_pk_bf16_f32 v194, v24, v25
v_cvt_pk_bf16_f32 v195, v26, v27
v_permlane16_swap_b32 v188, v190
v_permlane16_swap_b32 v189, v191
v_cvt_pk_bf16_f32 v196, v20, v21
v_cvt_pk_bf16_f32 v197, v22, v23
v_cvt_pk_bf16_f32 v198, v28, v29
v_cvt_pk_bf16_f32 v199, v30, v31
v_permlane16_swap_b32 v192, v194
)"
R"(v_permlane16_swap_b32 v193, v195
v_cvt_pk_bf16_f32 v200, v32, v33
v_cvt_pk_bf16_f32 v201, v34, v35
v_cvt_pk_bf16_f32 v202, v40, v41
v_cvt_pk_bf16_f32 v203, v42, v43
v_permlane16_swap_b32 v196, v198
v_permlane16_swap_b32 v197, v199
v_cvt_pk_bf16_f32 v204, v36, v37
v_cvt_pk_bf16_f32 v205, v38, v39
v_cvt_pk_bf16_f32 v206, v44, v45
v_cvt_pk_bf16_f32 v207, v46, v47
v_permlane16_swap_b32 v200, v202
v_permlane16_swap_b32 v201, v203
v_cvt_pk_bf16_f32 v208, v48, v49
v_cvt_pk_bf16_f32 v209, v50, v51
v_cvt_pk_bf16_f32 v210, v56, v57
v_cvt_pk_bf16_f32 v211, v58, v59
v_permlane16_swap_b32 v204, v206
v_permlane16_swap_b32 v205, v207
v_cvt_pk_bf16_f32 v212, v52, v53
v_cvt_pk_bf16_f32 v213, v54, v55
v_cvt_pk_bf16_f32 v214, v60, v61
v_cvt_pk_bf16_f32 v215, v62, v63
v_permlane16_swap_b32 v208, v210
)"
R"(v_permlane16_swap_b32 v209, v211
v_cvt_pk_bf16_f32 v216, v64, v65
v_cvt_pk_bf16_f32 v217, v66, v67
v_cvt_pk_bf16_f32 v218, v72, v73
v_cvt_pk_bf16_f32 v219, v74, v75
v_permlane16_swap_b32 v212, v214
v_permlane16_swap_b32 v213, v215
v_cvt_pk_bf16_f32 v220, v68, v69
v_cvt_pk_bf16_f32 v221, v70, v71
v_cvt_pk_bf16_f32 v222, v76, v77
v_cvt_pk_bf16_f32 v223, v78, v79
v_permlane16_swap_b32 v216, v218
v_permlane16_swap_b32 v217, v219
v_cvt_pk_bf16_f32 v224, v80, v81
v_cvt_pk_bf16_f32 v225, v82, v83
v_cvt_pk_bf16_f32 v226, v88, v89
v_cvt_pk_bf16_f32 v227, v90, v91
v_permlane16_swap_b32 v220, v222
v_permlane16_swap_b32 v221, v223
v_cvt_pk_bf16_f32 v228, v84, v85
v_cvt_pk_bf16_f32 v229, v86, v87
v_cvt_pk_bf16_f32 v230, v92, v93
v_cvt_pk_bf16_f32 v231, v94, v95
v_permlane16_swap_b32 v224, v226
)"
R"(v_permlane16_swap_b32 v225, v227
v_permlane16_swap_b32 v228, v230
v_permlane16_swap_b32 v229, v231
HEAD_%=:
s_cmp_eq_u32 s74, 2
s_cselect_b32 s86, 0x80000000, s66
s_waitcnt lgkmcnt(3)
v_mfma_f32_16x16x32_bf16 v[96:99], a[96:99], v[184:187], 0
ds_read_b128 a[120:123], v232 offset:6144
ds_read_b128 a[124:127], v233 offset:6144
v_mfma_f32_16x16x32_bf16 v[96:99], a[100:103], v[192:195], v[96:99]
v_mfma_f32_16x16x32_bf16 v[96:99], a[104:107], v[200:203], v[96:99]
s_add_u32 m0, s63, 1792
v_mfma_f32_16x16x32_bf16 v[100:103], a[96:99], v[188:191], 0
buffer_load_dwordx4 %[vV], %[srdV], s76 offen offset:256 nt lds
v_mfma_f32_16x16x32_bf16 v[100:103], a[100:103], v[196:199], v[100:103]
ds_read_b128 a[96:99], v232 offset:7168
ds_read_b128 a[100:103], v233 offset:7168
v_mfma_f32_16x16x32_bf16 v[100:103], a[104:107], v[204:207], v[100:103]
s_waitcnt lgkmcnt(4)
v_mfma_f32_16x16x32_bf16 v[96:99], a[108:111], v[208:211], v[96:99]
s_add_u32 m0, s63, 0
v_mfma_f32_16x16x32_bf16 v[96:99], a[112:115], v[216:219], v[96:99]
buffer_load_dwordx4 %[vV], %[srdV], s76 offen offset:3072 nt lds
)"
R"(v_mfma_f32_16x16x32_bf16 v[96:99], a[116:119], v[224:227], v[96:99]
v_mfma_f32_16x16x32_bf16 v[100:103], a[108:111], v[212:215], v[100:103]
ds_read_b128 a[104:107], v232 offset:8192
ds_read_b128 a[108:111], v233 offset:8192
v_mfma_f32_16x16x32_bf16 v[100:103], a[112:115], v[220:223], v[100:103]
s_add_u32 m0, s63, 896
v_mfma_f32_16x16x32_bf16 v[100:103], a[116:119], v[228:231], v[100:103]
s_waitcnt lgkmcnt(3)
buffer_load_dwordx4 %[vV], %[srdV], s76 offen offset:3200 nt lds
v_mfma_f32_16x16x32_bf16 v[104:107], a[120:123], v[184:187], 0
ds_read_b128 a[112:115], v232 offset:12288
ds_read_b128 a[116:119], v233 offset:12288
v_mfma_f32_16x16x32_bf16 v[104:107], a[124:127], v[192:195], v[104:107]
v_mfma_f32_16x16x32_bf16 v[104:107], a[96:99], v[200:203], v[104:107]
s_add_u32 m0, s63, 1792
v_max3_f32 v238, v96, v97, v98
v_max3_f32 v240, v100, v101, v102
v_max_f32 v238, v238, v99
v_max_f32 v240, v240, v103
v_mfma_f32_16x16x32_bf16 v[108:111], a[120:123], v[188:191], 0
buffer_load_dwordx4 %[vV], %[srdV], s76 offen offset:3328 nt lds
v_mov_b32 v239, v238
v_mov_b32 v241, v240
v_mfma_f32_16x16x32_bf16 v[108:111], a[124:127], v[196:199], v[108:111]
)"
R"(ds_read_b128 a[120:123], v232 offset:13312
ds_read_b128 a[124:127], v233 offset:13312
v_permlane16_swap_b32 v238, v239
v_mfma_f32_16x16x32_bf16 v[108:111], a[96:99], v[204:207], v[108:111]
s_waitcnt lgkmcnt(4)
v_permlane16_swap_b32 v240, v241
v_max_f32 v238, v238, v239
v_max_f32 v240, v240, v241
v_mfma_f32_16x16x32_bf16 v[104:107], a[100:103], v[208:211], v[104:107]
v_mov_b32 v239, v238
v_mov_b32 v241, v240
s_nop 0
v_permlane32_swap_b32 v238, v239
v_mfma_f32_16x16x32_bf16 v[104:107], a[104:107], v[216:219], v[104:107]
v_permlane32_swap_b32 v240, v241
v_max_f32 v238, v238, v239
v_max_f32 v240, v240, v241
v_mfma_f32_16x16x32_bf16 v[104:107], a[108:111], v[224:227], v[104:107]
v_sub_f32 v176, 0, v238
v_sub_f32 v180, 0, v240
v_sub_f32 v177, 0, v238
v_sub_f32 v181, 0, v240
v_mfma_f32_16x16x32_bf16 v[108:111], a[100:103], v[212:215], v[108:111]
ds_read_b128 a[96:99], v232 offset:14336
)"
R"(ds_read_b128 a[100:103], v233 offset:14336
v_sub_f32 v178, 0, v238
v_mfma_f32_16x16x32_bf16 v[108:111], a[104:107], v[220:223], v[108:111]
v_sub_f32 v182, 0, v240
v_sub_f32 v179, 0, v238
v_sub_f32 v183, 0, v240
v_mfma_f32_16x16x32_bf16 v[108:111], a[108:111], v[228:231], v[108:111]
s_waitcnt lgkmcnt(3)
v_mfma_f32_16x16x32_bf16 v[112:115], a[112:115], v[184:187], v[176:179]
ds_read_b128 a[104:107], v232 offset:18432
ds_read_b128 a[108:111], v233 offset:18432
v_mfma_f32_16x16x32_bf16 v[112:115], a[116:119], v[192:195], v[112:115]
v_mfma_f32_16x16x32_bf16 v[112:115], a[120:123], v[200:203], v[112:115]
v_mfma_f32_16x16x32_bf16 v[116:119], a[112:115], v[188:191], v[180:183]
v_add_f32 v96, v176, v96
v_add_f32 v97, v176, v97
v_add_f32 v98, v176, v98
v_add_f32 v99, v176, v99
v_mfma_f32_16x16x32_bf16 v[116:119], a[116:119], v[196:199], v[116:119]
ds_read_b128 a[112:115], v232 offset:19456
ds_read_b128 a[116:119], v233 offset:19456
v_add_f32 v100, v180, v100
v_add_f32 v101, v180, v101
v_mfma_f32_16x16x32_bf16 v[116:119], a[120:123], v[204:207], v[116:119]
)"
R"(s_waitcnt lgkmcnt(4)
v_add_f32 v102, v180, v102
v_add_f32 v103, v180, v103
v_add_f32 v104, v176, v104
v_mfma_f32_16x16x32_bf16 v[112:115], a[124:127], v[208:211], v[112:115]
v_add_f32 v105, v176, v105
v_add_f32 v106, v176, v106
v_add_f32 v107, v176, v107
v_mfma_f32_16x16x32_bf16 v[112:115], a[96:99], v[216:219], v[112:115]
s_waitcnt vmcnt(17)
s_barrier
v_add_f32 v108, v180, v108
v_mfma_f32_16x16x32_bf16 v[112:115], a[100:103], v[224:227], v[112:115]
s_add_u32 s75, s66, 786816
v_add_f32 v109, v180, v109
v_add_f32 v110, v180, v110
v_add_f32 v111, v180, v111
v_mfma_f32_16x16x32_bf16 v[116:119], a[124:127], v[212:215], v[116:119]
s_add_u32 m0, s58, 0
ds_read_b128 a[120:123], v232 offset:20480
ds_read_b128 a[124:127], v233 offset:20480
ds_read_b128 a[144:147], %[pX0] offset:0
ds_read_b128 a[148:151], %[pX0] offset:2048
v_max3_f32 v236, v96, v97, v98
)"
R"(v_mfma_f32_16x16x32_bf16 v[116:119], a[96:99], v[220:223], v[116:119]
buffer_load_dwordx4 %[vW], %[srdW], s75 offen offset:0 lds
v_max3_f32 v237, v100, v101, v102
v_mfma_f32_16x16x32_bf16 v[116:119], a[100:103], v[228:231], v[116:119]
s_waitcnt lgkmcnt(5)
ds_read_b128 a[152:155], %[pX1] offset:0
ds_read_b128 a[156:159], %[pX1] offset:2048
v_add_u32 v232, s49, %[pW]
v_xor_b32 v233, 32, v232
v_max_f32 v236, v236, v99
v_mfma_f32_16x16x32_bf16 v[120:123], a[104:107], v[184:187], v[176:179]
ds_read_b64_tr_b16 a[96:97], v232 offset:0
ds_read_b64_tr_b16 a[98:99], v232 offset:512
v_max_f32 v237, v237, v103
v_mfma_f32_16x16x32_bf16 v[120:123], a[108:111], v[192:195], v[120:123]
v_xor_b32 v234, 64, v232
v_xor_b32 v235, 96, v232
v_max3_f32 v236, v236, v104, v105
v_mfma_f32_16x16x32_bf16 v[120:123], a[112:115], v[200:203], v[120:123]
s_add_u32 m0, s58, 896
ds_read_b64_tr_b16 a[100:101], v232 offset:12288
ds_read_b64_tr_b16 a[102:103], v232 offset:12800
v_max3_f32 v237, v237, v108, v109
v_mfma_f32_16x16x32_bf16 v[124:127], a[104:107], v[188:191], v[180:183]
)"
R"(buffer_load_dwordx4 %[vW], %[srdW], s75 offen offset:128 lds
v_max3_f32 v236, v236, v106, v107
v_mfma_f32_16x16x32_bf16 v[124:127], a[108:111], v[196:199], v[124:127]
ds_read_b64_tr_b16 a[104:105], v233 offset:0
ds_read_b64_tr_b16 a[106:107], v233 offset:512
v_max3_f32 v237, v237, v110, v111
v_mfma_f32_16x16x32_bf16 v[124:127], a[112:115], v[204:207], v[124:127]
s_waitcnt lgkmcnt(10)
v_max3_f32 v236, v236, v112, v113
v_max3_f32 v237, v237, v116, v117
v_mfma_f32_16x16x32_bf16 v[120:123], a[116:119], v[208:211], v[120:123]
s_add_u32 m0, s58, 1792
ds_read_b64_tr_b16 a[108:109], v233 offset:12288
ds_read_b64_tr_b16 a[110:111], v233 offset:12800
v_mfma_f32_16x16x32_bf16 v[120:123], a[120:123], v[216:219], v[120:123]
buffer_load_dwordx4 %[vW], %[srdW], s75 offen offset:256 lds
v_mfma_f32_16x16x32_bf16 v[120:123], a[124:127], v[224:227], v[120:123]
ds_read_b64_tr_b16 a[112:113], v234 offset:0
ds_read_b64_tr_b16 a[114:115], v234 offset:512
v_mfma_f32_16x16x32_bf16 v[124:127], a[116:119], v[212:215], v[124:127]
s_add_u32 s75, s66, 1180032
v_max3_f32 v236, v236, v114, v115
v_max3_f32 v237, v237, v118, v119
v_mfma_f32_16x16x32_bf16 v[124:127], a[120:123], v[220:223], v[124:127]
)"
R"(s_add_u32 m0, s58, 12288
ds_read_b64_tr_b16 a[116:117], v234 offset:12288
ds_read_b64_tr_b16 a[118:119], v234 offset:12800
v_mfma_f32_16x16x32_bf16 v[124:127], a[124:127], v[228:231], v[124:127]
s_waitcnt lgkmcnt(4)
buffer_load_dwordx4 %[vW], %[srdW], s75 offen offset:0 lds
v_mfma_f32_16x16x32_bf16 a[160:163], a[96:99], a[144:147], 0
ds_read_b64_tr_b16 a[120:121], v235 offset:0
ds_read_b64_tr_b16 a[122:123], v235 offset:512
v_mfma_f32_16x16x32_bf16 a[160:163], a[100:103], a[152:155], a[160:163]
v_mfma_f32_16x16x32_bf16 a[164:167], a[96:99], a[148:151], 0
s_add_u32 m0, s58, 13184
ds_read_b64_tr_b16 a[124:125], v235 offset:12288
ds_read_b64_tr_b16 a[126:127], v235 offset:12800
v_mfma_f32_16x16x32_bf16 a[164:167], a[100:103], a[156:159], a[164:167]
buffer_load_dwordx4 %[vW], %[srdW], s75 offen offset:128 lds
v_mfma_f32_16x16x32_bf16 a[168:171], a[104:107], a[144:147], 0
ds_read_b64_tr_b16 a[96:97], v232 offset:1024
ds_read_b64_tr_b16 a[98:99], v232 offset:1536
v_mfma_f32_16x16x32_bf16 a[168:171], a[108:111], a[152:155], a[168:171]
v_max3_f32 v236, v236, v120, v121
v_max3_f32 v237, v237, v124, v125
v_mfma_f32_16x16x32_bf16 a[172:175], a[104:107], a[148:151], 0
s_add_u32 m0, s58, 14080
)"
R"(ds_read_b64_tr_b16 a[100:101], v232 offset:13312
ds_read_b64_tr_b16 a[102:103], v232 offset:13824
v_mfma_f32_16x16x32_bf16 a[172:175], a[108:111], a[156:159], a[172:175]
s_waitcnt lgkmcnt(4)
buffer_load_dwordx4 %[vW], %[srdW], s75 offen offset:256 lds
v_mfma_f32_16x16x32_bf16 a[176:179], a[112:115], a[144:147], 0
ds_read_b64_tr_b16 a[104:105], v233 offset:1024
ds_read_b64_tr_b16 a[106:107], v233 offset:1536
v_mfma_f32_16x16x32_bf16 a[176:179], a[116:119], a[152:155], a[176:179]
v_max3_f32 v236, v236, v122, v123
v_max3_f32 v237, v237, v126, v127
v_mfma_f32_16x16x32_bf16 a[180:183], a[112:115], a[148:151], 0
s_add_u32 m0, s73, -256
ds_read_b64_tr_b16 a[108:109], v233 offset:13312
ds_read_b64_tr_b16 a[110:111], v233 offset:13824
v_mfma_f32_16x16x32_bf16 a[180:183], a[116:119], a[156:159], a[180:183]
buffer_load_dwordx4 %[vX0], %[srdX], s69 offen offset:256 lds
v_mfma_f32_16x16x32_bf16 a[184:187], a[120:123], a[144:147], 0
ds_read_b64_tr_b16 a[112:113], v234 offset:1024
ds_read_b64_tr_b16 a[114:115], v234 offset:1536
v_mfma_f32_16x16x32_bf16 a[184:187], a[124:127], a[152:155], a[184:187]
v_max_f32 v236, v236, v237
v_cmp_lt_f32 vcc, s77, v236
v_mfma_f32_16x16x32_bf16 a[188:191], a[120:123], a[148:151], 0
)"
R"(s_add_u32 m0, s73, 768
ds_read_b64_tr_b16 a[116:117], v234 offset:13312
ds_read_b64_tr_b16 a[118:119], v234 offset:13824
v_mfma_f32_16x16x32_bf16 a[188:191], a[124:127], a[156:159], a[188:191]
s_waitcnt lgkmcnt(4)
buffer_load_dwordx4 %[vX1], %[srdX], s70 offen offset:256 lds
v_mfma_f32_16x16x32_bf16 a[192:195], a[96:99], a[144:147], 0
ds_read_b64_tr_b16 a[120:121], v235 offset:1024
ds_read_b64_tr_b16 a[122:123], v235 offset:1536
v_mfma_f32_16x16x32_bf16 a[192:195], a[100:103], a[152:155], a[192:195]
v_mfma_f32_16x16x32_bf16 a[196:199], a[96:99], a[148:151], 0
s_add_u32 m0, s73, 1792
ds_read_b64_tr_b16 a[124:125], v235 offset:13312
ds_read_b64_tr_b16 a[126:127], v235 offset:13824
v_mfma_f32_16x16x32_bf16 a[196:199], a[100:103], a[156:159], a[196:199]
buffer_load_dwordx4 %[vX0], %[srdX], s71 offen offset:256 lds
v_mfma_f32_16x16x32_bf16 a[200:203], a[104:107], a[144:147], 0
ds_read_b64_tr_b16 a[96:97], v232 offset:2048
ds_read_b64_tr_b16 a[98:99], v232 offset:2560
v_mfma_f32_16x16x32_bf16 a[200:203], a[108:111], a[152:155], a[200:203]
v_mfma_f32_16x16x32_bf16 a[204:207], a[104:107], a[148:151], 0
s_add_u32 m0, s73, 2816
ds_read_b64_tr_b16 a[100:101], v232 offset:14336
ds_read_b64_tr_b16 a[102:103], v232 offset:14848
)"
R"(v_mfma_f32_16x16x32_bf16 a[204:207], a[108:111], a[156:159], a[204:207]
s_waitcnt lgkmcnt(4)
buffer_load_dwordx4 %[vX1], %[srdX], s72 offen offset:256 lds
v_mfma_f32_16x16x32_bf16 a[208:211], a[112:115], a[144:147], 0
ds_read_b64_tr_b16 a[104:105], v233 offset:2048
ds_read_b64_tr_b16 a[106:107], v233 offset:2560
v_mfma_f32_16x16x32_bf16 a[208:211], a[116:119], a[152:155], a[208:211]
v_mfma_f32_16x16x32_bf16 a[212:215], a[112:115], a[148:151], 0
ds_read_b64_tr_b16 a[108:109], v233 offset:14336
ds_read_b64_tr_b16 a[110:111], v233 offset:14848
v_mfma_f32_16x16x32_bf16 a[212:215], a[116:119], a[156:159], a[212:215]
v_mfma_f32_16x16x32_bf16 a[216:219], a[120:123], a[144:147], 0
ds_read_b64_tr_b16 a[112:113], v234 offset:2048
ds_read_b64_tr_b16 a[114:115], v234 offset:2560
v_mfma_f32_16x16x32_bf16 a[216:219], a[124:127], a[152:155], a[216:219]
v_mfma_f32_16x16x32_bf16 a[220:223], a[120:123], a[148:151], 0
s_add_u32 s75, s67, 49152
ds_read_b64_tr_b16 a[116:117], v234 offset:14336
ds_read_b64_tr_b16 a[118:119], v234 offset:14848
v_mfma_f32_16x16x32_bf16 a[220:223], a[124:127], a[156:159], a[220:223]
s_waitcnt lgkmcnt(4)
s_waitcnt vmcnt(21)
s_barrier
v_mfma_f32_16x16x32_bf16 a[224:227], a[96:99], a[144:147], 0
)"
R"(s_add_u32 m0, s54, 0
ds_read_b64_tr_b16 a[120:121], v235 offset:2048
ds_read_b64_tr_b16 a[122:123], v235 offset:2560
v_mfma_f32_16x16x32_bf16 a[224:227], a[100:103], a[152:155], a[224:227]
buffer_load_dwordx4 %[vK], %[srdK], s75 offen offset:0 nt lds
v_mfma_f32_16x16x32_bf16 a[228:231], a[96:99], a[148:151], 0
ds_read_b64_tr_b16 a[124:125], v235 offset:14336
ds_read_b64_tr_b16 a[126:127], v235 offset:14848
v_mfma_f32_16x16x32_bf16 a[228:231], a[100:103], a[156:159], a[228:231]
v_add_u32 v232, s50, %[pK]
v_xor_b32 v233, 64, v232
v_mfma_f32_16x16x32_bf16 a[232:235], a[104:107], a[144:147], 0
s_add_u32 m0, s54, 896
ds_read_b128 a[96:99], v232 offset:0
ds_read_b128 a[100:103], v233 offset:0
v_mfma_f32_16x16x32_bf16 a[232:235], a[108:111], a[152:155], a[232:235]
buffer_load_dwordx4 %[vK], %[srdK], s75 offen offset:128 nt lds
v_mfma_f32_16x16x32_bf16 a[236:239], a[104:107], a[148:151], 0
v_mfma_f32_16x16x32_bf16 a[236:239], a[108:111], a[156:159], a[236:239]
s_waitcnt lgkmcnt(2)
v_mfma_f32_16x16x32_bf16 a[240:243], a[112:115], a[144:147], 0
s_add_u32 m0, s54, 1792
ds_read_b128 a[104:107], v232 offset:1024
ds_read_b128 a[108:111], v233 offset:1024
)"
R"(v_mfma_f32_16x16x32_bf16 a[240:243], a[116:119], a[152:155], a[240:243]
buffer_load_dwordx4 %[vK], %[srdK], s75 offen offset:256 nt lds
v_mfma_f32_16x16x32_bf16 a[244:247], a[112:115], a[148:151], 0
v_mfma_f32_16x16x32_bf16 a[244:247], a[116:119], a[156:159], a[244:247]
s_add_u32 s75, s67, 61440
v_mfma_f32_16x16x32_bf16 a[248:251], a[120:123], a[144:147], 0
s_add_u32 m0, s54, 12288
ds_read_b128 a[112:115], v232 offset:2048
ds_read_b128 a[116:119], v233 offset:2048
v_mfma_f32_16x16x32_bf16 a[248:251], a[124:127], a[152:155], a[248:251]
buffer_load_dwordx4 %[vK], %[srdK], s75 offen offset:0 nt lds
v_mfma_f32_16x16x32_bf16 a[252:255], a[120:123], a[148:151], 0
v_mfma_f32_16x16x32_bf16 a[252:255], a[124:127], a[156:159], a[252:255]
s_waitcnt lgkmcnt(3)
s_cbranch_vccnz RARE1_%=
BACK1_%=:
v_mfma_f32_16x16x32_bf16 v[128:131], a[96:99], v[184:187], v[176:179]
s_add_u32 m0, s54, 13184
ds_read_b128 a[120:123], v232 offset:6144
ds_read_b128 a[124:127], v233 offset:6144
v_exp_f32 v96, v96
v_mfma_f32_16x16x32_bf16 v[128:131], a[100:103], v[192:195], v[128:131]
buffer_load_dwordx4 %[vK], %[srdK], s75 offen offset:128 nt lds
v_exp_f32 v97, v97
)"
R"(v_mfma_f32_16x16x32_bf16 v[128:131], a[104:107], v[200:203], v[128:131]
v_exp_f32 v98, v98
v_exp_f32 v99, v99
v_mfma_f32_16x16x32_bf16 v[132:135], a[96:99], v[188:191], v[180:183]
v_exp_f32 v104, v104
v_exp_f32 v105, v105
v_mfma_f32_16x16x32_bf16 v[132:135], a[100:103], v[196:199], v[132:135]
s_add_u32 m0, s54, 14080
ds_read_b128 a[96:99], v232 offset:7168
ds_read_b128 a[100:103], v233 offset:7168
v_exp_f32 v106, v106
v_mfma_f32_16x16x32_bf16 v[132:135], a[104:107], v[204:207], v[132:135]
s_waitcnt lgkmcnt(4)
buffer_load_dwordx4 %[vK], %[srdK], s75 offen offset:256 nt lds
v_mfma_f32_16x16x32_bf16 v[128:131], a[108:111], v[208:211], v[128:131]
v_exp_f32 v107, v107
v_cvt_pk_bf16_f32 v160, v96, v97
v_mfma_f32_16x16x32_bf16 v[128:131], a[112:115], v[216:219], v[128:131]
v_cvt_pk_bf16_f32 v161, v98, v99
v_exp_f32 v100, v100
v_mfma_f32_16x16x32_bf16 v[128:131], a[116:119], v[224:227], v[128:131]
v_exp_f32 v101, v101
v_exp_f32 v102, v102
v_mfma_f32_16x16x32_bf16 v[132:135], a[108:111], v[212:215], v[132:135]
)"
R"(ds_read_b128 a[104:107], v232 offset:8192
ds_read_b128 a[108:111], v233 offset:8192
v_mfma_f32_16x16x32_bf16 v[132:135], a[112:115], v[220:223], v[132:135]
v_exp_f32 v103, v103
v_cvt_pk_bf16_f32 v162, v104, v105
v_mfma_f32_16x16x32_bf16 v[132:135], a[116:119], v[228:231], v[132:135]
s_waitcnt lgkmcnt(3)
v_cvt_pk_bf16_f32 v163, v106, v107
v_exp_f32 v108, v108
v_mfma_f32_16x16x32_bf16 v[136:139], a[120:123], v[184:187], v[176:179]
ds_read_b128 a[112:115], v232 offset:12288
ds_read_b128 a[116:119], v233 offset:12288
v_exp_f32 v109, v109
v_mfma_f32_16x16x32_bf16 v[136:139], a[124:127], v[192:195], v[136:139]
v_exp_f32 v110, v110
v_mfma_f32_16x16x32_bf16 v[136:139], a[96:99], v[200:203], v[136:139]
v_exp_f32 v111, v111
v_cvt_pk_bf16_f32 v164, v100, v101
v_mfma_f32_16x16x32_bf16 v[140:143], a[120:123], v[188:191], v[180:183]
v_cvt_pk_bf16_f32 v165, v102, v103
v_exp_f32 v112, v112
v_mfma_f32_16x16x32_bf16 v[140:143], a[124:127], v[196:199], v[140:143]
ds_read_b128 a[120:123], v232 offset:13312
ds_read_b128 a[124:127], v233 offset:13312
)"
R"(v_exp_f32 v113, v113
v_mfma_f32_16x16x32_bf16 v[140:143], a[96:99], v[204:207], v[140:143]
s_waitcnt lgkmcnt(4)
v_exp_f32 v114, v114
v_mfma_f32_16x16x32_bf16 v[136:139], a[100:103], v[208:211], v[136:139]
v_exp_f32 v115, v115
v_cvt_pk_bf16_f32 v166, v108, v109
v_mfma_f32_16x16x32_bf16 v[136:139], a[104:107], v[216:219], v[136:139]
v_cvt_pk_bf16_f32 v167, v110, v111
v_exp_f32 v120, v120
v_mfma_f32_16x16x32_bf16 v[136:139], a[108:111], v[224:227], v[136:139]
v_exp_f32 v121, v121
v_exp_f32 v122, v122
v_mfma_f32_16x16x32_bf16 v[140:143], a[100:103], v[212:215], v[140:143]
ds_read_b128 a[96:99], v232 offset:14336
ds_read_b128 a[100:103], v233 offset:14336
v_mfma_f32_16x16x32_bf16 v[140:143], a[104:107], v[220:223], v[140:143]
v_exp_f32 v123, v123
v_cvt_pk_bf16_f32 v168, v112, v113
v_mfma_f32_16x16x32_bf16 v[140:143], a[108:111], v[228:231], v[140:143]
s_waitcnt lgkmcnt(3)
v_cvt_pk_bf16_f32 v169, v114, v115
v_exp_f32 v116, v116
v_mfma_f32_16x16x32_bf16 v[144:147], a[112:115], v[184:187], v[176:179]
)"
R"(ds_read_b128 a[104:107], v232 offset:18432
ds_read_b128 a[108:111], v233 offset:18432
v_mfma_f32_16x16x32_bf16 v[144:147], a[116:119], v[192:195], v[144:147]
v_exp_f32 v117, v117
v_mfma_f32_16x16x32_bf16 v[144:147], a[120:123], v[200:203], v[144:147]
v_exp_f32 v118, v118
v_mfma_f32_16x16x32_bf16 v[148:151], a[112:115], v[188:191], v[180:183]
v_exp_f32 v119, v119
v_cvt_pk_bf16_f32 v170, v120, v121
v_mfma_f32_16x16x32_bf16 v[148:151], a[116:119], v[196:199], v[148:151]
ds_read_b128 a[112:115], v232 offset:19456
ds_read_b128 a[116:119], v233 offset:19456
v_cvt_pk_bf16_f32 v171, v122, v123
v_mfma_f32_16x16x32_bf16 v[148:151], a[120:123], v[204:207], v[148:151]
s_waitcnt lgkmcnt(4)
v_exp_f32 v124, v124
v_mfma_f32_16x16x32_bf16 v[144:147], a[124:127], v[208:211], v[144:147]
v_exp_f32 v125, v125
v_mfma_f32_16x16x32_bf16 v[144:147], a[96:99], v[216:219], v[144:147]
s_waitcnt vmcnt(16)
s_barrier
v_mfma_f32_16x16x32_bf16 v[144:147], a[100:103], v[224:227], v[144:147]
s_add_u32 s76, s68, 24576
v_exp_f32 v126, v126
)"
R"(v_mfma_f32_16x16x32_bf16 v[148:151], a[124:127], v[212:215], v[148:151]
s_add_u32 m0, s61, 0
ds_read_b128 a[120:123], v232 offset:20480
ds_read_b128 a[124:127], v233 offset:20480
v_mfma_f32_16x16x32_bf16 v[148:151], a[96:99], v[220:223], v[148:151]
buffer_load_dwordx4 %[vV], %[srdV], s76 offen offset:0 nt lds
v_mfma_f32_16x16x32_bf16 v[148:151], a[100:103], v[228:231], v[148:151]
s_waitcnt lgkmcnt(3)
v_add_u32 v232, s51, %[pV]
v_xor_b32 v233, 32, v232
v_mfma_f32_16x16x32_bf16 v[152:155], a[104:107], v[184:187], v[176:179]
ds_read_b64_tr_b16 a[96:97], v232 offset:0
ds_read_b64_tr_b16 a[98:99], v232 offset:6144
v_mfma_f32_16x16x32_bf16 v[152:155], a[108:111], v[192:195], v[152:155]
v_xor_b32 v234, 64, v232
v_xor_b32 v235, 96, v232
v_mfma_f32_16x16x32_bf16 v[152:155], a[112:115], v[200:203], v[152:155]
s_add_u32 m0, s61, 896
ds_read_b64_tr_b16 a[100:101], v232 offset:12288
ds_read_b64_tr_b16 a[102:103], v232 offset:18432
v_mfma_f32_16x16x32_bf16 v[156:159], a[104:107], v[188:191], v[180:183]
buffer_load_dwordx4 %[vV], %[srdV], s76 offen offset:128 nt lds
v_mfma_f32_16x16x32_bf16 v[156:159], a[108:111], v[196:199], v[156:159]
ds_read_b64_tr_b16 a[104:105], v233 offset:0
)"
R"(ds_read_b64_tr_b16 a[106:107], v233 offset:6144
v_mfma_f32_16x16x32_bf16 v[156:159], a[112:115], v[204:207], v[156:159]
s_waitcnt lgkmcnt(6)
v_exp_f32 v127, v127
v_cvt_pk_bf16_f32 v172, v116, v117
v_mfma_f32_16x16x32_bf16 v[152:155], a[116:119], v[208:211], v[152:155]
s_add_u32 m0, s61, 1792
ds_read_b64_tr_b16 a[108:109], v233 offset:12288
ds_read_b64_tr_b16 a[110:111], v233 offset:18432
v_cvt_pk_bf16_f32 v173, v118, v119
v_mfma_f32_16x16x32_bf16 v[152:155], a[120:123], v[216:219], v[152:155]
buffer_load_dwordx4 %[vV], %[srdV], s76 offen offset:256 nt lds
v_mfma_f32_16x16x32_bf16 v[152:155], a[124:127], v[224:227], v[152:155]
ds_read_b64_tr_b16 a[112:113], v234 offset:0
ds_read_b64_tr_b16 a[114:115], v234 offset:6144
v_mfma_f32_16x16x32_bf16 v[156:159], a[116:119], v[212:215], v[156:159]
v_cvt_pk_bf16_f32 v174, v124, v125
v_cvt_pk_bf16_f32 v175, v126, v127
v_mfma_f32_16x16x32_bf16 v[156:159], a[120:123], v[220:223], v[156:159]
s_add_u32 m0, s61, 0
ds_read_b64_tr_b16 a[116:117], v234 offset:12288
ds_read_b64_tr_b16 a[118:119], v234 offset:18432
v_mfma_f32_16x16x32_bf16 v[156:159], a[124:127], v[228:231], v[156:159]
buffer_load_dwordx4 %[vV], %[srdV], s76 offen offset:3072 nt lds
)"
R"(v_mfma_f32_16x16x32_bf16 a[132:135], a[128:131], v[160:163], 0
v_mfma_f32_16x16x32_bf16 a[132:135], a[128:131], v[168:171], a[132:135]
v_mfma_f32_16x16x32_bf16 a[136:139], a[128:131], v[164:167], 0
s_add_u32 m0, s61, 896
v_mfma_f32_16x16x32_bf16 a[136:139], a[128:131], v[172:175], a[136:139]
s_waitcnt lgkmcnt(4)
buffer_load_dwordx4 %[vV], %[srdV], s76 offen offset:3200 nt lds
v_mfma_f32_16x16x32_bf16 v[0:3], a[96:99], v[160:163], 0
ds_read_b64_tr_b16 a[120:121], v235 offset:0
ds_read_b64_tr_b16 a[122:123], v235 offset:6144
v_mfma_f32_16x16x32_bf16 v[0:3], a[100:103], v[168:171], v[0:3]
v_max3_f32 v236, v128, v129, v130
v_max3_f32 v237, v132, v133, v134
v_mfma_f32_16x16x32_bf16 v[4:7], a[96:99], v[164:167], 0
s_add_u32 m0, s61, 1792
ds_read_b64_tr_b16 a[124:125], v235 offset:12288
ds_read_b64_tr_b16 a[126:127], v235 offset:18432
v_mfma_f32_16x16x32_bf16 v[4:7], a[100:103], v[172:175], v[4:7]
buffer_load_dwordx4 %[vV], %[srdV], s76 offen offset:3328 nt lds
v_mfma_f32_16x16x32_bf16 v[8:11], a[104:107], v[160:163], 0
ds_read_b64_tr_b16 a[96:97], v232 offset:1024
ds_read_b64_tr_b16 a[98:99], v232 offset:7168
v_mfma_f32_16x16x32_bf16 v[8:11], a[108:111], v[168:171], v[8:11]
v_max_f32 v236, v236, v131
)"
R"(v_max_f32 v237, v237, v135
v_mfma_f32_16x16x32_bf16 v[12:15], a[104:107], v[164:167], 0
ds_read_b64_tr_b16 a[100:101], v232 offset:13312
ds_read_b64_tr_b16 a[102:103], v232 offset:19456
v_mfma_f32_16x16x32_bf16 v[12:15], a[108:111], v[172:175], v[12:15]
s_waitcnt lgkmcnt(4)
v_max3_f32 v236, v236, v136, v137
v_max3_f32 v237, v237, v140, v141
v_mfma_f32_16x16x32_bf16 v[16:19], a[112:115], v[160:163], 0
ds_read_b64_tr_b16 a[104:105], v233 offset:1024
ds_read_b64_tr_b16 a[106:107], v233 offset:7168
v_mfma_f32_16x16x32_bf16 v[16:19], a[116:119], v[168:171], v[16:19]
v_max3_f32 v236, v236, v138, v139
v_max3_f32 v237, v237, v142, v143
v_mfma_f32_16x16x32_bf16 v[20:23], a[112:115], v[164:167], 0
ds_read_b64_tr_b16 a[108:109], v233 offset:13312
ds_read_b64_tr_b16 a[110:111], v233 offset:19456
v_mfma_f32_16x16x32_bf16 v[20:23], a[116:119], v[172:175], v[20:23]
v_max3_f32 v236, v236, v144, v145
v_max3_f32 v237, v237, v148, v149
v_mfma_f32_16x16x32_bf16 v[24:27], a[120:123], v[160:163], 0
ds_read_b64_tr_b16 a[112:113], v234 offset:1024
ds_read_b64_tr_b16 a[114:115], v234 offset:7168
v_mfma_f32_16x16x32_bf16 v[24:27], a[124:127], v[168:171], v[24:27]
)"
R"(v_max3_f32 v236, v236, v146, v147
v_max3_f32 v237, v237, v150, v151
v_mfma_f32_16x16x32_bf16 v[28:31], a[120:123], v[164:167], 0
ds_read_b64_tr_b16 a[116:117], v234 offset:13312
ds_read_b64_tr_b16 a[118:119], v234 offset:19456
v_mfma_f32_16x16x32_bf16 v[28:31], a[124:127], v[172:175], v[28:31]
s_waitcnt lgkmcnt(4)
v_max3_f32 v236, v236, v152, v153
v_max3_f32 v237, v237, v156, v157
v_mfma_f32_16x16x32_bf16 v[32:35], a[96:99], v[160:163], 0
ds_read_b64_tr_b16 a[120:121], v235 offset:1024
ds_read_b64_tr_b16 a[122:123], v235 offset:7168
v_mfma_f32_16x16x32_bf16 v[32:35], a[100:103], v[168:171], v[32:35]
v_max3_f32 v236, v236, v154, v155
v_max3_f32 v237, v237, v158, v159
v_mfma_f32_16x16x32_bf16 v[36:39], a[96:99], v[164:167], 0
ds_read_b64_tr_b16 a[124:125], v235 offset:13312
ds_read_b64_tr_b16 a[126:127], v235 offset:19456
v_mfma_f32_16x16x32_bf16 v[36:39], a[100:103], v[172:175], v[36:39]
v_max_f32 v236, v236, v237
v_cmp_lt_f32 vcc, s77, v236
v_mfma_f32_16x16x32_bf16 v[40:43], a[104:107], v[160:163], 0
ds_read_b64_tr_b16 a[96:97], v232 offset:2048
ds_read_b64_tr_b16 a[98:99], v232 offset:8192
)"
R"(v_mfma_f32_16x16x32_bf16 v[40:43], a[108:111], v[168:171], v[40:43]
v_mfma_f32_16x16x32_bf16 v[44:47], a[104:107], v[164:167], 0
ds_read_b64_tr_b16 a[100:101], v232 offset:14336
ds_read_b64_tr_b16 a[102:103], v232 offset:20480
v_mfma_f32_16x16x32_bf16 v[44:47], a[108:111], v[172:175], v[44:47]
s_waitcnt lgkmcnt(4)
v_mfma_f32_16x16x32_bf16 v[48:51], a[112:115], v[160:163], 0
ds_read_b64_tr_b16 a[104:105], v233 offset:2048
ds_read_b64_tr_b16 a[106:107], v233 offset:8192
v_mfma_f32_16x16x32_bf16 v[48:51], a[116:119], v[168:171], v[48:51]
v_mfma_f32_16x16x32_bf16 v[52:55], a[112:115], v[164:167], 0
ds_read_b64_tr_b16 a[108:109], v233 offset:14336
ds_read_b64_tr_b16 a[110:111], v233 offset:20480
v_mfma_f32_16x16x32_bf16 v[52:55], a[116:119], v[172:175], v[52:55]
v_mfma_f32_16x16x32_bf16 v[56:59], a[120:123], v[160:163], 0
ds_read_b64_tr_b16 a[112:113], v234 offset:2048
ds_read_b64_tr_b16 a[114:115], v234 offset:8192
v_mfma_f32_16x16x32_bf16 v[56:59], a[124:127], v[168:171], v[56:59]
v_mfma_f32_16x16x32_bf16 v[60:63], a[120:123], v[164:167], 0
ds_read_b64_tr_b16 a[116:117], v234 offset:14336
ds_read_b64_tr_b16 a[118:119], v234 offset:20480
v_mfma_f32_16x16x32_bf16 v[60:63], a[124:127], v[172:175], v[60:63]
s_waitcnt lgkmcnt(4)
s_waitcnt vmcnt(16)
)"
R"(s_barrier
v_mfma_f32_16x16x32_bf16 v[64:67], a[96:99], v[160:163], 0
ds_read_b64_tr_b16 a[120:121], v235 offset:2048
ds_read_b64_tr_b16 a[122:123], v235 offset:8192
v_mfma_f32_16x16x32_bf16 v[64:67], a[100:103], v[168:171], v[64:67]
ds_read_b128 a[144:147], %[pX0] offset:16384
ds_read_b128 a[148:151], %[pX0] offset:18432
v_mfma_f32_16x16x32_bf16 v[68:71], a[96:99], v[164:167], 0
ds_read_b64_tr_b16 a[124:125], v235 offset:14336
ds_read_b64_tr_b16 a[126:127], v235 offset:20480
v_mfma_f32_16x16x32_bf16 v[68:71], a[100:103], v[172:175], v[68:71]
ds_read_b128 a[152:155], %[pX1] offset:16384
ds_read_b128 a[156:159], %[pX1] offset:18432
v_add_u32 v232, s52, %[pW]
v_xor_b32 v233, 32, v232
v_mfma_f32_16x16x32_bf16 v[72:75], a[104:107], v[160:163], 0
ds_read_b64_tr_b16 a[96:97], v232 offset:0
ds_read_b64_tr_b16 a[98:99], v232 offset:512
v_mfma_f32_16x16x32_bf16 v[72:75], a[108:111], v[168:171], v[72:75]
s_add_u32 s75, s66, 1573248
v_xor_b32 v234, 64, v232
v_xor_b32 v235, 96, v232
v_mfma_f32_16x16x32_bf16 v[76:79], a[104:107], v[164:167], 0
s_add_u32 m0, s56, 0
)"
R"(ds_read_b64_tr_b16 a[100:101], v232 offset:12288
ds_read_b64_tr_b16 a[102:103], v232 offset:12800
v_mfma_f32_16x16x32_bf16 v[76:79], a[108:111], v[172:175], v[76:79]
s_waitcnt lgkmcnt(6)
buffer_load_dwordx4 %[vW], %[srdW], s75 offen offset:0 lds
v_mfma_f32_16x16x32_bf16 v[80:83], a[112:115], v[160:163], 0
ds_read_b64_tr_b16 a[104:105], v233 offset:0
ds_read_b64_tr_b16 a[106:107], v233 offset:512
v_mfma_f32_16x16x32_bf16 v[80:83], a[116:119], v[168:171], v[80:83]
v_mfma_f32_16x16x32_bf16 v[84:87], a[112:115], v[164:167], 0
s_add_u32 m0, s56, 896
ds_read_b64_tr_b16 a[108:109], v233 offset:12288
ds_read_b64_tr_b16 a[110:111], v233 offset:12800
v_mfma_f32_16x16x32_bf16 v[84:87], a[116:119], v[172:175], v[84:87]
buffer_load_dwordx4 %[vW], %[srdW], s75 offen offset:128 lds
v_mfma_f32_16x16x32_bf16 v[88:91], a[120:123], v[160:163], 0
ds_read_b64_tr_b16 a[112:113], v234 offset:0
ds_read_b64_tr_b16 a[114:115], v234 offset:512
v_mfma_f32_16x16x32_bf16 v[88:91], a[124:127], v[168:171], v[88:91]
v_mfma_f32_16x16x32_bf16 v[92:95], a[120:123], v[164:167], 0
s_add_u32 m0, s56, 1792
ds_read_b64_tr_b16 a[116:117], v234 offset:12288
ds_read_b64_tr_b16 a[118:119], v234 offset:12800
v_mfma_f32_16x16x32_bf16 v[92:95], a[124:127], v[172:175], v[92:95]
)"
R"(s_waitcnt lgkmcnt(4)
buffer_load_dwordx4 %[vW], %[srdW], s75 offen offset:256 lds
s_cbranch_vccnz RARE2_%=
BACK2_%=:
v_mfma_f32_16x16x32_bf16 a[160:163], a[96:99], a[144:147], a[160:163]
ds_read_b64_tr_b16 a[120:121], v235 offset:0
ds_read_b64_tr_b16 a[122:123], v235 offset:512
v_mfma_f32_16x16x32_bf16 a[160:163], a[100:103], a[152:155], a[160:163]
s_add_u32 s75, s66, 1966464
v_exp_f32 v128, v128
v_mfma_f32_16x16x32_bf16 a[164:167], a[96:99], a[148:151], a[164:167]
s_add_u32 m0, s56, 12288
ds_read_b64_tr_b16 a[124:125], v235 offset:12288
ds_read_b64_tr_b16 a[126:127], v235 offset:12800
v_mfma_f32_16x16x32_bf16 a[164:167], a[100:103], a[156:159], a[164:167]
buffer_load_dwordx4 %[vW], %[srdW], s75 offen offset:0 lds
v_mfma_f32_16x16x32_bf16 a[168:171], a[104:107], a[144:147], a[168:171]
ds_read_b64_tr_b16 a[96:97], v232 offset:1024
ds_read_b64_tr_b16 a[98:99], v232 offset:1536
v_mfma_f32_16x16x32_bf16 a[168:171], a[108:111], a[152:155], a[168:171]
v_exp_f32 v129, v129
v_mfma_f32_16x16x32_bf16 a[172:175], a[104:107], a[148:151], a[172:175]
s_add_u32 m0, s56, 13184
ds_read_b64_tr_b16 a[100:101], v232 offset:13312
)"
R"(ds_read_b64_tr_b16 a[102:103], v232 offset:13824
v_mfma_f32_16x16x32_bf16 a[172:175], a[108:111], a[156:159], a[172:175]
s_waitcnt lgkmcnt(4)
buffer_load_dwordx4 %[vW], %[srdW], s75 offen offset:128 lds
v_mfma_f32_16x16x32_bf16 a[176:179], a[112:115], a[144:147], a[176:179]
ds_read_b64_tr_b16 a[104:105], v233 offset:1024
ds_read_b64_tr_b16 a[106:107], v233 offset:1536
v_mfma_f32_16x16x32_bf16 a[176:179], a[116:119], a[152:155], a[176:179]
v_exp_f32 v130, v130
v_mfma_f32_16x16x32_bf16 a[180:183], a[112:115], a[148:151], a[180:183]
s_add_u32 m0, s56, 14080
ds_read_b64_tr_b16 a[108:109], v233 offset:13312
ds_read_b64_tr_b16 a[110:111], v233 offset:13824
v_mfma_f32_16x16x32_bf16 a[180:183], a[116:119], a[156:159], a[180:183]
buffer_load_dwordx4 %[vW], %[srdW], s75 offen offset:256 lds
v_mfma_f32_16x16x32_bf16 a[184:187], a[120:123], a[144:147], a[184:187]
ds_read_b64_tr_b16 a[112:113], v234 offset:1024
ds_read_b64_tr_b16 a[114:115], v234 offset:1536
v_mfma_f32_16x16x32_bf16 a[184:187], a[124:127], a[152:155], a[184:187]
v_exp_f32 v131, v131
v_mfma_f32_16x16x32_bf16 a[188:191], a[120:123], a[148:151], a[188:191]
s_add_u32 m0, s73, 16000
ds_read_b64_tr_b16 a[116:117], v234 offset:13312
ds_read_b64_tr_b16 a[118:119], v234 offset:13824
)"
R"(v_mfma_f32_16x16x32_bf16 a[188:191], a[124:127], a[156:159], a[188:191]
s_waitcnt lgkmcnt(4)
buffer_load_dwordx4 %[vX0], %[srdX], s69 offen offset:384 lds
v_mfma_f32_16x16x32_bf16 a[192:195], a[96:99], a[144:147], a[192:195]
ds_read_b64_tr_b16 a[120:121], v235 offset:1024
ds_read_b64_tr_b16 a[122:123], v235 offset:1536
v_mfma_f32_16x16x32_bf16 a[192:195], a[100:103], a[152:155], a[192:195]
v_exp_f32 v136, v136
v_mfma_f32_16x16x32_bf16 a[196:199], a[96:99], a[148:151], a[196:199]
s_add_u32 m0, s73, 17024
ds_read_b64_tr_b16 a[124:125], v235 offset:13312
ds_read_b64_tr_b16 a[126:127], v235 offset:13824
v_mfma_f32_16x16x32_bf16 a[196:199], a[100:103], a[156:159], a[196:199]
buffer_load_dwordx4 %[vX1], %[srdX], s70 offen offset:384 lds
v_mfma_f32_16x16x32_bf16 a[200:203], a[104:107], a[144:147], a[200:203]
ds_read_b64_tr_b16 a[96:97], v232 offset:2048
ds_read_b64_tr_b16 a[98:99], v232 offset:2560
v_mfma_f32_16x16x32_bf16 a[200:203], a[108:111], a[152:155], a[200:203]
v_exp_f32 v137, v137
v_mfma_f32_16x16x32_bf16 a[204:207], a[104:107], a[148:151], a[204:207]
s_add_u32 m0, s73, 18048
ds_read_b64_tr_b16 a[100:101], v232 offset:14336
ds_read_b64_tr_b16 a[102:103], v232 offset:14848
v_mfma_f32_16x16x32_bf16 a[204:207], a[108:111], a[156:159], a[204:207]
)"
R"(s_waitcnt lgkmcnt(4)
buffer_load_dwordx4 %[vX0], %[srdX], s71 offen offset:384 lds
v_mfma_f32_16x16x32_bf16 a[208:211], a[112:115], a[144:147], a[208:211]
ds_read_b64_tr_b16 a[104:105], v233 offset:2048
ds_read_b64_tr_b16 a[106:107], v233 offset:2560
v_mfma_f32_16x16x32_bf16 a[208:211], a[116:119], a[152:155], a[208:211]
v_exp_f32 v138, v138
v_mfma_f32_16x16x32_bf16 a[212:215], a[112:115], a[148:151], a[212:215]
s_add_u32 m0, s73, 19072
ds_read_b64_tr_b16 a[108:109], v233 offset:14336
ds_read_b64_tr_b16 a[110:111], v233 offset:14848
v_mfma_f32_16x16x32_bf16 a[212:215], a[116:119], a[156:159], a[212:215]
buffer_load_dwordx4 %[vX1], %[srdX], s72 offen offset:384 lds
v_mfma_f32_16x16x32_bf16 a[216:219], a[120:123], a[144:147], a[216:219]
ds_read_b64_tr_b16 a[112:113], v234 offset:2048
ds_read_b64_tr_b16 a[114:115], v234 offset:2560
v_mfma_f32_16x16x32_bf16 a[216:219], a[124:127], a[152:155], a[216:219]
v_exp_f32 v139, v139
v_cvt_pk_bf16_f32 v160, v128, v129
v_mfma_f32_16x16x32_bf16 a[220:223], a[120:123], a[148:151], a[220:223]
s_add_u32 s75, s67, 73728
ds_read_b64_tr_b16 a[116:117], v234 offset:14336
ds_read_b64_tr_b16 a[118:119], v234 offset:14848
v_cvt_pk_bf16_f32 v161, v130, v131
)"
R"(v_mfma_f32_16x16x32_bf16 a[220:223], a[124:127], a[156:159], a[220:223]
s_waitcnt lgkmcnt(4)
s_waitcnt vmcnt(16)
s_barrier
v_mfma_f32_16x16x32_bf16 a[224:227], a[96:99], a[144:147], a[224:227]
s_add_u32 m0, s57, 0
ds_read_b64_tr_b16 a[120:121], v235 offset:2048
ds_read_b64_tr_b16 a[122:123], v235 offset:2560
v_mfma_f32_16x16x32_bf16 a[224:227], a[100:103], a[152:155], a[224:227]
buffer_load_dwordx4 %[vK], %[srdK], s75 offen offset:0 nt lds
v_mfma_f32_16x16x32_bf16 a[228:231], a[96:99], a[148:151], a[228:231]
ds_read_b64_tr_b16 a[124:125], v235 offset:14336
ds_read_b64_tr_b16 a[126:127], v235 offset:14848
v_mfma_f32_16x16x32_bf16 a[228:231], a[100:103], a[156:159], a[228:231]
v_add_u32 v232, s48, %[pK]
v_xor_b32 v233, 64, v232
v_mfma_f32_16x16x32_bf16 a[232:235], a[104:107], a[144:147], a[232:235]
s_add_u32 m0, s57, 896
ds_read_b128 a[96:99], v232 offset:0
ds_read_b128 a[100:103], v233 offset:0
v_mfma_f32_16x16x32_bf16 a[232:235], a[108:111], a[152:155], a[232:235]
buffer_load_dwordx4 %[vK], %[srdK], s75 offen offset:128 nt lds
v_mfma_f32_16x16x32_bf16 a[236:239], a[104:107], a[148:151], a[236:239]
v_exp_f32 v132, v132
)"
R"(v_mfma_f32_16x16x32_bf16 a[236:239], a[108:111], a[156:159], a[236:239]
s_waitcnt lgkmcnt(2)
v_exp_f32 v133, v133
v_mfma_f32_16x16x32_bf16 a[240:243], a[112:115], a[144:147], a[240:243]
s_add_u32 m0, s57, 1792
ds_read_b128 a[104:107], v232 offset:1024
ds_read_b128 a[108:111], v233 offset:1024
v_mfma_f32_16x16x32_bf16 a[240:243], a[116:119], a[152:155], a[240:243]
buffer_load_dwordx4 %[vK], %[srdK], s75 offen offset:256 nt lds
v_mfma_f32_16x16x32_bf16 a[244:247], a[112:115], a[148:151], a[244:247]
v_exp_f32 v134, v134
v_mfma_f32_16x16x32_bf16 a[244:247], a[116:119], a[156:159], a[244:247]
s_add_u32 s75, s67, 86016
v_exp_f32 v135, v135
v_cvt_pk_bf16_f32 v162, v136, v137
v_mfma_f32_16x16x32_bf16 a[248:251], a[120:123], a[144:147], a[248:251]
s_add_u32 m0, s57, 12288
ds_read_b128 a[112:115], v232 offset:2048
ds_read_b128 a[116:119], v233 offset:2048
v_cvt_pk_bf16_f32 v163, v138, v139
v_mfma_f32_16x16x32_bf16 a[248:251], a[124:127], a[152:155], a[248:251]
buffer_load_dwordx4 %[vK], %[srdK], s75 offen offset:0 nt lds
v_mfma_f32_16x16x32_bf16 a[252:255], a[120:123], a[148:151], a[252:255]
v_exp_f32 v140, v140
)"
R"(v_mfma_f32_16x16x32_bf16 a[252:255], a[124:127], a[156:159], a[252:255]
s_waitcnt lgkmcnt(3)
v_exp_f32 v141, v141
v_mfma_f32_16x16x32_bf16 v[96:99], a[96:99], v[184:187], v[176:179]
s_add_u32 m0, s57, 13184
ds_read_b128 a[120:123], v232 offset:6144
ds_read_b128 a[124:127], v233 offset:6144
v_mfma_f32_16x16x32_bf16 v[96:99], a[100:103], v[192:195], v[96:99]
buffer_load_dwordx4 %[vK], %[srdK], s75 offen offset:128 nt lds
v_mfma_f32_16x16x32_bf16 v[96:99], a[104:107], v[200:203], v[96:99]
v_exp_f32 v142, v142
v_mfma_f32_16x16x32_bf16 v[100:103], a[96:99], v[188:191], v[180:183]
v_exp_f32 v143, v143
v_mfma_f32_16x16x32_bf16 v[100:103], a[100:103], v[196:199], v[100:103]
s_add_u32 m0, s57, 14080
ds_read_b128 a[96:99], v232 offset:7168
ds_read_b128 a[100:103], v233 offset:7168
v_mfma_f32_16x16x32_bf16 v[100:103], a[104:107], v[204:207], v[100:103]
s_waitcnt lgkmcnt(4)
buffer_load_dwordx4 %[vK], %[srdK], s75 offen offset:256 nt lds
v_mfma_f32_16x16x32_bf16 v[96:99], a[108:111], v[208:211], v[96:99]
v_cvt_pk_bf16_f32 v164, v132, v133
v_cvt_pk_bf16_f32 v165, v134, v135
v_mfma_f32_16x16x32_bf16 v[96:99], a[112:115], v[216:219], v[96:99]
)"
R"(v_exp_f32 v144, v144
v_mfma_f32_16x16x32_bf16 v[96:99], a[116:119], v[224:227], v[96:99]
v_exp_f32 v145, v145
v_mfma_f32_16x16x32_bf16 v[100:103], a[108:111], v[212:215], v[100:103]
ds_read_b128 a[104:107], v232 offset:8192
ds_read_b128 a[108:111], v233 offset:8192
v_mfma_f32_16x16x32_bf16 v[100:103], a[112:115], v[220:223], v[100:103]
v_exp_f32 v146, v146
v_mfma_f32_16x16x32_bf16 v[100:103], a[116:119], v[228:231], v[100:103]
s_waitcnt lgkmcnt(3)
v_exp_f32 v147, v147
v_mfma_f32_16x16x32_bf16 v[104:107], a[120:123], v[184:187], v[176:179]
ds_read_b128 a[112:115], v232 offset:12288
ds_read_b128 a[116:119], v233 offset:12288
v_mfma_f32_16x16x32_bf16 v[104:107], a[124:127], v[192:195], v[104:107]
v_cvt_pk_bf16_f32 v166, v140, v141
v_cvt_pk_bf16_f32 v167, v142, v143
v_mfma_f32_16x16x32_bf16 v[104:107], a[96:99], v[200:203], v[104:107]
v_exp_f32 v152, v152
v_mfma_f32_16x16x32_bf16 v[108:111], a[120:123], v[188:191], v[180:183]
v_exp_f32 v153, v153
v_mfma_f32_16x16x32_bf16 v[108:111], a[124:127], v[196:199], v[108:111]
ds_read_b128 a[120:123], v232 offset:13312
ds_read_b128 a[124:127], v233 offset:13312
)"
R"(v_mfma_f32_16x16x32_bf16 v[108:111], a[96:99], v[204:207], v[108:111]
s_waitcnt lgkmcnt(4)
v_exp_f32 v154, v154
v_mfma_f32_16x16x32_bf16 v[104:107], a[100:103], v[208:211], v[104:107]
v_exp_f32 v155, v155
v_mfma_f32_16x16x32_bf16 v[104:107], a[104:107], v[216:219], v[104:107]
v_cvt_pk_bf16_f32 v168, v144, v145
v_cvt_pk_bf16_f32 v169, v146, v147
v_mfma_f32_16x16x32_bf16 v[104:107], a[108:111], v[224:227], v[104:107]
v_exp_f32 v148, v148
v_mfma_f32_16x16x32_bf16 v[108:111], a[100:103], v[212:215], v[108:111]
ds_read_b128 a[96:99], v232 offset:14336
ds_read_b128 a[100:103], v233 offset:14336
v_mfma_f32_16x16x32_bf16 v[108:111], a[104:107], v[220:223], v[108:111]
v_exp_f32 v149, v149
v_mfma_f32_16x16x32_bf16 v[108:111], a[108:111], v[228:231], v[108:111]
s_waitcnt lgkmcnt(3)
v_exp_f32 v150, v150
v_mfma_f32_16x16x32_bf16 v[112:115], a[112:115], v[184:187], v[176:179]
ds_read_b128 a[104:107], v232 offset:18432
ds_read_b128 a[108:111], v233 offset:18432
v_mfma_f32_16x16x32_bf16 v[112:115], a[116:119], v[192:195], v[112:115]
v_exp_f32 v151, v151
v_mfma_f32_16x16x32_bf16 v[112:115], a[120:123], v[200:203], v[112:115]
)"
R"(v_cvt_pk_bf16_f32 v170, v152, v153
v_cvt_pk_bf16_f32 v171, v154, v155
v_mfma_f32_16x16x32_bf16 v[116:119], a[112:115], v[188:191], v[180:183]
v_exp_f32 v156, v156
v_mfma_f32_16x16x32_bf16 v[116:119], a[116:119], v[196:199], v[116:119]
ds_read_b128 a[112:115], v232 offset:19456
ds_read_b128 a[116:119], v233 offset:19456
v_mfma_f32_16x16x32_bf16 v[116:119], a[120:123], v[204:207], v[116:119]
s_waitcnt lgkmcnt(4)
v_exp_f32 v157, v157
v_mfma_f32_16x16x32_bf16 v[112:115], a[124:127], v[208:211], v[112:115]
v_exp_f32 v158, v158
v_mfma_f32_16x16x32_bf16 v[112:115], a[96:99], v[216:219], v[112:115]
s_waitcnt vmcnt(16)
s_barrier
v_mfma_f32_16x16x32_bf16 v[112:115], a[100:103], v[224:227], v[112:115]
s_add_u32 s75, s66, 2359680
v_exp_f32 v159, v159
v_mfma_f32_16x16x32_bf16 v[116:119], a[124:127], v[212:215], v[116:119]
s_add_u32 m0, s58, 0
ds_read_b128 a[120:123], v232 offset:20480
ds_read_b128 a[124:127], v233 offset:20480
v_mfma_f32_16x16x32_bf16 v[116:119], a[96:99], v[220:223], v[116:119]
buffer_load_dwordx4 %[vW], %[srdW], s75 offen offset:0 lds
)"
R"(v_mfma_f32_16x16x32_bf16 v[116:119], a[100:103], v[228:231], v[116:119]
s_waitcnt lgkmcnt(3)
v_add_u32 v232, s49, %[pV]
v_xor_b32 v233, 32, v232
v_mfma_f32_16x16x32_bf16 v[120:123], a[104:107], v[184:187], v[176:179]
ds_read_b64_tr_b16 a[96:97], v232 offset:0
ds_read_b64_tr_b16 a[98:99], v232 offset:6144
v_mfma_f32_16x16x32_bf16 v[120:123], a[108:111], v[192:195], v[120:123]
v_xor_b32 v234, 64, v232
v_xor_b32 v235, 96, v232
v_mfma_f32_16x16x32_bf16 v[120:123], a[112:115], v[200:203], v[120:123]
s_add_u32 m0, s58, 896
ds_read_b64_tr_b16 a[100:101], v232 offset:12288
ds_read_b64_tr_b16 a[102:103], v232 offset:18432
v_mfma_f32_16x16x32_bf16 v[124:127], a[104:107], v[188:191], v[180:183]
buffer_load_dwordx4 %[vW], %[srdW], s75 offen offset:128 lds
v_mfma_f32_16x16x32_bf16 v[124:127], a[108:111], v[196:199], v[124:127]
ds_read_b64_tr_b16 a[104:105], v233 offset:0
ds_read_b64_tr_b16 a[106:107], v233 offset:6144
v_mfma_f32_16x16x32_bf16 v[124:127], a[112:115], v[204:207], v[124:127]
s_waitcnt lgkmcnt(6)
v_cvt_pk_bf16_f32 v172, v148, v149
v_cvt_pk_bf16_f32 v173, v150, v151
v_mfma_f32_16x16x32_bf16 v[120:123], a[116:119], v[208:211], v[120:123]
)"
R"(s_add_u32 m0, s58, 1792
ds_read_b64_tr_b16 a[108:109], v233 offset:12288
ds_read_b64_tr_b16 a[110:111], v233 offset:18432
v_mfma_f32_16x16x32_bf16 v[120:123], a[120:123], v[216:219], v[120:123]
buffer_load_dwordx4 %[vW], %[srdW], s75 offen offset:256 lds
v_mfma_f32_16x16x32_bf16 v[120:123], a[124:127], v[224:227], v[120:123]
ds_read_b64_tr_b16 a[112:113], v234 offset:0
ds_read_b64_tr_b16 a[114:115], v234 offset:6144
v_mfma_f32_16x16x32_bf16 v[124:127], a[116:119], v[212:215], v[124:127]
s_add_u32 s75, s66, 2752896
v_cvt_pk_bf16_f32 v174, v156, v157
v_cvt_pk_bf16_f32 v175, v158, v159
v_mfma_f32_16x16x32_bf16 v[124:127], a[120:123], v[220:223], v[124:127]
s_add_u32 m0, s58, 12288
ds_read_b64_tr_b16 a[116:117], v234 offset:12288
ds_read_b64_tr_b16 a[118:119], v234 offset:18432
v_mfma_f32_16x16x32_bf16 v[124:127], a[124:127], v[228:231], v[124:127]
buffer_load_dwordx4 %[vW], %[srdW], s75 offen offset:0 lds
v_mfma_f32_16x16x32_bf16 a[132:135], a[128:131], v[160:163], a[132:135]
v_mfma_f32_16x16x32_bf16 a[132:135], a[128:131], v[168:171], a[132:135]
v_mfma_f32_16x16x32_bf16 a[136:139], a[128:131], v[164:167], a[136:139]
s_add_u32 m0, s58, 13184
v_mfma_f32_16x16x32_bf16 a[136:139], a[128:131], v[172:175], a[136:139]
s_waitcnt lgkmcnt(4)
)"
R"(buffer_load_dwordx4 %[vW], %[srdW], s75 offen offset:128 lds
v_mfma_f32_16x16x32_bf16 v[0:3], a[96:99], v[160:163], v[0:3]
ds_read_b64_tr_b16 a[120:121], v235 offset:0
ds_read_b64_tr_b16 a[122:123], v235 offset:6144
v_mfma_f32_16x16x32_bf16 v[0:3], a[100:103], v[168:171], v[0:3]
v_max3_f32 v236, v96, v97, v98
v_max3_f32 v237, v100, v101, v102
v_mfma_f32_16x16x32_bf16 v[4:7], a[96:99], v[164:167], v[4:7]
s_add_u32 m0, s58, 14080
ds_read_b64_tr_b16 a[124:125], v235 offset:12288
ds_read_b64_tr_b16 a[126:127], v235 offset:18432
v_mfma_f32_16x16x32_bf16 v[4:7], a[100:103], v[172:175], v[4:7]
buffer_load_dwordx4 %[vW], %[srdW], s75 offen offset:256 lds
v_mfma_f32_16x16x32_bf16 v[8:11], a[104:107], v[160:163], v[8:11]
ds_read_b64_tr_b16 a[96:97], v232 offset:1024
ds_read_b64_tr_b16 a[98:99], v232 offset:7168
v_mfma_f32_16x16x32_bf16 v[8:11], a[108:111], v[168:171], v[8:11]
v_max_f32 v236, v236, v99
v_max_f32 v237, v237, v103
v_mfma_f32_16x16x32_bf16 v[12:15], a[104:107], v[164:167], v[12:15]
ds_read_b64_tr_b16 a[100:101], v232 offset:13312
ds_read_b64_tr_b16 a[102:103], v232 offset:19456
v_mfma_f32_16x16x32_bf16 v[12:15], a[108:111], v[172:175], v[12:15]
s_waitcnt lgkmcnt(4)
)"
R"(v_max3_f32 v236, v236, v104, v105
v_max3_f32 v237, v237, v108, v109
v_mfma_f32_16x16x32_bf16 v[16:19], a[112:115], v[160:163], v[16:19]
ds_read_b64_tr_b16 a[104:105], v233 offset:1024
ds_read_b64_tr_b16 a[106:107], v233 offset:7168
v_mfma_f32_16x16x32_bf16 v[16:19], a[116:119], v[168:171], v[16:19]
v_max3_f32 v236, v236, v106, v107
v_max3_f32 v237, v237, v110, v111
v_mfma_f32_16x16x32_bf16 v[20:23], a[112:115], v[164:167], v[20:23]
ds_read_b64_tr_b16 a[108:109], v233 offset:13312
ds_read_b64_tr_b16 a[110:111], v233 offset:19456
v_mfma_f32_16x16x32_bf16 v[20:23], a[116:119], v[172:175], v[20:23]
v_max3_f32 v236, v236, v112, v113
v_max3_f32 v237, v237, v116, v117
v_mfma_f32_16x16x32_bf16 v[24:27], a[120:123], v[160:163], v[24:27]
ds_read_b64_tr_b16 a[112:113], v234 offset:1024
ds_read_b64_tr_b16 a[114:115], v234 offset:7168
v_mfma_f32_16x16x32_bf16 v[24:27], a[124:127], v[168:171], v[24:27]
v_max3_f32 v236, v236, v114, v115
v_max3_f32 v237, v237, v118, v119
v_mfma_f32_16x16x32_bf16 v[28:31], a[120:123], v[164:167], v[28:31]
ds_read_b64_tr_b16 a[116:117], v234 offset:13312
ds_read_b64_tr_b16 a[118:119], v234 offset:19456
v_mfma_f32_16x16x32_bf16 v[28:31], a[124:127], v[172:175], v[28:31]
)"
R"(s_waitcnt lgkmcnt(4)
v_max3_f32 v236, v236, v120, v121
v_max3_f32 v237, v237, v124, v125
v_mfma_f32_16x16x32_bf16 v[32:35], a[96:99], v[160:163], v[32:35]
ds_read_b64_tr_b16 a[120:121], v235 offset:1024
ds_read_b64_tr_b16 a[122:123], v235 offset:7168
v_mfma_f32_16x16x32_bf16 v[32:35], a[100:103], v[168:171], v[32:35]
v_max3_f32 v236, v236, v122, v123
v_max3_f32 v237, v237, v126, v127
v_mfma_f32_16x16x32_bf16 v[36:39], a[96:99], v[164:167], v[36:39]
ds_read_b64_tr_b16 a[124:125], v235 offset:13312
ds_read_b64_tr_b16 a[126:127], v235 offset:19456
v_mfma_f32_16x16x32_bf16 v[36:39], a[100:103], v[172:175], v[36:39]
v_max_f32 v236, v236, v237
v_cmp_lt_f32 vcc, s77, v236
v_mfma_f32_16x16x32_bf16 v[40:43], a[104:107], v[160:163], v[40:43]
ds_read_b64_tr_b16 a[96:97], v232 offset:2048
ds_read_b64_tr_b16 a[98:99], v232 offset:8192
v_mfma_f32_16x16x32_bf16 v[40:43], a[108:111], v[168:171], v[40:43]
v_mfma_f32_16x16x32_bf16 v[44:47], a[104:107], v[164:167], v[44:47]
ds_read_b64_tr_b16 a[100:101], v232 offset:14336
ds_read_b64_tr_b16 a[102:103], v232 offset:20480
v_mfma_f32_16x16x32_bf16 v[44:47], a[108:111], v[172:175], v[44:47]
s_waitcnt lgkmcnt(4)
)"
R"(v_mfma_f32_16x16x32_bf16 v[48:51], a[112:115], v[160:163], v[48:51]
ds_read_b64_tr_b16 a[104:105], v233 offset:2048
ds_read_b64_tr_b16 a[106:107], v233 offset:8192
v_mfma_f32_16x16x32_bf16 v[48:51], a[116:119], v[168:171], v[48:51]
v_mfma_f32_16x16x32_bf16 v[52:55], a[112:115], v[164:167], v[52:55]
ds_read_b64_tr_b16 a[108:109], v233 offset:14336
ds_read_b64_tr_b16 a[110:111], v233 offset:20480
v_mfma_f32_16x16x32_bf16 v[52:55], a[116:119], v[172:175], v[52:55]
v_mfma_f32_16x16x32_bf16 v[56:59], a[120:123], v[160:163], v[56:59]
ds_read_b64_tr_b16 a[112:113], v234 offset:2048
ds_read_b64_tr_b16 a[114:115], v234 offset:8192
v_mfma_f32_16x16x32_bf16 v[56:59], a[124:127], v[168:171], v[56:59]
v_mfma_f32_16x16x32_bf16 v[60:63], a[120:123], v[164:167], v[60:63]
ds_read_b64_tr_b16 a[116:117], v234 offset:14336
ds_read_b64_tr_b16 a[118:119], v234 offset:20480
v_mfma_f32_16x16x32_bf16 v[60:63], a[124:127], v[172:175], v[60:63]
s_waitcnt lgkmcnt(4)
s_waitcnt vmcnt(16)
s_barrier
v_mfma_f32_16x16x32_bf16 v[64:67], a[96:99], v[160:163], v[64:67]
ds_read_b64_tr_b16 a[120:121], v235 offset:2048
ds_read_b64_tr_b16 a[122:123], v235 offset:8192
v_mfma_f32_16x16x32_bf16 v[64:67], a[100:103], v[168:171], v[64:67]
ds_read_b128 a[144:147], %[pX0] offset:0
)"
R"(ds_read_b128 a[148:151], %[pX0] offset:2048
v_mfma_f32_16x16x32_bf16 v[68:71], a[96:99], v[164:167], v[68:71]
ds_read_b64_tr_b16 a[124:125], v235 offset:14336
ds_read_b64_tr_b16 a[126:127], v235 offset:20480
v_mfma_f32_16x16x32_bf16 v[68:71], a[100:103], v[172:175], v[68:71]
ds_read_b128 a[152:155], %[pX1] offset:0
ds_read_b128 a[156:159], %[pX1] offset:2048
v_add_u32 v232, s50, %[pW]
v_xor_b32 v233, 32, v232
v_mfma_f32_16x16x32_bf16 v[72:75], a[104:107], v[160:163], v[72:75]
ds_read_b64_tr_b16 a[96:97], v232 offset:0
ds_read_b64_tr_b16 a[98:99], v232 offset:512
v_mfma_f32_16x16x32_bf16 v[72:75], a[108:111], v[168:171], v[72:75]
s_add_u32 s76, s68, 49152
v_xor_b32 v234, 64, v232
v_xor_b32 v235, 96, v232
v_mfma_f32_16x16x32_bf16 v[76:79], a[104:107], v[164:167], v[76:79]
s_add_u32 m0, s60, 0
ds_read_b64_tr_b16 a[100:101], v232 offset:12288
ds_read_b64_tr_b16 a[102:103], v232 offset:12800
v_mfma_f32_16x16x32_bf16 v[76:79], a[108:111], v[172:175], v[76:79]
s_waitcnt lgkmcnt(6)
buffer_load_dwordx4 %[vV], %[srdV], s76 offen offset:0 nt lds
v_mfma_f32_16x16x32_bf16 v[80:83], a[112:115], v[160:163], v[80:83]
)"
R"(ds_read_b64_tr_b16 a[104:105], v233 offset:0
ds_read_b64_tr_b16 a[106:107], v233 offset:512
v_mfma_f32_16x16x32_bf16 v[80:83], a[116:119], v[168:171], v[80:83]
v_mfma_f32_16x16x32_bf16 v[84:87], a[112:115], v[164:167], v[84:87]
s_add_u32 m0, s60, 896
ds_read_b64_tr_b16 a[108:109], v233 offset:12288
ds_read_b64_tr_b16 a[110:111], v233 offset:12800
v_mfma_f32_16x16x32_bf16 v[84:87], a[116:119], v[172:175], v[84:87]
buffer_load_dwordx4 %[vV], %[srdV], s76 offen offset:128 nt lds
v_mfma_f32_16x16x32_bf16 v[88:91], a[120:123], v[160:163], v[88:91]
ds_read_b64_tr_b16 a[112:113], v234 offset:0
ds_read_b64_tr_b16 a[114:115], v234 offset:512
v_mfma_f32_16x16x32_bf16 v[88:91], a[124:127], v[168:171], v[88:91]
v_mfma_f32_16x16x32_bf16 v[92:95], a[120:123], v[164:167], v[92:95]
s_add_u32 m0, s60, 1792
ds_read_b64_tr_b16 a[116:117], v234 offset:12288
ds_read_b64_tr_b16 a[118:119], v234 offset:12800
v_mfma_f32_16x16x32_bf16 v[92:95], a[124:127], v[172:175], v[92:95]
s_waitcnt lgkmcnt(4)
buffer_load_dwordx4 %[vV], %[srdV], s76 offen offset:256 nt lds
s_cbranch_vccnz RARE3_%=
BACK3_%=:
v_mfma_f32_16x16x32_bf16 a[160:163], a[96:99], a[144:147], a[160:163]
ds_read_b64_tr_b16 a[120:121], v235 offset:0
)"
R"(ds_read_b64_tr_b16 a[122:123], v235 offset:512
v_mfma_f32_16x16x32_bf16 a[160:163], a[100:103], a[152:155], a[160:163]
v_exp_f32 v96, v96
v_mfma_f32_16x16x32_bf16 a[164:167], a[96:99], a[148:151], a[164:167]
s_add_u32 m0, s60, 0
ds_read_b64_tr_b16 a[124:125], v235 offset:12288
ds_read_b64_tr_b16 a[126:127], v235 offset:12800
v_mfma_f32_16x16x32_bf16 a[164:167], a[100:103], a[156:159], a[164:167]
buffer_load_dwordx4 %[vV], %[srdV], s76 offen offset:3072 nt lds
v_mfma_f32_16x16x32_bf16 a[168:171], a[104:107], a[144:147], a[168:171]
ds_read_b64_tr_b16 a[96:97], v232 offset:1024
ds_read_b64_tr_b16 a[98:99], v232 offset:1536
v_mfma_f32_16x16x32_bf16 a[168:171], a[108:111], a[152:155], a[168:171]
v_exp_f32 v97, v97
v_mfma_f32_16x16x32_bf16 a[172:175], a[104:107], a[148:151], a[172:175]
s_add_u32 m0, s60, 896
ds_read_b64_tr_b16 a[100:101], v232 offset:13312
ds_read_b64_tr_b16 a[102:103], v232 offset:13824
v_mfma_f32_16x16x32_bf16 a[172:175], a[108:111], a[156:159], a[172:175]
s_waitcnt lgkmcnt(4)
buffer_load_dwordx4 %[vV], %[srdV], s76 offen offset:3200 nt lds
v_mfma_f32_16x16x32_bf16 a[176:179], a[112:115], a[144:147], a[176:179]
ds_read_b64_tr_b16 a[104:105], v233 offset:1024
ds_read_b64_tr_b16 a[106:107], v233 offset:1536
)"
R"(v_mfma_f32_16x16x32_bf16 a[176:179], a[116:119], a[152:155], a[176:179]
v_exp_f32 v98, v98
v_mfma_f32_16x16x32_bf16 a[180:183], a[112:115], a[148:151], a[180:183]
s_add_u32 m0, s60, 1792
ds_read_b64_tr_b16 a[108:109], v233 offset:13312
ds_read_b64_tr_b16 a[110:111], v233 offset:13824
v_mfma_f32_16x16x32_bf16 a[180:183], a[116:119], a[156:159], a[180:183]
buffer_load_dwordx4 %[vV], %[srdV], s76 offen offset:3328 nt lds
v_mfma_f32_16x16x32_bf16 a[184:187], a[120:123], a[144:147], a[184:187]
ds_read_b64_tr_b16 a[112:113], v234 offset:1024
ds_read_b64_tr_b16 a[114:115], v234 offset:1536
v_mfma_f32_16x16x32_bf16 a[184:187], a[124:127], a[152:155], a[184:187]
v_exp_f32 v99, v99
v_mfma_f32_16x16x32_bf16 a[188:191], a[120:123], a[148:151], a[188:191]
s_add_u32 m0, s73, -512
ds_read_b64_tr_b16 a[116:117], v234 offset:13312
ds_read_b64_tr_b16 a[118:119], v234 offset:13824
v_mfma_f32_16x16x32_bf16 a[188:191], a[124:127], a[156:159], a[188:191]
s_waitcnt lgkmcnt(4)
buffer_load_dwordx4 %[vX0], %[srdX], s69 offen offset:512 lds
v_mfma_f32_16x16x32_bf16 a[192:195], a[96:99], a[144:147], a[192:195]
ds_read_b64_tr_b16 a[120:121], v235 offset:1024
ds_read_b64_tr_b16 a[122:123], v235 offset:1536
v_mfma_f32_16x16x32_bf16 a[192:195], a[100:103], a[152:155], a[192:195]
)"
R"(v_exp_f32 v104, v104
v_mfma_f32_16x16x32_bf16 a[196:199], a[96:99], a[148:151], a[196:199]
s_add_u32 m0, s73, 512
ds_read_b64_tr_b16 a[124:125], v235 offset:13312
ds_read_b64_tr_b16 a[126:127], v235 offset:13824
v_mfma_f32_16x16x32_bf16 a[196:199], a[100:103], a[156:159], a[196:199]
buffer_load_dwordx4 %[vX1], %[srdX], s70 offen offset:512 lds
v_mfma_f32_16x16x32_bf16 a[200:203], a[104:107], a[144:147], a[200:203]
ds_read_b64_tr_b16 a[96:97], v232 offset:2048
ds_read_b64_tr_b16 a[98:99], v232 offset:2560
v_mfma_f32_16x16x32_bf16 a[200:203], a[108:111], a[152:155], a[200:203]
v_exp_f32 v105, v105
v_mfma_f32_16x16x32_bf16 a[204:207], a[104:107], a[148:151], a[204:207]
s_add_u32 m0, s73, 1536
ds_read_b64_tr_b16 a[100:101], v232 offset:14336
ds_read_b64_tr_b16 a[102:103], v232 offset:14848
v_mfma_f32_16x16x32_bf16 a[204:207], a[108:111], a[156:159], a[204:207]
s_waitcnt lgkmcnt(4)
buffer_load_dwordx4 %[vX0], %[srdX], s71 offen offset:512 lds
v_mfma_f32_16x16x32_bf16 a[208:211], a[112:115], a[144:147], a[208:211]
ds_read_b64_tr_b16 a[104:105], v233 offset:2048
ds_read_b64_tr_b16 a[106:107], v233 offset:2560
v_mfma_f32_16x16x32_bf16 a[208:211], a[116:119], a[152:155], a[208:211]
v_exp_f32 v106, v106
)"
R"(v_mfma_f32_16x16x32_bf16 a[212:215], a[112:115], a[148:151], a[212:215]
s_add_u32 m0, s73, 2560
ds_read_b64_tr_b16 a[108:109], v233 offset:14336
ds_read_b64_tr_b16 a[110:111], v233 offset:14848
v_mfma_f32_16x16x32_bf16 a[212:215], a[116:119], a[156:159], a[212:215]
buffer_load_dwordx4 %[vX1], %[srdX], s72 offen offset:512 lds
v_mfma_f32_16x16x32_bf16 a[216:219], a[120:123], a[144:147], a[216:219]
ds_read_b64_tr_b16 a[112:113], v234 offset:2048
ds_read_b64_tr_b16 a[114:115], v234 offset:2560
v_mfma_f32_16x16x32_bf16 a[216:219], a[124:127], a[152:155], a[216:219]
v_exp_f32 v107, v107
v_mfma_f32_16x16x32_bf16 a[220:223], a[120:123], a[148:151], a[220:223]
s_add_u32 s75, s67, 98304
ds_read_b64_tr_b16 a[116:117], v234 offset:14336
ds_read_b64_tr_b16 a[118:119], v234 offset:14848
v_mfma_f32_16x16x32_bf16 a[220:223], a[124:127], a[156:159], a[220:223]
s_waitcnt lgkmcnt(4)
s_waitcnt vmcnt(16)
s_barrier
v_mfma_f32_16x16x32_bf16 a[224:227], a[96:99], a[144:147], a[224:227]
s_add_u32 m0, s55, 0
ds_read_b64_tr_b16 a[120:121], v235 offset:2048
ds_read_b64_tr_b16 a[122:123], v235 offset:2560
v_mfma_f32_16x16x32_bf16 a[224:227], a[100:103], a[152:155], a[224:227]
)"
R"(buffer_load_dwordx4 %[vK], %[srdK], s75 offen offset:0 nt lds
v_mfma_f32_16x16x32_bf16 a[228:231], a[96:99], a[148:151], a[228:231]
ds_read_b64_tr_b16 a[124:125], v235 offset:14336
ds_read_b64_tr_b16 a[126:127], v235 offset:14848
v_mfma_f32_16x16x32_bf16 a[228:231], a[100:103], a[156:159], a[228:231]
v_add_u32 v232, s51, %[pK]
v_xor_b32 v233, 64, v232
v_mfma_f32_16x16x32_bf16 a[232:235], a[104:107], a[144:147], a[232:235]
s_add_u32 m0, s55, 896
ds_read_b128 a[96:99], v232 offset:0
ds_read_b128 a[100:103], v233 offset:0
v_mfma_f32_16x16x32_bf16 a[232:235], a[108:111], a[152:155], a[232:235]
buffer_load_dwordx4 %[vK], %[srdK], s75 offen offset:128 nt lds
v_mfma_f32_16x16x32_bf16 a[236:239], a[104:107], a[148:151], a[236:239]
v_cvt_pk_bf16_f32 v160, v96, v97
v_cvt_pk_bf16_f32 v161, v98, v99
v_mfma_f32_16x16x32_bf16 a[236:239], a[108:111], a[156:159], a[236:239]
s_waitcnt lgkmcnt(2)
v_exp_f32 v100, v100
v_mfma_f32_16x16x32_bf16 a[240:243], a[112:115], a[144:147], a[240:243]
s_add_u32 m0, s55, 1792
ds_read_b128 a[104:107], v232 offset:1024
ds_read_b128 a[108:111], v233 offset:1024
v_mfma_f32_16x16x32_bf16 a[240:243], a[116:119], a[152:155], a[240:243]
)"
R"(buffer_load_dwordx4 %[vK], %[srdK], s75 offen offset:256 nt lds
v_mfma_f32_16x16x32_bf16 a[244:247], a[112:115], a[148:151], a[244:247]
v_exp_f32 v101, v101
v_mfma_f32_16x16x32_bf16 a[244:247], a[116:119], a[156:159], a[244:247]
s_add_u32 s75, s67, 110592
v_exp_f32 v102, v102
v_mfma_f32_16x16x32_bf16 a[248:251], a[120:123], a[144:147], a[248:251]
s_add_u32 m0, s55, 12288
ds_read_b128 a[112:115], v232 offset:2048
ds_read_b128 a[116:119], v233 offset:2048
v_mfma_f32_16x16x32_bf16 a[248:251], a[124:127], a[152:155], a[248:251]
buffer_load_dwordx4 %[vK], %[srdK], s75 offen offset:0 nt lds
v_mfma_f32_16x16x32_bf16 a[252:255], a[120:123], a[148:151], a[252:255]
v_exp_f32 v103, v103
v_mfma_f32_16x16x32_bf16 a[252:255], a[124:127], a[156:159], a[252:255]
s_waitcnt lgkmcnt(3)
v_cvt_pk_bf16_f32 v162, v104, v105
v_cvt_pk_bf16_f32 v163, v106, v107
v_mfma_f32_16x16x32_bf16 v[128:131], a[96:99], v[184:187], v[176:179]
s_add_u32 m0, s55, 13184
ds_read_b128 a[120:123], v232 offset:6144
ds_read_b128 a[124:127], v233 offset:6144
v_mfma_f32_16x16x32_bf16 v[128:131], a[100:103], v[192:195], v[128:131]
buffer_load_dwordx4 %[vK], %[srdK], s75 offen offset:128 nt lds
)"
R"(v_mfma_f32_16x16x32_bf16 v[128:131], a[104:107], v[200:203], v[128:131]
v_exp_f32 v108, v108
v_mfma_f32_16x16x32_bf16 v[132:135], a[96:99], v[188:191], v[180:183]
v_exp_f32 v109, v109
v_mfma_f32_16x16x32_bf16 v[132:135], a[100:103], v[196:199], v[132:135]
s_add_u32 m0, s55, 14080
ds_read_b128 a[96:99], v232 offset:7168
ds_read_b128 a[100:103], v233 offset:7168
v_mfma_f32_16x16x32_bf16 v[132:135], a[104:107], v[204:207], v[132:135]
s_waitcnt lgkmcnt(4)
buffer_load_dwordx4 %[vK], %[srdK], s75 offen offset:256 nt lds
v_mfma_f32_16x16x32_bf16 v[128:131], a[108:111], v[208:211], v[128:131]
v_exp_f32 v110, v110
v_mfma_f32_16x16x32_bf16 v[128:131], a[112:115], v[216:219], v[128:131]
v_exp_f32 v111, v111
v_mfma_f32_16x16x32_bf16 v[128:131], a[116:119], v[224:227], v[128:131]
v_cvt_pk_bf16_f32 v164, v100, v101
v_cvt_pk_bf16_f32 v165, v102, v103
v_mfma_f32_16x16x32_bf16 v[132:135], a[108:111], v[212:215], v[132:135]
ds_read_b128 a[104:107], v232 offset:8192
ds_read_b128 a[108:111], v233 offset:8192
v_mfma_f32_16x16x32_bf16 v[132:135], a[112:115], v[220:223], v[132:135]
v_exp_f32 v112, v112
v_mfma_f32_16x16x32_bf16 v[132:135], a[116:119], v[228:231], v[132:135]
)"
R"(s_waitcnt lgkmcnt(3)
v_exp_f32 v113, v113
v_mfma_f32_16x16x32_bf16 v[136:139], a[120:123], v[184:187], v[176:179]
ds_read_b128 a[112:115], v232 offset:12288
ds_read_b128 a[116:119], v233 offset:12288
v_mfma_f32_16x16x32_bf16 v[136:139], a[124:127], v[192:195], v[136:139]
v_exp_f32 v114, v114
v_mfma_f32_16x16x32_bf16 v[136:139], a[96:99], v[200:203], v[136:139]
v_exp_f32 v115, v115
v_mfma_f32_16x16x32_bf16 v[140:143], a[120:123], v[188:191], v[180:183]
v_cvt_pk_bf16_f32 v166, v108, v109
v_cvt_pk_bf16_f32 v167, v110, v111
v_mfma_f32_16x16x32_bf16 v[140:143], a[124:127], v[196:199], v[140:143]
ds_read_b128 a[120:123], v232 offset:13312
ds_read_b128 a[124:127], v233 offset:13312
v_mfma_f32_16x16x32_bf16 v[140:143], a[96:99], v[204:207], v[140:143]
s_waitcnt lgkmcnt(4)
v_exp_f32 v120, v120
v_mfma_f32_16x16x32_bf16 v[136:139], a[100:103], v[208:211], v[136:139]
v_exp_f32 v121, v121
v_mfma_f32_16x16x32_bf16 v[136:139], a[104:107], v[216:219], v[136:139]
v_exp_f32 v122, v122
v_mfma_f32_16x16x32_bf16 v[136:139], a[108:111], v[224:227], v[136:139]
v_exp_f32 v123, v123
)"
R"(v_mfma_f32_16x16x32_bf16 v[140:143], a[100:103], v[212:215], v[140:143]
ds_read_b128 a[96:99], v232 offset:14336
ds_read_b128 a[100:103], v233 offset:14336
v_mfma_f32_16x16x32_bf16 v[140:143], a[104:107], v[220:223], v[140:143]
v_cvt_pk_bf16_f32 v168, v112, v113
v_cvt_pk_bf16_f32 v169, v114, v115
v_mfma_f32_16x16x32_bf16 v[140:143], a[108:111], v[228:231], v[140:143]
s_waitcnt lgkmcnt(3)
v_exp_f32 v116, v116
v_mfma_f32_16x16x32_bf16 v[144:147], a[112:115], v[184:187], v[176:179]
ds_read_b128 a[104:107], v232 offset:18432
ds_read_b128 a[108:111], v233 offset:18432
v_mfma_f32_16x16x32_bf16 v[144:147], a[116:119], v[192:195], v[144:147]
v_exp_f32 v117, v117
v_mfma_f32_16x16x32_bf16 v[144:147], a[120:123], v[200:203], v[144:147]
v_exp_f32 v118, v118
v_mfma_f32_16x16x32_bf16 v[148:151], a[112:115], v[188:191], v[180:183]
v_exp_f32 v119, v119
v_mfma_f32_16x16x32_bf16 v[148:151], a[116:119], v[196:199], v[148:151]
ds_read_b128 a[112:115], v232 offset:19456
ds_read_b128 a[116:119], v233 offset:19456
v_mfma_f32_16x16x32_bf16 v[148:151], a[120:123], v[204:207], v[148:151]
s_waitcnt lgkmcnt(4)
v_cvt_pk_bf16_f32 v170, v120, v121
)"
R"(v_cvt_pk_bf16_f32 v171, v122, v123
v_mfma_f32_16x16x32_bf16 v[144:147], a[124:127], v[208:211], v[144:147]
v_exp_f32 v124, v124
v_mfma_f32_16x16x32_bf16 v[144:147], a[96:99], v[216:219], v[144:147]
s_waitcnt vmcnt(16)
s_barrier
v_mfma_f32_16x16x32_bf16 v[144:147], a[100:103], v[224:227], v[144:147]
s_add_u32 s75, s66, 3146112
v_exp_f32 v125, v125
v_mfma_f32_16x16x32_bf16 v[148:151], a[124:127], v[212:215], v[148:151]
s_add_u32 m0, s56, 0
ds_read_b128 a[120:123], v232 offset:20480
ds_read_b128 a[124:127], v233 offset:20480
ds_read_b128 a[144:147], %[pX0] offset:16384
ds_read_b128 a[148:151], %[pX0] offset:18432
v_mfma_f32_16x16x32_bf16 v[148:151], a[96:99], v[220:223], v[148:151]
buffer_load_dwordx4 %[vW], %[srdW], s75 offen offset:0 lds
v_mfma_f32_16x16x32_bf16 v[148:151], a[100:103], v[228:231], v[148:151]
s_waitcnt lgkmcnt(5)
ds_read_b128 a[152:155], %[pX1] offset:16384
ds_read_b128 a[156:159], %[pX1] offset:18432
v_add_u32 v232, s52, %[pW]
v_xor_b32 v233, 32, v232
v_mfma_f32_16x16x32_bf16 v[152:155], a[104:107], v[184:187], v[176:179]
)"
R"(ds_read_b64_tr_b16 a[96:97], v232 offset:0
ds_read_b64_tr_b16 a[98:99], v232 offset:512
v_mfma_f32_16x16x32_bf16 v[152:155], a[108:111], v[192:195], v[152:155]
v_xor_b32 v234, 64, v232
v_xor_b32 v235, 96, v232
v_mfma_f32_16x16x32_bf16 v[152:155], a[112:115], v[200:203], v[152:155]
s_add_u32 m0, s56, 896
ds_read_b64_tr_b16 a[100:101], v232 offset:12288
ds_read_b64_tr_b16 a[102:103], v232 offset:12800
v_mfma_f32_16x16x32_bf16 v[156:159], a[104:107], v[188:191], v[180:183]
buffer_load_dwordx4 %[vW], %[srdW], s75 offen offset:128 lds
v_mfma_f32_16x16x32_bf16 v[156:159], a[108:111], v[196:199], v[156:159]
ds_read_b64_tr_b16 a[104:105], v233 offset:0
ds_read_b64_tr_b16 a[106:107], v233 offset:512
v_mfma_f32_16x16x32_bf16 v[156:159], a[112:115], v[204:207], v[156:159]
s_waitcnt lgkmcnt(10)
v_exp_f32 v126, v126
v_mfma_f32_16x16x32_bf16 v[152:155], a[116:119], v[208:211], v[152:155]
s_add_u32 m0, s56, 1792
ds_read_b64_tr_b16 a[108:109], v233 offset:12288
ds_read_b64_tr_b16 a[110:111], v233 offset:12800
v_mfma_f32_16x16x32_bf16 v[152:155], a[120:123], v[216:219], v[152:155]
buffer_load_dwordx4 %[vW], %[srdW], s75 offen offset:256 lds
v_mfma_f32_16x16x32_bf16 v[152:155], a[124:127], v[224:227], v[152:155]
)"
R"(ds_read_b64_tr_b16 a[112:113], v234 offset:0
ds_read_b64_tr_b16 a[114:115], v234 offset:512
v_mfma_f32_16x16x32_bf16 v[156:159], a[116:119], v[212:215], v[156:159]
s_add_u32 s75, s66, 3539328
v_exp_f32 v127, v127
v_mfma_f32_16x16x32_bf16 v[156:159], a[120:123], v[220:223], v[156:159]
s_add_u32 m0, s56, 12288
ds_read_b64_tr_b16 a[116:117], v234 offset:12288
ds_read_b64_tr_b16 a[118:119], v234 offset:12800
v_mfma_f32_16x16x32_bf16 v[156:159], a[124:127], v[228:231], v[156:159]
s_waitcnt lgkmcnt(4)
buffer_load_dwordx4 %[vW], %[srdW], s75 offen offset:0 lds
v_mfma_f32_16x16x32_bf16 a[160:163], a[96:99], a[144:147], a[160:163]
ds_read_b64_tr_b16 a[120:121], v235 offset:0
ds_read_b64_tr_b16 a[122:123], v235 offset:512
v_mfma_f32_16x16x32_bf16 a[160:163], a[100:103], a[152:155], a[160:163]
v_cvt_pk_bf16_f32 v172, v116, v117
v_cvt_pk_bf16_f32 v173, v118, v119
v_mfma_f32_16x16x32_bf16 a[164:167], a[96:99], a[148:151], a[164:167]
s_add_u32 m0, s56, 13184
ds_read_b64_tr_b16 a[124:125], v235 offset:12288
ds_read_b64_tr_b16 a[126:127], v235 offset:12800
v_mfma_f32_16x16x32_bf16 a[164:167], a[100:103], a[156:159], a[164:167]
buffer_load_dwordx4 %[vW], %[srdW], s75 offen offset:128 lds
)"
R"(v_mfma_f32_16x16x32_bf16 a[168:171], a[104:107], a[144:147], a[168:171]
ds_read_b64_tr_b16 a[96:97], v232 offset:1024
ds_read_b64_tr_b16 a[98:99], v232 offset:1536
v_mfma_f32_16x16x32_bf16 a[168:171], a[108:111], a[152:155], a[168:171]
v_cvt_pk_bf16_f32 v174, v124, v125
v_cvt_pk_bf16_f32 v175, v126, v127
v_mfma_f32_16x16x32_bf16 a[172:175], a[104:107], a[148:151], a[172:175]
s_add_u32 m0, s56, 14080
ds_read_b64_tr_b16 a[100:101], v232 offset:13312
ds_read_b64_tr_b16 a[102:103], v232 offset:13824
v_mfma_f32_16x16x32_bf16 a[172:175], a[108:111], a[156:159], a[172:175]
s_waitcnt lgkmcnt(4)
buffer_load_dwordx4 %[vW], %[srdW], s75 offen offset:256 lds
v_mfma_f32_16x16x32_bf16 a[176:179], a[112:115], a[144:147], a[176:179]
ds_read_b64_tr_b16 a[104:105], v233 offset:1024
ds_read_b64_tr_b16 a[106:107], v233 offset:1536
v_mfma_f32_16x16x32_bf16 a[176:179], a[116:119], a[152:155], a[176:179]
v_max3_f32 v236, v128, v129, v130
v_max3_f32 v237, v132, v133, v134
v_mfma_f32_16x16x32_bf16 a[180:183], a[112:115], a[148:151], a[180:183]
s_add_u32 m0, s73, 15744
ds_read_b64_tr_b16 a[108:109], v233 offset:13312
ds_read_b64_tr_b16 a[110:111], v233 offset:13824
v_mfma_f32_16x16x32_bf16 a[180:183], a[116:119], a[156:159], a[180:183]
)"
R"(buffer_load_dwordx4 %[vX0], %[srdX], s69 offen offset:640 lds
v_mfma_f32_16x16x32_bf16 a[184:187], a[120:123], a[144:147], a[184:187]
ds_read_b64_tr_b16 a[112:113], v234 offset:1024
ds_read_b64_tr_b16 a[114:115], v234 offset:1536
v_mfma_f32_16x16x32_bf16 a[184:187], a[124:127], a[152:155], a[184:187]
v_max_f32 v236, v236, v131
v_max_f32 v237, v237, v135
v_mfma_f32_16x16x32_bf16 a[188:191], a[120:123], a[148:151], a[188:191]
s_add_u32 m0, s73, 16768
ds_read_b64_tr_b16 a[116:117], v234 offset:13312
ds_read_b64_tr_b16 a[118:119], v234 offset:13824
v_mfma_f32_16x16x32_bf16 a[188:191], a[124:127], a[156:159], a[188:191]
s_waitcnt lgkmcnt(4)
buffer_load_dwordx4 %[vX1], %[srdX], s70 offen offset:640 lds
v_mfma_f32_16x16x32_bf16 a[192:195], a[96:99], a[144:147], a[192:195]
ds_read_b64_tr_b16 a[120:121], v235 offset:1024
ds_read_b64_tr_b16 a[122:123], v235 offset:1536
v_mfma_f32_16x16x32_bf16 a[192:195], a[100:103], a[152:155], a[192:195]
v_max3_f32 v236, v236, v136, v137
v_max3_f32 v237, v237, v140, v141
v_mfma_f32_16x16x32_bf16 a[196:199], a[96:99], a[148:151], a[196:199]
s_add_u32 m0, s73, 17792
ds_read_b64_tr_b16 a[124:125], v235 offset:13312
ds_read_b64_tr_b16 a[126:127], v235 offset:13824
)"
R"(v_mfma_f32_16x16x32_bf16 a[196:199], a[100:103], a[156:159], a[196:199]
buffer_load_dwordx4 %[vX0], %[srdX], s71 offen offset:640 lds
v_mfma_f32_16x16x32_bf16 a[200:203], a[104:107], a[144:147], a[200:203]
ds_read_b64_tr_b16 a[96:97], v232 offset:2048
ds_read_b64_tr_b16 a[98:99], v232 offset:2560
v_mfma_f32_16x16x32_bf16 a[200:203], a[108:111], a[152:155], a[200:203]
v_max3_f32 v236, v236, v138, v139
v_max3_f32 v237, v237, v142, v143
v_mfma_f32_16x16x32_bf16 a[204:207], a[104:107], a[148:151], a[204:207]
s_add_u32 m0, s73, 18816
ds_read_b64_tr_b16 a[100:101], v232 offset:14336
ds_read_b64_tr_b16 a[102:103], v232 offset:14848
v_mfma_f32_16x16x32_bf16 a[204:207], a[108:111], a[156:159], a[204:207]
s_waitcnt lgkmcnt(4)
buffer_load_dwordx4 %[vX1], %[srdX], s72 offen offset:640 lds
v_mfma_f32_16x16x32_bf16 a[208:211], a[112:115], a[144:147], a[208:211]
ds_read_b64_tr_b16 a[104:105], v233 offset:2048
ds_read_b64_tr_b16 a[106:107], v233 offset:2560
v_mfma_f32_16x16x32_bf16 a[208:211], a[116:119], a[152:155], a[208:211]
v_max3_f32 v236, v236, v144, v145
v_max3_f32 v237, v237, v148, v149
v_mfma_f32_16x16x32_bf16 a[212:215], a[112:115], a[148:151], a[212:215]
ds_read_b64_tr_b16 a[108:109], v233 offset:14336
ds_read_b64_tr_b16 a[110:111], v233 offset:14848
)"
R"(v_mfma_f32_16x16x32_bf16 a[212:215], a[116:119], a[156:159], a[212:215]
v_max3_f32 v236, v236, v146, v147
v_max3_f32 v237, v237, v150, v151
v_mfma_f32_16x16x32_bf16 a[216:219], a[120:123], a[144:147], a[216:219]
ds_read_b64_tr_b16 a[112:113], v234 offset:2048
ds_read_b64_tr_b16 a[114:115], v234 offset:2560
v_mfma_f32_16x16x32_bf16 a[216:219], a[124:127], a[152:155], a[216:219]
v_max3_f32 v236, v236, v152, v153
v_max3_f32 v237, v237, v156, v157
v_mfma_f32_16x16x32_bf16 a[220:223], a[120:123], a[148:151], a[220:223]
s_add_u32 s76, s68, 73728
ds_read_b64_tr_b16 a[116:117], v234 offset:14336
ds_read_b64_tr_b16 a[118:119], v234 offset:14848
v_mfma_f32_16x16x32_bf16 a[220:223], a[124:127], a[156:159], a[220:223]
s_waitcnt lgkmcnt(4)
s_waitcnt vmcnt(20)
s_barrier
v_mfma_f32_16x16x32_bf16 a[224:227], a[96:99], a[144:147], a[224:227]
s_add_u32 m0, s63, 0
ds_read_b64_tr_b16 a[120:121], v235 offset:2048
ds_read_b64_tr_b16 a[122:123], v235 offset:2560
v_mfma_f32_16x16x32_bf16 a[224:227], a[100:103], a[152:155], a[224:227]
buffer_load_dwordx4 %[vV], %[srdV], s76 offen offset:0 nt lds
v_mfma_f32_16x16x32_bf16 a[228:231], a[96:99], a[148:151], a[228:231]
)"
R"(ds_read_b64_tr_b16 a[124:125], v235 offset:14336
ds_read_b64_tr_b16 a[126:127], v235 offset:14848
v_mfma_f32_16x16x32_bf16 a[228:231], a[100:103], a[156:159], a[228:231]
v_add_u32 v232, s48, %[pV]
v_xor_b32 v233, 32, v232
v_mfma_f32_16x16x32_bf16 a[232:235], a[104:107], a[144:147], a[232:235]
ds_read_b64_tr_b16 a[96:97], v232 offset:0
ds_read_b64_tr_b16 a[98:99], v232 offset:6144
v_mfma_f32_16x16x32_bf16 a[232:235], a[108:111], a[152:155], a[232:235]
v_xor_b32 v234, 64, v232
v_xor_b32 v235, 96, v232
v_mfma_f32_16x16x32_bf16 a[236:239], a[104:107], a[148:151], a[236:239]
s_add_u32 m0, s63, 896
ds_read_b64_tr_b16 a[100:101], v232 offset:12288
ds_read_b64_tr_b16 a[102:103], v232 offset:18432
v_mfma_f32_16x16x32_bf16 a[236:239], a[108:111], a[156:159], a[236:239]
s_waitcnt lgkmcnt(4)
buffer_load_dwordx4 %[vV], %[srdV], s76 offen offset:128 nt lds
v_mfma_f32_16x16x32_bf16 a[240:243], a[112:115], a[144:147], a[240:243]
ds_read_b64_tr_b16 a[104:105], v233 offset:0
ds_read_b64_tr_b16 a[106:107], v233 offset:6144
v_mfma_f32_16x16x32_bf16 a[240:243], a[116:119], a[152:155], a[240:243]
v_max3_f32 v236, v236, v154, v155
v_max3_f32 v237, v237, v158, v159
)"
R"(v_mfma_f32_16x16x32_bf16 a[244:247], a[112:115], a[148:151], a[244:247]
s_add_u32 m0, s63, 1792
ds_read_b64_tr_b16 a[108:109], v233 offset:12288
ds_read_b64_tr_b16 a[110:111], v233 offset:18432
v_mfma_f32_16x16x32_bf16 a[244:247], a[116:119], a[156:159], a[244:247]
buffer_load_dwordx4 %[vV], %[srdV], s76 offen offset:256 nt lds
v_mfma_f32_16x16x32_bf16 a[248:251], a[120:123], a[144:147], a[248:251]
ds_read_b64_tr_b16 a[112:113], v234 offset:0
ds_read_b64_tr_b16 a[114:115], v234 offset:6144
v_mfma_f32_16x16x32_bf16 a[248:251], a[124:127], a[152:155], a[248:251]
v_max_f32 v236, v236, v237
v_cmp_lt_f32 vcc, s77, v236
v_mfma_f32_16x16x32_bf16 a[252:255], a[120:123], a[148:151], a[252:255]
s_add_u32 m0, s63, 0
ds_read_b64_tr_b16 a[116:117], v234 offset:12288
ds_read_b64_tr_b16 a[118:119], v234 offset:18432
v_mfma_f32_16x16x32_bf16 a[252:255], a[124:127], a[156:159], a[252:255]
buffer_load_dwordx4 %[vV], %[srdV], s76 offen offset:3072 nt lds
v_mfma_f32_16x16x32_bf16 a[132:135], a[128:131], v[160:163], a[132:135]
v_mfma_f32_16x16x32_bf16 a[132:135], a[128:131], v[168:171], a[132:135]
v_mfma_f32_16x16x32_bf16 a[136:139], a[128:131], v[164:167], a[136:139]
s_add_u32 m0, s63, 896
v_mfma_f32_16x16x32_bf16 a[136:139], a[128:131], v[172:175], a[136:139]
s_waitcnt lgkmcnt(4)
)"
R"(buffer_load_dwordx4 %[vV], %[srdV], s76 offen offset:3200 nt lds
v_mfma_f32_16x16x32_bf16 v[0:3], a[96:99], v[160:163], v[0:3]
ds_read_b64_tr_b16 a[120:121], v235 offset:0
ds_read_b64_tr_b16 a[122:123], v235 offset:6144
v_mfma_f32_16x16x32_bf16 v[0:3], a[100:103], v[168:171], v[0:3]
v_mfma_f32_16x16x32_bf16 v[4:7], a[96:99], v[164:167], v[4:7]
s_add_u32 m0, s63, 1792
ds_read_b64_tr_b16 a[124:125], v235 offset:12288
ds_read_b64_tr_b16 a[126:127], v235 offset:18432
v_mfma_f32_16x16x32_bf16 v[4:7], a[100:103], v[172:175], v[4:7]
buffer_load_dwordx4 %[vV], %[srdV], s76 offen offset:3328 nt lds
v_mfma_f32_16x16x32_bf16 v[8:11], a[104:107], v[160:163], v[8:11]
ds_read_b64_tr_b16 a[96:97], v232 offset:1024
ds_read_b64_tr_b16 a[98:99], v232 offset:7168
v_mfma_f32_16x16x32_bf16 v[8:11], a[108:111], v[168:171], v[8:11]
v_mfma_f32_16x16x32_bf16 v[12:15], a[104:107], v[164:167], v[12:15]
ds_read_b64_tr_b16 a[100:101], v232 offset:13312
ds_read_b64_tr_b16 a[102:103], v232 offset:19456
v_mfma_f32_16x16x32_bf16 v[12:15], a[108:111], v[172:175], v[12:15]
s_waitcnt lgkmcnt(4)
v_mfma_f32_16x16x32_bf16 v[16:19], a[112:115], v[160:163], v[16:19]
ds_read_b64_tr_b16 a[104:105], v233 offset:1024
ds_read_b64_tr_b16 a[106:107], v233 offset:7168
v_mfma_f32_16x16x32_bf16 v[16:19], a[116:119], v[168:171], v[16:19]
)"
R"(v_mfma_f32_16x16x32_bf16 v[20:23], a[112:115], v[164:167], v[20:23]
ds_read_b64_tr_b16 a[108:109], v233 offset:13312
ds_read_b64_tr_b16 a[110:111], v233 offset:19456
v_mfma_f32_16x16x32_bf16 v[20:23], a[116:119], v[172:175], v[20:23]
v_mfma_f32_16x16x32_bf16 v[24:27], a[120:123], v[160:163], v[24:27]
ds_read_b64_tr_b16 a[112:113], v234 offset:1024
ds_read_b64_tr_b16 a[114:115], v234 offset:7168
v_mfma_f32_16x16x32_bf16 v[24:27], a[124:127], v[168:171], v[24:27]
v_mfma_f32_16x16x32_bf16 v[28:31], a[120:123], v[164:167], v[28:31]
ds_read_b64_tr_b16 a[116:117], v234 offset:13312
ds_read_b64_tr_b16 a[118:119], v234 offset:19456
v_mfma_f32_16x16x32_bf16 v[28:31], a[124:127], v[172:175], v[28:31]
s_waitcnt lgkmcnt(4)
v_mfma_f32_16x16x32_bf16 v[32:35], a[96:99], v[160:163], v[32:35]
ds_read_b64_tr_b16 a[120:121], v235 offset:1024
ds_read_b64_tr_b16 a[122:123], v235 offset:7168
v_mfma_f32_16x16x32_bf16 v[32:35], a[100:103], v[168:171], v[32:35]
v_mfma_f32_16x16x32_bf16 v[36:39], a[96:99], v[164:167], v[36:39]
ds_read_b64_tr_b16 a[124:125], v235 offset:13312
ds_read_b64_tr_b16 a[126:127], v235 offset:19456
v_mfma_f32_16x16x32_bf16 v[36:39], a[100:103], v[172:175], v[36:39]
v_mfma_f32_16x16x32_bf16 v[40:43], a[104:107], v[160:163], v[40:43]
ds_read_b64_tr_b16 a[96:97], v232 offset:2048
ds_read_b64_tr_b16 a[98:99], v232 offset:8192
)"
R"(v_mfma_f32_16x16x32_bf16 v[40:43], a[108:111], v[168:171], v[40:43]
v_mfma_f32_16x16x32_bf16 v[44:47], a[104:107], v[164:167], v[44:47]
ds_read_b64_tr_b16 a[100:101], v232 offset:14336
ds_read_b64_tr_b16 a[102:103], v232 offset:20480
v_mfma_f32_16x16x32_bf16 v[44:47], a[108:111], v[172:175], v[44:47]
s_waitcnt lgkmcnt(4)
v_mfma_f32_16x16x32_bf16 v[48:51], a[112:115], v[160:163], v[48:51]
ds_read_b64_tr_b16 a[104:105], v233 offset:2048
ds_read_b64_tr_b16 a[106:107], v233 offset:8192
v_mfma_f32_16x16x32_bf16 v[48:51], a[116:119], v[168:171], v[48:51]
v_mfma_f32_16x16x32_bf16 v[52:55], a[112:115], v[164:167], v[52:55]
ds_read_b64_tr_b16 a[108:109], v233 offset:14336
ds_read_b64_tr_b16 a[110:111], v233 offset:20480
v_mfma_f32_16x16x32_bf16 v[52:55], a[116:119], v[172:175], v[52:55]
v_mfma_f32_16x16x32_bf16 v[56:59], a[120:123], v[160:163], v[56:59]
ds_read_b64_tr_b16 a[112:113], v234 offset:2048
ds_read_b64_tr_b16 a[114:115], v234 offset:8192
v_mfma_f32_16x16x32_bf16 v[56:59], a[124:127], v[168:171], v[56:59]
v_mfma_f32_16x16x32_bf16 v[60:63], a[120:123], v[164:167], v[60:63]
s_add_u32 s75, s67, 122880
ds_read_b64_tr_b16 a[116:117], v234 offset:14336
ds_read_b64_tr_b16 a[118:119], v234 offset:20480
v_mfma_f32_16x16x32_bf16 v[60:63], a[124:127], v[172:175], v[60:63]
s_waitcnt lgkmcnt(4)
)"
R"(s_waitcnt vmcnt(16)
s_barrier
v_mfma_f32_16x16x32_bf16 v[64:67], a[96:99], v[160:163], v[64:67]
s_add_u32 m0, s58, 0
ds_read_b64_tr_b16 a[120:121], v235 offset:2048
ds_read_b64_tr_b16 a[122:123], v235 offset:8192
v_mfma_f32_16x16x32_bf16 v[64:67], a[100:103], v[168:171], v[64:67]
buffer_load_dwordx4 %[vK], %[srdK], s75 offen offset:0 nt lds
v_mfma_f32_16x16x32_bf16 v[68:71], a[96:99], v[164:167], v[68:71]
ds_read_b64_tr_b16 a[124:125], v235 offset:14336
ds_read_b64_tr_b16 a[126:127], v235 offset:20480
v_mfma_f32_16x16x32_bf16 v[68:71], a[100:103], v[172:175], v[68:71]
v_add_u32 v232, s49, %[pK]
v_xor_b32 v233, 64, v232
v_mfma_f32_16x16x32_bf16 v[72:75], a[104:107], v[160:163], v[72:75]
s_add_u32 m0, s58, 896
ds_read_b128 a[96:99], v232 offset:0
ds_read_b128 a[100:103], v233 offset:0
v_mfma_f32_16x16x32_bf16 v[72:75], a[108:111], v[168:171], v[72:75]
buffer_load_dwordx4 %[vK], %[srdK], s75 offen offset:128 nt lds
v_mfma_f32_16x16x32_bf16 v[76:79], a[104:107], v[164:167], v[76:79]
v_mfma_f32_16x16x32_bf16 v[76:79], a[108:111], v[172:175], v[76:79]
s_waitcnt lgkmcnt(2)
v_mfma_f32_16x16x32_bf16 v[80:83], a[112:115], v[160:163], v[80:83]
)"
R"(s_add_u32 m0, s58, 1792
ds_read_b128 a[104:107], v232 offset:1024
ds_read_b128 a[108:111], v233 offset:1024
v_mfma_f32_16x16x32_bf16 v[80:83], a[116:119], v[168:171], v[80:83]
buffer_load_dwordx4 %[vK], %[srdK], s75 offen offset:256 nt lds
v_mfma_f32_16x16x32_bf16 v[84:87], a[112:115], v[164:167], v[84:87]
v_mfma_f32_16x16x32_bf16 v[84:87], a[116:119], v[172:175], v[84:87]
s_add_u32 s75, s67, 135168
v_mfma_f32_16x16x32_bf16 v[88:91], a[120:123], v[160:163], v[88:91]
s_add_u32 m0, s58, 12288
ds_read_b128 a[112:115], v232 offset:2048
ds_read_b128 a[116:119], v233 offset:2048
v_mfma_f32_16x16x32_bf16 v[88:91], a[124:127], v[168:171], v[88:91]
buffer_load_dwordx4 %[vK], %[srdK], s75 offen offset:0 nt lds
v_mfma_f32_16x16x32_bf16 v[92:95], a[120:123], v[164:167], v[92:95]
v_mfma_f32_16x16x32_bf16 v[92:95], a[124:127], v[172:175], v[92:95]
s_waitcnt lgkmcnt(3)
s_cbranch_vccnz RARE4_%=
BACK4_%=:
v_mfma_f32_16x16x32_bf16 v[96:99], a[96:99], v[184:187], v[176:179]
s_add_u32 m0, s58, 13184
ds_read_b128 a[120:123], v232 offset:6144
ds_read_b128 a[124:127], v233 offset:6144
v_mfma_f32_16x16x32_bf16 v[96:99], a[100:103], v[192:195], v[96:99]
)"
R"(buffer_load_dwordx4 %[vK], %[srdK], s75 offen offset:128 nt lds
v_mfma_f32_16x16x32_bf16 v[96:99], a[104:107], v[200:203], v[96:99]
v_exp_f32 v128, v128
v_mfma_f32_16x16x32_bf16 v[100:103], a[96:99], v[188:191], v[180:183]
v_exp_f32 v129, v129
v_mfma_f32_16x16x32_bf16 v[100:103], a[100:103], v[196:199], v[100:103]
s_add_u32 m0, s58, 14080
ds_read_b128 a[96:99], v232 offset:7168
ds_read_b128 a[100:103], v233 offset:7168
v_mfma_f32_16x16x32_bf16 v[100:103], a[104:107], v[204:207], v[100:103]
s_waitcnt lgkmcnt(4)
buffer_load_dwordx4 %[vK], %[srdK], s75 offen offset:256 nt lds
v_mfma_f32_16x16x32_bf16 v[96:99], a[108:111], v[208:211], v[96:99]
v_exp_f32 v130, v130
v_mfma_f32_16x16x32_bf16 v[96:99], a[112:115], v[216:219], v[96:99]
v_exp_f32 v131, v131
v_mfma_f32_16x16x32_bf16 v[96:99], a[116:119], v[224:227], v[96:99]
v_exp_f32 v136, v136
v_mfma_f32_16x16x32_bf16 v[100:103], a[108:111], v[212:215], v[100:103]
ds_read_b128 a[104:107], v232 offset:8192
ds_read_b128 a[108:111], v233 offset:8192
v_mfma_f32_16x16x32_bf16 v[100:103], a[112:115], v[220:223], v[100:103]
v_exp_f32 v137, v137
v_mfma_f32_16x16x32_bf16 v[100:103], a[116:119], v[228:231], v[100:103]
)"
R"(s_waitcnt lgkmcnt(3)
v_exp_f32 v138, v138
v_mfma_f32_16x16x32_bf16 v[104:107], a[120:123], v[184:187], v[176:179]
ds_read_b128 a[112:115], v232 offset:12288
ds_read_b128 a[116:119], v233 offset:12288
v_mfma_f32_16x16x32_bf16 v[104:107], a[124:127], v[192:195], v[104:107]
v_exp_f32 v139, v139
v_cvt_pk_bf16_f32 v160, v128, v129
v_mfma_f32_16x16x32_bf16 v[104:107], a[96:99], v[200:203], v[104:107]
v_cvt_pk_bf16_f32 v161, v130, v131
v_exp_f32 v132, v132
v_mfma_f32_16x16x32_bf16 v[108:111], a[120:123], v[188:191], v[180:183]
v_exp_f32 v133, v133
v_mfma_f32_16x16x32_bf16 v[108:111], a[124:127], v[196:199], v[108:111]
ds_read_b128 a[120:123], v232 offset:13312
ds_read_b128 a[124:127], v233 offset:13312
v_mfma_f32_16x16x32_bf16 v[108:111], a[96:99], v[204:207], v[108:111]
s_waitcnt lgkmcnt(4)
v_exp_f32 v134, v134
v_mfma_f32_16x16x32_bf16 v[104:107], a[100:103], v[208:211], v[104:107]
v_exp_f32 v135, v135
v_cvt_pk_bf16_f32 v162, v136, v137
v_mfma_f32_16x16x32_bf16 v[104:107], a[104:107], v[216:219], v[104:107]
v_cvt_pk_bf16_f32 v163, v138, v139
)"
R"(v_exp_f32 v140, v140
v_mfma_f32_16x16x32_bf16 v[104:107], a[108:111], v[224:227], v[104:107]
v_exp_f32 v141, v141
v_mfma_f32_16x16x32_bf16 v[108:111], a[100:103], v[212:215], v[108:111]
ds_read_b128 a[96:99], v232 offset:14336
ds_read_b128 a[100:103], v233 offset:14336
v_mfma_f32_16x16x32_bf16 v[108:111], a[104:107], v[220:223], v[108:111]
v_exp_f32 v142, v142
v_mfma_f32_16x16x32_bf16 v[108:111], a[108:111], v[228:231], v[108:111]
s_waitcnt lgkmcnt(3)
v_exp_f32 v143, v143
v_cvt_pk_bf16_f32 v164, v132, v133
v_mfma_f32_16x16x32_bf16 v[112:115], a[112:115], v[184:187], v[176:179]
ds_read_b128 a[104:107], v232 offset:18432
ds_read_b128 a[108:111], v233 offset:18432
v_cvt_pk_bf16_f32 v165, v134, v135
v_mfma_f32_16x16x32_bf16 v[112:115], a[116:119], v[192:195], v[112:115]
v_exp_f32 v144, v144
v_mfma_f32_16x16x32_bf16 v[112:115], a[120:123], v[200:203], v[112:115]
v_exp_f32 v145, v145
v_mfma_f32_16x16x32_bf16 v[116:119], a[112:115], v[188:191], v[180:183]
v_exp_f32 v146, v146
v_mfma_f32_16x16x32_bf16 v[116:119], a[116:119], v[196:199], v[116:119]
ds_read_b128 a[112:115], v232 offset:19456
)"
R"(ds_read_b128 a[116:119], v233 offset:19456
v_mfma_f32_16x16x32_bf16 v[116:119], a[120:123], v[204:207], v[116:119]
s_waitcnt lgkmcnt(4)
v_exp_f32 v147, v147
v_cvt_pk_bf16_f32 v166, v140, v141
v_mfma_f32_16x16x32_bf16 v[112:115], a[124:127], v[208:211], v[112:115]
v_cvt_pk_bf16_f32 v167, v142, v143
v_exp_f32 v152, v152
v_mfma_f32_16x16x32_bf16 v[112:115], a[96:99], v[216:219], v[112:115]
s_waitcnt vmcnt(16)
s_barrier
v_mfma_f32_16x16x32_bf16 v[112:115], a[100:103], v[224:227], v[112:115]
s_add_u32 s75, s66, 3932544
v_exp_f32 v153, v153
v_mfma_f32_16x16x32_bf16 v[116:119], a[124:127], v[212:215], v[116:119]
s_add_u32 m0, s54, 0
ds_read_b128 a[120:123], v232 offset:20480
ds_read_b128 a[124:127], v233 offset:20480
ds_read_b128 a[144:147], %[pX0] offset:0
ds_read_b128 a[148:151], %[pX0] offset:2048
v_mfma_f32_16x16x32_bf16 v[116:119], a[96:99], v[220:223], v[116:119]
buffer_load_dwordx4 %[vW], %[srdW], s75 offen offset:0 lds
v_mfma_f32_16x16x32_bf16 v[116:119], a[100:103], v[228:231], v[116:119]
s_waitcnt lgkmcnt(5)
)"
R"(ds_read_b128 a[152:155], %[pX1] offset:0
ds_read_b128 a[156:159], %[pX1] offset:2048
v_add_u32 v232, s50, %[pW]
v_xor_b32 v233, 32, v232
v_mfma_f32_16x16x32_bf16 v[120:123], a[104:107], v[184:187], v[176:179]
ds_read_b64_tr_b16 a[96:97], v232 offset:0
ds_read_b64_tr_b16 a[98:99], v232 offset:512
v_mfma_f32_16x16x32_bf16 v[120:123], a[108:111], v[192:195], v[120:123]
v_xor_b32 v234, 64, v232
v_xor_b32 v235, 96, v232
v_mfma_f32_16x16x32_bf16 v[120:123], a[112:115], v[200:203], v[120:123]
s_add_u32 m0, s54, 896
ds_read_b64_tr_b16 a[100:101], v232 offset:12288
ds_read_b64_tr_b16 a[102:103], v232 offset:12800
v_mfma_f32_16x16x32_bf16 v[124:127], a[104:107], v[188:191], v[180:183]
buffer_load_dwordx4 %[vW], %[srdW], s75 offen offset:128 lds
v_mfma_f32_16x16x32_bf16 v[124:127], a[108:111], v[196:199], v[124:127]
ds_read_b64_tr_b16 a[104:105], v233 offset:0
ds_read_b64_tr_b16 a[106:107], v233 offset:512
v_mfma_f32_16x16x32_bf16 v[124:127], a[112:115], v[204:207], v[124:127]
s_waitcnt lgkmcnt(10)
v_exp_f32 v154, v154
v_mfma_f32_16x16x32_bf16 v[120:123], a[116:119], v[208:211], v[120:123]
s_add_u32 m0, s54, 1792
)"
R"(ds_read_b64_tr_b16 a[108:109], v233 offset:12288
ds_read_b64_tr_b16 a[110:111], v233 offset:12800
v_mfma_f32_16x16x32_bf16 v[120:123], a[120:123], v[216:219], v[120:123]
buffer_load_dwordx4 %[vW], %[srdW], s75 offen offset:256 lds
v_mfma_f32_16x16x32_bf16 v[120:123], a[124:127], v[224:227], v[120:123]
ds_read_b64_tr_b16 a[112:113], v234 offset:0
ds_read_b64_tr_b16 a[114:115], v234 offset:512
v_mfma_f32_16x16x32_bf16 v[124:127], a[116:119], v[212:215], v[124:127]
s_add_u32 s75, s66, 4325760
v_exp_f32 v155, v155
v_cvt_pk_bf16_f32 v168, v144, v145
v_mfma_f32_16x16x32_bf16 v[124:127], a[120:123], v[220:223], v[124:127]
s_add_u32 m0, s54, 12288
ds_read_b64_tr_b16 a[116:117], v234 offset:12288
ds_read_b64_tr_b16 a[118:119], v234 offset:12800
v_cvt_pk_bf16_f32 v169, v146, v147
v_mfma_f32_16x16x32_bf16 v[124:127], a[124:127], v[228:231], v[124:127]
s_waitcnt lgkmcnt(4)
buffer_load_dwordx4 %[vW], %[srdW], s75 offen offset:0 lds
v_mfma_f32_16x16x32_bf16 a[160:163], a[96:99], a[144:147], a[160:163]
ds_read_b64_tr_b16 a[120:121], v235 offset:0
ds_read_b64_tr_b16 a[122:123], v235 offset:512
v_mfma_f32_16x16x32_bf16 a[160:163], a[100:103], a[152:155], a[160:163]
v_exp_f32 v148, v148
)"
R"(v_mfma_f32_16x16x32_bf16 a[164:167], a[96:99], a[148:151], a[164:167]
s_add_u32 m0, s54, 13184
ds_read_b64_tr_b16 a[124:125], v235 offset:12288
ds_read_b64_tr_b16 a[126:127], v235 offset:12800
v_mfma_f32_16x16x32_bf16 a[164:167], a[100:103], a[156:159], a[164:167]
buffer_load_dwordx4 %[vW], %[srdW], s75 offen offset:128 lds
v_mfma_f32_16x16x32_bf16 a[168:171], a[104:107], a[144:147], a[168:171]
ds_read_b64_tr_b16 a[96:97], v232 offset:1024
ds_read_b64_tr_b16 a[98:99], v232 offset:1536
v_mfma_f32_16x16x32_bf16 a[168:171], a[108:111], a[152:155], a[168:171]
v_exp_f32 v149, v149
v_mfma_f32_16x16x32_bf16 a[172:175], a[104:107], a[148:151], a[172:175]
s_add_u32 m0, s54, 14080
ds_read_b64_tr_b16 a[100:101], v232 offset:13312
ds_read_b64_tr_b16 a[102:103], v232 offset:13824
v_mfma_f32_16x16x32_bf16 a[172:175], a[108:111], a[156:159], a[172:175]
s_waitcnt lgkmcnt(4)
buffer_load_dwordx4 %[vW], %[srdW], s75 offen offset:256 lds
v_mfma_f32_16x16x32_bf16 a[176:179], a[112:115], a[144:147], a[176:179]
ds_read_b64_tr_b16 a[104:105], v233 offset:1024
ds_read_b64_tr_b16 a[106:107], v233 offset:1536
v_mfma_f32_16x16x32_bf16 a[176:179], a[116:119], a[152:155], a[176:179]
v_exp_f32 v150, v150
v_mfma_f32_16x16x32_bf16 a[180:183], a[112:115], a[148:151], a[180:183]
)"
R"(s_add_u32 m0, s73, -768
ds_read_b64_tr_b16 a[108:109], v233 offset:13312
ds_read_b64_tr_b16 a[110:111], v233 offset:13824
v_mfma_f32_16x16x32_bf16 a[180:183], a[116:119], a[156:159], a[180:183]
buffer_load_dwordx4 %[vX0], %[srdX], s69 offen offset:768 lds
v_mfma_f32_16x16x32_bf16 a[184:187], a[120:123], a[144:147], a[184:187]
ds_read_b64_tr_b16 a[112:113], v234 offset:1024
ds_read_b64_tr_b16 a[114:115], v234 offset:1536
v_mfma_f32_16x16x32_bf16 a[184:187], a[124:127], a[152:155], a[184:187]
v_exp_f32 v151, v151
v_mfma_f32_16x16x32_bf16 a[188:191], a[120:123], a[148:151], a[188:191]
s_add_u32 m0, s73, 256
ds_read_b64_tr_b16 a[116:117], v234 offset:13312
ds_read_b64_tr_b16 a[118:119], v234 offset:13824
v_mfma_f32_16x16x32_bf16 a[188:191], a[124:127], a[156:159], a[188:191]
s_waitcnt lgkmcnt(4)
buffer_load_dwordx4 %[vX1], %[srdX], s70 offen offset:768 lds
v_mfma_f32_16x16x32_bf16 a[192:195], a[96:99], a[144:147], a[192:195]
ds_read_b64_tr_b16 a[120:121], v235 offset:1024
ds_read_b64_tr_b16 a[122:123], v235 offset:1536
v_mfma_f32_16x16x32_bf16 a[192:195], a[100:103], a[152:155], a[192:195]
v_cvt_pk_bf16_f32 v170, v152, v153
v_cvt_pk_bf16_f32 v171, v154, v155
v_mfma_f32_16x16x32_bf16 a[196:199], a[96:99], a[148:151], a[196:199]
)"
R"(s_add_u32 m0, s73, 1280
ds_read_b64_tr_b16 a[124:125], v235 offset:13312
ds_read_b64_tr_b16 a[126:127], v235 offset:13824
v_mfma_f32_16x16x32_bf16 a[196:199], a[100:103], a[156:159], a[196:199]
buffer_load_dwordx4 %[vX0], %[srdX], s71 offen offset:768 lds
v_mfma_f32_16x16x32_bf16 a[200:203], a[104:107], a[144:147], a[200:203]
ds_read_b64_tr_b16 a[96:97], v232 offset:2048
ds_read_b64_tr_b16 a[98:99], v232 offset:2560
v_mfma_f32_16x16x32_bf16 a[200:203], a[108:111], a[152:155], a[200:203]
v_exp_f32 v156, v156
v_mfma_f32_16x16x32_bf16 a[204:207], a[104:107], a[148:151], a[204:207]
s_add_u32 m0, s73, 2304
ds_read_b64_tr_b16 a[100:101], v232 offset:14336
ds_read_b64_tr_b16 a[102:103], v232 offset:14848
v_mfma_f32_16x16x32_bf16 a[204:207], a[108:111], a[156:159], a[204:207]
s_waitcnt lgkmcnt(4)
buffer_load_dwordx4 %[vX1], %[srdX], s72 offen offset:768 lds
v_mfma_f32_16x16x32_bf16 a[208:211], a[112:115], a[144:147], a[208:211]
ds_read_b64_tr_b16 a[104:105], v233 offset:2048
ds_read_b64_tr_b16 a[106:107], v233 offset:2560
v_mfma_f32_16x16x32_bf16 a[208:211], a[116:119], a[152:155], a[208:211]
v_exp_f32 v157, v157
v_mfma_f32_16x16x32_bf16 a[212:215], a[112:115], a[148:151], a[212:215]
ds_read_b64_tr_b16 a[108:109], v233 offset:14336
)"
R"(ds_read_b64_tr_b16 a[110:111], v233 offset:14848
v_mfma_f32_16x16x32_bf16 a[212:215], a[116:119], a[156:159], a[212:215]
v_exp_f32 v158, v158
v_mfma_f32_16x16x32_bf16 a[216:219], a[120:123], a[144:147], a[216:219]
ds_read_b64_tr_b16 a[112:113], v234 offset:2048
ds_read_b64_tr_b16 a[114:115], v234 offset:2560
v_mfma_f32_16x16x32_bf16 a[216:219], a[124:127], a[152:155], a[216:219]
v_exp_f32 v159, v159
v_mfma_f32_16x16x32_bf16 a[220:223], a[120:123], a[148:151], a[220:223]
s_add_u32 s76, s68, 98304
ds_read_b64_tr_b16 a[116:117], v234 offset:14336
ds_read_b64_tr_b16 a[118:119], v234 offset:14848
v_mfma_f32_16x16x32_bf16 a[220:223], a[124:127], a[156:159], a[220:223]
s_waitcnt lgkmcnt(4)
s_waitcnt vmcnt(16)
s_barrier
v_mfma_f32_16x16x32_bf16 a[224:227], a[96:99], a[144:147], a[224:227]
s_add_u32 m0, s61, 0
ds_read_b64_tr_b16 a[120:121], v235 offset:2048
ds_read_b64_tr_b16 a[122:123], v235 offset:2560
v_mfma_f32_16x16x32_bf16 a[224:227], a[100:103], a[152:155], a[224:227]
buffer_load_dwordx4 %[vV], %[srdV], s76 offen offset:0 nt lds
v_mfma_f32_16x16x32_bf16 a[228:231], a[96:99], a[148:151], a[228:231]
ds_read_b64_tr_b16 a[124:125], v235 offset:14336
)"
R"(ds_read_b64_tr_b16 a[126:127], v235 offset:14848
v_mfma_f32_16x16x32_bf16 a[228:231], a[100:103], a[156:159], a[228:231]
v_add_u32 v232, s51, %[pV]
v_xor_b32 v233, 32, v232
v_mfma_f32_16x16x32_bf16 a[232:235], a[104:107], a[144:147], a[232:235]
ds_read_b64_tr_b16 a[96:97], v232 offset:0
ds_read_b64_tr_b16 a[98:99], v232 offset:6144
v_mfma_f32_16x16x32_bf16 a[232:235], a[108:111], a[152:155], a[232:235]
v_xor_b32 v234, 64, v232
v_xor_b32 v235, 96, v232
v_mfma_f32_16x16x32_bf16 a[236:239], a[104:107], a[148:151], a[236:239]
s_add_u32 m0, s61, 896
ds_read_b64_tr_b16 a[100:101], v232 offset:12288
ds_read_b64_tr_b16 a[102:103], v232 offset:18432
v_mfma_f32_16x16x32_bf16 a[236:239], a[108:111], a[156:159], a[236:239]
s_waitcnt lgkmcnt(4)
buffer_load_dwordx4 %[vV], %[srdV], s76 offen offset:128 nt lds
v_mfma_f32_16x16x32_bf16 a[240:243], a[112:115], a[144:147], a[240:243]
ds_read_b64_tr_b16 a[104:105], v233 offset:0
ds_read_b64_tr_b16 a[106:107], v233 offset:6144
v_mfma_f32_16x16x32_bf16 a[240:243], a[116:119], a[152:155], a[240:243]
v_cvt_pk_bf16_f32 v172, v148, v149
v_cvt_pk_bf16_f32 v173, v150, v151
v_mfma_f32_16x16x32_bf16 a[244:247], a[112:115], a[148:151], a[244:247]
)"
R"(s_add_u32 m0, s61, 1792
ds_read_b64_tr_b16 a[108:109], v233 offset:12288
ds_read_b64_tr_b16 a[110:111], v233 offset:18432
v_mfma_f32_16x16x32_bf16 a[244:247], a[116:119], a[156:159], a[244:247]
buffer_load_dwordx4 %[vV], %[srdV], s76 offen offset:256 nt lds
v_mfma_f32_16x16x32_bf16 a[248:251], a[120:123], a[144:147], a[248:251]
ds_read_b64_tr_b16 a[112:113], v234 offset:0
ds_read_b64_tr_b16 a[114:115], v234 offset:6144
v_mfma_f32_16x16x32_bf16 a[248:251], a[124:127], a[152:155], a[248:251]
v_cvt_pk_bf16_f32 v174, v156, v157
v_cvt_pk_bf16_f32 v175, v158, v159
v_mfma_f32_16x16x32_bf16 a[252:255], a[120:123], a[148:151], a[252:255]
s_add_u32 m0, s61, 0
ds_read_b64_tr_b16 a[116:117], v234 offset:12288
ds_read_b64_tr_b16 a[118:119], v234 offset:18432
v_mfma_f32_16x16x32_bf16 a[252:255], a[124:127], a[156:159], a[252:255]
buffer_load_dwordx4 %[vV], %[srdV], s76 offen offset:3072 nt lds
v_mfma_f32_16x16x32_bf16 a[132:135], a[128:131], v[160:163], a[132:135]
v_max3_f32 v236, v96, v97, v98
v_max3_f32 v237, v100, v101, v102
v_mfma_f32_16x16x32_bf16 a[132:135], a[128:131], v[168:171], a[132:135]
v_max_f32 v236, v236, v99
v_max_f32 v237, v237, v103
v_mfma_f32_16x16x32_bf16 a[136:139], a[128:131], v[164:167], a[136:139]
)"
R"(s_add_u32 m0, s61, 896
v_max3_f32 v236, v236, v104, v105
v_max3_f32 v237, v237, v108, v109
v_mfma_f32_16x16x32_bf16 a[136:139], a[128:131], v[172:175], a[136:139]
s_waitcnt lgkmcnt(4)
buffer_load_dwordx4 %[vV], %[srdV], s76 offen offset:3200 nt lds
v_mfma_f32_16x16x32_bf16 v[0:3], a[96:99], v[160:163], v[0:3]
ds_read_b64_tr_b16 a[120:121], v235 offset:0
ds_read_b64_tr_b16 a[122:123], v235 offset:6144
v_mfma_f32_16x16x32_bf16 v[0:3], a[100:103], v[168:171], v[0:3]
v_max3_f32 v236, v236, v106, v107
v_max3_f32 v237, v237, v110, v111
v_mfma_f32_16x16x32_bf16 v[4:7], a[96:99], v[164:167], v[4:7]
s_add_u32 m0, s61, 1792
ds_read_b64_tr_b16 a[124:125], v235 offset:12288
ds_read_b64_tr_b16 a[126:127], v235 offset:18432
v_mfma_f32_16x16x32_bf16 v[4:7], a[100:103], v[172:175], v[4:7]
buffer_load_dwordx4 %[vV], %[srdV], s76 offen offset:3328 nt lds
v_mfma_f32_16x16x32_bf16 v[8:11], a[104:107], v[160:163], v[8:11]
ds_read_b64_tr_b16 a[96:97], v232 offset:1024
ds_read_b64_tr_b16 a[98:99], v232 offset:7168
v_mfma_f32_16x16x32_bf16 v[8:11], a[108:111], v[168:171], v[8:11]
v_max3_f32 v236, v236, v112, v113
v_max3_f32 v237, v237, v116, v117
)"
R"(v_mfma_f32_16x16x32_bf16 v[12:15], a[104:107], v[164:167], v[12:15]
ds_read_b64_tr_b16 a[100:101], v232 offset:13312
ds_read_b64_tr_b16 a[102:103], v232 offset:19456
v_mfma_f32_16x16x32_bf16 v[12:15], a[108:111], v[172:175], v[12:15]
s_waitcnt lgkmcnt(4)
v_max3_f32 v236, v236, v114, v115
v_max3_f32 v237, v237, v118, v119
v_mfma_f32_16x16x32_bf16 v[16:19], a[112:115], v[160:163], v[16:19]
ds_read_b64_tr_b16 a[104:105], v233 offset:1024
ds_read_b64_tr_b16 a[106:107], v233 offset:7168
v_mfma_f32_16x16x32_bf16 v[16:19], a[116:119], v[168:171], v[16:19]
v_max3_f32 v236, v236, v120, v121
v_max3_f32 v237, v237, v124, v125
v_mfma_f32_16x16x32_bf16 v[20:23], a[112:115], v[164:167], v[20:23]
ds_read_b64_tr_b16 a[108:109], v233 offset:13312
ds_read_b64_tr_b16 a[110:111], v233 offset:19456
v_mfma_f32_16x16x32_bf16 v[20:23], a[116:119], v[172:175], v[20:23]
v_max3_f32 v236, v236, v122, v123
v_max3_f32 v237, v237, v126, v127
v_mfma_f32_16x16x32_bf16 v[24:27], a[120:123], v[160:163], v[24:27]
ds_read_b64_tr_b16 a[112:113], v234 offset:1024
ds_read_b64_tr_b16 a[114:115], v234 offset:7168
v_mfma_f32_16x16x32_bf16 v[24:27], a[124:127], v[168:171], v[24:27]
v_max_f32 v236, v236, v237
)"
R"(v_cmp_lt_f32 vcc, s77, v236
v_mfma_f32_16x16x32_bf16 v[28:31], a[120:123], v[164:167], v[28:31]
ds_read_b64_tr_b16 a[116:117], v234 offset:13312
ds_read_b64_tr_b16 a[118:119], v234 offset:19456
v_mfma_f32_16x16x32_bf16 v[28:31], a[124:127], v[172:175], v[28:31]
s_waitcnt lgkmcnt(4)
v_mfma_f32_16x16x32_bf16 v[32:35], a[96:99], v[160:163], v[32:35]
ds_read_b64_tr_b16 a[120:121], v235 offset:1024
ds_read_b64_tr_b16 a[122:123], v235 offset:7168
v_mfma_f32_16x16x32_bf16 v[32:35], a[100:103], v[168:171], v[32:35]
v_mfma_f32_16x16x32_bf16 v[36:39], a[96:99], v[164:167], v[36:39]
ds_read_b64_tr_b16 a[124:125], v235 offset:13312
ds_read_b64_tr_b16 a[126:127], v235 offset:19456
v_mfma_f32_16x16x32_bf16 v[36:39], a[100:103], v[172:175], v[36:39]
v_mfma_f32_16x16x32_bf16 v[40:43], a[104:107], v[160:163], v[40:43]
ds_read_b64_tr_b16 a[96:97], v232 offset:2048
ds_read_b64_tr_b16 a[98:99], v232 offset:8192
v_mfma_f32_16x16x32_bf16 v[40:43], a[108:111], v[168:171], v[40:43]
v_mfma_f32_16x16x32_bf16 v[44:47], a[104:107], v[164:167], v[44:47]
ds_read_b64_tr_b16 a[100:101], v232 offset:14336
ds_read_b64_tr_b16 a[102:103], v232 offset:20480
v_mfma_f32_16x16x32_bf16 v[44:47], a[108:111], v[172:175], v[44:47]
s_waitcnt lgkmcnt(4)
v_mfma_f32_16x16x32_bf16 v[48:51], a[112:115], v[160:163], v[48:51]
)"
R"(ds_read_b64_tr_b16 a[104:105], v233 offset:2048
ds_read_b64_tr_b16 a[106:107], v233 offset:8192
v_mfma_f32_16x16x32_bf16 v[48:51], a[116:119], v[168:171], v[48:51]
v_mfma_f32_16x16x32_bf16 v[52:55], a[112:115], v[164:167], v[52:55]
ds_read_b64_tr_b16 a[108:109], v233 offset:14336
ds_read_b64_tr_b16 a[110:111], v233 offset:20480
v_mfma_f32_16x16x32_bf16 v[52:55], a[116:119], v[172:175], v[52:55]
v_mfma_f32_16x16x32_bf16 v[56:59], a[120:123], v[160:163], v[56:59]
ds_read_b64_tr_b16 a[112:113], v234 offset:2048
ds_read_b64_tr_b16 a[114:115], v234 offset:8192
v_mfma_f32_16x16x32_bf16 v[56:59], a[124:127], v[168:171], v[56:59]
v_mfma_f32_16x16x32_bf16 v[60:63], a[120:123], v[164:167], v[60:63]
s_add_u32 s75, s66, 4718976
ds_read_b64_tr_b16 a[116:117], v234 offset:14336
ds_read_b64_tr_b16 a[118:119], v234 offset:20480
v_mfma_f32_16x16x32_bf16 v[60:63], a[124:127], v[172:175], v[60:63]
s_waitcnt lgkmcnt(4)
s_waitcnt vmcnt(16)
s_barrier
v_mfma_f32_16x16x32_bf16 v[64:67], a[96:99], v[160:163], v[64:67]
s_add_u32 m0, s56, 0
ds_read_b64_tr_b16 a[120:121], v235 offset:2048
ds_read_b64_tr_b16 a[122:123], v235 offset:8192
v_mfma_f32_16x16x32_bf16 v[64:67], a[100:103], v[168:171], v[64:67]
)"
R"(buffer_load_dwordx4 %[vW], %[srdW], s75 offen offset:0 lds
v_mfma_f32_16x16x32_bf16 v[68:71], a[96:99], v[164:167], v[68:71]
ds_read_b64_tr_b16 a[124:125], v235 offset:14336
ds_read_b64_tr_b16 a[126:127], v235 offset:20480
v_mfma_f32_16x16x32_bf16 v[68:71], a[100:103], v[172:175], v[68:71]
v_add_u32 v232, s52, %[pK]
v_xor_b32 v233, 64, v232
v_mfma_f32_16x16x32_bf16 v[72:75], a[104:107], v[160:163], v[72:75]
s_add_u32 m0, s56, 896
ds_read_b128 a[96:99], v232 offset:0
ds_read_b128 a[100:103], v233 offset:0
v_mfma_f32_16x16x32_bf16 v[72:75], a[108:111], v[168:171], v[72:75]
buffer_load_dwordx4 %[vW], %[srdW], s75 offen offset:128 lds
v_mfma_f32_16x16x32_bf16 v[76:79], a[104:107], v[164:167], v[76:79]
v_mfma_f32_16x16x32_bf16 v[76:79], a[108:111], v[172:175], v[76:79]
s_waitcnt lgkmcnt(2)
v_mfma_f32_16x16x32_bf16 v[80:83], a[112:115], v[160:163], v[80:83]
s_add_u32 m0, s56, 1792
ds_read_b128 a[104:107], v232 offset:1024
ds_read_b128 a[108:111], v233 offset:1024
v_mfma_f32_16x16x32_bf16 v[80:83], a[116:119], v[168:171], v[80:83]
buffer_load_dwordx4 %[vW], %[srdW], s75 offen offset:256 lds
v_mfma_f32_16x16x32_bf16 v[84:87], a[112:115], v[164:167], v[84:87]
v_mfma_f32_16x16x32_bf16 v[84:87], a[116:119], v[172:175], v[84:87]
)"
R"(s_add_u32 s75, s66, 5112192
v_mfma_f32_16x16x32_bf16 v[88:91], a[120:123], v[160:163], v[88:91]
s_add_u32 m0, s56, 12288
ds_read_b128 a[112:115], v232 offset:2048
ds_read_b128 a[116:119], v233 offset:2048
v_mfma_f32_16x16x32_bf16 v[88:91], a[124:127], v[168:171], v[88:91]
buffer_load_dwordx4 %[vW], %[srdW], s75 offen offset:0 lds
v_mfma_f32_16x16x32_bf16 v[92:95], a[120:123], v[164:167], v[92:95]
v_mfma_f32_16x16x32_bf16 v[92:95], a[124:127], v[172:175], v[92:95]
s_waitcnt lgkmcnt(3)
s_cbranch_vccnz RARE5_%=
BACK5_%=:
v_mfma_f32_16x16x32_bf16 v[128:131], a[96:99], v[184:187], v[176:179]
s_add_u32 m0, s56, 13184
ds_read_b128 a[120:123], v232 offset:6144
ds_read_b128 a[124:127], v233 offset:6144
v_mfma_f32_16x16x32_bf16 v[128:131], a[100:103], v[192:195], v[128:131]
buffer_load_dwordx4 %[vW], %[srdW], s75 offen offset:128 lds
v_mfma_f32_16x16x32_bf16 v[128:131], a[104:107], v[200:203], v[128:131]
v_exp_f32 v96, v96
v_mfma_f32_16x16x32_bf16 v[132:135], a[96:99], v[188:191], v[180:183]
v_exp_f32 v97, v97
v_mfma_f32_16x16x32_bf16 v[132:135], a[100:103], v[196:199], v[132:135]
s_add_u32 m0, s56, 14080
)"
R"(ds_read_b128 a[96:99], v232 offset:7168
ds_read_b128 a[100:103], v233 offset:7168
v_mfma_f32_16x16x32_bf16 v[132:135], a[104:107], v[204:207], v[132:135]
s_waitcnt lgkmcnt(4)
buffer_load_dwordx4 %[vW], %[srdW], s75 offen offset:256 lds
v_mfma_f32_16x16x32_bf16 v[128:131], a[108:111], v[208:211], v[128:131]
v_exp_f32 v98, v98
v_mfma_f32_16x16x32_bf16 v[128:131], a[112:115], v[216:219], v[128:131]
v_exp_f32 v99, v99
v_mfma_f32_16x16x32_bf16 v[128:131], a[116:119], v[224:227], v[128:131]
v_exp_f32 v104, v104
v_mfma_f32_16x16x32_bf16 v[132:135], a[108:111], v[212:215], v[132:135]
ds_read_b128 a[104:107], v232 offset:8192
ds_read_b128 a[108:111], v233 offset:8192
v_mfma_f32_16x16x32_bf16 v[132:135], a[112:115], v[220:223], v[132:135]
v_exp_f32 v105, v105
v_mfma_f32_16x16x32_bf16 v[132:135], a[116:119], v[228:231], v[132:135]
s_waitcnt lgkmcnt(3)
v_exp_f32 v106, v106
v_mfma_f32_16x16x32_bf16 v[136:139], a[120:123], v[184:187], v[176:179]
ds_read_b128 a[112:115], v232 offset:12288
ds_read_b128 a[116:119], v233 offset:12288
v_mfma_f32_16x16x32_bf16 v[136:139], a[124:127], v[192:195], v[136:139]
v_exp_f32 v107, v107
)"
R"(v_cvt_pk_bf16_f32 v160, v96, v97
v_mfma_f32_16x16x32_bf16 v[136:139], a[96:99], v[200:203], v[136:139]
v_cvt_pk_bf16_f32 v161, v98, v99
v_exp_f32 v100, v100
v_mfma_f32_16x16x32_bf16 v[140:143], a[120:123], v[188:191], v[180:183]
v_exp_f32 v101, v101
v_mfma_f32_16x16x32_bf16 v[140:143], a[124:127], v[196:199], v[140:143]
ds_read_b128 a[120:123], v232 offset:13312
ds_read_b128 a[124:127], v233 offset:13312
v_mfma_f32_16x16x32_bf16 v[140:143], a[96:99], v[204:207], v[140:143]
s_waitcnt lgkmcnt(4)
v_exp_f32 v102, v102
v_mfma_f32_16x16x32_bf16 v[136:139], a[100:103], v[208:211], v[136:139]
v_exp_f32 v103, v103
v_cvt_pk_bf16_f32 v162, v104, v105
v_mfma_f32_16x16x32_bf16 v[136:139], a[104:107], v[216:219], v[136:139]
v_cvt_pk_bf16_f32 v163, v106, v107
v_exp_f32 v108, v108
v_mfma_f32_16x16x32_bf16 v[136:139], a[108:111], v[224:227], v[136:139]
v_exp_f32 v109, v109
v_mfma_f32_16x16x32_bf16 v[140:143], a[100:103], v[212:215], v[140:143]
ds_read_b128 a[96:99], v232 offset:14336
ds_read_b128 a[100:103], v233 offset:14336
v_mfma_f32_16x16x32_bf16 v[140:143], a[104:107], v[220:223], v[140:143]
)"
R"(v_exp_f32 v110, v110
v_mfma_f32_16x16x32_bf16 v[140:143], a[108:111], v[228:231], v[140:143]
s_waitcnt lgkmcnt(3)
v_exp_f32 v111, v111
v_cvt_pk_bf16_f32 v164, v100, v101
v_mfma_f32_16x16x32_bf16 v[144:147], a[112:115], v[184:187], v[176:179]
ds_read_b128 a[104:107], v232 offset:18432
ds_read_b128 a[108:111], v233 offset:18432
v_cvt_pk_bf16_f32 v165, v102, v103
v_mfma_f32_16x16x32_bf16 v[144:147], a[116:119], v[192:195], v[144:147]
v_exp_f32 v112, v112
v_mfma_f32_16x16x32_bf16 v[144:147], a[120:123], v[200:203], v[144:147]
v_exp_f32 v113, v113
v_mfma_f32_16x16x32_bf16 v[148:151], a[112:115], v[188:191], v[180:183]
v_exp_f32 v114, v114
v_mfma_f32_16x16x32_bf16 v[148:151], a[116:119], v[196:199], v[148:151]
ds_read_b128 a[112:115], v232 offset:19456
ds_read_b128 a[116:119], v233 offset:19456
v_mfma_f32_16x16x32_bf16 v[148:151], a[120:123], v[204:207], v[148:151]
s_waitcnt lgkmcnt(4)
v_exp_f32 v115, v115
v_cvt_pk_bf16_f32 v166, v108, v109
v_mfma_f32_16x16x32_bf16 v[144:147], a[124:127], v[208:211], v[144:147]
v_cvt_pk_bf16_f32 v167, v110, v111
)"
R"(v_exp_f32 v120, v120
v_mfma_f32_16x16x32_bf16 v[144:147], a[96:99], v[216:219], v[144:147]
s_waitcnt vmcnt(16)
s_barrier
v_mfma_f32_16x16x32_bf16 v[144:147], a[100:103], v[224:227], v[144:147]
s_add_u32 s75, s67, 147456
v_exp_f32 v121, v121
v_mfma_f32_16x16x32_bf16 v[148:151], a[124:127], v[212:215], v[148:151]
s_add_u32 m0, s57, 0
ds_read_b128 a[120:123], v232 offset:20480
ds_read_b128 a[124:127], v233 offset:20480
ds_read_b128 a[144:147], %[pX0] offset:16384
ds_read_b128 a[148:151], %[pX0] offset:18432
v_mfma_f32_16x16x32_bf16 v[148:151], a[96:99], v[220:223], v[148:151]
buffer_load_dwordx4 %[vK], %[srdK], s75 offen offset:0 nt lds
v_mfma_f32_16x16x32_bf16 v[148:151], a[100:103], v[228:231], v[148:151]
s_waitcnt lgkmcnt(5)
ds_read_b128 a[152:155], %[pX1] offset:16384
ds_read_b128 a[156:159], %[pX1] offset:18432
v_add_u32 v232, s48, %[pW]
v_xor_b32 v233, 32, v232
v_mfma_f32_16x16x32_bf16 v[152:155], a[104:107], v[184:187], v[176:179]
ds_read_b64_tr_b16 a[96:97], v232 offset:0
ds_read_b64_tr_b16 a[98:99], v232 offset:512
)"
R"(v_mfma_f32_16x16x32_bf16 v[152:155], a[108:111], v[192:195], v[152:155]
v_xor_b32 v234, 64, v232
v_xor_b32 v235, 96, v232
v_mfma_f32_16x16x32_bf16 v[152:155], a[112:115], v[200:203], v[152:155]
s_add_u32 m0, s57, 896
ds_read_b64_tr_b16 a[100:101], v232 offset:12288
ds_read_b64_tr_b16 a[102:103], v232 offset:12800
v_mfma_f32_16x16x32_bf16 v[156:159], a[104:107], v[188:191], v[180:183]
buffer_load_dwordx4 %[vK], %[srdK], s75 offen offset:128 nt lds
v_mfma_f32_16x16x32_bf16 v[156:159], a[108:111], v[196:199], v[156:159]
ds_read_b64_tr_b16 a[104:105], v233 offset:0
ds_read_b64_tr_b16 a[106:107], v233 offset:512
v_mfma_f32_16x16x32_bf16 v[156:159], a[112:115], v[204:207], v[156:159]
s_waitcnt lgkmcnt(10)
v_exp_f32 v122, v122
v_mfma_f32_16x16x32_bf16 v[152:155], a[116:119], v[208:211], v[152:155]
s_add_u32 m0, s57, 1792
ds_read_b64_tr_b16 a[108:109], v233 offset:12288
ds_read_b64_tr_b16 a[110:111], v233 offset:12800
v_mfma_f32_16x16x32_bf16 v[152:155], a[120:123], v[216:219], v[152:155]
buffer_load_dwordx4 %[vK], %[srdK], s75 offen offset:256 nt lds
v_mfma_f32_16x16x32_bf16 v[152:155], a[124:127], v[224:227], v[152:155]
ds_read_b64_tr_b16 a[112:113], v234 offset:0
ds_read_b64_tr_b16 a[114:115], v234 offset:512
)"
R"(v_mfma_f32_16x16x32_bf16 v[156:159], a[116:119], v[212:215], v[156:159]
s_add_u32 s75, s67, 159744
v_exp_f32 v123, v123
v_cvt_pk_bf16_f32 v168, v112, v113
v_mfma_f32_16x16x32_bf16 v[156:159], a[120:123], v[220:223], v[156:159]
s_add_u32 m0, s57, 12288
ds_read_b64_tr_b16 a[116:117], v234 offset:12288
ds_read_b64_tr_b16 a[118:119], v234 offset:12800
v_cvt_pk_bf16_f32 v169, v114, v115
v_mfma_f32_16x16x32_bf16 v[156:159], a[124:127], v[228:231], v[156:159]
s_waitcnt lgkmcnt(4)
buffer_load_dwordx4 %[vK], %[srdK], s75 offen offset:0 nt lds
v_mfma_f32_16x16x32_bf16 a[160:163], a[96:99], a[144:147], a[160:163]
ds_read_b64_tr_b16 a[120:121], v235 offset:0
ds_read_b64_tr_b16 a[122:123], v235 offset:512
v_mfma_f32_16x16x32_bf16 a[160:163], a[100:103], a[152:155], a[160:163]
v_exp_f32 v116, v116
v_mfma_f32_16x16x32_bf16 a[164:167], a[96:99], a[148:151], a[164:167]
s_add_u32 m0, s57, 13184
ds_read_b64_tr_b16 a[124:125], v235 offset:12288
ds_read_b64_tr_b16 a[126:127], v235 offset:12800
v_mfma_f32_16x16x32_bf16 a[164:167], a[100:103], a[156:159], a[164:167]
buffer_load_dwordx4 %[vK], %[srdK], s75 offen offset:128 nt lds
v_mfma_f32_16x16x32_bf16 a[168:171], a[104:107], a[144:147], a[168:171]
)"
R"(ds_read_b64_tr_b16 a[96:97], v232 offset:1024
ds_read_b64_tr_b16 a[98:99], v232 offset:1536
v_mfma_f32_16x16x32_bf16 a[168:171], a[108:111], a[152:155], a[168:171]
v_exp_f32 v117, v117
v_mfma_f32_16x16x32_bf16 a[172:175], a[104:107], a[148:151], a[172:175]
s_add_u32 m0, s57, 14080
ds_read_b64_tr_b16 a[100:101], v232 offset:13312
ds_read_b64_tr_b16 a[102:103], v232 offset:13824
v_mfma_f32_16x16x32_bf16 a[172:175], a[108:111], a[156:159], a[172:175]
s_waitcnt lgkmcnt(4)
buffer_load_dwordx4 %[vK], %[srdK], s75 offen offset:256 nt lds
v_mfma_f32_16x16x32_bf16 a[176:179], a[112:115], a[144:147], a[176:179]
ds_read_b64_tr_b16 a[104:105], v233 offset:1024
ds_read_b64_tr_b16 a[106:107], v233 offset:1536
v_mfma_f32_16x16x32_bf16 a[176:179], a[116:119], a[152:155], a[176:179]
v_exp_f32 v118, v118
v_mfma_f32_16x16x32_bf16 a[180:183], a[112:115], a[148:151], a[180:183]
s_add_u32 m0, s73, 15488
ds_read_b64_tr_b16 a[108:109], v233 offset:13312
ds_read_b64_tr_b16 a[110:111], v233 offset:13824
v_mfma_f32_16x16x32_bf16 a[180:183], a[116:119], a[156:159], a[180:183]
buffer_load_dwordx4 %[vX0], %[srdX], s69 offen offset:896 lds
v_mfma_f32_16x16x32_bf16 a[184:187], a[120:123], a[144:147], a[184:187]
ds_read_b64_tr_b16 a[112:113], v234 offset:1024
)"
R"(ds_read_b64_tr_b16 a[114:115], v234 offset:1536
v_mfma_f32_16x16x32_bf16 a[184:187], a[124:127], a[152:155], a[184:187]
v_exp_f32 v119, v119
v_mfma_f32_16x16x32_bf16 a[188:191], a[120:123], a[148:151], a[188:191]
s_add_u32 m0, s73, 16512
ds_read_b64_tr_b16 a[116:117], v234 offset:13312
ds_read_b64_tr_b16 a[118:119], v234 offset:13824
v_mfma_f32_16x16x32_bf16 a[188:191], a[124:127], a[156:159], a[188:191]
s_waitcnt lgkmcnt(4)
buffer_load_dwordx4 %[vX1], %[srdX], s70 offen offset:896 lds
v_mfma_f32_16x16x32_bf16 a[192:195], a[96:99], a[144:147], a[192:195]
ds_read_b64_tr_b16 a[120:121], v235 offset:1024
ds_read_b64_tr_b16 a[122:123], v235 offset:1536
v_mfma_f32_16x16x32_bf16 a[192:195], a[100:103], a[152:155], a[192:195]
v_cvt_pk_bf16_f32 v170, v120, v121
v_cvt_pk_bf16_f32 v171, v122, v123
v_mfma_f32_16x16x32_bf16 a[196:199], a[96:99], a[148:151], a[196:199]
s_add_u32 m0, s73, 17536
ds_read_b64_tr_b16 a[124:125], v235 offset:13312
ds_read_b64_tr_b16 a[126:127], v235 offset:13824
v_mfma_f32_16x16x32_bf16 a[196:199], a[100:103], a[156:159], a[196:199]
buffer_load_dwordx4 %[vX0], %[srdX], s71 offen offset:896 lds
v_mfma_f32_16x16x32_bf16 a[200:203], a[104:107], a[144:147], a[200:203]
ds_read_b64_tr_b16 a[96:97], v232 offset:2048
)"
R"(ds_read_b64_tr_b16 a[98:99], v232 offset:2560
v_mfma_f32_16x16x32_bf16 a[200:203], a[108:111], a[152:155], a[200:203]
v_exp_f32 v124, v124
v_mfma_f32_16x16x32_bf16 a[204:207], a[104:107], a[148:151], a[204:207]
s_add_u32 m0, s73, 18560
ds_read_b64_tr_b16 a[100:101], v232 offset:14336
ds_read_b64_tr_b16 a[102:103], v232 offset:14848
v_mfma_f32_16x16x32_bf16 a[204:207], a[108:111], a[156:159], a[204:207]
s_waitcnt lgkmcnt(4)
buffer_load_dwordx4 %[vX1], %[srdX], s72 offen offset:896 lds
v_mfma_f32_16x16x32_bf16 a[208:211], a[112:115], a[144:147], a[208:211]
ds_read_b64_tr_b16 a[104:105], v233 offset:2048
ds_read_b64_tr_b16 a[106:107], v233 offset:2560
v_mfma_f32_16x16x32_bf16 a[208:211], a[116:119], a[152:155], a[208:211]
v_exp_f32 v125, v125
v_mfma_f32_16x16x32_bf16 a[212:215], a[112:115], a[148:151], a[212:215]
ds_read_b64_tr_b16 a[108:109], v233 offset:14336
ds_read_b64_tr_b16 a[110:111], v233 offset:14848
v_mfma_f32_16x16x32_bf16 a[212:215], a[116:119], a[156:159], a[212:215]
v_exp_f32 v126, v126
v_mfma_f32_16x16x32_bf16 a[216:219], a[120:123], a[144:147], a[216:219]
ds_read_b64_tr_b16 a[112:113], v234 offset:2048
ds_read_b64_tr_b16 a[114:115], v234 offset:2560
v_mfma_f32_16x16x32_bf16 a[216:219], a[124:127], a[152:155], a[216:219]
)"
R"(v_exp_f32 v127, v127
v_mfma_f32_16x16x32_bf16 a[220:223], a[120:123], a[148:151], a[220:223]
s_add_u32 s76, s68, 122880
ds_read_b64_tr_b16 a[116:117], v234 offset:14336
ds_read_b64_tr_b16 a[118:119], v234 offset:14848
v_mfma_f32_16x16x32_bf16 a[220:223], a[124:127], a[156:159], a[220:223]
s_waitcnt lgkmcnt(4)
s_waitcnt vmcnt(16)
s_barrier
v_mfma_f32_16x16x32_bf16 a[224:227], a[96:99], a[144:147], a[224:227]
s_add_u32 m0, s64, 0
ds_read_b64_tr_b16 a[120:121], v235 offset:2048
ds_read_b64_tr_b16 a[122:123], v235 offset:2560
v_mfma_f32_16x16x32_bf16 a[224:227], a[100:103], a[152:155], a[224:227]
buffer_load_dwordx4 %[vV], %[srdV], s76 offen offset:0 nt lds
v_mfma_f32_16x16x32_bf16 a[228:231], a[96:99], a[148:151], a[228:231]
ds_read_b64_tr_b16 a[124:125], v235 offset:14336
ds_read_b64_tr_b16 a[126:127], v235 offset:14848
v_mfma_f32_16x16x32_bf16 a[228:231], a[100:103], a[156:159], a[228:231]
v_add_u32 v232, s49, %[pV]
v_xor_b32 v233, 32, v232
v_mfma_f32_16x16x32_bf16 a[232:235], a[104:107], a[144:147], a[232:235]
ds_read_b64_tr_b16 a[96:97], v232 offset:0
ds_read_b64_tr_b16 a[98:99], v232 offset:6144
)"
R"(v_mfma_f32_16x16x32_bf16 a[232:235], a[108:111], a[152:155], a[232:235]
v_xor_b32 v234, 64, v232
v_xor_b32 v235, 96, v232
v_mfma_f32_16x16x32_bf16 a[236:239], a[104:107], a[148:151], a[236:239]
s_add_u32 m0, s64, 896
ds_read_b64_tr_b16 a[100:101], v232 offset:12288
ds_read_b64_tr_b16 a[102:103], v232 offset:18432
v_mfma_f32_16x16x32_bf16 a[236:239], a[108:111], a[156:159], a[236:239]
s_waitcnt lgkmcnt(4)
buffer_load_dwordx4 %[vV], %[srdV], s76 offen offset:128 nt lds
v_mfma_f32_16x16x32_bf16 a[240:243], a[112:115], a[144:147], a[240:243]
ds_read_b64_tr_b16 a[104:105], v233 offset:0
ds_read_b64_tr_b16 a[106:107], v233 offset:6144
v_mfma_f32_16x16x32_bf16 a[240:243], a[116:119], a[152:155], a[240:243]
v_cvt_pk_bf16_f32 v172, v116, v117
v_cvt_pk_bf16_f32 v173, v118, v119
v_mfma_f32_16x16x32_bf16 a[244:247], a[112:115], a[148:151], a[244:247]
s_add_u32 m0, s64, 1792
ds_read_b64_tr_b16 a[108:109], v233 offset:12288
ds_read_b64_tr_b16 a[110:111], v233 offset:18432
v_mfma_f32_16x16x32_bf16 a[244:247], a[116:119], a[156:159], a[244:247]
buffer_load_dwordx4 %[vV], %[srdV], s76 offen offset:256 nt lds
v_mfma_f32_16x16x32_bf16 a[248:251], a[120:123], a[144:147], a[248:251]
ds_read_b64_tr_b16 a[112:113], v234 offset:0
)"
R"(ds_read_b64_tr_b16 a[114:115], v234 offset:6144
v_mfma_f32_16x16x32_bf16 a[248:251], a[124:127], a[152:155], a[248:251]
v_cvt_pk_bf16_f32 v174, v124, v125
v_cvt_pk_bf16_f32 v175, v126, v127
v_mfma_f32_16x16x32_bf16 a[252:255], a[120:123], a[148:151], a[252:255]
s_add_u32 m0, s64, 0
ds_read_b64_tr_b16 a[116:117], v234 offset:12288
ds_read_b64_tr_b16 a[118:119], v234 offset:18432
v_mfma_f32_16x16x32_bf16 a[252:255], a[124:127], a[156:159], a[252:255]
buffer_load_dwordx4 %[vV], %[srdV], s76 offen offset:3072 nt lds
v_mfma_f32_16x16x32_bf16 a[132:135], a[128:131], v[160:163], a[132:135]
v_max3_f32 v236, v128, v129, v130
v_max3_f32 v237, v132, v133, v134
v_mfma_f32_16x16x32_bf16 a[132:135], a[128:131], v[168:171], a[132:135]
v_max_f32 v236, v236, v131
v_max_f32 v237, v237, v135
v_mfma_f32_16x16x32_bf16 a[136:139], a[128:131], v[164:167], a[136:139]
s_add_u32 m0, s64, 896
v_max3_f32 v236, v236, v136, v137
v_max3_f32 v237, v237, v140, v141
v_mfma_f32_16x16x32_bf16 a[136:139], a[128:131], v[172:175], a[136:139]
s_waitcnt lgkmcnt(4)
buffer_load_dwordx4 %[vV], %[srdV], s76 offen offset:3200 nt lds
v_mfma_f32_16x16x32_bf16 v[0:3], a[96:99], v[160:163], v[0:3]
)"
R"(ds_read_b64_tr_b16 a[120:121], v235 offset:0
ds_read_b64_tr_b16 a[122:123], v235 offset:6144
v_mfma_f32_16x16x32_bf16 v[0:3], a[100:103], v[168:171], v[0:3]
v_max3_f32 v236, v236, v138, v139
v_max3_f32 v237, v237, v142, v143
v_mfma_f32_16x16x32_bf16 v[4:7], a[96:99], v[164:167], v[4:7]
s_add_u32 m0, s64, 1792
ds_read_b64_tr_b16 a[124:125], v235 offset:12288
ds_read_b64_tr_b16 a[126:127], v235 offset:18432
v_mfma_f32_16x16x32_bf16 v[4:7], a[100:103], v[172:175], v[4:7]
buffer_load_dwordx4 %[vV], %[srdV], s76 offen offset:3328 nt lds
v_mfma_f32_16x16x32_bf16 v[8:11], a[104:107], v[160:163], v[8:11]
ds_read_b64_tr_b16 a[96:97], v232 offset:1024
ds_read_b64_tr_b16 a[98:99], v232 offset:7168
v_mfma_f32_16x16x32_bf16 v[8:11], a[108:111], v[168:171], v[8:11]
v_max3_f32 v236, v236, v144, v145
v_max3_f32 v237, v237, v148, v149
v_mfma_f32_16x16x32_bf16 v[12:15], a[104:107], v[164:167], v[12:15]
ds_read_b64_tr_b16 a[100:101], v232 offset:13312
ds_read_b64_tr_b16 a[102:103], v232 offset:19456
v_mfma_f32_16x16x32_bf16 v[12:15], a[108:111], v[172:175], v[12:15]
s_waitcnt lgkmcnt(4)
v_max3_f32 v236, v236, v146, v147
v_max3_f32 v237, v237, v150, v151
)"
R"(v_mfma_f32_16x16x32_bf16 v[16:19], a[112:115], v[160:163], v[16:19]
ds_read_b64_tr_b16 a[104:105], v233 offset:1024
ds_read_b64_tr_b16 a[106:107], v233 offset:7168
v_mfma_f32_16x16x32_bf16 v[16:19], a[116:119], v[168:171], v[16:19]
v_max3_f32 v236, v236, v152, v153
v_max3_f32 v237, v237, v156, v157
v_mfma_f32_16x16x32_bf16 v[20:23], a[112:115], v[164:167], v[20:23]
ds_read_b64_tr_b16 a[108:109], v233 offset:13312
ds_read_b64_tr_b16 a[110:111], v233 offset:19456
v_mfma_f32_16x16x32_bf16 v[20:23], a[116:119], v[172:175], v[20:23]
v_max3_f32 v236, v236, v154, v155
v_max3_f32 v237, v237, v158, v159
v_mfma_f32_16x16x32_bf16 v[24:27], a[120:123], v[160:163], v[24:27]
ds_read_b64_tr_b16 a[112:113], v234 offset:1024
ds_read_b64_tr_b16 a[114:115], v234 offset:7168
v_mfma_f32_16x16x32_bf16 v[24:27], a[124:127], v[168:171], v[24:27]
v_max_f32 v236, v236, v237
v_cmp_lt_f32 vcc, s77, v236
v_mfma_f32_16x16x32_bf16 v[28:31], a[120:123], v[164:167], v[28:31]
ds_read_b64_tr_b16 a[116:117], v234 offset:13312
ds_read_b64_tr_b16 a[118:119], v234 offset:19456
v_mfma_f32_16x16x32_bf16 v[28:31], a[124:127], v[172:175], v[28:31]
s_waitcnt lgkmcnt(4)
v_mfma_f32_16x16x32_bf16 v[32:35], a[96:99], v[160:163], v[32:35]
)"
R"(ds_read_b64_tr_b16 a[120:121], v235 offset:1024
ds_read_b64_tr_b16 a[122:123], v235 offset:7168
v_mfma_f32_16x16x32_bf16 v[32:35], a[100:103], v[168:171], v[32:35]
v_mfma_f32_16x16x32_bf16 v[36:39], a[96:99], v[164:167], v[36:39]
ds_read_b64_tr_b16 a[124:125], v235 offset:13312
ds_read_b64_tr_b16 a[126:127], v235 offset:19456
v_mfma_f32_16x16x32_bf16 v[36:39], a[100:103], v[172:175], v[36:39]
v_mfma_f32_16x16x32_bf16 v[40:43], a[104:107], v[160:163], v[40:43]
ds_read_b64_tr_b16 a[96:97], v232 offset:2048
ds_read_b64_tr_b16 a[98:99], v232 offset:8192
v_mfma_f32_16x16x32_bf16 v[40:43], a[108:111], v[168:171], v[40:43]
v_mfma_f32_16x16x32_bf16 v[44:47], a[104:107], v[164:167], v[44:47]
ds_read_b64_tr_b16 a[100:101], v232 offset:14336
ds_read_b64_tr_b16 a[102:103], v232 offset:20480
v_mfma_f32_16x16x32_bf16 v[44:47], a[108:111], v[172:175], v[44:47]
s_waitcnt lgkmcnt(4)
v_mfma_f32_16x16x32_bf16 v[48:51], a[112:115], v[160:163], v[48:51]
ds_read_b64_tr_b16 a[104:105], v233 offset:2048
ds_read_b64_tr_b16 a[106:107], v233 offset:8192
v_mfma_f32_16x16x32_bf16 v[48:51], a[116:119], v[168:171], v[48:51]
v_mfma_f32_16x16x32_bf16 v[52:55], a[112:115], v[164:167], v[52:55]
ds_read_b64_tr_b16 a[108:109], v233 offset:14336
ds_read_b64_tr_b16 a[110:111], v233 offset:20480
v_mfma_f32_16x16x32_bf16 v[52:55], a[116:119], v[172:175], v[52:55]
)"
R"(v_mfma_f32_16x16x32_bf16 v[56:59], a[120:123], v[160:163], v[56:59]
ds_read_b64_tr_b16 a[112:113], v234 offset:2048
ds_read_b64_tr_b16 a[114:115], v234 offset:8192
v_mfma_f32_16x16x32_bf16 v[56:59], a[124:127], v[168:171], v[56:59]
v_mfma_f32_16x16x32_bf16 v[60:63], a[120:123], v[164:167], v[60:63]
ds_read_b64_tr_b16 a[116:117], v234 offset:14336
ds_read_b64_tr_b16 a[118:119], v234 offset:20480
v_mfma_f32_16x16x32_bf16 v[60:63], a[124:127], v[172:175], v[60:63]
s_waitcnt lgkmcnt(4)
s_waitcnt vmcnt(16)
s_barrier
v_mfma_f32_16x16x32_bf16 v[64:67], a[96:99], v[160:163], v[64:67]
ds_read_b64_tr_b16 a[120:121], v235 offset:2048
ds_read_b64_tr_b16 a[122:123], v235 offset:8192
v_mfma_f32_16x16x32_bf16 v[64:67], a[100:103], v[168:171], v[64:67]
ds_read_b128 a[144:147], %[pX0] offset:0
ds_read_b128 a[148:151], %[pX0] offset:2048
v_mfma_f32_16x16x32_bf16 v[68:71], a[96:99], v[164:167], v[68:71]
ds_read_b64_tr_b16 a[124:125], v235 offset:14336
ds_read_b64_tr_b16 a[126:127], v235 offset:20480
v_mfma_f32_16x16x32_bf16 v[68:71], a[100:103], v[172:175], v[68:71]
ds_read_b128 a[152:155], %[pX1] offset:0
ds_read_b128 a[156:159], %[pX1] offset:2048
v_add_u32 v232, s50, %[pW]
)"
R"(v_xor_b32 v233, 32, v232
v_mfma_f32_16x16x32_bf16 v[72:75], a[104:107], v[160:163], v[72:75]
ds_read_b64_tr_b16 a[96:97], v232 offset:0
ds_read_b64_tr_b16 a[98:99], v232 offset:512
v_mfma_f32_16x16x32_bf16 v[72:75], a[108:111], v[168:171], v[72:75]
s_add_u32 s75, s66, 5505408
v_xor_b32 v234, 64, v232
v_xor_b32 v235, 96, v232
v_mfma_f32_16x16x32_bf16 v[76:79], a[104:107], v[164:167], v[76:79]
s_add_u32 m0, s54, 0
ds_read_b64_tr_b16 a[100:101], v232 offset:12288
ds_read_b64_tr_b16 a[102:103], v232 offset:12800
v_mfma_f32_16x16x32_bf16 v[76:79], a[108:111], v[172:175], v[76:79]
s_waitcnt lgkmcnt(6)
buffer_load_dwordx4 %[vW], %[srdW], s75 offen offset:0 lds
v_mfma_f32_16x16x32_bf16 v[80:83], a[112:115], v[160:163], v[80:83]
ds_read_b64_tr_b16 a[104:105], v233 offset:0
ds_read_b64_tr_b16 a[106:107], v233 offset:512
v_mfma_f32_16x16x32_bf16 v[80:83], a[116:119], v[168:171], v[80:83]
v_mfma_f32_16x16x32_bf16 v[84:87], a[112:115], v[164:167], v[84:87]
s_add_u32 m0, s54, 896
ds_read_b64_tr_b16 a[108:109], v233 offset:12288
ds_read_b64_tr_b16 a[110:111], v233 offset:12800
v_mfma_f32_16x16x32_bf16 v[84:87], a[116:119], v[172:175], v[84:87]
)"
R"(buffer_load_dwordx4 %[vW], %[srdW], s75 offen offset:128 lds
v_mfma_f32_16x16x32_bf16 v[88:91], a[120:123], v[160:163], v[88:91]
ds_read_b64_tr_b16 a[112:113], v234 offset:0
ds_read_b64_tr_b16 a[114:115], v234 offset:512
v_mfma_f32_16x16x32_bf16 v[88:91], a[124:127], v[168:171], v[88:91]
v_mfma_f32_16x16x32_bf16 v[92:95], a[120:123], v[164:167], v[92:95]
s_add_u32 m0, s54, 1792
ds_read_b64_tr_b16 a[116:117], v234 offset:12288
ds_read_b64_tr_b16 a[118:119], v234 offset:12800
v_mfma_f32_16x16x32_bf16 v[92:95], a[124:127], v[172:175], v[92:95]
s_waitcnt lgkmcnt(4)
buffer_load_dwordx4 %[vW], %[srdW], s75 offen offset:256 lds
s_cbranch_vccnz RARE6_%=
BACK6_%=:
v_mfma_f32_16x16x32_bf16 a[160:163], a[96:99], a[144:147], a[160:163]
ds_read_b64_tr_b16 a[120:121], v235 offset:0
ds_read_b64_tr_b16 a[122:123], v235 offset:512
v_mfma_f32_16x16x32_bf16 a[160:163], a[100:103], a[152:155], a[160:163]
s_add_u32 s75, s66, 5898624
v_exp_f32 v128, v128
v_mfma_f32_16x16x32_bf16 a[164:167], a[96:99], a[148:151], a[164:167]
s_add_u32 m0, s54, 12288
ds_read_b64_tr_b16 a[124:125], v235 offset:12288
ds_read_b64_tr_b16 a[126:127], v235 offset:12800
)"
R"(v_mfma_f32_16x16x32_bf16 a[164:167], a[100:103], a[156:159], a[164:167]
buffer_load_dwordx4 %[vW], %[srdW], s75 offen offset:0 lds
v_mfma_f32_16x16x32_bf16 a[168:171], a[104:107], a[144:147], a[168:171]
ds_read_b64_tr_b16 a[96:97], v232 offset:1024
ds_read_b64_tr_b16 a[98:99], v232 offset:1536
v_mfma_f32_16x16x32_bf16 a[168:171], a[108:111], a[152:155], a[168:171]
v_exp_f32 v129, v129
v_mfma_f32_16x16x32_bf16 a[172:175], a[104:107], a[148:151], a[172:175]
s_add_u32 m0, s54, 13184
ds_read_b64_tr_b16 a[100:101], v232 offset:13312
ds_read_b64_tr_b16 a[102:103], v232 offset:13824
v_mfma_f32_16x16x32_bf16 a[172:175], a[108:111], a[156:159], a[172:175]
s_waitcnt lgkmcnt(4)
buffer_load_dwordx4 %[vW], %[srdW], s75 offen offset:128 lds
v_mfma_f32_16x16x32_bf16 a[176:179], a[112:115], a[144:147], a[176:179]
ds_read_b64_tr_b16 a[104:105], v233 offset:1024
ds_read_b64_tr_b16 a[106:107], v233 offset:1536
v_mfma_f32_16x16x32_bf16 a[176:179], a[116:119], a[152:155], a[176:179]
v_exp_f32 v130, v130
v_mfma_f32_16x16x32_bf16 a[180:183], a[112:115], a[148:151], a[180:183]
s_add_u32 m0, s54, 14080
ds_read_b64_tr_b16 a[108:109], v233 offset:13312
ds_read_b64_tr_b16 a[110:111], v233 offset:13824
v_mfma_f32_16x16x32_bf16 a[180:183], a[116:119], a[156:159], a[180:183]
)"
R"(buffer_load_dwordx4 %[vW], %[srdW], s75 offen offset:256 lds
v_mfma_f32_16x16x32_bf16 a[184:187], a[120:123], a[144:147], a[184:187]
ds_read_b64_tr_b16 a[112:113], v234 offset:1024
ds_read_b64_tr_b16 a[114:115], v234 offset:1536
v_mfma_f32_16x16x32_bf16 a[184:187], a[124:127], a[152:155], a[184:187]
v_exp_f32 v131, v131
v_mfma_f32_16x16x32_bf16 a[188:191], a[120:123], a[148:151], a[188:191]
s_add_u32 m0, s73, -1024
ds_read_b64_tr_b16 a[116:117], v234 offset:13312
ds_read_b64_tr_b16 a[118:119], v234 offset:13824
v_mfma_f32_16x16x32_bf16 a[188:191], a[124:127], a[156:159], a[188:191]
s_waitcnt lgkmcnt(4)
buffer_load_dwordx4 %[vX0], %[srdX], s69 offen offset:1024 lds
v_mfma_f32_16x16x32_bf16 a[192:195], a[96:99], a[144:147], a[192:195]
ds_read_b64_tr_b16 a[120:121], v235 offset:1024
ds_read_b64_tr_b16 a[122:123], v235 offset:1536
v_mfma_f32_16x16x32_bf16 a[192:195], a[100:103], a[152:155], a[192:195]
v_exp_f32 v136, v136
v_mfma_f32_16x16x32_bf16 a[196:199], a[96:99], a[148:151], a[196:199]
s_add_u32 m0, s73, 0
ds_read_b64_tr_b16 a[124:125], v235 offset:13312
ds_read_b64_tr_b16 a[126:127], v235 offset:13824
v_mfma_f32_16x16x32_bf16 a[196:199], a[100:103], a[156:159], a[196:199]
buffer_load_dwordx4 %[vX1], %[srdX], s70 offen offset:1024 lds
)"
R"(v_mfma_f32_16x16x32_bf16 a[200:203], a[104:107], a[144:147], a[200:203]
ds_read_b64_tr_b16 a[96:97], v232 offset:2048
ds_read_b64_tr_b16 a[98:99], v232 offset:2560
v_mfma_f32_16x16x32_bf16 a[200:203], a[108:111], a[152:155], a[200:203]
v_exp_f32 v137, v137
v_mfma_f32_16x16x32_bf16 a[204:207], a[104:107], a[148:151], a[204:207]
s_add_u32 m0, s73, 1024
ds_read_b64_tr_b16 a[100:101], v232 offset:14336
ds_read_b64_tr_b16 a[102:103], v232 offset:14848
v_mfma_f32_16x16x32_bf16 a[204:207], a[108:111], a[156:159], a[204:207]
s_waitcnt lgkmcnt(4)
buffer_load_dwordx4 %[vX0], %[srdX], s71 offen offset:1024 lds
v_mfma_f32_16x16x32_bf16 a[208:211], a[112:115], a[144:147], a[208:211]
ds_read_b64_tr_b16 a[104:105], v233 offset:2048
ds_read_b64_tr_b16 a[106:107], v233 offset:2560
v_mfma_f32_16x16x32_bf16 a[208:211], a[116:119], a[152:155], a[208:211]
v_exp_f32 v138, v138
v_mfma_f32_16x16x32_bf16 a[212:215], a[112:115], a[148:151], a[212:215]
s_add_u32 m0, s73, 2048
ds_read_b64_tr_b16 a[108:109], v233 offset:14336
ds_read_b64_tr_b16 a[110:111], v233 offset:14848
v_mfma_f32_16x16x32_bf16 a[212:215], a[116:119], a[156:159], a[212:215]
buffer_load_dwordx4 %[vX1], %[srdX], s72 offen offset:1024 lds
v_mfma_f32_16x16x32_bf16 a[216:219], a[120:123], a[144:147], a[216:219]
)"
R"(ds_read_b64_tr_b16 a[112:113], v234 offset:2048
ds_read_b64_tr_b16 a[114:115], v234 offset:2560
v_mfma_f32_16x16x32_bf16 a[216:219], a[124:127], a[152:155], a[216:219]
v_exp_f32 v139, v139
v_cvt_pk_bf16_f32 v160, v128, v129
v_mfma_f32_16x16x32_bf16 a[220:223], a[120:123], a[148:151], a[220:223]
s_add_u32 s75, s67, 172032
ds_read_b64_tr_b16 a[116:117], v234 offset:14336
ds_read_b64_tr_b16 a[118:119], v234 offset:14848
v_cvt_pk_bf16_f32 v161, v130, v131
v_mfma_f32_16x16x32_bf16 a[220:223], a[124:127], a[156:159], a[220:223]
s_waitcnt lgkmcnt(4)
s_waitcnt vmcnt(20)
s_barrier
v_mfma_f32_16x16x32_bf16 a[224:227], a[96:99], a[144:147], a[224:227]
s_add_u32 m0, s55, 0
ds_read_b64_tr_b16 a[120:121], v235 offset:2048
ds_read_b64_tr_b16 a[122:123], v235 offset:2560
v_mfma_f32_16x16x32_bf16 a[224:227], a[100:103], a[152:155], a[224:227]
buffer_load_dwordx4 %[vK], %[srdK], s75 offen offset:0 nt lds
v_mfma_f32_16x16x32_bf16 a[228:231], a[96:99], a[148:151], a[228:231]
ds_read_b64_tr_b16 a[124:125], v235 offset:14336
ds_read_b64_tr_b16 a[126:127], v235 offset:14848
v_mfma_f32_16x16x32_bf16 a[228:231], a[100:103], a[156:159], a[228:231]
)"
R"(v_add_u32 v232, s51, %[pK]
v_xor_b32 v233, 64, v232
v_mfma_f32_16x16x32_bf16 a[232:235], a[104:107], a[144:147], a[232:235]
s_add_u32 m0, s55, 896
ds_read_b128 a[96:99], v232 offset:0
ds_read_b128 a[100:103], v233 offset:0
v_mfma_f32_16x16x32_bf16 a[232:235], a[108:111], a[152:155], a[232:235]
buffer_load_dwordx4 %[vK], %[srdK], s75 offen offset:128 nt lds
v_mfma_f32_16x16x32_bf16 a[236:239], a[104:107], a[148:151], a[236:239]
v_exp_f32 v132, v132
v_mfma_f32_16x16x32_bf16 a[236:239], a[108:111], a[156:159], a[236:239]
s_waitcnt lgkmcnt(2)
v_exp_f32 v133, v133
v_mfma_f32_16x16x32_bf16 a[240:243], a[112:115], a[144:147], a[240:243]
s_add_u32 m0, s55, 1792
ds_read_b128 a[104:107], v232 offset:1024
ds_read_b128 a[108:111], v233 offset:1024
v_mfma_f32_16x16x32_bf16 a[240:243], a[116:119], a[152:155], a[240:243]
buffer_load_dwordx4 %[vK], %[srdK], s75 offen offset:256 nt lds
v_mfma_f32_16x16x32_bf16 a[244:247], a[112:115], a[148:151], a[244:247]
v_exp_f32 v134, v134
v_mfma_f32_16x16x32_bf16 a[244:247], a[116:119], a[156:159], a[244:247]
s_add_u32 s75, s67, 184320
v_exp_f32 v135, v135
)"
R"(v_cvt_pk_bf16_f32 v162, v136, v137
v_mfma_f32_16x16x32_bf16 a[248:251], a[120:123], a[144:147], a[248:251]
s_add_u32 m0, s55, 12288
ds_read_b128 a[112:115], v232 offset:2048
ds_read_b128 a[116:119], v233 offset:2048
v_cvt_pk_bf16_f32 v163, v138, v139
v_mfma_f32_16x16x32_bf16 a[248:251], a[124:127], a[152:155], a[248:251]
buffer_load_dwordx4 %[vK], %[srdK], s75 offen offset:0 nt lds
v_mfma_f32_16x16x32_bf16 a[252:255], a[120:123], a[148:151], a[252:255]
v_exp_f32 v140, v140
v_mfma_f32_16x16x32_bf16 a[252:255], a[124:127], a[156:159], a[252:255]
s_waitcnt lgkmcnt(3)
v_exp_f32 v141, v141
v_mfma_f32_16x16x32_bf16 v[96:99], a[96:99], v[184:187], v[176:179]
s_add_u32 m0, s55, 13184
ds_read_b128 a[120:123], v232 offset:6144
ds_read_b128 a[124:127], v233 offset:6144
v_mfma_f32_16x16x32_bf16 v[96:99], a[100:103], v[192:195], v[96:99]
buffer_load_dwordx4 %[vK], %[srdK], s75 offen offset:128 nt lds
v_mfma_f32_16x16x32_bf16 v[96:99], a[104:107], v[200:203], v[96:99]
v_exp_f32 v142, v142
v_mfma_f32_16x16x32_bf16 v[100:103], a[96:99], v[188:191], v[180:183]
v_exp_f32 v143, v143
v_mfma_f32_16x16x32_bf16 v[100:103], a[100:103], v[196:199], v[100:103]
)"
R"(s_add_u32 m0, s55, 14080
ds_read_b128 a[96:99], v232 offset:7168
ds_read_b128 a[100:103], v233 offset:7168
v_mfma_f32_16x16x32_bf16 v[100:103], a[104:107], v[204:207], v[100:103]
s_waitcnt lgkmcnt(4)
buffer_load_dwordx4 %[vK], %[srdK], s75 offen offset:256 nt lds
v_mfma_f32_16x16x32_bf16 v[96:99], a[108:111], v[208:211], v[96:99]
v_cvt_pk_bf16_f32 v164, v132, v133
v_cvt_pk_bf16_f32 v165, v134, v135
v_mfma_f32_16x16x32_bf16 v[96:99], a[112:115], v[216:219], v[96:99]
v_exp_f32 v144, v144
v_mfma_f32_16x16x32_bf16 v[96:99], a[116:119], v[224:227], v[96:99]
v_exp_f32 v145, v145
v_mfma_f32_16x16x32_bf16 v[100:103], a[108:111], v[212:215], v[100:103]
ds_read_b128 a[104:107], v232 offset:8192
ds_read_b128 a[108:111], v233 offset:8192
v_mfma_f32_16x16x32_bf16 v[100:103], a[112:115], v[220:223], v[100:103]
v_exp_f32 v146, v146
v_mfma_f32_16x16x32_bf16 v[100:103], a[116:119], v[228:231], v[100:103]
s_waitcnt lgkmcnt(3)
v_exp_f32 v147, v147
v_mfma_f32_16x16x32_bf16 v[104:107], a[120:123], v[184:187], v[176:179]
ds_read_b128 a[112:115], v232 offset:12288
ds_read_b128 a[116:119], v233 offset:12288
)"
R"(v_mfma_f32_16x16x32_bf16 v[104:107], a[124:127], v[192:195], v[104:107]
v_cvt_pk_bf16_f32 v166, v140, v141
v_cvt_pk_bf16_f32 v167, v142, v143
v_mfma_f32_16x16x32_bf16 v[104:107], a[96:99], v[200:203], v[104:107]
v_exp_f32 v152, v152
v_mfma_f32_16x16x32_bf16 v[108:111], a[120:123], v[188:191], v[180:183]
v_exp_f32 v153, v153
v_mfma_f32_16x16x32_bf16 v[108:111], a[124:127], v[196:199], v[108:111]
ds_read_b128 a[120:123], v232 offset:13312
ds_read_b128 a[124:127], v233 offset:13312
v_mfma_f32_16x16x32_bf16 v[108:111], a[96:99], v[204:207], v[108:111]
s_waitcnt lgkmcnt(4)
v_exp_f32 v154, v154
v_mfma_f32_16x16x32_bf16 v[104:107], a[100:103], v[208:211], v[104:107]
v_exp_f32 v155, v155
v_mfma_f32_16x16x32_bf16 v[104:107], a[104:107], v[216:219], v[104:107]
v_cvt_pk_bf16_f32 v168, v144, v145
v_cvt_pk_bf16_f32 v169, v146, v147
v_mfma_f32_16x16x32_bf16 v[104:107], a[108:111], v[224:227], v[104:107]
v_exp_f32 v148, v148
v_mfma_f32_16x16x32_bf16 v[108:111], a[100:103], v[212:215], v[108:111]
ds_read_b128 a[96:99], v232 offset:14336
ds_read_b128 a[100:103], v233 offset:14336
v_mfma_f32_16x16x32_bf16 v[108:111], a[104:107], v[220:223], v[108:111]
)"
R"(v_exp_f32 v149, v149
v_mfma_f32_16x16x32_bf16 v[108:111], a[108:111], v[228:231], v[108:111]
s_waitcnt lgkmcnt(3)
v_exp_f32 v150, v150
v_mfma_f32_16x16x32_bf16 v[112:115], a[112:115], v[184:187], v[176:179]
ds_read_b128 a[104:107], v232 offset:18432
ds_read_b128 a[108:111], v233 offset:18432
v_mfma_f32_16x16x32_bf16 v[112:115], a[116:119], v[192:195], v[112:115]
v_exp_f32 v151, v151
v_mfma_f32_16x16x32_bf16 v[112:115], a[120:123], v[200:203], v[112:115]
v_cvt_pk_bf16_f32 v170, v152, v153
v_cvt_pk_bf16_f32 v171, v154, v155
v_mfma_f32_16x16x32_bf16 v[116:119], a[112:115], v[188:191], v[180:183]
v_exp_f32 v156, v156
v_mfma_f32_16x16x32_bf16 v[116:119], a[116:119], v[196:199], v[116:119]
ds_read_b128 a[112:115], v232 offset:19456
ds_read_b128 a[116:119], v233 offset:19456
v_mfma_f32_16x16x32_bf16 v[116:119], a[120:123], v[204:207], v[116:119]
s_waitcnt lgkmcnt(4)
v_exp_f32 v157, v157
v_mfma_f32_16x16x32_bf16 v[112:115], a[124:127], v[208:211], v[112:115]
v_exp_f32 v158, v158
v_mfma_f32_16x16x32_bf16 v[112:115], a[96:99], v[216:219], v[112:115]
s_waitcnt vmcnt(16)
)"
R"(s_barrier
v_mfma_f32_16x16x32_bf16 v[112:115], a[100:103], v[224:227], v[112:115]
s_add_u32 s76, s68, 147456
v_exp_f32 v159, v159
v_mfma_f32_16x16x32_bf16 v[116:119], a[124:127], v[212:215], v[116:119]
s_add_u32 m0, s62, 0
ds_read_b128 a[120:123], v232 offset:20480
ds_read_b128 a[124:127], v233 offset:20480
v_mfma_f32_16x16x32_bf16 v[116:119], a[96:99], v[220:223], v[116:119]
buffer_load_dwordx4 %[vV], %[srdV], s76 offen offset:0 nt lds
v_mfma_f32_16x16x32_bf16 v[116:119], a[100:103], v[228:231], v[116:119]
s_waitcnt lgkmcnt(3)
v_add_u32 v232, s52, %[pV]
v_xor_b32 v233, 32, v232
v_mfma_f32_16x16x32_bf16 v[120:123], a[104:107], v[184:187], v[176:179]
ds_read_b64_tr_b16 a[96:97], v232 offset:0
ds_read_b64_tr_b16 a[98:99], v232 offset:6144
v_mfma_f32_16x16x32_bf16 v[120:123], a[108:111], v[192:195], v[120:123]
v_xor_b32 v234, 64, v232
v_xor_b32 v235, 96, v232
v_mfma_f32_16x16x32_bf16 v[120:123], a[112:115], v[200:203], v[120:123]
s_add_u32 m0, s62, 896
ds_read_b64_tr_b16 a[100:101], v232 offset:12288
ds_read_b64_tr_b16 a[102:103], v232 offset:18432
)"
R"(v_mfma_f32_16x16x32_bf16 v[124:127], a[104:107], v[188:191], v[180:183]
buffer_load_dwordx4 %[vV], %[srdV], s76 offen offset:128 nt lds
v_mfma_f32_16x16x32_bf16 v[124:127], a[108:111], v[196:199], v[124:127]
ds_read_b64_tr_b16 a[104:105], v233 offset:0
ds_read_b64_tr_b16 a[106:107], v233 offset:6144
v_mfma_f32_16x16x32_bf16 v[124:127], a[112:115], v[204:207], v[124:127]
s_waitcnt lgkmcnt(6)
v_cvt_pk_bf16_f32 v172, v148, v149
v_cvt_pk_bf16_f32 v173, v150, v151
v_mfma_f32_16x16x32_bf16 v[120:123], a[116:119], v[208:211], v[120:123]
s_add_u32 m0, s62, 1792
ds_read_b64_tr_b16 a[108:109], v233 offset:12288
ds_read_b64_tr_b16 a[110:111], v233 offset:18432
v_mfma_f32_16x16x32_bf16 v[120:123], a[120:123], v[216:219], v[120:123]
buffer_load_dwordx4 %[vV], %[srdV], s76 offen offset:256 nt lds
v_mfma_f32_16x16x32_bf16 v[120:123], a[124:127], v[224:227], v[120:123]
ds_read_b64_tr_b16 a[112:113], v234 offset:0
ds_read_b64_tr_b16 a[114:115], v234 offset:6144
v_mfma_f32_16x16x32_bf16 v[124:127], a[116:119], v[212:215], v[124:127]
v_cvt_pk_bf16_f32 v174, v156, v157
v_cvt_pk_bf16_f32 v175, v158, v159
v_mfma_f32_16x16x32_bf16 v[124:127], a[120:123], v[220:223], v[124:127]
s_add_u32 m0, s62, 0
ds_read_b64_tr_b16 a[116:117], v234 offset:12288
)"
R"(ds_read_b64_tr_b16 a[118:119], v234 offset:18432
v_mfma_f32_16x16x32_bf16 v[124:127], a[124:127], v[228:231], v[124:127]
buffer_load_dwordx4 %[vV], %[srdV], s76 offen offset:3072 nt lds
v_mfma_f32_16x16x32_bf16 a[132:135], a[128:131], v[160:163], a[132:135]
v_mfma_f32_16x16x32_bf16 a[132:135], a[128:131], v[168:171], a[132:135]
v_mfma_f32_16x16x32_bf16 a[136:139], a[128:131], v[164:167], a[136:139]
s_add_u32 m0, s62, 896
v_mfma_f32_16x16x32_bf16 a[136:139], a[128:131], v[172:175], a[136:139]
s_waitcnt lgkmcnt(4)
buffer_load_dwordx4 %[vV], %[srdV], s76 offen offset:3200 nt lds
v_mfma_f32_16x16x32_bf16 v[0:3], a[96:99], v[160:163], v[0:3]
ds_read_b64_tr_b16 a[120:121], v235 offset:0
ds_read_b64_tr_b16 a[122:123], v235 offset:6144
v_mfma_f32_16x16x32_bf16 v[0:3], a[100:103], v[168:171], v[0:3]
v_max3_f32 v236, v96, v97, v98
v_max3_f32 v237, v100, v101, v102
v_mfma_f32_16x16x32_bf16 v[4:7], a[96:99], v[164:167], v[4:7]
s_add_u32 m0, s62, 1792
ds_read_b64_tr_b16 a[124:125], v235 offset:12288
ds_read_b64_tr_b16 a[126:127], v235 offset:18432
v_mfma_f32_16x16x32_bf16 v[4:7], a[100:103], v[172:175], v[4:7]
buffer_load_dwordx4 %[vV], %[srdV], s76 offen offset:3328 nt lds
v_mfma_f32_16x16x32_bf16 v[8:11], a[104:107], v[160:163], v[8:11]
ds_read_b64_tr_b16 a[96:97], v232 offset:1024
)"
R"(ds_read_b64_tr_b16 a[98:99], v232 offset:7168
v_mfma_f32_16x16x32_bf16 v[8:11], a[108:111], v[168:171], v[8:11]
v_max_f32 v236, v236, v99
v_max_f32 v237, v237, v103
v_mfma_f32_16x16x32_bf16 v[12:15], a[104:107], v[164:167], v[12:15]
ds_read_b64_tr_b16 a[100:101], v232 offset:13312
ds_read_b64_tr_b16 a[102:103], v232 offset:19456
v_mfma_f32_16x16x32_bf16 v[12:15], a[108:111], v[172:175], v[12:15]
s_waitcnt lgkmcnt(4)
v_max3_f32 v236, v236, v104, v105
v_max3_f32 v237, v237, v108, v109
v_mfma_f32_16x16x32_bf16 v[16:19], a[112:115], v[160:163], v[16:19]
ds_read_b64_tr_b16 a[104:105], v233 offset:1024
ds_read_b64_tr_b16 a[106:107], v233 offset:7168
v_mfma_f32_16x16x32_bf16 v[16:19], a[116:119], v[168:171], v[16:19]
v_max3_f32 v236, v236, v106, v107
v_max3_f32 v237, v237, v110, v111
v_mfma_f32_16x16x32_bf16 v[20:23], a[112:115], v[164:167], v[20:23]
ds_read_b64_tr_b16 a[108:109], v233 offset:13312
ds_read_b64_tr_b16 a[110:111], v233 offset:19456
v_mfma_f32_16x16x32_bf16 v[20:23], a[116:119], v[172:175], v[20:23]
v_max3_f32 v236, v236, v112, v113
v_max3_f32 v237, v237, v116, v117
v_mfma_f32_16x16x32_bf16 v[24:27], a[120:123], v[160:163], v[24:27]
)"
R"(ds_read_b64_tr_b16 a[112:113], v234 offset:1024
ds_read_b64_tr_b16 a[114:115], v234 offset:7168
v_mfma_f32_16x16x32_bf16 v[24:27], a[124:127], v[168:171], v[24:27]
v_max3_f32 v236, v236, v114, v115
v_max3_f32 v237, v237, v118, v119
v_mfma_f32_16x16x32_bf16 v[28:31], a[120:123], v[164:167], v[28:31]
ds_read_b64_tr_b16 a[116:117], v234 offset:13312
ds_read_b64_tr_b16 a[118:119], v234 offset:19456
v_mfma_f32_16x16x32_bf16 v[28:31], a[124:127], v[172:175], v[28:31]
s_waitcnt lgkmcnt(4)
v_max3_f32 v236, v236, v120, v121
v_max3_f32 v237, v237, v124, v125
v_mfma_f32_16x16x32_bf16 v[32:35], a[96:99], v[160:163], v[32:35]
ds_read_b64_tr_b16 a[120:121], v235 offset:1024
ds_read_b64_tr_b16 a[122:123], v235 offset:7168
v_mfma_f32_16x16x32_bf16 v[32:35], a[100:103], v[168:171], v[32:35]
v_max3_f32 v236, v236, v122, v123
v_max3_f32 v237, v237, v126, v127
v_mfma_f32_16x16x32_bf16 v[36:39], a[96:99], v[164:167], v[36:39]
ds_read_b64_tr_b16 a[124:125], v235 offset:13312
ds_read_b64_tr_b16 a[126:127], v235 offset:19456
v_mfma_f32_16x16x32_bf16 v[36:39], a[100:103], v[172:175], v[36:39]
v_max_f32 v236, v236, v237
v_cmp_lt_f32 vcc, s77, v236
)"
R"(v_mfma_f32_16x16x32_bf16 v[40:43], a[104:107], v[160:163], v[40:43]
ds_read_b64_tr_b16 a[96:97], v232 offset:2048
ds_read_b64_tr_b16 a[98:99], v232 offset:8192
v_mfma_f32_16x16x32_bf16 v[40:43], a[108:111], v[168:171], v[40:43]
v_mfma_f32_16x16x32_bf16 v[44:47], a[104:107], v[164:167], v[44:47]
ds_read_b64_tr_b16 a[100:101], v232 offset:14336
ds_read_b64_tr_b16 a[102:103], v232 offset:20480
v_mfma_f32_16x16x32_bf16 v[44:47], a[108:111], v[172:175], v[44:47]
s_waitcnt lgkmcnt(4)
v_mfma_f32_16x16x32_bf16 v[48:51], a[112:115], v[160:163], v[48:51]
ds_read_b64_tr_b16 a[104:105], v233 offset:2048
ds_read_b64_tr_b16 a[106:107], v233 offset:8192
v_mfma_f32_16x16x32_bf16 v[48:51], a[116:119], v[168:171], v[48:51]
v_mfma_f32_16x16x32_bf16 v[52:55], a[112:115], v[164:167], v[52:55]
ds_read_b64_tr_b16 a[108:109], v233 offset:14336
ds_read_b64_tr_b16 a[110:111], v233 offset:20480
v_mfma_f32_16x16x32_bf16 v[52:55], a[116:119], v[172:175], v[52:55]
v_mfma_f32_16x16x32_bf16 v[56:59], a[120:123], v[160:163], v[56:59]
ds_read_b64_tr_b16 a[112:113], v234 offset:2048
ds_read_b64_tr_b16 a[114:115], v234 offset:8192
v_mfma_f32_16x16x32_bf16 v[56:59], a[124:127], v[168:171], v[56:59]
v_mfma_f32_16x16x32_bf16 v[60:63], a[120:123], v[164:167], v[60:63]
ds_read_b64_tr_b16 a[116:117], v234 offset:14336
ds_read_b64_tr_b16 a[118:119], v234 offset:20480
)"
R"(v_mfma_f32_16x16x32_bf16 v[60:63], a[124:127], v[172:175], v[60:63]
s_waitcnt lgkmcnt(4)
s_waitcnt vmcnt(16)
s_barrier
v_mfma_f32_16x16x32_bf16 v[64:67], a[96:99], v[160:163], v[64:67]
ds_read_b64_tr_b16 a[120:121], v235 offset:2048
ds_read_b64_tr_b16 a[122:123], v235 offset:8192
v_mfma_f32_16x16x32_bf16 v[64:67], a[100:103], v[168:171], v[64:67]
ds_read_b128 a[144:147], %[pX0] offset:16384
ds_read_b128 a[148:151], %[pX0] offset:18432
v_mfma_f32_16x16x32_bf16 v[68:71], a[96:99], v[164:167], v[68:71]
ds_read_b64_tr_b16 a[124:125], v235 offset:14336
ds_read_b64_tr_b16 a[126:127], v235 offset:20480
v_mfma_f32_16x16x32_bf16 v[68:71], a[100:103], v[172:175], v[68:71]
ds_read_b128 a[152:155], %[pX1] offset:16384
ds_read_b128 a[156:159], %[pX1] offset:18432
v_add_u32 v232, s48, %[pW]
v_xor_b32 v233, 32, v232
v_mfma_f32_16x16x32_bf16 v[72:75], a[104:107], v[160:163], v[72:75]
ds_read_b64_tr_b16 a[96:97], v232 offset:0
ds_read_b64_tr_b16 a[98:99], v232 offset:512
v_mfma_f32_16x16x32_bf16 v[72:75], a[108:111], v[168:171], v[72:75]
s_add_u32 s75, s66, 6291840
v_xor_b32 v234, 64, v232
)"
R"(v_xor_b32 v235, 96, v232
v_mfma_f32_16x16x32_bf16 v[76:79], a[104:107], v[164:167], v[76:79]
s_add_u32 m0, s57, 0
ds_read_b64_tr_b16 a[100:101], v232 offset:12288
ds_read_b64_tr_b16 a[102:103], v232 offset:12800
v_mfma_f32_16x16x32_bf16 v[76:79], a[108:111], v[172:175], v[76:79]
s_waitcnt lgkmcnt(6)
buffer_load_dwordx4 %[vW], %[srdW], s75 offen offset:0 lds
v_mfma_f32_16x16x32_bf16 v[80:83], a[112:115], v[160:163], v[80:83]
ds_read_b64_tr_b16 a[104:105], v233 offset:0
ds_read_b64_tr_b16 a[106:107], v233 offset:512
v_mfma_f32_16x16x32_bf16 v[80:83], a[116:119], v[168:171], v[80:83]
v_mfma_f32_16x16x32_bf16 v[84:87], a[112:115], v[164:167], v[84:87]
s_add_u32 m0, s57, 896
ds_read_b64_tr_b16 a[108:109], v233 offset:12288
ds_read_b64_tr_b16 a[110:111], v233 offset:12800
v_mfma_f32_16x16x32_bf16 v[84:87], a[116:119], v[172:175], v[84:87]
buffer_load_dwordx4 %[vW], %[srdW], s75 offen offset:128 lds
v_mfma_f32_16x16x32_bf16 v[88:91], a[120:123], v[160:163], v[88:91]
ds_read_b64_tr_b16 a[112:113], v234 offset:0
ds_read_b64_tr_b16 a[114:115], v234 offset:512
v_mfma_f32_16x16x32_bf16 v[88:91], a[124:127], v[168:171], v[88:91]
v_mfma_f32_16x16x32_bf16 v[92:95], a[120:123], v[164:167], v[92:95]
s_add_u32 m0, s57, 1792
)"
R"(ds_read_b64_tr_b16 a[116:117], v234 offset:12288
ds_read_b64_tr_b16 a[118:119], v234 offset:12800
v_mfma_f32_16x16x32_bf16 v[92:95], a[124:127], v[172:175], v[92:95]
s_waitcnt lgkmcnt(4)
buffer_load_dwordx4 %[vW], %[srdW], s75 offen offset:256 lds
s_cbranch_vccnz RARE7_%=
BACK7_%=:
v_mfma_f32_16x16x32_bf16 a[160:163], a[96:99], a[144:147], a[160:163]
ds_read_b64_tr_b16 a[120:121], v235 offset:0
ds_read_b64_tr_b16 a[122:123], v235 offset:512
v_mfma_f32_16x16x32_bf16 a[160:163], a[100:103], a[152:155], a[160:163]
s_add_u32 s75, s66, 6685056
v_exp_f32 v96, v96
v_mfma_f32_16x16x32_bf16 a[164:167], a[96:99], a[148:151], a[164:167]
s_add_u32 m0, s57, 12288
ds_read_b64_tr_b16 a[124:125], v235 offset:12288
ds_read_b64_tr_b16 a[126:127], v235 offset:12800
v_mfma_f32_16x16x32_bf16 a[164:167], a[100:103], a[156:159], a[164:167]
buffer_load_dwordx4 %[vW], %[srdW], s75 offen offset:0 lds
v_mfma_f32_16x16x32_bf16 a[168:171], a[104:107], a[144:147], a[168:171]
ds_read_b64_tr_b16 a[96:97], v232 offset:1024
ds_read_b64_tr_b16 a[98:99], v232 offset:1536
v_mfma_f32_16x16x32_bf16 a[168:171], a[108:111], a[152:155], a[168:171]
v_exp_f32 v97, v97
)"
R"(v_mfma_f32_16x16x32_bf16 a[172:175], a[104:107], a[148:151], a[172:175]
s_add_u32 m0, s57, 13184
ds_read_b64_tr_b16 a[100:101], v232 offset:13312
ds_read_b64_tr_b16 a[102:103], v232 offset:13824
v_mfma_f32_16x16x32_bf16 a[172:175], a[108:111], a[156:159], a[172:175]
s_waitcnt lgkmcnt(4)
buffer_load_dwordx4 %[vW], %[srdW], s75 offen offset:128 lds
v_mfma_f32_16x16x32_bf16 a[176:179], a[112:115], a[144:147], a[176:179]
ds_read_b64_tr_b16 a[104:105], v233 offset:1024
ds_read_b64_tr_b16 a[106:107], v233 offset:1536
v_mfma_f32_16x16x32_bf16 a[176:179], a[116:119], a[152:155], a[176:179]
v_exp_f32 v98, v98
v_mfma_f32_16x16x32_bf16 a[180:183], a[112:115], a[148:151], a[180:183]
s_add_u32 m0, s57, 14080
ds_read_b64_tr_b16 a[108:109], v233 offset:13312
ds_read_b64_tr_b16 a[110:111], v233 offset:13824
v_mfma_f32_16x16x32_bf16 a[180:183], a[116:119], a[156:159], a[180:183]
buffer_load_dwordx4 %[vW], %[srdW], s75 offen offset:256 lds
v_mfma_f32_16x16x32_bf16 a[184:187], a[120:123], a[144:147], a[184:187]
ds_read_b64_tr_b16 a[112:113], v234 offset:1024
ds_read_b64_tr_b16 a[114:115], v234 offset:1536
v_mfma_f32_16x16x32_bf16 a[184:187], a[124:127], a[152:155], a[184:187]
v_exp_f32 v99, v99
v_mfma_f32_16x16x32_bf16 a[188:191], a[120:123], a[148:151], a[188:191]
)"
R"(s_add_u32 m0, s73, 15232
ds_read_b64_tr_b16 a[116:117], v234 offset:13312
ds_read_b64_tr_b16 a[118:119], v234 offset:13824
v_mfma_f32_16x16x32_bf16 a[188:191], a[124:127], a[156:159], a[188:191]
s_waitcnt lgkmcnt(4)
buffer_load_dwordx4 %[vX0], %[srdX], s69 offen offset:1152 lds
v_mfma_f32_16x16x32_bf16 a[192:195], a[96:99], a[144:147], a[192:195]
ds_read_b64_tr_b16 a[120:121], v235 offset:1024
ds_read_b64_tr_b16 a[122:123], v235 offset:1536
v_mfma_f32_16x16x32_bf16 a[192:195], a[100:103], a[152:155], a[192:195]
v_exp_f32 v104, v104
v_mfma_f32_16x16x32_bf16 a[196:199], a[96:99], a[148:151], a[196:199]
s_add_u32 m0, s73, 16256
ds_read_b64_tr_b16 a[124:125], v235 offset:13312
ds_read_b64_tr_b16 a[126:127], v235 offset:13824
v_mfma_f32_16x16x32_bf16 a[196:199], a[100:103], a[156:159], a[196:199]
buffer_load_dwordx4 %[vX1], %[srdX], s70 offen offset:1152 lds
v_mfma_f32_16x16x32_bf16 a[200:203], a[104:107], a[144:147], a[200:203]
ds_read_b64_tr_b16 a[96:97], v232 offset:2048
ds_read_b64_tr_b16 a[98:99], v232 offset:2560
v_mfma_f32_16x16x32_bf16 a[200:203], a[108:111], a[152:155], a[200:203]
v_exp_f32 v105, v105
v_mfma_f32_16x16x32_bf16 a[204:207], a[104:107], a[148:151], a[204:207]
s_add_u32 m0, s73, 17280
)"
R"(ds_read_b64_tr_b16 a[100:101], v232 offset:14336
ds_read_b64_tr_b16 a[102:103], v232 offset:14848
v_mfma_f32_16x16x32_bf16 a[204:207], a[108:111], a[156:159], a[204:207]
s_waitcnt lgkmcnt(4)
buffer_load_dwordx4 %[vX0], %[srdX], s71 offen offset:1152 lds
v_mfma_f32_16x16x32_bf16 a[208:211], a[112:115], a[144:147], a[208:211]
ds_read_b64_tr_b16 a[104:105], v233 offset:2048
ds_read_b64_tr_b16 a[106:107], v233 offset:2560
v_mfma_f32_16x16x32_bf16 a[208:211], a[116:119], a[152:155], a[208:211]
v_exp_f32 v106, v106
v_mfma_f32_16x16x32_bf16 a[212:215], a[112:115], a[148:151], a[212:215]
s_add_u32 m0, s73, 18304
ds_read_b64_tr_b16 a[108:109], v233 offset:14336
ds_read_b64_tr_b16 a[110:111], v233 offset:14848
v_mfma_f32_16x16x32_bf16 a[212:215], a[116:119], a[156:159], a[212:215]
buffer_load_dwordx4 %[vX1], %[srdX], s72 offen offset:1152 lds
v_mfma_f32_16x16x32_bf16 a[216:219], a[120:123], a[144:147], a[216:219]
ds_read_b64_tr_b16 a[112:113], v234 offset:2048
ds_read_b64_tr_b16 a[114:115], v234 offset:2560
v_mfma_f32_16x16x32_bf16 a[216:219], a[124:127], a[152:155], a[216:219]
v_exp_f32 v107, v107
v_cvt_pk_bf16_f32 v160, v96, v97
v_mfma_f32_16x16x32_bf16 a[220:223], a[120:123], a[148:151], a[220:223]
s_add_u32 s76, s68, 172032
)"
R"(ds_read_b64_tr_b16 a[116:117], v234 offset:14336
ds_read_b64_tr_b16 a[118:119], v234 offset:14848
v_cvt_pk_bf16_f32 v161, v98, v99
v_mfma_f32_16x16x32_bf16 a[220:223], a[124:127], a[156:159], a[220:223]
s_waitcnt lgkmcnt(4)
s_waitcnt vmcnt(16)
s_barrier
v_mfma_f32_16x16x32_bf16 a[224:227], a[96:99], a[144:147], a[224:227]
s_add_u32 m0, s64, 0
ds_read_b64_tr_b16 a[120:121], v235 offset:2048
ds_read_b64_tr_b16 a[122:123], v235 offset:2560
v_mfma_f32_16x16x32_bf16 a[224:227], a[100:103], a[152:155], a[224:227]
buffer_load_dwordx4 %[vV], %[srdV], s76 offen offset:0 nt lds
v_mfma_f32_16x16x32_bf16 a[228:231], a[96:99], a[148:151], a[228:231]
ds_read_b64_tr_b16 a[124:125], v235 offset:14336
ds_read_b64_tr_b16 a[126:127], v235 offset:14848
v_mfma_f32_16x16x32_bf16 a[228:231], a[100:103], a[156:159], a[228:231]
v_add_u32 v232, s49, %[pK]
v_xor_b32 v233, 64, v232
v_mfma_f32_16x16x32_bf16 a[232:235], a[104:107], a[144:147], a[232:235]
s_add_u32 m0, s64, 896
ds_read_b128 a[96:99], v232 offset:0
ds_read_b128 a[100:103], v233 offset:0
v_mfma_f32_16x16x32_bf16 a[232:235], a[108:111], a[152:155], a[232:235]
)"
R"(buffer_load_dwordx4 %[vV], %[srdV], s76 offen offset:128 nt lds
v_mfma_f32_16x16x32_bf16 a[236:239], a[104:107], a[148:151], a[236:239]
v_exp_f32 v100, v100
v_mfma_f32_16x16x32_bf16 a[236:239], a[108:111], a[156:159], a[236:239]
s_waitcnt lgkmcnt(2)
v_exp_f32 v101, v101
v_mfma_f32_16x16x32_bf16 a[240:243], a[112:115], a[144:147], a[240:243]
s_add_u32 m0, s64, 1792
ds_read_b128 a[104:107], v232 offset:1024
ds_read_b128 a[108:111], v233 offset:1024
v_mfma_f32_16x16x32_bf16 a[240:243], a[116:119], a[152:155], a[240:243]
buffer_load_dwordx4 %[vV], %[srdV], s76 offen offset:256 nt lds
v_mfma_f32_16x16x32_bf16 a[244:247], a[112:115], a[148:151], a[244:247]
v_exp_f32 v102, v102
v_mfma_f32_16x16x32_bf16 a[244:247], a[116:119], a[156:159], a[244:247]
v_exp_f32 v103, v103
v_cvt_pk_bf16_f32 v162, v104, v105
v_mfma_f32_16x16x32_bf16 a[248:251], a[120:123], a[144:147], a[248:251]
s_add_u32 m0, s64, 0
ds_read_b128 a[112:115], v232 offset:2048
ds_read_b128 a[116:119], v233 offset:2048
v_cvt_pk_bf16_f32 v163, v106, v107
v_mfma_f32_16x16x32_bf16 a[248:251], a[124:127], a[152:155], a[248:251]
buffer_load_dwordx4 %[vV], %[srdV], s76 offen offset:3072 nt lds
)"
R"(v_mfma_f32_16x16x32_bf16 a[252:255], a[120:123], a[148:151], a[252:255]
v_exp_f32 v108, v108
v_mfma_f32_16x16x32_bf16 a[252:255], a[124:127], a[156:159], a[252:255]
s_waitcnt lgkmcnt(3)
v_exp_f32 v109, v109
v_mfma_f32_16x16x32_bf16 v[128:131], a[96:99], v[184:187], v[176:179]
s_add_u32 m0, s64, 896
ds_read_b128 a[120:123], v232 offset:6144
ds_read_b128 a[124:127], v233 offset:6144
v_mfma_f32_16x16x32_bf16 v[128:131], a[100:103], v[192:195], v[128:131]
buffer_load_dwordx4 %[vV], %[srdV], s76 offen offset:3200 nt lds
v_mfma_f32_16x16x32_bf16 v[128:131], a[104:107], v[200:203], v[128:131]
v_exp_f32 v110, v110
v_mfma_f32_16x16x32_bf16 v[132:135], a[96:99], v[188:191], v[180:183]
v_exp_f32 v111, v111
v_mfma_f32_16x16x32_bf16 v[132:135], a[100:103], v[196:199], v[132:135]
s_add_u32 m0, s64, 1792
ds_read_b128 a[96:99], v232 offset:7168
ds_read_b128 a[100:103], v233 offset:7168
v_mfma_f32_16x16x32_bf16 v[132:135], a[104:107], v[204:207], v[132:135]
s_waitcnt lgkmcnt(4)
buffer_load_dwordx4 %[vV], %[srdV], s76 offen offset:3328 nt lds
v_mfma_f32_16x16x32_bf16 v[128:131], a[108:111], v[208:211], v[128:131]
v_cvt_pk_bf16_f32 v164, v100, v101
)"
R"(v_cvt_pk_bf16_f32 v165, v102, v103
v_mfma_f32_16x16x32_bf16 v[128:131], a[112:115], v[216:219], v[128:131]
v_exp_f32 v112, v112
v_mfma_f32_16x16x32_bf16 v[128:131], a[116:119], v[224:227], v[128:131]
v_exp_f32 v113, v113
v_mfma_f32_16x16x32_bf16 v[132:135], a[108:111], v[212:215], v[132:135]
ds_read_b128 a[104:107], v232 offset:8192
ds_read_b128 a[108:111], v233 offset:8192
v_mfma_f32_16x16x32_bf16 v[132:135], a[112:115], v[220:223], v[132:135]
v_exp_f32 v114, v114
v_mfma_f32_16x16x32_bf16 v[132:135], a[116:119], v[228:231], v[132:135]
s_waitcnt lgkmcnt(3)
v_exp_f32 v115, v115
v_mfma_f32_16x16x32_bf16 v[136:139], a[120:123], v[184:187], v[176:179]
ds_read_b128 a[112:115], v232 offset:12288
ds_read_b128 a[116:119], v233 offset:12288
v_mfma_f32_16x16x32_bf16 v[136:139], a[124:127], v[192:195], v[136:139]
v_cvt_pk_bf16_f32 v166, v108, v109
v_cvt_pk_bf16_f32 v167, v110, v111
v_mfma_f32_16x16x32_bf16 v[136:139], a[96:99], v[200:203], v[136:139]
v_exp_f32 v120, v120
v_mfma_f32_16x16x32_bf16 v[140:143], a[120:123], v[188:191], v[180:183]
v_exp_f32 v121, v121
v_mfma_f32_16x16x32_bf16 v[140:143], a[124:127], v[196:199], v[140:143]
)"
R"(ds_read_b128 a[120:123], v232 offset:13312
ds_read_b128 a[124:127], v233 offset:13312
v_mfma_f32_16x16x32_bf16 v[140:143], a[96:99], v[204:207], v[140:143]
s_waitcnt lgkmcnt(4)
v_exp_f32 v122, v122
v_mfma_f32_16x16x32_bf16 v[136:139], a[100:103], v[208:211], v[136:139]
v_exp_f32 v123, v123
v_mfma_f32_16x16x32_bf16 v[136:139], a[104:107], v[216:219], v[136:139]
v_cvt_pk_bf16_f32 v168, v112, v113
v_cvt_pk_bf16_f32 v169, v114, v115
v_mfma_f32_16x16x32_bf16 v[136:139], a[108:111], v[224:227], v[136:139]
v_exp_f32 v116, v116
v_mfma_f32_16x16x32_bf16 v[140:143], a[100:103], v[212:215], v[140:143]
ds_read_b128 a[96:99], v232 offset:14336
ds_read_b128 a[100:103], v233 offset:14336
v_mfma_f32_16x16x32_bf16 v[140:143], a[104:107], v[220:223], v[140:143]
v_exp_f32 v117, v117
v_mfma_f32_16x16x32_bf16 v[140:143], a[108:111], v[228:231], v[140:143]
s_waitcnt lgkmcnt(3)
v_exp_f32 v118, v118
v_mfma_f32_16x16x32_bf16 v[144:147], a[112:115], v[184:187], v[176:179]
ds_read_b128 a[104:107], v232 offset:18432
ds_read_b128 a[108:111], v233 offset:18432
v_mfma_f32_16x16x32_bf16 v[144:147], a[116:119], v[192:195], v[144:147]
)"
R"(v_exp_f32 v119, v119
v_mfma_f32_16x16x32_bf16 v[144:147], a[120:123], v[200:203], v[144:147]
v_cvt_pk_bf16_f32 v170, v120, v121
v_cvt_pk_bf16_f32 v171, v122, v123
v_mfma_f32_16x16x32_bf16 v[148:151], a[112:115], v[188:191], v[180:183]
v_exp_f32 v124, v124
v_mfma_f32_16x16x32_bf16 v[148:151], a[116:119], v[196:199], v[148:151]
ds_read_b128 a[112:115], v232 offset:19456
ds_read_b128 a[116:119], v233 offset:19456
v_mfma_f32_16x16x32_bf16 v[148:151], a[120:123], v[204:207], v[148:151]
s_waitcnt lgkmcnt(4)
v_exp_f32 v125, v125
v_mfma_f32_16x16x32_bf16 v[144:147], a[124:127], v[208:211], v[144:147]
v_exp_f32 v126, v126
v_mfma_f32_16x16x32_bf16 v[144:147], a[96:99], v[216:219], v[144:147]
s_waitcnt vmcnt(16)
s_barrier
v_mfma_f32_16x16x32_bf16 v[144:147], a[100:103], v[224:227], v[144:147]
s_add_u32 s75, s66, 7078272
v_exp_f32 v127, v127
v_mfma_f32_16x16x32_bf16 v[148:151], a[124:127], v[212:215], v[148:151]
s_add_u32 m0, s54, 0
ds_read_b128 a[120:123], v232 offset:20480
ds_read_b128 a[124:127], v233 offset:20480
)"
R"(v_mfma_f32_16x16x32_bf16 v[148:151], a[96:99], v[220:223], v[148:151]
buffer_load_dwordx4 %[vW], %[srdW], s75 offen offset:0 lds
v_mfma_f32_16x16x32_bf16 v[148:151], a[100:103], v[228:231], v[148:151]
s_waitcnt lgkmcnt(3)
v_add_u32 v232, s50, %[pV]
v_xor_b32 v233, 32, v232
v_mfma_f32_16x16x32_bf16 v[152:155], a[104:107], v[184:187], v[176:179]
ds_read_b64_tr_b16 a[96:97], v232 offset:0
ds_read_b64_tr_b16 a[98:99], v232 offset:6144
v_mfma_f32_16x16x32_bf16 v[152:155], a[108:111], v[192:195], v[152:155]
v_xor_b32 v234, 64, v232
v_xor_b32 v235, 96, v232
v_mfma_f32_16x16x32_bf16 v[152:155], a[112:115], v[200:203], v[152:155]
s_add_u32 m0, s54, 896
ds_read_b64_tr_b16 a[100:101], v232 offset:12288
ds_read_b64_tr_b16 a[102:103], v232 offset:18432
v_mfma_f32_16x16x32_bf16 v[156:159], a[104:107], v[188:191], v[180:183]
buffer_load_dwordx4 %[vW], %[srdW], s75 offen offset:128 lds
v_mfma_f32_16x16x32_bf16 v[156:159], a[108:111], v[196:199], v[156:159]
ds_read_b64_tr_b16 a[104:105], v233 offset:0
ds_read_b64_tr_b16 a[106:107], v233 offset:6144
v_mfma_f32_16x16x32_bf16 v[156:159], a[112:115], v[204:207], v[156:159]
s_waitcnt lgkmcnt(6)
v_cvt_pk_bf16_f32 v172, v116, v117
)"
R"(v_cvt_pk_bf16_f32 v173, v118, v119
v_mfma_f32_16x16x32_bf16 v[152:155], a[116:119], v[208:211], v[152:155]
s_add_u32 m0, s54, 1792
ds_read_b64_tr_b16 a[108:109], v233 offset:12288
ds_read_b64_tr_b16 a[110:111], v233 offset:18432
v_mfma_f32_16x16x32_bf16 v[152:155], a[120:123], v[216:219], v[152:155]
buffer_load_dwordx4 %[vW], %[srdW], s75 offen offset:256 lds
v_mfma_f32_16x16x32_bf16 v[152:155], a[124:127], v[224:227], v[152:155]
ds_read_b64_tr_b16 a[112:113], v234 offset:0
ds_read_b64_tr_b16 a[114:115], v234 offset:6144
v_mfma_f32_16x16x32_bf16 v[156:159], a[116:119], v[212:215], v[156:159]
s_add_u32 s75, s66, 7471488
v_cvt_pk_bf16_f32 v174, v124, v125
v_cvt_pk_bf16_f32 v175, v126, v127
v_mfma_f32_16x16x32_bf16 v[156:159], a[120:123], v[220:223], v[156:159]
s_add_u32 m0, s54, 12288
ds_read_b64_tr_b16 a[116:117], v234 offset:12288
ds_read_b64_tr_b16 a[118:119], v234 offset:18432
v_mfma_f32_16x16x32_bf16 v[156:159], a[124:127], v[228:231], v[156:159]
buffer_load_dwordx4 %[vW], %[srdW], s75 offen offset:0 lds
v_mfma_f32_16x16x32_bf16 a[132:135], a[128:131], v[160:163], a[132:135]
v_mfma_f32_16x16x32_bf16 a[132:135], a[128:131], v[168:171], a[132:135]
v_mfma_f32_16x16x32_bf16 a[136:139], a[128:131], v[164:167], a[136:139]
s_add_u32 m0, s54, 13184
)"
R"(v_mfma_f32_16x16x32_bf16 a[136:139], a[128:131], v[172:175], a[136:139]
s_waitcnt lgkmcnt(4)
buffer_load_dwordx4 %[vW], %[srdW], s75 offen offset:128 lds
v_mfma_f32_16x16x32_bf16 v[0:3], a[96:99], v[160:163], v[0:3]
ds_read_b64_tr_b16 a[120:121], v235 offset:0
ds_read_b64_tr_b16 a[122:123], v235 offset:6144
v_mfma_f32_16x16x32_bf16 v[0:3], a[100:103], v[168:171], v[0:3]
v_max3_f32 v236, v128, v129, v130
v_max3_f32 v237, v132, v133, v134
v_mfma_f32_16x16x32_bf16 v[4:7], a[96:99], v[164:167], v[4:7]
s_add_u32 m0, s54, 14080
ds_read_b64_tr_b16 a[124:125], v235 offset:12288
ds_read_b64_tr_b16 a[126:127], v235 offset:18432
v_mfma_f32_16x16x32_bf16 v[4:7], a[100:103], v[172:175], v[4:7]
buffer_load_dwordx4 %[vW], %[srdW], s75 offen offset:256 lds
v_mfma_f32_16x16x32_bf16 v[8:11], a[104:107], v[160:163], v[8:11]
ds_read_b64_tr_b16 a[96:97], v232 offset:1024
ds_read_b64_tr_b16 a[98:99], v232 offset:7168
v_mfma_f32_16x16x32_bf16 v[8:11], a[108:111], v[168:171], v[8:11]
v_max_f32 v236, v236, v131
v_max_f32 v237, v237, v135
v_mfma_f32_16x16x32_bf16 v[12:15], a[104:107], v[164:167], v[12:15]
ds_read_b64_tr_b16 a[100:101], v232 offset:13312
ds_read_b64_tr_b16 a[102:103], v232 offset:19456
)"
R"(v_mfma_f32_16x16x32_bf16 v[12:15], a[108:111], v[172:175], v[12:15]
s_waitcnt lgkmcnt(4)
v_max3_f32 v236, v236, v136, v137
v_max3_f32 v237, v237, v140, v141
v_mfma_f32_16x16x32_bf16 v[16:19], a[112:115], v[160:163], v[16:19]
ds_read_b64_tr_b16 a[104:105], v233 offset:1024
ds_read_b64_tr_b16 a[106:107], v233 offset:7168
v_mfma_f32_16x16x32_bf16 v[16:19], a[116:119], v[168:171], v[16:19]
v_max3_f32 v236, v236, v138, v139
v_max3_f32 v237, v237, v142, v143
v_mfma_f32_16x16x32_bf16 v[20:23], a[112:115], v[164:167], v[20:23]
ds_read_b64_tr_b16 a[108:109], v233 offset:13312
ds_read_b64_tr_b16 a[110:111], v233 offset:19456
v_mfma_f32_16x16x32_bf16 v[20:23], a[116:119], v[172:175], v[20:23]
v_max3_f32 v236, v236, v144, v145
v_max3_f32 v237, v237, v148, v149
v_mfma_f32_16x16x32_bf16 v[24:27], a[120:123], v[160:163], v[24:27]
ds_read_b64_tr_b16 a[112:113], v234 offset:1024
ds_read_b64_tr_b16 a[114:115], v234 offset:7168
v_mfma_f32_16x16x32_bf16 v[24:27], a[124:127], v[168:171], v[24:27]
v_max3_f32 v236, v236, v146, v147
v_max3_f32 v237, v237, v150, v151
v_mfma_f32_16x16x32_bf16 v[28:31], a[120:123], v[164:167], v[28:31]
ds_read_b64_tr_b16 a[116:117], v234 offset:13312
)"
R"(ds_read_b64_tr_b16 a[118:119], v234 offset:19456
v_mfma_f32_16x16x32_bf16 v[28:31], a[124:127], v[172:175], v[28:31]
s_waitcnt lgkmcnt(4)
v_max3_f32 v236, v236, v152, v153
v_max3_f32 v237, v237, v156, v157
v_mfma_f32_16x16x32_bf16 v[32:35], a[96:99], v[160:163], v[32:35]
ds_read_b64_tr_b16 a[120:121], v235 offset:1024
ds_read_b64_tr_b16 a[122:123], v235 offset:7168
v_mfma_f32_16x16x32_bf16 v[32:35], a[100:103], v[168:171], v[32:35]
v_max3_f32 v236, v236, v154, v155
v_max3_f32 v237, v237, v158, v159
v_mfma_f32_16x16x32_bf16 v[36:39], a[96:99], v[164:167], v[36:39]
ds_read_b64_tr_b16 a[124:125], v235 offset:13312
ds_read_b64_tr_b16 a[126:127], v235 offset:19456
v_mfma_f32_16x16x32_bf16 v[36:39], a[100:103], v[172:175], v[36:39]
v_max_f32 v236, v236, v237
v_cmp_lt_f32 vcc, s77, v236
v_mfma_f32_16x16x32_bf16 v[40:43], a[104:107], v[160:163], v[40:43]
ds_read_b64_tr_b16 a[96:97], v232 offset:2048
ds_read_b64_tr_b16 a[98:99], v232 offset:8192
v_mfma_f32_16x16x32_bf16 v[40:43], a[108:111], v[168:171], v[40:43]
v_mfma_f32_16x16x32_bf16 v[44:47], a[104:107], v[164:167], v[44:47]
ds_read_b64_tr_b16 a[100:101], v232 offset:14336
ds_read_b64_tr_b16 a[102:103], v232 offset:20480
)"
R"(v_mfma_f32_16x16x32_bf16 v[44:47], a[108:111], v[172:175], v[44:47]
s_waitcnt lgkmcnt(4)
v_mfma_f32_16x16x32_bf16 v[48:51], a[112:115], v[160:163], v[48:51]
ds_read_b64_tr_b16 a[104:105], v233 offset:2048
ds_read_b64_tr_b16 a[106:107], v233 offset:8192
v_mfma_f32_16x16x32_bf16 v[48:51], a[116:119], v[168:171], v[48:51]
v_mfma_f32_16x16x32_bf16 v[52:55], a[112:115], v[164:167], v[52:55]
ds_read_b64_tr_b16 a[108:109], v233 offset:14336
ds_read_b64_tr_b16 a[110:111], v233 offset:20480
v_mfma_f32_16x16x32_bf16 v[52:55], a[116:119], v[172:175], v[52:55]
v_mfma_f32_16x16x32_bf16 v[56:59], a[120:123], v[160:163], v[56:59]
ds_read_b64_tr_b16 a[112:113], v234 offset:2048
ds_read_b64_tr_b16 a[114:115], v234 offset:8192
v_mfma_f32_16x16x32_bf16 v[56:59], a[124:127], v[168:171], v[56:59]
v_mfma_f32_16x16x32_bf16 v[60:63], a[120:123], v[164:167], v[60:63]
ds_read_b64_tr_b16 a[116:117], v234 offset:14336
ds_read_b64_tr_b16 a[118:119], v234 offset:20480
v_mfma_f32_16x16x32_bf16 v[60:63], a[124:127], v[172:175], v[60:63]
s_waitcnt lgkmcnt(4)
s_waitcnt vmcnt(16)
s_barrier
v_mfma_f32_16x16x32_bf16 v[64:67], a[96:99], v[160:163], v[64:67]
ds_read_b64_tr_b16 a[120:121], v235 offset:2048
ds_read_b64_tr_b16 a[122:123], v235 offset:8192
)"
R"(v_mfma_f32_16x16x32_bf16 v[64:67], a[100:103], v[168:171], v[64:67]
ds_read_b128 a[144:147], %[pX0] offset:0
ds_read_b128 a[148:151], %[pX0] offset:2048
v_mfma_f32_16x16x32_bf16 v[68:71], a[96:99], v[164:167], v[68:71]
ds_read_b64_tr_b16 a[124:125], v235 offset:14336
ds_read_b64_tr_b16 a[126:127], v235 offset:20480
v_mfma_f32_16x16x32_bf16 v[68:71], a[100:103], v[172:175], v[68:71]
ds_read_b128 a[152:155], %[pX1] offset:0
ds_read_b128 a[156:159], %[pX1] offset:2048
v_add_u32 v232, s51, %[pW]
v_xor_b32 v233, 32, v232
v_mfma_f32_16x16x32_bf16 v[72:75], a[104:107], v[160:163], v[72:75]
ds_read_b64_tr_b16 a[96:97], v232 offset:0
ds_read_b64_tr_b16 a[98:99], v232 offset:512
v_mfma_f32_16x16x32_bf16 v[72:75], a[108:111], v[168:171], v[72:75]
s_add_u32 s75, s67, 196608
v_xor_b32 v234, 64, v232
v_xor_b32 v235, 96, v232
v_mfma_f32_16x16x32_bf16 v[76:79], a[104:107], v[164:167], v[76:79]
s_add_u32 m0, s55, 0
ds_read_b64_tr_b16 a[100:101], v232 offset:12288
ds_read_b64_tr_b16 a[102:103], v232 offset:12800
v_mfma_f32_16x16x32_bf16 v[76:79], a[108:111], v[172:175], v[76:79]
s_waitcnt lgkmcnt(6)
)"
R"(buffer_load_dwordx4 %[vK], %[srdK], s75 offen offset:0 nt lds
v_mfma_f32_16x16x32_bf16 v[80:83], a[112:115], v[160:163], v[80:83]
ds_read_b64_tr_b16 a[104:105], v233 offset:0
ds_read_b64_tr_b16 a[106:107], v233 offset:512
v_mfma_f32_16x16x32_bf16 v[80:83], a[116:119], v[168:171], v[80:83]
v_mfma_f32_16x16x32_bf16 v[84:87], a[112:115], v[164:167], v[84:87]
s_add_u32 m0, s55, 896
ds_read_b64_tr_b16 a[108:109], v233 offset:12288
ds_read_b64_tr_b16 a[110:111], v233 offset:12800
v_mfma_f32_16x16x32_bf16 v[84:87], a[116:119], v[172:175], v[84:87]
buffer_load_dwordx4 %[vK], %[srdK], s75 offen offset:128 nt lds
v_mfma_f32_16x16x32_bf16 v[88:91], a[120:123], v[160:163], v[88:91]
ds_read_b64_tr_b16 a[112:113], v234 offset:0
ds_read_b64_tr_b16 a[114:115], v234 offset:512
v_mfma_f32_16x16x32_bf16 v[88:91], a[124:127], v[168:171], v[88:91]
v_mfma_f32_16x16x32_bf16 v[92:95], a[120:123], v[164:167], v[92:95]
s_add_u32 m0, s55, 1792
ds_read_b64_tr_b16 a[116:117], v234 offset:12288
ds_read_b64_tr_b16 a[118:119], v234 offset:12800
v_mfma_f32_16x16x32_bf16 v[92:95], a[124:127], v[172:175], v[92:95]
s_waitcnt lgkmcnt(4)
buffer_load_dwordx4 %[vK], %[srdK], s75 offen offset:256 nt lds
s_cbranch_vccnz RARE8_%=
BACK8_%=:
)"
R"(v_mfma_f32_16x16x32_bf16 a[160:163], a[96:99], a[144:147], a[160:163]
ds_read_b64_tr_b16 a[120:121], v235 offset:0
ds_read_b64_tr_b16 a[122:123], v235 offset:512
v_exp_f32 v128, v128
v_mfma_f32_16x16x32_bf16 a[160:163], a[100:103], a[152:155], a[160:163]
s_add_u32 s75, s67, 208896
v_exp_f32 v129, v129
v_exp_f32 v130, v130
v_mfma_f32_16x16x32_bf16 a[164:167], a[96:99], a[148:151], a[164:167]
s_add_u32 m0, s55, 12288
ds_read_b64_tr_b16 a[124:125], v235 offset:12288
ds_read_b64_tr_b16 a[126:127], v235 offset:12800
v_exp_f32 v131, v131
v_mfma_f32_16x16x32_bf16 a[164:167], a[100:103], a[156:159], a[164:167]
buffer_load_dwordx4 %[vK], %[srdK], s75 offen offset:0 nt lds
v_exp_f32 v136, v136
v_mfma_f32_16x16x32_bf16 a[168:171], a[104:107], a[144:147], a[168:171]
ds_read_b64_tr_b16 a[96:97], v232 offset:1024
ds_read_b64_tr_b16 a[98:99], v232 offset:1536
v_exp_f32 v137, v137
v_mfma_f32_16x16x32_bf16 a[168:171], a[108:111], a[152:155], a[168:171]
v_exp_f32 v138, v138
v_exp_f32 v139, v139
v_mfma_f32_16x16x32_bf16 a[172:175], a[104:107], a[148:151], a[172:175]
)"
R"(s_add_u32 m0, s55, 13184
ds_read_b64_tr_b16 a[100:101], v232 offset:13312
ds_read_b64_tr_b16 a[102:103], v232 offset:13824
v_cvt_pk_bf16_f32 v160, v128, v129
v_mfma_f32_16x16x32_bf16 a[172:175], a[108:111], a[156:159], a[172:175]
s_waitcnt lgkmcnt(4)
buffer_load_dwordx4 %[vK], %[srdK], s75 offen offset:128 nt lds
v_cvt_pk_bf16_f32 v161, v130, v131
v_mfma_f32_16x16x32_bf16 a[176:179], a[112:115], a[144:147], a[176:179]
ds_read_b64_tr_b16 a[104:105], v233 offset:1024
ds_read_b64_tr_b16 a[106:107], v233 offset:1536
v_exp_f32 v132, v132
v_mfma_f32_16x16x32_bf16 a[176:179], a[116:119], a[152:155], a[176:179]
v_exp_f32 v133, v133
v_exp_f32 v134, v134
v_mfma_f32_16x16x32_bf16 a[180:183], a[112:115], a[148:151], a[180:183]
s_add_u32 m0, s55, 14080
ds_read_b64_tr_b16 a[108:109], v233 offset:13312
ds_read_b64_tr_b16 a[110:111], v233 offset:13824
v_exp_f32 v135, v135
v_mfma_f32_16x16x32_bf16 a[180:183], a[116:119], a[156:159], a[180:183]
buffer_load_dwordx4 %[vK], %[srdK], s75 offen offset:256 nt lds
v_cvt_pk_bf16_f32 v162, v136, v137
v_mfma_f32_16x16x32_bf16 a[184:187], a[120:123], a[144:147], a[184:187]
)"
R"(ds_read_b64_tr_b16 a[112:113], v234 offset:1024
ds_read_b64_tr_b16 a[114:115], v234 offset:1536
v_cvt_pk_bf16_f32 v163, v138, v139
v_mfma_f32_16x16x32_bf16 a[184:187], a[124:127], a[152:155], a[184:187]
v_exp_f32 v140, v140
v_exp_f32 v141, v141
v_mfma_f32_16x16x32_bf16 a[188:191], a[120:123], a[148:151], a[188:191]
s_add_u32 m0, s73, 0
ds_read_b64_tr_b16 a[116:117], v234 offset:13312
ds_read_b64_tr_b16 a[118:119], v234 offset:13824
v_exp_f32 v142, v142
v_mfma_f32_16x16x32_bf16 a[188:191], a[124:127], a[156:159], a[188:191]
s_waitcnt lgkmcnt(4)
buffer_load_dwordx4 %[vX0], %[srdX], s69 offen offset:0 lds
v_exp_f32 v143, v143
v_mfma_f32_16x16x32_bf16 a[192:195], a[96:99], a[144:147], a[192:195]
ds_read_b64_tr_b16 a[120:121], v235 offset:1024
ds_read_b64_tr_b16 a[122:123], v235 offset:1536
v_cvt_pk_bf16_f32 v164, v132, v133
v_mfma_f32_16x16x32_bf16 a[192:195], a[100:103], a[152:155], a[192:195]
v_cvt_pk_bf16_f32 v165, v134, v135
v_exp_f32 v144, v144
v_mfma_f32_16x16x32_bf16 a[196:199], a[96:99], a[148:151], a[196:199]
s_add_u32 m0, s73, 1024
)"
R"(ds_read_b64_tr_b16 a[124:125], v235 offset:13312
ds_read_b64_tr_b16 a[126:127], v235 offset:13824
v_exp_f32 v145, v145
v_mfma_f32_16x16x32_bf16 a[196:199], a[100:103], a[156:159], a[196:199]
buffer_load_dwordx4 %[vX1], %[srdX], s70 offen offset:0 lds
v_exp_f32 v146, v146
v_mfma_f32_16x16x32_bf16 a[200:203], a[104:107], a[144:147], a[200:203]
ds_read_b64_tr_b16 a[96:97], v232 offset:2048
ds_read_b64_tr_b16 a[98:99], v232 offset:2560
v_mfma_f32_16x16x32_bf16 a[200:203], a[108:111], a[152:155], a[200:203]
v_exp_f32 v147, v147
v_cvt_pk_bf16_f32 v166, v140, v141
v_mfma_f32_16x16x32_bf16 a[204:207], a[104:107], a[148:151], a[204:207]
s_add_u32 m0, s73, 2048
ds_read_b64_tr_b16 a[100:101], v232 offset:14336
ds_read_b64_tr_b16 a[102:103], v232 offset:14848
v_cvt_pk_bf16_f32 v167, v142, v143
v_mfma_f32_16x16x32_bf16 a[204:207], a[108:111], a[156:159], a[204:207]
s_waitcnt lgkmcnt(4)
buffer_load_dwordx4 %[vX0], %[srdX], s71 offen offset:0 lds
v_exp_f32 v152, v152
v_mfma_f32_16x16x32_bf16 a[208:211], a[112:115], a[144:147], a[208:211]
ds_read_b64_tr_b16 a[104:105], v233 offset:2048
ds_read_b64_tr_b16 a[106:107], v233 offset:2560
)"
R"(v_exp_f32 v153, v153
v_mfma_f32_16x16x32_bf16 a[208:211], a[116:119], a[152:155], a[208:211]
v_exp_f32 v154, v154
v_exp_f32 v155, v155
v_mfma_f32_16x16x32_bf16 a[212:215], a[112:115], a[148:151], a[212:215]
s_add_u32 m0, s73, 3072
ds_read_b64_tr_b16 a[108:109], v233 offset:14336
ds_read_b64_tr_b16 a[110:111], v233 offset:14848
v_cvt_pk_bf16_f32 v168, v144, v145
v_mfma_f32_16x16x32_bf16 a[212:215], a[116:119], a[156:159], a[212:215]
buffer_load_dwordx4 %[vX1], %[srdX], s72 offen offset:0 lds
v_cvt_pk_bf16_f32 v169, v146, v147
v_mfma_f32_16x16x32_bf16 a[216:219], a[120:123], a[144:147], a[216:219]
ds_read_b64_tr_b16 a[112:113], v234 offset:2048
ds_read_b64_tr_b16 a[114:115], v234 offset:2560
v_exp_f32 v148, v148
v_mfma_f32_16x16x32_bf16 a[216:219], a[124:127], a[152:155], a[216:219]
v_exp_f32 v149, v149
v_exp_f32 v150, v150
v_mfma_f32_16x16x32_bf16 a[220:223], a[120:123], a[148:151], a[220:223]
s_add_u32 s75, s86, 768
ds_read_b64_tr_b16 a[116:117], v234 offset:14336
ds_read_b64_tr_b16 a[118:119], v234 offset:14848
v_exp_f32 v151, v151
)"
R"(v_mfma_f32_16x16x32_bf16 a[220:223], a[124:127], a[156:159], a[220:223]
s_waitcnt lgkmcnt(4)
s_waitcnt vmcnt(16)
s_barrier
v_cvt_pk_bf16_f32 v170, v152, v153
v_mfma_f32_16x16x32_bf16 a[224:227], a[96:99], a[144:147], a[224:227]
s_add_u32 m0, s56, 0
ds_read_b64_tr_b16 a[120:121], v235 offset:2048
ds_read_b64_tr_b16 a[122:123], v235 offset:2560
v_cvt_pk_bf16_f32 v171, v154, v155
v_mfma_f32_16x16x32_bf16 a[224:227], a[100:103], a[152:155], a[224:227]
buffer_load_dwordx4 %[vW], %[srdW], s75 offen offset:0 lds
v_exp_f32 v156, v156
v_mfma_f32_16x16x32_bf16 a[228:231], a[96:99], a[148:151], a[228:231]
ds_read_b64_tr_b16 a[124:125], v235 offset:14336
ds_read_b64_tr_b16 a[126:127], v235 offset:14848
v_exp_f32 v157, v157
v_mfma_f32_16x16x32_bf16 a[228:231], a[100:103], a[156:159], a[228:231]
v_add_u32 v232, s52, %[pV]
v_xor_b32 v233, 32, v232
v_exp_f32 v158, v158
v_mfma_f32_16x16x32_bf16 a[232:235], a[104:107], a[144:147], a[232:235]
ds_read_b64_tr_b16 a[96:97], v232 offset:0
ds_read_b64_tr_b16 a[98:99], v232 offset:6144
)"
R"(v_mfma_f32_16x16x32_bf16 a[232:235], a[108:111], a[152:155], a[232:235]
v_xor_b32 v234, 64, v232
v_xor_b32 v235, 96, v232
v_mfma_f32_16x16x32_bf16 a[236:239], a[104:107], a[148:151], a[236:239]
s_add_u32 m0, s56, 896
ds_read_b64_tr_b16 a[100:101], v232 offset:12288
ds_read_b64_tr_b16 a[102:103], v232 offset:18432
v_mfma_f32_16x16x32_bf16 a[236:239], a[108:111], a[156:159], a[236:239]
s_waitcnt lgkmcnt(4)
buffer_load_dwordx4 %[vW], %[srdW], s75 offen offset:128 lds
v_mfma_f32_16x16x32_bf16 a[240:243], a[112:115], a[144:147], a[240:243]
ds_read_b64_tr_b16 a[104:105], v233 offset:0
ds_read_b64_tr_b16 a[106:107], v233 offset:6144
v_mfma_f32_16x16x32_bf16 a[240:243], a[116:119], a[152:155], a[240:243]
v_exp_f32 v159, v159
v_cvt_pk_bf16_f32 v172, v148, v149
v_mfma_f32_16x16x32_bf16 a[244:247], a[112:115], a[148:151], a[244:247]
s_add_u32 m0, s56, 1792
ds_read_b64_tr_b16 a[108:109], v233 offset:12288
ds_read_b64_tr_b16 a[110:111], v233 offset:18432
v_cvt_pk_bf16_f32 v173, v150, v151
v_mfma_f32_16x16x32_bf16 a[244:247], a[116:119], a[156:159], a[244:247]
buffer_load_dwordx4 %[vW], %[srdW], s75 offen offset:256 lds
v_mfma_f32_16x16x32_bf16 a[248:251], a[120:123], a[144:147], a[248:251]
)"
R"(ds_read_b64_tr_b16 a[112:113], v234 offset:0
ds_read_b64_tr_b16 a[114:115], v234 offset:6144
v_mfma_f32_16x16x32_bf16 a[248:251], a[124:127], a[152:155], a[248:251]
s_add_u32 s75, s86, 393984
v_cvt_pk_bf16_f32 v174, v156, v157
v_cvt_pk_bf16_f32 v175, v158, v159
v_mfma_f32_16x16x32_bf16 a[252:255], a[120:123], a[148:151], a[252:255]
s_add_u32 m0, s56, 12288
ds_read_b64_tr_b16 a[116:117], v234 offset:12288
ds_read_b64_tr_b16 a[118:119], v234 offset:18432
v_mfma_f32_16x16x32_bf16 a[252:255], a[124:127], a[156:159], a[252:255]
buffer_load_dwordx4 %[vW], %[srdW], s75 offen offset:0 lds
v_mfma_f32_16x16x32_bf16 a[132:135], a[128:131], v[160:163], a[132:135]
v_mfma_f32_16x16x32_bf16 a[132:135], a[128:131], v[168:171], a[132:135]
v_mfma_f32_16x16x32_bf16 a[136:139], a[128:131], v[164:167], a[136:139]
s_add_u32 m0, s56, 13184
v_mfma_f32_16x16x32_bf16 a[136:139], a[128:131], v[172:175], a[136:139]
s_waitcnt lgkmcnt(4)
buffer_load_dwordx4 %[vW], %[srdW], s75 offen offset:128 lds
v_mfma_f32_16x16x32_bf16 v[0:3], a[96:99], v[160:163], v[0:3]
ds_read_b64_tr_b16 a[120:121], v235 offset:0
ds_read_b64_tr_b16 a[122:123], v235 offset:6144
v_mfma_f32_16x16x32_bf16 v[0:3], a[100:103], v[168:171], v[0:3]
v_mfma_f32_16x16x32_bf16 v[4:7], a[96:99], v[164:167], v[4:7]
)"
R"(s_add_u32 m0, s56, 14080
ds_read_b64_tr_b16 a[124:125], v235 offset:12288
ds_read_b64_tr_b16 a[126:127], v235 offset:18432
v_mfma_f32_16x16x32_bf16 v[4:7], a[100:103], v[172:175], v[4:7]
buffer_load_dwordx4 %[vW], %[srdW], s75 offen offset:256 lds
v_mfma_f32_16x16x32_bf16 v[8:11], a[104:107], v[160:163], v[8:11]
ds_read_b64_tr_b16 a[96:97], v232 offset:1024
ds_read_b64_tr_b16 a[98:99], v232 offset:7168
v_mfma_f32_16x16x32_bf16 v[8:11], a[108:111], v[168:171], v[8:11]
v_mfma_f32_16x16x32_bf16 v[12:15], a[104:107], v[164:167], v[12:15]
ds_read_b64_tr_b16 a[100:101], v232 offset:13312
ds_read_b64_tr_b16 a[102:103], v232 offset:19456
v_mfma_f32_16x16x32_bf16 v[12:15], a[108:111], v[172:175], v[12:15]
s_waitcnt lgkmcnt(4)
v_mfma_f32_16x16x32_bf16 v[16:19], a[112:115], v[160:163], v[16:19]
ds_read_b64_tr_b16 a[104:105], v233 offset:1024
ds_read_b64_tr_b16 a[106:107], v233 offset:7168
v_mfma_f32_16x16x32_bf16 v[16:19], a[116:119], v[168:171], v[16:19]
v_mfma_f32_16x16x32_bf16 v[20:23], a[112:115], v[164:167], v[20:23]
ds_read_b64_tr_b16 a[108:109], v233 offset:13312
ds_read_b64_tr_b16 a[110:111], v233 offset:19456
v_mfma_f32_16x16x32_bf16 v[20:23], a[116:119], v[172:175], v[20:23]
v_mfma_f32_16x16x32_bf16 v[24:27], a[120:123], v[160:163], v[24:27]
ds_read_b64_tr_b16 a[112:113], v234 offset:1024
)"
R"(ds_read_b64_tr_b16 a[114:115], v234 offset:7168
v_mfma_f32_16x16x32_bf16 v[24:27], a[124:127], v[168:171], v[24:27]
v_accvgpr_read_b32 v238, a132
v_accvgpr_read_b32 v240, a136
v_mfma_f32_16x16x32_bf16 v[28:31], a[120:123], v[164:167], v[28:31]
ds_read_b64_tr_b16 a[116:117], v234 offset:13312
ds_read_b64_tr_b16 a[118:119], v234 offset:19456
v_mfma_f32_16x16x32_bf16 v[28:31], a[124:127], v[172:175], v[28:31]
s_waitcnt lgkmcnt(4)
v_rcp_f32 v238, v238
v_mfma_f32_16x16x32_bf16 v[32:35], a[96:99], v[160:163], v[32:35]
ds_read_b64_tr_b16 a[120:121], v235 offset:1024
ds_read_b64_tr_b16 a[122:123], v235 offset:7168
v_mfma_f32_16x16x32_bf16 v[32:35], a[100:103], v[168:171], v[32:35]
v_rcp_f32 v240, v240
v_mfma_f32_16x16x32_bf16 v[36:39], a[96:99], v[164:167], v[36:39]
ds_read_b64_tr_b16 a[124:125], v235 offset:13312
ds_read_b64_tr_b16 a[126:127], v235 offset:19456
v_mfma_f32_16x16x32_bf16 v[36:39], a[100:103], v[172:175], v[36:39]
v_mfma_f32_16x16x32_bf16 v[40:43], a[104:107], v[160:163], v[40:43]
ds_read_b64_tr_b16 a[96:97], v232 offset:2048
ds_read_b64_tr_b16 a[98:99], v232 offset:8192
v_mfma_f32_16x16x32_bf16 v[40:43], a[108:111], v[168:171], v[40:43]
v_mfma_f32_16x16x32_bf16 v[44:47], a[104:107], v[164:167], v[44:47]
)"
R"(ds_read_b64_tr_b16 a[100:101], v232 offset:14336
ds_read_b64_tr_b16 a[102:103], v232 offset:20480
v_mfma_f32_16x16x32_bf16 v[44:47], a[108:111], v[172:175], v[44:47]
s_waitcnt lgkmcnt(4)
v_mfma_f32_16x16x32_bf16 v[48:51], a[112:115], v[160:163], v[48:51]
ds_read_b64_tr_b16 a[104:105], v233 offset:2048
ds_read_b64_tr_b16 a[106:107], v233 offset:8192
v_mfma_f32_16x16x32_bf16 v[48:51], a[116:119], v[168:171], v[48:51]
v_mfma_f32_16x16x32_bf16 v[52:55], a[112:115], v[164:167], v[52:55]
ds_read_b64_tr_b16 a[108:109], v233 offset:14336
ds_read_b64_tr_b16 a[110:111], v233 offset:20480
v_mfma_f32_16x16x32_bf16 v[52:55], a[116:119], v[172:175], v[52:55]
v_mfma_f32_16x16x32_bf16 v[56:59], a[120:123], v[160:163], v[56:59]
ds_read_b64_tr_b16 a[112:113], v234 offset:2048
ds_read_b64_tr_b16 a[114:115], v234 offset:8192
v_mfma_f32_16x16x32_bf16 v[56:59], a[124:127], v[168:171], v[56:59]
v_mfma_f32_16x16x32_bf16 v[60:63], a[120:123], v[164:167], v[60:63]
ds_read_b64_tr_b16 a[116:117], v234 offset:14336
ds_read_b64_tr_b16 a[118:119], v234 offset:20480
v_mfma_f32_16x16x32_bf16 v[60:63], a[124:127], v[172:175], v[60:63]
s_waitcnt lgkmcnt(4)
s_waitcnt vmcnt(16)
s_barrier
v_mfma_f32_16x16x32_bf16 v[64:67], a[96:99], v[160:163], v[64:67]
)"
R"(ds_read_b64_tr_b16 a[120:121], v235 offset:2048
ds_read_b64_tr_b16 a[122:123], v235 offset:8192
v_mfma_f32_16x16x32_bf16 v[64:67], a[100:103], v[168:171], v[64:67]
ds_read_b128 a[144:147], %[pX0] offset:16384
ds_read_b128 a[148:151], %[pX0] offset:18432
v_mfma_f32_16x16x32_bf16 v[68:71], a[96:99], v[164:167], v[68:71]
ds_read_b64_tr_b16 a[124:125], v235 offset:14336
ds_read_b64_tr_b16 a[126:127], v235 offset:20480
v_mfma_f32_16x16x32_bf16 v[68:71], a[100:103], v[172:175], v[68:71]
ds_read_b128 a[152:155], %[pX1] offset:16384
ds_read_b128 a[156:159], %[pX1] offset:18432
v_add_u32 v232, s48, %[pW]
v_xor_b32 v233, 32, v232
v_mfma_f32_16x16x32_bf16 v[72:75], a[104:107], v[160:163], v[72:75]
ds_read_b64_tr_b16 a[96:97], v232 offset:0
ds_read_b64_tr_b16 a[98:99], v232 offset:512
v_mfma_f32_16x16x32_bf16 v[72:75], a[108:111], v[168:171], v[72:75]
s_add_u32 s75, s67, 221184
v_xor_b32 v234, 64, v232
v_xor_b32 v235, 96, v232
v_mfma_f32_16x16x32_bf16 v[76:79], a[104:107], v[164:167], v[76:79]
s_add_u32 m0, s57, 0
ds_read_b64_tr_b16 a[100:101], v232 offset:12288
ds_read_b64_tr_b16 a[102:103], v232 offset:12800
)"
R"(v_mfma_f32_16x16x32_bf16 v[76:79], a[108:111], v[172:175], v[76:79]
s_waitcnt lgkmcnt(6)
buffer_load_dwordx4 %[vK], %[srdK], s75 offen offset:0 nt lds
v_mfma_f32_16x16x32_bf16 v[80:83], a[112:115], v[160:163], v[80:83]
ds_read_b64_tr_b16 a[104:105], v233 offset:0
ds_read_b64_tr_b16 a[106:107], v233 offset:512
v_mfma_f32_16x16x32_bf16 v[80:83], a[116:119], v[168:171], v[80:83]
v_mfma_f32_16x16x32_bf16 v[84:87], a[112:115], v[164:167], v[84:87]
s_add_u32 m0, s57, 896
ds_read_b64_tr_b16 a[108:109], v233 offset:12288
ds_read_b64_tr_b16 a[110:111], v233 offset:12800
v_mfma_f32_16x16x32_bf16 v[84:87], a[116:119], v[172:175], v[84:87]
buffer_load_dwordx4 %[vK], %[srdK], s75 offen offset:128 nt lds
v_mfma_f32_16x16x32_bf16 v[88:91], a[120:123], v[160:163], v[88:91]
ds_read_b64_tr_b16 a[112:113], v234 offset:0
ds_read_b64_tr_b16 a[114:115], v234 offset:512
v_mfma_f32_16x16x32_bf16 v[88:91], a[124:127], v[168:171], v[88:91]
v_mfma_f32_16x16x32_bf16 v[92:95], a[120:123], v[164:167], v[92:95]
s_add_u32 m0, s57, 1792
ds_read_b64_tr_b16 a[116:117], v234 offset:12288
ds_read_b64_tr_b16 a[118:119], v234 offset:12800
v_mfma_f32_16x16x32_bf16 v[92:95], a[124:127], v[172:175], v[92:95]
s_waitcnt lgkmcnt(4)
buffer_load_dwordx4 %[vK], %[srdK], s75 offen offset:256 nt lds
)"
R"(v_pk_mul_f32 v[0:1], v[0:1], v[238:239] op_sel_hi:[1,0]
v_pk_mul_f32 v[2:3], v[2:3], v[238:239] op_sel_hi:[1,0]
v_pk_mul_f32 v[4:5], v[4:5], v[240:241] op_sel_hi:[1,0]
v_pk_mul_f32 v[6:7], v[6:7], v[240:241] op_sel_hi:[1,0]
v_pk_mul_f32 v[8:9], v[8:9], v[238:239] op_sel_hi:[1,0]
v_pk_mul_f32 v[10:11], v[10:11], v[238:239] op_sel_hi:[1,0]
v_pk_mul_f32 v[12:13], v[12:13], v[240:241] op_sel_hi:[1,0]
v_pk_mul_f32 v[14:15], v[14:15], v[240:241] op_sel_hi:[1,0]
v_pk_mul_f32 v[16:17], v[16:17], v[238:239] op_sel_hi:[1,0]
v_pk_mul_f32 v[18:19], v[18:19], v[238:239] op_sel_hi:[1,0]
v_pk_mul_f32 v[20:21], v[20:21], v[240:241] op_sel_hi:[1,0]
v_pk_mul_f32 v[22:23], v[22:23], v[240:241] op_sel_hi:[1,0]
v_pk_mul_f32 v[24:25], v[24:25], v[238:239] op_sel_hi:[1,0]
v_pk_mul_f32 v[26:27], v[26:27], v[238:239] op_sel_hi:[1,0]
v_pk_mul_f32 v[28:29], v[28:29], v[240:241] op_sel_hi:[1,0]
v_pk_mul_f32 v[30:31], v[30:31], v[240:241] op_sel_hi:[1,0]
v_pk_mul_f32 v[32:33], v[32:33], v[238:239] op_sel_hi:[1,0]
v_pk_mul_f32 v[34:35], v[34:35], v[238:239] op_sel_hi:[1,0]
v_pk_mul_f32 v[36:37], v[36:37], v[240:241] op_sel_hi:[1,0]
v_pk_mul_f32 v[38:39], v[38:39], v[240:241] op_sel_hi:[1,0]
v_pk_mul_f32 v[40:41], v[40:41], v[238:239] op_sel_hi:[1,0]
v_pk_mul_f32 v[42:43], v[42:43], v[238:239] op_sel_hi:[1,0]
v_pk_mul_f32 v[44:45], v[44:45], v[240:241] op_sel_hi:[1,0]
v_pk_mul_f32 v[46:47], v[46:47], v[240:241] op_sel_hi:[1,0]
)"
R"(v_pk_mul_f32 v[48:49], v[48:49], v[238:239] op_sel_hi:[1,0]
v_pk_mul_f32 v[50:51], v[50:51], v[238:239] op_sel_hi:[1,0]
v_pk_mul_f32 v[52:53], v[52:53], v[240:241] op_sel_hi:[1,0]
v_pk_mul_f32 v[54:55], v[54:55], v[240:241] op_sel_hi:[1,0]
v_pk_mul_f32 v[56:57], v[56:57], v[238:239] op_sel_hi:[1,0]
v_pk_mul_f32 v[58:59], v[58:59], v[238:239] op_sel_hi:[1,0]
v_pk_mul_f32 v[60:61], v[60:61], v[240:241] op_sel_hi:[1,0]
v_pk_mul_f32 v[62:63], v[62:63], v[240:241] op_sel_hi:[1,0]
v_pk_mul_f32 v[64:65], v[64:65], v[238:239] op_sel_hi:[1,0]
v_pk_mul_f32 v[66:67], v[66:67], v[238:239] op_sel_hi:[1,0]
v_pk_mul_f32 v[68:69], v[68:69], v[240:241] op_sel_hi:[1,0]
v_pk_mul_f32 v[70:71], v[70:71], v[240:241] op_sel_hi:[1,0]
v_pk_mul_f32 v[72:73], v[72:73], v[238:239] op_sel_hi:[1,0]
v_pk_mul_f32 v[74:75], v[74:75], v[238:239] op_sel_hi:[1,0]
v_pk_mul_f32 v[76:77], v[76:77], v[240:241] op_sel_hi:[1,0]
v_pk_mul_f32 v[78:79], v[78:79], v[240:241] op_sel_hi:[1,0]
v_pk_mul_f32 v[80:81], v[80:81], v[238:239] op_sel_hi:[1,0]
v_pk_mul_f32 v[82:83], v[82:83], v[238:239] op_sel_hi:[1,0]
v_pk_mul_f32 v[84:85], v[84:85], v[240:241] op_sel_hi:[1,0]
v_pk_mul_f32 v[86:87], v[86:87], v[240:241] op_sel_hi:[1,0]
v_pk_mul_f32 v[88:89], v[88:89], v[238:239] op_sel_hi:[1,0]
v_pk_mul_f32 v[90:91], v[90:91], v[238:239] op_sel_hi:[1,0]
v_pk_mul_f32 v[92:93], v[92:93], v[240:241] op_sel_hi:[1,0]
v_pk_mul_f32 v[94:95], v[94:95], v[240:241] op_sel_hi:[1,0]
)"
R"(v_mfma_f32_16x16x32_bf16 a[160:163], a[96:99], a[144:147], a[160:163]
ds_read_b64_tr_b16 a[120:121], v235 offset:0
ds_read_b64_tr_b16 a[122:123], v235 offset:512
v_cvt_pk_bf16_f32 v128, v0, v1
v_mfma_f32_16x16x32_bf16 a[160:163], a[100:103], a[152:155], a[160:163]
s_add_u32 s75, s67, 233472
v_cvt_pk_bf16_f32 v129, v2, v3
v_cvt_pk_bf16_f32 v132, v4, v5
v_cvt_pk_bf16_f32 v133, v6, v7
v_mfma_f32_16x16x32_bf16 a[164:167], a[96:99], a[148:151], a[164:167]
s_add_u32 m0, s57, 12288
ds_read_b64_tr_b16 a[124:125], v235 offset:12288
ds_read_b64_tr_b16 a[126:127], v235 offset:12800
v_cvt_pk_bf16_f32 v130, v8, v9
v_mfma_f32_16x16x32_bf16 a[164:167], a[100:103], a[156:159], a[164:167]
buffer_load_dwordx4 %[vK], %[srdK], s75 offen offset:0 nt lds
v_cvt_pk_bf16_f32 v131, v10, v11
v_mfma_f32_16x16x32_bf16 a[168:171], a[104:107], a[144:147], a[168:171]
ds_read_b64_tr_b16 a[96:97], v232 offset:1024
ds_read_b64_tr_b16 a[98:99], v232 offset:1536
v_cvt_pk_bf16_f32 v136, v16, v17
v_mfma_f32_16x16x32_bf16 a[168:171], a[108:111], a[152:155], a[168:171]
v_cvt_pk_bf16_f32 v134, v12, v13
v_cvt_pk_bf16_f32 v135, v14, v15
)"
R"(v_cvt_pk_bf16_f32 v137, v18, v19
v_mfma_f32_16x16x32_bf16 a[172:175], a[104:107], a[148:151], a[172:175]
s_add_u32 m0, s57, 13184
ds_read_b64_tr_b16 a[100:101], v232 offset:13312
ds_read_b64_tr_b16 a[102:103], v232 offset:13824
v_cvt_pk_bf16_f32 v140, v20, v21
v_mfma_f32_16x16x32_bf16 a[172:175], a[108:111], a[156:159], a[172:175]
s_waitcnt lgkmcnt(4)
buffer_load_dwordx4 %[vK], %[srdK], s75 offen offset:128 nt lds
v_cvt_pk_bf16_f32 v141, v22, v23
v_mfma_f32_16x16x32_bf16 a[176:179], a[112:115], a[144:147], a[176:179]
ds_read_b64_tr_b16 a[104:105], v233 offset:1024
ds_read_b64_tr_b16 a[106:107], v233 offset:1536
v_cvt_pk_bf16_f32 v138, v24, v25
v_cvt_pk_bf16_f32 v144, v32, v33
v_mfma_f32_16x16x32_bf16 a[176:179], a[116:119], a[152:155], a[176:179]
v_cvt_pk_bf16_f32 v139, v26, v27
v_cvt_pk_bf16_f32 v142, v28, v29
v_cvt_pk_bf16_f32 v143, v30, v31
v_cvt_pk_bf16_f32 v145, v34, v35
v_mfma_f32_16x16x32_bf16 a[180:183], a[112:115], a[148:151], a[180:183]
s_add_u32 m0, s57, 14080
ds_read_b64_tr_b16 a[108:109], v233 offset:13312
ds_read_b64_tr_b16 a[110:111], v233 offset:13824
)"
R"(v_cvt_pk_bf16_f32 v148, v36, v37
v_mfma_f32_16x16x32_bf16 a[180:183], a[116:119], a[156:159], a[180:183]
buffer_load_dwordx4 %[vK], %[srdK], s75 offen offset:256 nt lds
v_cvt_pk_bf16_f32 v149, v38, v39
v_accvgpr_read_b32 v0, a160
v_accvgpr_read_b32 v1, a161
v_mfma_f32_16x16x32_bf16 a[184:187], a[120:123], a[144:147], a[184:187]
ds_read_b64_tr_b16 a[112:113], v234 offset:1024
ds_read_b64_tr_b16 a[114:115], v234 offset:1536
v_cvt_pk_bf16_f32 v146, v40, v41
v_cvt_pk_bf16_f32 v152, v48, v49
v_accvgpr_read_b32 v2, a162
v_accvgpr_read_b32 v3, a163
v_mfma_f32_16x16x32_bf16 a[184:187], a[124:127], a[152:155], a[184:187]
v_cvt_pk_bf16_f32 v147, v42, v43
v_cvt_pk_bf16_f32 v150, v44, v45
v_cvt_pk_bf16_f32 v151, v46, v47
v_cvt_pk_bf16_f32 v153, v50, v51
v_accvgpr_read_b32 v4, a164
v_accvgpr_read_b32 v5, a165
v_mfma_f32_16x16x32_bf16 a[188:191], a[120:123], a[148:151], a[188:191]
s_add_u32 m0, s73, 16256
ds_read_b64_tr_b16 a[116:117], v234 offset:13312
ds_read_b64_tr_b16 a[118:119], v234 offset:13824
)"
R"(v_cvt_pk_bf16_f32 v156, v52, v53
v_accvgpr_read_b32 v6, a166
v_accvgpr_read_b32 v7, a167
v_mfma_f32_16x16x32_bf16 a[188:191], a[124:127], a[156:159], a[188:191]
s_waitcnt lgkmcnt(4)
buffer_load_dwordx4 %[vX0], %[srdX], s69 offen offset:128 lds
v_cvt_pk_bf16_f32 v157, v54, v55
v_accvgpr_read_b32 v8, a168
v_accvgpr_read_b32 v9, a169
v_mfma_f32_16x16x32_bf16 a[192:195], a[96:99], a[144:147], a[192:195]
ds_read_b64_tr_b16 a[120:121], v235 offset:1024
ds_read_b64_tr_b16 a[122:123], v235 offset:1536
v_cvt_pk_bf16_f32 v154, v56, v57
v_cvt_pk_bf16_f32 v160, v64, v65
v_accvgpr_read_b32 v10, a170
v_accvgpr_read_b32 v11, a171
v_mfma_f32_16x16x32_bf16 a[192:195], a[100:103], a[152:155], a[192:195]
v_cvt_pk_bf16_f32 v155, v58, v59
v_cvt_pk_bf16_f32 v158, v60, v61
v_cvt_pk_bf16_f32 v159, v62, v63
v_cvt_pk_bf16_f32 v161, v66, v67
v_accvgpr_read_b32 v12, a172
v_accvgpr_read_b32 v13, a173
v_mfma_f32_16x16x32_bf16 a[196:199], a[96:99], a[148:151], a[196:199]
)"
R"(s_add_u32 m0, s73, 17280
ds_read_b64_tr_b16 a[124:125], v235 offset:13312
ds_read_b64_tr_b16 a[126:127], v235 offset:13824
v_cvt_pk_bf16_f32 v164, v68, v69
v_accvgpr_read_b32 v14, a174
v_accvgpr_read_b32 v15, a175
v_mfma_f32_16x16x32_bf16 a[196:199], a[100:103], a[156:159], a[196:199]
buffer_load_dwordx4 %[vX1], %[srdX], s70 offen offset:128 lds
v_cvt_pk_bf16_f32 v165, v70, v71
v_accvgpr_read_b32 v16, a176
v_accvgpr_read_b32 v17, a177
v_mfma_f32_16x16x32_bf16 a[200:203], a[104:107], a[144:147], a[200:203]
ds_read_b64_tr_b16 a[96:97], v232 offset:2048
ds_read_b64_tr_b16 a[98:99], v232 offset:2560
v_cvt_pk_bf16_f32 v162, v72, v73
v_cvt_pk_bf16_f32 v168, v80, v81
v_accvgpr_read_b32 v18, a178
v_accvgpr_read_b32 v19, a179
v_mfma_f32_16x16x32_bf16 a[200:203], a[108:111], a[152:155], a[200:203]
v_cvt_pk_bf16_f32 v163, v74, v75
v_cvt_pk_bf16_f32 v166, v76, v77
v_cvt_pk_bf16_f32 v167, v78, v79
v_cvt_pk_bf16_f32 v169, v82, v83
v_accvgpr_read_b32 v20, a180
)"
R"(v_accvgpr_read_b32 v21, a181
v_mfma_f32_16x16x32_bf16 a[204:207], a[104:107], a[148:151], a[204:207]
s_add_u32 m0, s73, 18304
ds_read_b64_tr_b16 a[100:101], v232 offset:14336
ds_read_b64_tr_b16 a[102:103], v232 offset:14848
v_cvt_pk_bf16_f32 v172, v84, v85
v_accvgpr_read_b32 v22, a182
v_accvgpr_read_b32 v23, a183
v_mfma_f32_16x16x32_bf16 a[204:207], a[108:111], a[156:159], a[204:207]
s_waitcnt lgkmcnt(4)
buffer_load_dwordx4 %[vX0], %[srdX], s71 offen offset:128 lds
v_cvt_pk_bf16_f32 v173, v86, v87
v_accvgpr_read_b32 v24, a184
v_accvgpr_read_b32 v25, a185
v_mfma_f32_16x16x32_bf16 a[208:211], a[112:115], a[144:147], a[208:211]
ds_read_b64_tr_b16 a[104:105], v233 offset:2048
ds_read_b64_tr_b16 a[106:107], v233 offset:2560
v_cvt_pk_bf16_f32 v170, v88, v89
v_accvgpr_read_b32 v26, a186
v_accvgpr_read_b32 v27, a187
v_mfma_f32_16x16x32_bf16 a[208:211], a[116:119], a[152:155], a[208:211]
v_cvt_pk_bf16_f32 v171, v90, v91
v_cvt_pk_bf16_f32 v174, v92, v93
v_cvt_pk_bf16_f32 v175, v94, v95
)"
R"(v_accvgpr_read_b32 v28, a188
v_accvgpr_read_b32 v29, a189
v_mfma_f32_16x16x32_bf16 a[212:215], a[112:115], a[148:151], a[212:215]
s_add_u32 m0, s73, 19328
ds_read_b64_tr_b16 a[108:109], v233 offset:14336
ds_read_b64_tr_b16 a[110:111], v233 offset:14848
v_accvgpr_read_b32 v30, a190
v_accvgpr_read_b32 v31, a191
v_mfma_f32_16x16x32_bf16 a[212:215], a[116:119], a[156:159], a[212:215]
buffer_load_dwordx4 %[vX1], %[srdX], s72 offen offset:128 lds
v_accvgpr_read_b32 v32, a192
v_accvgpr_read_b32 v33, a193
v_mfma_f32_16x16x32_bf16 a[216:219], a[120:123], a[144:147], a[216:219]
ds_read_b64_tr_b16 a[112:113], v234 offset:2048
ds_read_b64_tr_b16 a[114:115], v234 offset:2560
v_accvgpr_read_b32 v34, a194
v_accvgpr_read_b32 v35, a195
v_mfma_f32_16x16x32_bf16 a[216:219], a[124:127], a[152:155], a[216:219]
v_accvgpr_read_b32 v36, a196
v_accvgpr_read_b32 v37, a197
v_accvgpr_read_b32 v38, a198
v_accvgpr_read_b32 v39, a199
v_mfma_f32_16x16x32_bf16 a[220:223], a[120:123], a[148:151], a[220:223]
s_add_u32 s76, s68, 196608
)"
R"(ds_read_b64_tr_b16 a[116:117], v234 offset:14336
ds_read_b64_tr_b16 a[118:119], v234 offset:14848
v_accvgpr_read_b32 v40, a200
v_accvgpr_read_b32 v41, a201
v_mfma_f32_16x16x32_bf16 a[220:223], a[124:127], a[156:159], a[220:223]
s_waitcnt lgkmcnt(4)
s_waitcnt vmcnt(20)
s_barrier
v_accvgpr_read_b32 v42, a202
v_accvgpr_read_b32 v43, a203
v_mfma_f32_16x16x32_bf16 a[224:227], a[96:99], a[144:147], a[224:227]
s_add_u32 m0, s64, 0
ds_read_b64_tr_b16 a[120:121], v235 offset:2048
ds_read_b64_tr_b16 a[122:123], v235 offset:2560
v_accvgpr_read_b32 v44, a204
v_accvgpr_read_b32 v45, a205
v_mfma_f32_16x16x32_bf16 a[224:227], a[100:103], a[152:155], a[224:227]
buffer_load_dwordx4 %[vV], %[srdV], s76 offen offset:0 nt lds
v_accvgpr_read_b32 v46, a206
v_mfma_f32_16x16x32_bf16 a[228:231], a[96:99], a[148:151], a[228:231]
ds_read_b64_tr_b16 a[124:125], v235 offset:14336
ds_read_b64_tr_b16 a[126:127], v235 offset:14848
v_accvgpr_read_b32 v47, a207
v_mfma_f32_16x16x32_bf16 a[228:231], a[100:103], a[156:159], a[228:231]
)"
R"(v_add_u32 v232, s49, %[pK]
v_xor_b32 v233, 64, v232
v_accvgpr_read_b32 v48, a208
v_mfma_f32_16x16x32_bf16 a[232:235], a[104:107], a[144:147], a[232:235]
s_add_u32 m0, s64, 896
ds_read_b128 a[96:99], v232 offset:0
ds_read_b128 a[100:103], v233 offset:0
v_accvgpr_read_b32 v49, a209
v_mfma_f32_16x16x32_bf16 a[232:235], a[108:111], a[152:155], a[232:235]
buffer_load_dwordx4 %[vV], %[srdV], s76 offen offset:128 nt lds
v_accvgpr_read_b32 v50, a210
v_mfma_f32_16x16x32_bf16 a[236:239], a[104:107], a[148:151], a[236:239]
v_accvgpr_read_b32 v51, a211
v_accvgpr_read_b32 v52, a212
v_accvgpr_read_b32 v53, a213
v_mfma_f32_16x16x32_bf16 a[236:239], a[108:111], a[156:159], a[236:239]
s_waitcnt lgkmcnt(2)
v_accvgpr_read_b32 v54, a214
v_accvgpr_read_b32 v55, a215
v_accvgpr_read_b32 v56, a216
v_mfma_f32_16x16x32_bf16 a[240:243], a[112:115], a[144:147], a[240:243]
ds_read_b128 a[104:107], v232 offset:1024
ds_read_b128 a[108:111], v233 offset:1024
v_accvgpr_read_b32 v57, a217
)"
R"(v_mfma_f32_16x16x32_bf16 a[240:243], a[116:119], a[152:155], a[240:243]
v_accvgpr_read_b32 v58, a218
v_accvgpr_read_b32 v59, a219
v_accvgpr_read_b32 v60, a220
v_mfma_f32_16x16x32_bf16 a[244:247], a[112:115], a[148:151], a[244:247]
v_accvgpr_read_b32 v61, a221
v_accvgpr_read_b32 v62, a222
v_accvgpr_read_b32 v63, a223
v_mfma_f32_16x16x32_bf16 a[244:247], a[116:119], a[156:159], a[244:247]
v_mfma_f32_16x16x32_bf16 a[248:251], a[120:123], a[144:147], a[248:251]
ds_read_b128 a[112:115], v232 offset:2048
ds_read_b128 a[116:119], v233 offset:2048
v_mfma_f32_16x16x32_bf16 a[248:251], a[124:127], a[152:155], a[248:251]
v_mfma_f32_16x16x32_bf16 a[252:255], a[120:123], a[148:151], a[252:255]
v_mfma_f32_16x16x32_bf16 a[252:255], a[124:127], a[156:159], a[252:255]
v_bfe_u32 v236, %[vP], 4, 6
v_min_u32 v236, 29, v236
v_mul_u32_u24 v237, 43, v236
v_lshrrev_b32 v237, 7, v237
v_lshlrev_b32 v236, 7, v236
v_mul_u32_u24 v237, 11904, v237
v_add_u32 v236, v236, v237
s_cmp_eq_u32 s74, 0
s_cselect_b32 s46, s85, 0x80000000
)"
R"(s_mov_b32 m0, s84
s_nop 0
buffer_load_dword v236, %[srdW], s46 offen offset:1152 lds
s_cmp_eq_u32 s74, 2
s_cselect_b32 s46, s83, 0x80000000
s_cselect_b32 s47, %[po2], 0x80000000
s_mov_b32 m0, s84
s_nop 0
buffer_load_dword %[vWo], %[srdWo], s46 offen offset:1024 lds
buffer_load_dword %[vWo], %[srdWo], s46 offen offset:1152 lds
buffer_load_dword %[vWo], %[srdWo], s47 offen offset:1024 lds
v_accvgpr_read_b32 v64, a224
v_accvgpr_read_b32 v65, a225
v_accvgpr_read_b32 v66, a226
v_accvgpr_read_b32 v67, a227
v_accvgpr_read_b32 v68, a228
v_accvgpr_read_b32 v69, a229
v_accvgpr_read_b32 v70, a230
v_accvgpr_read_b32 v71, a231
v_accvgpr_read_b32 v72, a232
v_accvgpr_read_b32 v73, a233
v_accvgpr_read_b32 v74, a234
v_accvgpr_read_b32 v75, a235
v_accvgpr_read_b32 v76, a236
)"
R"(v_accvgpr_read_b32 v77, a237
v_accvgpr_read_b32 v78, a238
v_accvgpr_read_b32 v79, a239
v_accvgpr_read_b32 v80, a240
v_accvgpr_read_b32 v81, a241
v_accvgpr_read_b32 v82, a242
v_accvgpr_read_b32 v83, a243
v_accvgpr_read_b32 v84, a244
v_accvgpr_read_b32 v85, a245
v_accvgpr_read_b32 v86, a246
v_accvgpr_read_b32 v87, a247
v_accvgpr_read_b32 v88, a248
v_accvgpr_read_b32 v89, a249
v_accvgpr_read_b32 v90, a250
v_accvgpr_read_b32 v91, a251
v_accvgpr_read_b32 v92, a252
v_accvgpr_read_b32 v93, a253
v_accvgpr_read_b32 v94, a254
v_accvgpr_read_b32 v95, a255
v_pk_mul_f32 v[0:1], v[0:1], s[80:81]
v_pk_mul_f32 v[2:3], v[2:3], s[80:81]
v_pk_mul_f32 v[4:5], v[4:5], s[80:81]
v_pk_mul_f32 v[6:7], v[6:7], s[80:81]
v_pk_mul_f32 v[8:9], v[8:9], s[80:81]
)"
R"(v_pk_mul_f32 v[10:11], v[10:11], s[80:81]
v_pk_mul_f32 v[12:13], v[12:13], s[80:81]
v_pk_mul_f32 v[14:15], v[14:15], s[80:81]
v_pk_mul_f32 v[16:17], v[16:17], s[80:81]
v_pk_mul_f32 v[18:19], v[18:19], s[80:81]
v_pk_mul_f32 v[20:21], v[20:21], s[80:81]
v_pk_mul_f32 v[22:23], v[22:23], s[80:81]
v_pk_mul_f32 v[24:25], v[24:25], s[80:81]
v_pk_mul_f32 v[26:27], v[26:27], s[80:81]
v_pk_mul_f32 v[28:29], v[28:29], s[80:81]
v_pk_mul_f32 v[30:31], v[30:31], s[80:81]
v_pk_mul_f32 v[32:33], v[32:33], s[80:81]
v_pk_mul_f32 v[34:35], v[34:35], s[80:81]
v_pk_mul_f32 v[36:37], v[36:37], s[80:81]
v_pk_mul_f32 v[38:39], v[38:39], s[80:81]
v_pk_mul_f32 v[40:41], v[40:41], s[80:81]
v_pk_mul_f32 v[42:43], v[42:43], s[80:81]
v_pk_mul_f32 v[44:45], v[44:45], s[80:81]
v_pk_mul_f32 v[46:47], v[46:47], s[80:81]
v_pk_mul_f32 v[48:49], v[48:49], s[80:81]
v_pk_mul_f32 v[50:51], v[50:51], s[80:81]
v_pk_mul_f32 v[52:53], v[52:53], s[80:81]
v_pk_mul_f32 v[54:55], v[54:55], s[80:81]
v_pk_mul_f32 v[56:57], v[56:57], s[80:81]
)"
R"(v_pk_mul_f32 v[58:59], v[58:59], s[80:81]
v_pk_mul_f32 v[60:61], v[60:61], s[80:81]
v_pk_mul_f32 v[62:63], v[62:63], s[80:81]
v_pk_mul_f32 v[64:65], v[64:65], s[80:81]
v_pk_mul_f32 v[66:67], v[66:67], s[80:81]
v_pk_mul_f32 v[68:69], v[68:69], s[80:81]
v_pk_mul_f32 v[70:71], v[70:71], s[80:81]
v_pk_mul_f32 v[72:73], v[72:73], s[80:81]
v_pk_mul_f32 v[74:75], v[74:75], s[80:81]
v_pk_mul_f32 v[76:77], v[76:77], s[80:81]
v_pk_mul_f32 v[78:79], v[78:79], s[80:81]
v_pk_mul_f32 v[80:81], v[80:81], s[80:81]
v_pk_mul_f32 v[82:83], v[82:83], s[80:81]
v_pk_mul_f32 v[84:85], v[84:85], s[80:81]
v_pk_mul_f32 v[86:87], v[86:87], s[80:81]
v_pk_mul_f32 v[88:89], v[88:89], s[80:81]
v_pk_mul_f32 v[90:91], v[90:91], s[80:81]
v_pk_mul_f32 v[92:93], v[92:93], s[80:81]
v_pk_mul_f32 v[94:95], v[94:95], s[80:81]
v_cvt_pk_bf16_f32 v184, v0, v1
v_cvt_pk_bf16_f32 v185, v2, v3
v_cvt_pk_bf16_f32 v186, v8, v9
v_cvt_pk_bf16_f32 v187, v10, v11
v_cvt_pk_bf16_f32 v188, v4, v5
)"
R"(v_cvt_pk_bf16_f32 v189, v6, v7
v_cvt_pk_bf16_f32 v190, v12, v13
v_cvt_pk_bf16_f32 v191, v14, v15
v_permlane16_swap_b32 v184, v186
v_permlane16_swap_b32 v185, v187
v_cvt_pk_bf16_f32 v192, v16, v17
v_cvt_pk_bf16_f32 v193, v18, v19
v_cvt_pk_bf16_f32 v194, v24, v25
v_cvt_pk_bf16_f32 v195, v26, v27
v_permlane16_swap_b32 v188, v190
v_permlane16_swap_b32 v189, v191
v_cvt_pk_bf16_f32 v196, v20, v21
v_cvt_pk_bf16_f32 v197, v22, v23
v_cvt_pk_bf16_f32 v198, v28, v29
v_cvt_pk_bf16_f32 v199, v30, v31
v_permlane16_swap_b32 v192, v194
v_permlane16_swap_b32 v193, v195
v_cvt_pk_bf16_f32 v200, v32, v33
v_cvt_pk_bf16_f32 v201, v34, v35
v_cvt_pk_bf16_f32 v202, v40, v41
v_cvt_pk_bf16_f32 v203, v42, v43
v_permlane16_swap_b32 v196, v198
v_permlane16_swap_b32 v197, v199
v_cvt_pk_bf16_f32 v204, v36, v37
)"
R"(v_cvt_pk_bf16_f32 v205, v38, v39
v_cvt_pk_bf16_f32 v206, v44, v45
v_cvt_pk_bf16_f32 v207, v46, v47
v_permlane16_swap_b32 v200, v202
v_permlane16_swap_b32 v201, v203
v_cvt_pk_bf16_f32 v208, v48, v49
v_cvt_pk_bf16_f32 v209, v50, v51
v_cvt_pk_bf16_f32 v210, v56, v57
v_cvt_pk_bf16_f32 v211, v58, v59
v_permlane16_swap_b32 v204, v206
v_permlane16_swap_b32 v205, v207
v_cvt_pk_bf16_f32 v212, v52, v53
v_cvt_pk_bf16_f32 v213, v54, v55
v_cvt_pk_bf16_f32 v214, v60, v61
v_cvt_pk_bf16_f32 v215, v62, v63
v_permlane16_swap_b32 v208, v210
v_permlane16_swap_b32 v209, v211
v_cvt_pk_bf16_f32 v216, v64, v65
v_cvt_pk_bf16_f32 v217, v66, v67
v_cvt_pk_bf16_f32 v218, v72, v73
v_cvt_pk_bf16_f32 v219, v74, v75
v_permlane16_swap_b32 v212, v214
v_permlane16_swap_b32 v213, v215
v_cvt_pk_bf16_f32 v220, v68, v69
)"
R"(v_cvt_pk_bf16_f32 v221, v70, v71
v_cvt_pk_bf16_f32 v222, v76, v77
v_cvt_pk_bf16_f32 v223, v78, v79
v_permlane16_swap_b32 v216, v218
v_permlane16_swap_b32 v217, v219
v_cvt_pk_bf16_f32 v224, v80, v81
v_cvt_pk_bf16_f32 v225, v82, v83
v_cvt_pk_bf16_f32 v226, v88, v89
v_cvt_pk_bf16_f32 v227, v90, v91
v_permlane16_swap_b32 v220, v222
v_permlane16_swap_b32 v221, v223
v_cvt_pk_bf16_f32 v228, v84, v85
v_cvt_pk_bf16_f32 v229, v86, v87
v_cvt_pk_bf16_f32 v230, v92, v93
v_cvt_pk_bf16_f32 v231, v94, v95
v_permlane16_swap_b32 v224, v226
v_permlane16_swap_b32 v225, v227
v_permlane16_swap_b32 v228, v230
v_permlane16_swap_b32 v229, v231
s_cmp_lg_u32 s74, 0
s_cbranch_scc1 PK0_%=
v_accvgpr_write_b32 a0, v128
v_accvgpr_write_b32 a1, v129
v_accvgpr_write_b32 a2, v130
)"
R"(v_accvgpr_write_b32 a3, v131
v_accvgpr_write_b32 a4, v132
v_accvgpr_write_b32 a5, v133
v_accvgpr_write_b32 a6, v134
v_accvgpr_write_b32 a7, v135
v_accvgpr_write_b32 a8, v136
v_accvgpr_write_b32 a9, v137
v_accvgpr_write_b32 a10, v138
v_accvgpr_write_b32 a11, v139
v_accvgpr_write_b32 a12, v140
v_accvgpr_write_b32 a13, v141
v_accvgpr_write_b32 a14, v142
v_accvgpr_write_b32 a15, v143
v_accvgpr_write_b32 a16, v144
v_accvgpr_write_b32 a17, v145
v_accvgpr_write_b32 a18, v146
v_accvgpr_write_b32 a19, v147
v_accvgpr_write_b32 a20, v148
v_accvgpr_write_b32 a21, v149
v_accvgpr_write_b32 a22, v150
v_accvgpr_write_b32 a23, v151
v_accvgpr_write_b32 a24, v152
v_accvgpr_write_b32 a25, v153
v_accvgpr_write_b32 a26, v154
)"
R"(v_accvgpr_write_b32 a27, v155
v_accvgpr_write_b32 a28, v156
v_accvgpr_write_b32 a29, v157
v_accvgpr_write_b32 a30, v158
v_accvgpr_write_b32 a31, v159
v_accvgpr_write_b32 a32, v160
v_accvgpr_write_b32 a33, v161
v_accvgpr_write_b32 a34, v162
v_accvgpr_write_b32 a35, v163
v_accvgpr_write_b32 a36, v164
v_accvgpr_write_b32 a37, v165
v_accvgpr_write_b32 a38, v166
v_accvgpr_write_b32 a39, v167
v_accvgpr_write_b32 a40, v168
v_accvgpr_write_b32 a41, v169
v_accvgpr_write_b32 a42, v170
v_accvgpr_write_b32 a43, v171
v_accvgpr_write_b32 a44, v172
v_accvgpr_write_b32 a45, v173
v_accvgpr_write_b32 a46, v174
v_accvgpr_write_b32 a47, v175
PK0_%=:
s_cmp_lg_u32 s74, 1
s_cbranch_scc1 PK1_%=
)"
R"(v_accvgpr_write_b32 a48, v128
v_accvgpr_write_b32 a49, v129
v_accvgpr_write_b32 a50, v130
v_accvgpr_write_b32 a51, v131
v_accvgpr_write_b32 a52, v132
v_accvgpr_write_b32 a53, v133
v_accvgpr_write_b32 a54, v134
v_accvgpr_write_b32 a55, v135
v_accvgpr_write_b32 a56, v136
v_accvgpr_write_b32 a57, v137
v_accvgpr_write_b32 a58, v138
v_accvgpr_write_b32 a59, v139
v_accvgpr_write_b32 a60, v140
v_accvgpr_write_b32 a61, v141
v_accvgpr_write_b32 a62, v142
v_accvgpr_write_b32 a63, v143
v_accvgpr_write_b32 a64, v144
v_accvgpr_write_b32 a65, v145
v_accvgpr_write_b32 a66, v146
v_accvgpr_write_b32 a67, v147
v_accvgpr_write_b32 a68, v148
v_accvgpr_write_b32 a69, v149
v_accvgpr_write_b32 a70, v150
v_accvgpr_write_b32 a71, v151
)"
R"(v_accvgpr_write_b32 a72, v152
v_accvgpr_write_b32 a73, v153
v_accvgpr_write_b32 a74, v154
v_accvgpr_write_b32 a75, v155
v_accvgpr_write_b32 a76, v156
v_accvgpr_write_b32 a77, v157
v_accvgpr_write_b32 a78, v158
v_accvgpr_write_b32 a79, v159
v_accvgpr_write_b32 a80, v160
v_accvgpr_write_b32 a81, v161
v_accvgpr_write_b32 a82, v162
v_accvgpr_write_b32 a83, v163
v_accvgpr_write_b32 a84, v164
v_accvgpr_write_b32 a85, v165
v_accvgpr_write_b32 a86, v166
v_accvgpr_write_b32 a87, v167
v_accvgpr_write_b32 a88, v168
v_accvgpr_write_b32 a89, v169
v_accvgpr_write_b32 a90, v170
v_accvgpr_write_b32 a91, v171
v_accvgpr_write_b32 a92, v172
v_accvgpr_write_b32 a93, v173
v_accvgpr_write_b32 a94, v174
v_accvgpr_write_b32 a95, v175
)"
R"(PK1_%=:
s_cmp_lg_u32 s74, 2
s_cbranch_scc1 PK2_%=
v_accvgpr_write_b32 a140, v128
v_accvgpr_write_b32 a141, v129
v_accvgpr_write_b32 a142, v130
v_accvgpr_write_b32 a143, v131
v_accvgpr_write_b32 a144, v132
v_accvgpr_write_b32 a145, v133
v_accvgpr_write_b32 a146, v134
v_accvgpr_write_b32 a147, v135
v_accvgpr_write_b32 a148, v136
v_accvgpr_write_b32 a149, v137
v_accvgpr_write_b32 a150, v138
v_accvgpr_write_b32 a151, v139
v_accvgpr_write_b32 a152, v140
v_accvgpr_write_b32 a153, v141
v_accvgpr_write_b32 a154, v142
v_accvgpr_write_b32 a155, v143
v_accvgpr_write_b32 a156, v144
v_accvgpr_write_b32 a157, v145
v_accvgpr_write_b32 a158, v146
v_accvgpr_write_b32 a159, v147
v_accvgpr_write_b32 a160, v148
)"
R"(v_accvgpr_write_b32 a161, v149
v_accvgpr_write_b32 a162, v150
v_accvgpr_write_b32 a163, v151
v_accvgpr_write_b32 a164, v152
v_accvgpr_write_b32 a165, v153
v_accvgpr_write_b32 a166, v154
v_accvgpr_write_b32 a167, v155
v_accvgpr_write_b32 a168, v156
v_accvgpr_write_b32 a169, v157
v_accvgpr_write_b32 a170, v158
v_accvgpr_write_b32 a171, v159
v_accvgpr_write_b32 a172, v160
v_accvgpr_write_b32 a173, v161
v_accvgpr_write_b32 a174, v162
v_accvgpr_write_b32 a175, v163
v_accvgpr_write_b32 a176, v164
v_accvgpr_write_b32 a177, v165
v_accvgpr_write_b32 a178, v166
v_accvgpr_write_b32 a179, v167
v_accvgpr_write_b32 a180, v168
v_accvgpr_write_b32 a181, v169
v_accvgpr_write_b32 a182, v170
v_accvgpr_write_b32 a183, v171
v_accvgpr_write_b32 a184, v172
)"
R"(v_accvgpr_write_b32 a185, v173
v_accvgpr_write_b32 a186, v174
v_accvgpr_write_b32 a187, v175
PK2_%=:
s_mov_b32 s79, s48
s_mov_b32 s48, s49
s_mov_b32 s49, s50
s_mov_b32 s50, s51
s_mov_b32 s51, s52
s_mov_b32 s52, s79
s_mov_b32 s79, s54
s_mov_b32 s54, s55
s_mov_b32 s55, s56
s_mov_b32 s56, s57
s_mov_b32 s57, s58
s_mov_b32 s58, s79
s_mov_b32 s79, s60
s_mov_b32 s60, s61
s_mov_b32 s61, s62
s_mov_b32 s62, s63
s_mov_b32 s63, s64
s_mov_b32 s64, s79
s_add_u32 s66, s66, 384
s_add_u32 s67, s67, 196608
)"
R"(s_add_u32 s68, s68, 196608
s_add_u32 s74, s74, 1
s_cmp_lg_u32 s74, 3
s_cbranch_scc1 HEAD_%=
v_add_u32 %[vX1], 10240, %[vWo]
v_add_u32 %[vW], 20480, %[vWo]
s_add_u32 s66, %[hgi], 1
s_and_b32 s66, s66, 7
s_lshl_b32 s66, s66, 7
s_add_u32 s66, s66, s78
v_bfe_u32 %[vX0], %[vP], 7, 2
v_mul_u32_u24 %[vX0], 1280, %[vX0]
v_bfe_u32 v236, %[vP], 4, 3
v_lshl_add_u32 %[vX0], v236, 7, %[vX0]
s_mul_i32 s46, %[bq], 15360
s_waitcnt lgkmcnt(3)
v_mfma_f32_16x16x32_bf16 v[96:99], a[96:99], v[184:187], 0
ds_read_b128 a[120:123], v232 offset:6144
ds_read_b128 a[124:127], v233 offset:6144
v_mfma_f32_16x16x32_bf16 v[96:99], a[100:103], v[192:195], v[96:99]
v_mfma_f32_16x16x32_bf16 v[96:99], a[104:107], v[200:203], v[96:99]
s_add_u32 m0, s63, 1792
v_mfma_f32_16x16x32_bf16 v[100:103], a[96:99], v[188:191], 0
buffer_load_dwordx4 %[vV], %[srdV], s76 offen offset:256 nt lds
)"
R"(v_mfma_f32_16x16x32_bf16 v[100:103], a[100:103], v[196:199], v[100:103]
ds_read_b128 a[96:99], v232 offset:7168
ds_read_b128 a[100:103], v233 offset:7168
v_mfma_f32_16x16x32_bf16 v[100:103], a[104:107], v[204:207], v[100:103]
s_waitcnt lgkmcnt(4)
v_mfma_f32_16x16x32_bf16 v[96:99], a[108:111], v[208:211], v[96:99]
s_add_u32 m0, s63, 0
v_mfma_f32_16x16x32_bf16 v[96:99], a[112:115], v[216:219], v[96:99]
buffer_load_dwordx4 %[vV], %[srdV], s76 offen offset:3072 nt lds
v_mfma_f32_16x16x32_bf16 v[96:99], a[116:119], v[224:227], v[96:99]
v_mfma_f32_16x16x32_bf16 v[100:103], a[108:111], v[212:215], v[100:103]
ds_read_b128 a[104:107], v232 offset:8192
ds_read_b128 a[108:111], v233 offset:8192
v_mfma_f32_16x16x32_bf16 v[100:103], a[112:115], v[220:223], v[100:103]
s_add_u32 m0, s63, 896
v_mfma_f32_16x16x32_bf16 v[100:103], a[116:119], v[228:231], v[100:103]
s_waitcnt lgkmcnt(3)
buffer_load_dwordx4 %[vV], %[srdV], s76 offen offset:3200 nt lds
v_mfma_f32_16x16x32_bf16 v[104:107], a[120:123], v[184:187], 0
ds_read_b128 a[112:115], v232 offset:12288
ds_read_b128 a[116:119], v233 offset:12288
v_mfma_f32_16x16x32_bf16 v[104:107], a[124:127], v[192:195], v[104:107]
v_mfma_f32_16x16x32_bf16 v[104:107], a[96:99], v[200:203], v[104:107]
s_add_u32 m0, s63, 1792
)"
R"(v_max3_f32 v238, v96, v97, v98
v_max3_f32 v240, v100, v101, v102
v_max_f32 v238, v238, v99
v_max_f32 v240, v240, v103
v_mfma_f32_16x16x32_bf16 v[108:111], a[120:123], v[188:191], 0
buffer_load_dwordx4 %[vV], %[srdV], s76 offen offset:3328 nt lds
v_mov_b32 v239, v238
v_mov_b32 v241, v240
v_mfma_f32_16x16x32_bf16 v[108:111], a[124:127], v[196:199], v[108:111]
ds_read_b128 a[120:123], v232 offset:13312
ds_read_b128 a[124:127], v233 offset:13312
v_permlane16_swap_b32 v238, v239
v_mfma_f32_16x16x32_bf16 v[108:111], a[96:99], v[204:207], v[108:111]
s_waitcnt lgkmcnt(4)
v_permlane16_swap_b32 v240, v241
v_max_f32 v238, v238, v239
v_max_f32 v240, v240, v241
v_mfma_f32_16x16x32_bf16 v[104:107], a[100:103], v[208:211], v[104:107]
v_mov_b32 v239, v238
v_mov_b32 v241, v240
s_nop 0
v_permlane32_swap_b32 v238, v239
v_mfma_f32_16x16x32_bf16 v[104:107], a[104:107], v[216:219], v[104:107]
v_permlane32_swap_b32 v240, v241
)"
R"(v_max_f32 v238, v238, v239
v_max_f32 v240, v240, v241
v_mfma_f32_16x16x32_bf16 v[104:107], a[108:111], v[224:227], v[104:107]
v_sub_f32 v176, 0, v238
v_sub_f32 v180, 0, v240
v_sub_f32 v177, 0, v238
v_sub_f32 v181, 0, v240
v_mfma_f32_16x16x32_bf16 v[108:111], a[100:103], v[212:215], v[108:111]
ds_read_b128 a[96:99], v232 offset:14336
ds_read_b128 a[100:103], v233 offset:14336
v_sub_f32 v178, 0, v238
v_mfma_f32_16x16x32_bf16 v[108:111], a[104:107], v[220:223], v[108:111]
v_sub_f32 v182, 0, v240
v_sub_f32 v179, 0, v238
v_sub_f32 v183, 0, v240
v_mfma_f32_16x16x32_bf16 v[108:111], a[108:111], v[228:231], v[108:111]
s_waitcnt lgkmcnt(3)
v_mfma_f32_16x16x32_bf16 v[112:115], a[112:115], v[184:187], v[176:179]
ds_read_b128 a[104:107], v232 offset:18432
ds_read_b128 a[108:111], v233 offset:18432
v_mfma_f32_16x16x32_bf16 v[112:115], a[116:119], v[192:195], v[112:115]
v_mfma_f32_16x16x32_bf16 v[112:115], a[120:123], v[200:203], v[112:115]
v_mfma_f32_16x16x32_bf16 v[116:119], a[112:115], v[188:191], v[180:183]
v_add_f32 v96, v176, v96
)"
R"(v_add_f32 v97, v176, v97
v_add_f32 v98, v176, v98
v_mfma_f32_16x16x32_bf16 v[116:119], a[116:119], v[196:199], v[116:119]
ds_read_b128 a[112:115], v232 offset:19456
ds_read_b128 a[116:119], v233 offset:19456
v_add_f32 v99, v176, v99
v_mfma_f32_16x16x32_bf16 v[116:119], a[120:123], v[204:207], v[116:119]
s_waitcnt lgkmcnt(4)
v_add_f32 v100, v180, v100
v_add_f32 v101, v180, v101
v_add_f32 v102, v180, v102
v_mfma_f32_16x16x32_bf16 v[112:115], a[124:127], v[208:211], v[112:115]
v_add_f32 v103, v180, v103
v_add_f32 v104, v176, v104
v_add_f32 v105, v176, v105
v_mfma_f32_16x16x32_bf16 v[112:115], a[96:99], v[216:219], v[112:115]
s_waitcnt vmcnt(10)
s_barrier
v_add_f32 v106, v176, v106
v_mfma_f32_16x16x32_bf16 v[112:115], a[100:103], v[224:227], v[112:115]
s_add_u32 s75, s78, 1024
v_add_f32 v107, v176, v107
v_add_f32 v108, v180, v108
v_add_f32 v109, v180, v109
)"
R"(v_mfma_f32_16x16x32_bf16 v[116:119], a[124:127], v[212:215], v[116:119]
s_add_u32 m0, s64, 0
ds_read_b128 a[120:123], v232 offset:20480
ds_read_b128 a[124:127], v233 offset:20480
v_add_f32 v110, v180, v110
v_mfma_f32_16x16x32_bf16 v[116:119], a[96:99], v[220:223], v[116:119]
buffer_load_dwordx4 %[vWo], %[srdWo], s75 offen offset:0 lds
v_add_f32 v111, v180, v111
v_mfma_f32_16x16x32_bf16 v[116:119], a[100:103], v[228:231], v[116:119]
s_waitcnt lgkmcnt(3)
v_add_u32 v232, s50, %[pK]
v_xor_b32 v233, 64, v232
v_max3_f32 v236, v96, v97, v98
v_mfma_f32_16x16x32_bf16 v[120:123], a[104:107], v[184:187], v[176:179]
ds_read_b128 a[96:99], v232 offset:0
ds_read_b128 a[100:103], v233 offset:0
v_max3_f32 v237, v100, v101, v102
v_mfma_f32_16x16x32_bf16 v[120:123], a[108:111], v[192:195], v[120:123]
s_add_u32 m0, s64, 1024
v_max_f32 v236, v236, v99
v_max_f32 v237, v237, v103
v_max3_f32 v236, v236, v104, v105
v_mfma_f32_16x16x32_bf16 v[120:123], a[112:115], v[200:203], v[120:123]
buffer_load_dwordx4 %[vX1], %[srdWo], s75 offen offset:0 lds
)"
R"(v_max3_f32 v237, v237, v108, v109
v_mfma_f32_16x16x32_bf16 v[124:127], a[104:107], v[188:191], v[180:183]
v_max3_f32 v236, v236, v106, v107
v_max3_f32 v237, v237, v110, v111
v_mfma_f32_16x16x32_bf16 v[124:127], a[108:111], v[196:199], v[124:127]
s_add_u32 m0, s64, 2048
ds_read_b128 a[104:107], v232 offset:1024
ds_read_b128 a[108:111], v233 offset:1024
v_mfma_f32_16x16x32_bf16 v[124:127], a[112:115], v[204:207], v[124:127]
s_waitcnt lgkmcnt(4)
v_max3_f32 v236, v236, v112, v113
v_max3_f32 v237, v237, v116, v117
v_mfma_f32_16x16x32_bf16 v[120:123], a[116:119], v[208:211], v[120:123]
buffer_load_dwordx4 %[vW], %[srdWo], s75 offen offset:0 lds
v_mfma_f32_16x16x32_bf16 v[120:123], a[120:123], v[216:219], v[120:123]
v_max3_f32 v236, v236, v114, v115
v_max3_f32 v237, v237, v118, v119
v_mfma_f32_16x16x32_bf16 v[120:123], a[124:127], v[224:227], v[120:123]
s_add_u32 s75, s78, 31744
v_mfma_f32_16x16x32_bf16 v[124:127], a[116:119], v[212:215], v[124:127]
s_add_u32 m0, s64, 3072
ds_read_b128 a[112:115], v232 offset:2048
ds_read_b128 a[116:119], v233 offset:2048
v_mfma_f32_16x16x32_bf16 v[124:127], a[120:123], v[220:223], v[124:127]
)"
R"(buffer_load_dwordx4 %[vWo], %[srdWo], s75 offen offset:0 lds
v_mfma_f32_16x16x32_bf16 v[124:127], a[124:127], v[228:231], v[124:127]
s_waitcnt lgkmcnt(3)
v_max3_f32 v236, v236, v120, v121
s_nop 5
v_max3_f32 v237, v237, v124, v125
v_max3_f32 v236, v236, v122, v123
v_max3_f32 v237, v237, v126, v127
v_max_f32 v236, v236, v237
v_cmp_lt_f32 vcc, s77, v236
s_nop 1
s_cbranch_vccnz RARE9_%=
BACK9_%=:
v_mfma_f32_16x16x32_bf16 v[128:131], a[96:99], v[184:187], v[176:179]
ds_read_b128 a[120:123], v232 offset:6144
ds_read_b128 a[124:127], v233 offset:6144
v_exp_f32 v96, v96
v_mfma_f32_16x16x32_bf16 v[128:131], a[100:103], v[192:195], v[128:131]
s_add_u32 m0, s64, 4096
v_exp_f32 v97, v97
v_exp_f32 v98, v98
v_mfma_f32_16x16x32_bf16 v[128:131], a[104:107], v[200:203], v[128:131]
buffer_load_dwordx4 %[vX1], %[srdWo], s75 offen offset:0 lds
v_exp_f32 v99, v99
)"
R"(v_mfma_f32_16x16x32_bf16 v[132:135], a[96:99], v[188:191], v[180:183]
v_exp_f32 v104, v104
v_exp_f32 v105, v105
v_mfma_f32_16x16x32_bf16 v[132:135], a[100:103], v[196:199], v[132:135]
s_add_u32 m0, s64, 5120
ds_read_b128 a[96:99], v232 offset:7168
ds_read_b128 a[100:103], v233 offset:7168
v_exp_f32 v106, v106
v_mfma_f32_16x16x32_bf16 v[132:135], a[104:107], v[204:207], v[132:135]
s_waitcnt lgkmcnt(4)
v_exp_f32 v107, v107
v_cvt_pk_bf16_f32 v160, v96, v97
v_mfma_f32_16x16x32_bf16 v[128:131], a[108:111], v[208:211], v[128:131]
buffer_load_dwordx4 %[vW], %[srdWo], s75 offen offset:0 lds
v_cvt_pk_bf16_f32 v161, v98, v99
v_mfma_f32_16x16x32_bf16 v[128:131], a[112:115], v[216:219], v[128:131]
v_exp_f32 v100, v100
v_exp_f32 v101, v101
v_mfma_f32_16x16x32_bf16 v[128:131], a[116:119], v[224:227], v[128:131]
s_add_u32 s75, s67, 49152
v_exp_f32 v102, v102
v_exp_f32 v103, v103
v_mfma_f32_16x16x32_bf16 v[132:135], a[108:111], v[212:215], v[132:135]
s_add_u32 m0, s55, 0
)"
R"(ds_read_b128 a[104:107], v232 offset:8192
ds_read_b128 a[108:111], v233 offset:8192
v_cvt_pk_bf16_f32 v162, v104, v105
v_mfma_f32_16x16x32_bf16 v[132:135], a[112:115], v[220:223], v[132:135]
buffer_load_dwordx4 %[vK], %[srdK], s75 offen offset:0 nt lds
v_cvt_pk_bf16_f32 v163, v106, v107
v_mfma_f32_16x16x32_bf16 v[132:135], a[116:119], v[228:231], v[132:135]
s_waitcnt lgkmcnt(3)
v_exp_f32 v108, v108
v_exp_f32 v109, v109
v_mfma_f32_16x16x32_bf16 v[136:139], a[120:123], v[184:187], v[176:179]
ds_read_b128 a[112:115], v232 offset:12288
ds_read_b128 a[116:119], v233 offset:12288
v_exp_f32 v110, v110
v_mfma_f32_16x16x32_bf16 v[136:139], a[124:127], v[192:195], v[136:139]
s_add_u32 m0, s55, 896
v_exp_f32 v111, v111
v_cvt_pk_bf16_f32 v164, v100, v101
v_mfma_f32_16x16x32_bf16 v[136:139], a[96:99], v[200:203], v[136:139]
buffer_load_dwordx4 %[vK], %[srdK], s75 offen offset:128 nt lds
v_cvt_pk_bf16_f32 v165, v102, v103
v_mfma_f32_16x16x32_bf16 v[140:143], a[120:123], v[188:191], v[180:183]
v_exp_f32 v112, v112
v_exp_f32 v113, v113
)"
R"(v_mfma_f32_16x16x32_bf16 v[140:143], a[124:127], v[196:199], v[140:143]
s_add_u32 m0, s55, 1792
ds_read_b128 a[120:123], v232 offset:13312
ds_read_b128 a[124:127], v233 offset:13312
v_exp_f32 v114, v114
v_mfma_f32_16x16x32_bf16 v[140:143], a[96:99], v[204:207], v[140:143]
s_waitcnt lgkmcnt(4)
v_exp_f32 v115, v115
v_cvt_pk_bf16_f32 v166, v108, v109
v_mfma_f32_16x16x32_bf16 v[136:139], a[100:103], v[208:211], v[136:139]
buffer_load_dwordx4 %[vK], %[srdK], s75 offen offset:256 nt lds
v_cvt_pk_bf16_f32 v167, v110, v111
v_mfma_f32_16x16x32_bf16 v[136:139], a[104:107], v[216:219], v[136:139]
v_exp_f32 v120, v120
v_exp_f32 v121, v121
v_mfma_f32_16x16x32_bf16 v[136:139], a[108:111], v[224:227], v[136:139]
s_add_u32 s75, s67, 61440
v_exp_f32 v122, v122
v_exp_f32 v123, v123
v_mfma_f32_16x16x32_bf16 v[140:143], a[100:103], v[212:215], v[140:143]
s_add_u32 m0, s55, 12288
ds_read_b128 a[96:99], v232 offset:14336
ds_read_b128 a[100:103], v233 offset:14336
v_cvt_pk_bf16_f32 v168, v112, v113
)"
R"(v_mfma_f32_16x16x32_bf16 v[140:143], a[104:107], v[220:223], v[140:143]
buffer_load_dwordx4 %[vK], %[srdK], s75 offen offset:0 nt lds
v_cvt_pk_bf16_f32 v169, v114, v115
v_mfma_f32_16x16x32_bf16 v[140:143], a[108:111], v[228:231], v[140:143]
s_waitcnt lgkmcnt(3)
v_exp_f32 v116, v116
v_exp_f32 v117, v117
v_mfma_f32_16x16x32_bf16 v[144:147], a[112:115], v[184:187], v[176:179]
ds_read_b128 a[104:107], v232 offset:18432
ds_read_b128 a[108:111], v233 offset:18432
v_exp_f32 v118, v118
v_mfma_f32_16x16x32_bf16 v[144:147], a[116:119], v[192:195], v[144:147]
s_add_u32 m0, s55, 13184
v_exp_f32 v119, v119
v_cvt_pk_bf16_f32 v170, v120, v121
v_mfma_f32_16x16x32_bf16 v[144:147], a[120:123], v[200:203], v[144:147]
buffer_load_dwordx4 %[vK], %[srdK], s75 offen offset:128 nt lds
v_cvt_pk_bf16_f32 v171, v122, v123
v_mfma_f32_16x16x32_bf16 v[148:151], a[112:115], v[188:191], v[180:183]
v_exp_f32 v124, v124
v_mfma_f32_16x16x32_bf16 v[148:151], a[116:119], v[196:199], v[148:151]
s_add_u32 m0, s55, 14080
ds_read_b128 a[112:115], v232 offset:19456
ds_read_b128 a[116:119], v233 offset:19456
)"
R"(v_mfma_f32_16x16x32_bf16 v[148:151], a[120:123], v[204:207], v[148:151]
s_waitcnt lgkmcnt(4)
v_exp_f32 v125, v125
v_mfma_f32_16x16x32_bf16 v[144:147], a[124:127], v[208:211], v[144:147]
buffer_load_dwordx4 %[vK], %[srdK], s75 offen offset:256 nt lds
v_mfma_f32_16x16x32_bf16 v[144:147], a[96:99], v[216:219], v[144:147]
s_waitcnt vmcnt(12)
s_barrier
v_mfma_f32_16x16x32_bf16 v[144:147], a[100:103], v[224:227], v[144:147]
s_add_u32 s76, s68, 24576
v_exp_f32 v126, v126
v_mfma_f32_16x16x32_bf16 v[148:151], a[124:127], v[212:215], v[148:151]
s_add_u32 m0, s65, 0
ds_read_b128 a[120:123], v232 offset:20480
ds_read_b128 a[124:127], v233 offset:20480
v_mfma_f32_16x16x32_bf16 v[148:151], a[96:99], v[220:223], v[148:151]
buffer_load_dwordx4 %[vV], %[srdV], s76 offen offset:0 nt lds
v_mfma_f32_16x16x32_bf16 v[148:151], a[100:103], v[228:231], v[148:151]
s_waitcnt lgkmcnt(3)
v_add_u32 v232, s51, %[pV]
v_xor_b32 v233, 32, v232
v_mfma_f32_16x16x32_bf16 v[152:155], a[104:107], v[184:187], v[176:179]
ds_read_b64_tr_b16 a[96:97], v232 offset:0
ds_read_b64_tr_b16 a[98:99], v232 offset:6144
)"
R"(v_mfma_f32_16x16x32_bf16 v[152:155], a[108:111], v[192:195], v[152:155]
v_xor_b32 v234, 64, v232
v_xor_b32 v235, 96, v232
v_mfma_f32_16x16x32_bf16 v[152:155], a[112:115], v[200:203], v[152:155]
s_add_u32 m0, s65, 896
ds_read_b64_tr_b16 a[100:101], v232 offset:12288
ds_read_b64_tr_b16 a[102:103], v232 offset:18432
v_mfma_f32_16x16x32_bf16 v[156:159], a[104:107], v[188:191], v[180:183]
buffer_load_dwordx4 %[vV], %[srdV], s76 offen offset:128 nt lds
v_mfma_f32_16x16x32_bf16 v[156:159], a[108:111], v[196:199], v[156:159]
ds_read_b64_tr_b16 a[104:105], v233 offset:0
ds_read_b64_tr_b16 a[106:107], v233 offset:6144
v_mfma_f32_16x16x32_bf16 v[156:159], a[112:115], v[204:207], v[156:159]
s_waitcnt lgkmcnt(6)
v_exp_f32 v127, v127
v_cvt_pk_bf16_f32 v172, v116, v117
v_mfma_f32_16x16x32_bf16 v[152:155], a[116:119], v[208:211], v[152:155]
s_add_u32 m0, s65, 1792
ds_read_b64_tr_b16 a[108:109], v233 offset:12288
ds_read_b64_tr_b16 a[110:111], v233 offset:18432
v_cvt_pk_bf16_f32 v173, v118, v119
v_mfma_f32_16x16x32_bf16 v[152:155], a[120:123], v[216:219], v[152:155]
buffer_load_dwordx4 %[vV], %[srdV], s76 offen offset:256 nt lds
v_mfma_f32_16x16x32_bf16 v[152:155], a[124:127], v[224:227], v[152:155]
)"
R"(ds_read_b64_tr_b16 a[112:113], v234 offset:0
ds_read_b64_tr_b16 a[114:115], v234 offset:6144
v_mfma_f32_16x16x32_bf16 v[156:159], a[116:119], v[212:215], v[156:159]
v_cvt_pk_bf16_f32 v174, v124, v125
v_cvt_pk_bf16_f32 v175, v126, v127
v_mfma_f32_16x16x32_bf16 v[156:159], a[120:123], v[220:223], v[156:159]
s_add_u32 m0, s65, 0
ds_read_b64_tr_b16 a[116:117], v234 offset:12288
ds_read_b64_tr_b16 a[118:119], v234 offset:18432
v_mfma_f32_16x16x32_bf16 v[156:159], a[124:127], v[228:231], v[156:159]
buffer_load_dwordx4 %[vV], %[srdV], s76 offen offset:3072 nt lds
v_mfma_f32_16x16x32_bf16 a[132:135], a[128:131], v[160:163], 0
v_mfma_f32_16x16x32_bf16 a[132:135], a[128:131], v[168:171], a[132:135]
v_mfma_f32_16x16x32_bf16 a[136:139], a[128:131], v[164:167], 0
s_add_u32 m0, s65, 896
v_mfma_f32_16x16x32_bf16 a[136:139], a[128:131], v[172:175], a[136:139]
s_waitcnt lgkmcnt(4)
buffer_load_dwordx4 %[vV], %[srdV], s76 offen offset:3200 nt lds
v_mfma_f32_16x16x32_bf16 v[0:3], a[96:99], v[160:163], 0
ds_read_b64_tr_b16 a[120:121], v235 offset:0
ds_read_b64_tr_b16 a[122:123], v235 offset:6144
v_mfma_f32_16x16x32_bf16 v[0:3], a[100:103], v[168:171], v[0:3]
v_max3_f32 v236, v128, v129, v130
v_max3_f32 v237, v132, v133, v134
)"
R"(v_mfma_f32_16x16x32_bf16 v[4:7], a[96:99], v[164:167], 0
s_add_u32 m0, s65, 1792
ds_read_b64_tr_b16 a[124:125], v235 offset:12288
ds_read_b64_tr_b16 a[126:127], v235 offset:18432
v_mfma_f32_16x16x32_bf16 v[4:7], a[100:103], v[172:175], v[4:7]
buffer_load_dwordx4 %[vV], %[srdV], s76 offen offset:3328 nt lds
v_mfma_f32_16x16x32_bf16 v[8:11], a[104:107], v[160:163], 0
ds_read_b64_tr_b16 a[96:97], v232 offset:1024
ds_read_b64_tr_b16 a[98:99], v232 offset:7168
v_mfma_f32_16x16x32_bf16 v[8:11], a[108:111], v[168:171], v[8:11]
s_add_u32 s75, s78, 1152
v_max_f32 v236, v236, v131
v_max_f32 v237, v237, v135
v_mfma_f32_16x16x32_bf16 v[12:15], a[104:107], v[164:167], 0
s_add_u32 m0, s60, 0
ds_read_b64_tr_b16 a[100:101], v232 offset:13312
ds_read_b64_tr_b16 a[102:103], v232 offset:19456
v_mfma_f32_16x16x32_bf16 v[12:15], a[108:111], v[172:175], v[12:15]
s_waitcnt lgkmcnt(4)
buffer_load_dwordx4 %[vWo], %[srdWo], s75 offen offset:0 lds
v_mfma_f32_16x16x32_bf16 v[16:19], a[112:115], v[160:163], 0
ds_read_b64_tr_b16 a[104:105], v233 offset:1024
ds_read_b64_tr_b16 a[106:107], v233 offset:7168
v_mfma_f32_16x16x32_bf16 v[16:19], a[116:119], v[168:171], v[16:19]
)"
R"(v_max3_f32 v236, v236, v136, v137
v_max3_f32 v237, v237, v140, v141
v_mfma_f32_16x16x32_bf16 v[20:23], a[112:115], v[164:167], 0
s_add_u32 m0, s60, 1024
ds_read_b64_tr_b16 a[108:109], v233 offset:13312
ds_read_b64_tr_b16 a[110:111], v233 offset:19456
v_mfma_f32_16x16x32_bf16 v[20:23], a[116:119], v[172:175], v[20:23]
buffer_load_dwordx4 %[vX1], %[srdWo], s75 offen offset:0 lds
v_mfma_f32_16x16x32_bf16 v[24:27], a[120:123], v[160:163], 0
ds_read_b64_tr_b16 a[112:113], v234 offset:1024
ds_read_b64_tr_b16 a[114:115], v234 offset:7168
v_mfma_f32_16x16x32_bf16 v[24:27], a[124:127], v[168:171], v[24:27]
v_max3_f32 v236, v236, v138, v139
v_max3_f32 v237, v237, v142, v143
v_mfma_f32_16x16x32_bf16 v[28:31], a[120:123], v[164:167], 0
s_add_u32 m0, s60, 2048
ds_read_b64_tr_b16 a[116:117], v234 offset:13312
ds_read_b64_tr_b16 a[118:119], v234 offset:19456
v_mfma_f32_16x16x32_bf16 v[28:31], a[124:127], v[172:175], v[28:31]
s_waitcnt lgkmcnt(4)
buffer_load_dwordx4 %[vW], %[srdWo], s75 offen offset:0 lds
v_mfma_f32_16x16x32_bf16 v[32:35], a[96:99], v[160:163], 0
ds_read_b64_tr_b16 a[120:121], v235 offset:1024
ds_read_b64_tr_b16 a[122:123], v235 offset:7168
)"
R"(v_mfma_f32_16x16x32_bf16 v[32:35], a[100:103], v[168:171], v[32:35]
s_add_u32 s75, s78, 31872
v_max3_f32 v236, v236, v144, v145
v_max3_f32 v237, v237, v148, v149
v_mfma_f32_16x16x32_bf16 v[36:39], a[96:99], v[164:167], 0
s_add_u32 m0, s60, 3072
ds_read_b64_tr_b16 a[124:125], v235 offset:13312
ds_read_b64_tr_b16 a[126:127], v235 offset:19456
v_mfma_f32_16x16x32_bf16 v[36:39], a[100:103], v[172:175], v[36:39]
buffer_load_dwordx4 %[vWo], %[srdWo], s75 offen offset:0 lds
v_mfma_f32_16x16x32_bf16 v[40:43], a[104:107], v[160:163], 0
ds_read_b64_tr_b16 a[96:97], v232 offset:2048
ds_read_b64_tr_b16 a[98:99], v232 offset:8192
v_mfma_f32_16x16x32_bf16 v[40:43], a[108:111], v[168:171], v[40:43]
v_max3_f32 v236, v236, v146, v147
v_max3_f32 v237, v237, v150, v151
v_mfma_f32_16x16x32_bf16 v[44:47], a[104:107], v[164:167], 0
s_add_u32 m0, s60, 4096
ds_read_b64_tr_b16 a[100:101], v232 offset:14336
ds_read_b64_tr_b16 a[102:103], v232 offset:20480
v_mfma_f32_16x16x32_bf16 v[44:47], a[108:111], v[172:175], v[44:47]
s_waitcnt lgkmcnt(4)
buffer_load_dwordx4 %[vX1], %[srdWo], s75 offen offset:0 lds
v_mfma_f32_16x16x32_bf16 v[48:51], a[112:115], v[160:163], 0
)"
R"(ds_read_b64_tr_b16 a[104:105], v233 offset:2048
ds_read_b64_tr_b16 a[106:107], v233 offset:8192
v_mfma_f32_16x16x32_bf16 v[48:51], a[116:119], v[168:171], v[48:51]
v_max3_f32 v236, v236, v152, v153
v_max3_f32 v237, v237, v156, v157
v_mfma_f32_16x16x32_bf16 v[52:55], a[112:115], v[164:167], 0
s_add_u32 m0, s60, 5120
ds_read_b64_tr_b16 a[108:109], v233 offset:14336
ds_read_b64_tr_b16 a[110:111], v233 offset:20480
v_mfma_f32_16x16x32_bf16 v[52:55], a[116:119], v[172:175], v[52:55]
buffer_load_dwordx4 %[vW], %[srdWo], s75 offen offset:0 lds
v_mfma_f32_16x16x32_bf16 v[56:59], a[120:123], v[160:163], 0
ds_read_b64_tr_b16 a[112:113], v234 offset:2048
ds_read_b64_tr_b16 a[114:115], v234 offset:8192
v_mfma_f32_16x16x32_bf16 v[56:59], a[124:127], v[168:171], v[56:59]
v_max3_f32 v236, v236, v154, v155
v_max3_f32 v237, v237, v158, v159
v_mfma_f32_16x16x32_bf16 v[60:63], a[120:123], v[164:167], 0
s_add_u32 s75, s67, 73728
ds_read_b64_tr_b16 a[116:117], v234 offset:14336
ds_read_b64_tr_b16 a[118:119], v234 offset:20480
v_mfma_f32_16x16x32_bf16 v[60:63], a[124:127], v[172:175], v[60:63]
s_waitcnt lgkmcnt(4)
s_waitcnt vmcnt(18)
)"
R"(s_barrier
v_mfma_f32_16x16x32_bf16 v[64:67], a[96:99], v[160:163], 0
s_add_u32 m0, s56, 0
ds_read_b64_tr_b16 a[120:121], v235 offset:2048
ds_read_b64_tr_b16 a[122:123], v235 offset:8192
v_mfma_f32_16x16x32_bf16 v[64:67], a[100:103], v[168:171], v[64:67]
buffer_load_dwordx4 %[vK], %[srdK], s75 offen offset:0 nt lds
v_mfma_f32_16x16x32_bf16 v[68:71], a[96:99], v[164:167], 0
ds_read_b64_tr_b16 a[124:125], v235 offset:14336
ds_read_b64_tr_b16 a[126:127], v235 offset:20480
v_mfma_f32_16x16x32_bf16 v[68:71], a[100:103], v[172:175], v[68:71]
v_add_u32 v232, s52, %[pWo]
v_xor_b32 v233, 32, v232
v_mfma_f32_16x16x32_bf16 v[72:75], a[104:107], v[160:163], 0
ds_read_b64_tr_b16 a[96:97], v232 offset:0
ds_read_b64_tr_b16 a[98:99], v232 offset:2048
v_mfma_f32_16x16x32_bf16 v[72:75], a[108:111], v[168:171], v[72:75]
v_xor_b32 v234, 64, v232
v_xor_b32 v235, 96, v232
v_mfma_f32_16x16x32_bf16 v[76:79], a[104:107], v[164:167], 0
s_add_u32 m0, s56, 896
ds_read_b64_tr_b16 a[100:101], v232 offset:4096
ds_read_b64_tr_b16 a[102:103], v232 offset:6144
v_mfma_f32_16x16x32_bf16 v[76:79], a[108:111], v[172:175], v[76:79]
)"
R"(s_waitcnt lgkmcnt(4)
buffer_load_dwordx4 %[vK], %[srdK], s75 offen offset:128 nt lds
v_mfma_f32_16x16x32_bf16 v[80:83], a[112:115], v[160:163], 0
ds_read_b64_tr_b16 a[104:105], v232 offset:8192
ds_read_b64_tr_b16 a[106:107], v232 offset:10240
v_mfma_f32_16x16x32_bf16 v[80:83], a[116:119], v[168:171], v[80:83]
v_max_f32 v236, v236, v237
v_cmp_lt_f32 vcc, s77, v236
v_mfma_f32_16x16x32_bf16 v[84:87], a[112:115], v[164:167], 0
s_add_u32 m0, s56, 1792
ds_read_b64_tr_b16 a[108:109], v232 offset:12288
ds_read_b64_tr_b16 a[110:111], v232 offset:14336
v_mfma_f32_16x16x32_bf16 v[84:87], a[116:119], v[172:175], v[84:87]
buffer_load_dwordx4 %[vK], %[srdK], s75 offen offset:256 nt lds
v_mfma_f32_16x16x32_bf16 v[88:91], a[120:123], v[160:163], 0
ds_read_b64_tr_b16 a[112:113], v232 offset:16384
ds_read_b64_tr_b16 a[114:115], v232 offset:18432
v_mfma_f32_16x16x32_bf16 v[88:91], a[124:127], v[168:171], v[88:91]
s_add_u32 s75, s67, 86016
v_mfma_f32_16x16x32_bf16 v[92:95], a[120:123], v[164:167], 0
s_add_u32 m0, s56, 12288
ds_read_b64_tr_b16 a[116:117], v232 offset:20480
ds_read_b64_tr_b16 a[118:119], v232 offset:22528
v_mfma_f32_16x16x32_bf16 v[92:95], a[124:127], v[172:175], v[92:95]
)"
R"(s_waitcnt lgkmcnt(6)
buffer_load_dwordx4 %[vK], %[srdK], s75 offen offset:0 nt lds
s_cbranch_vccnz RARE10_%=
BACK10_%=:
v_mfma_f32_16x16x32_bf16 a[192:195], a[96:99], a[0:3], 0
ds_read_b64_tr_b16 a[120:121], v233 offset:0
ds_read_b64_tr_b16 a[122:123], v233 offset:2048
v_mfma_f32_16x16x32_bf16 a[192:195], a[100:103], a[8:11], a[192:195]
v_exp_f32 v128, v128
v_mfma_f32_16x16x32_bf16 a[192:195], a[104:107], a[16:19], a[192:195]
s_add_u32 m0, s56, 13184
ds_read_b64_tr_b16 a[124:125], v233 offset:4096
ds_read_b64_tr_b16 a[126:127], v233 offset:6144
v_mfma_f32_16x16x32_bf16 a[196:199], a[96:99], a[4:7], 0
buffer_load_dwordx4 %[vK], %[srdK], s75 offen offset:128 nt lds
v_mfma_f32_16x16x32_bf16 a[196:199], a[100:103], a[12:15], a[196:199]
ds_read_b64_tr_b16 a[96:97], v233 offset:8192
ds_read_b64_tr_b16 a[98:99], v233 offset:10240
v_mfma_f32_16x16x32_bf16 a[196:199], a[104:107], a[20:23], a[196:199]
s_waitcnt lgkmcnt(6)
v_exp_f32 v129, v129
v_mfma_f32_16x16x32_bf16 a[192:195], a[108:111], a[24:27], a[192:195]
s_add_u32 m0, s56, 14080
ds_read_b64_tr_b16 a[100:101], v233 offset:12288
)"
R"(ds_read_b64_tr_b16 a[102:103], v233 offset:14336
v_mfma_f32_16x16x32_bf16 a[192:195], a[112:115], a[32:35], a[192:195]
buffer_load_dwordx4 %[vK], %[srdK], s75 offen offset:256 nt lds
v_mfma_f32_16x16x32_bf16 a[192:195], a[116:119], a[40:43], a[192:195]
ds_read_b64_tr_b16 a[104:105], v233 offset:16384
ds_read_b64_tr_b16 a[106:107], v233 offset:18432
v_mfma_f32_16x16x32_bf16 a[196:199], a[108:111], a[28:31], a[196:199]
v_exp_f32 v130, v130
v_mfma_f32_16x16x32_bf16 a[196:199], a[112:115], a[36:39], a[196:199]
ds_read_b64_tr_b16 a[108:109], v233 offset:20480
ds_read_b64_tr_b16 a[110:111], v233 offset:22528
v_mfma_f32_16x16x32_bf16 a[196:199], a[116:119], a[44:47], a[196:199]
s_waitcnt lgkmcnt(6)
v_exp_f32 v131, v131
v_mfma_f32_16x16x32_bf16 a[200:203], a[120:123], a[0:3], 0
ds_read_b64_tr_b16 a[112:113], v234 offset:0
ds_read_b64_tr_b16 a[114:115], v234 offset:2048
v_mfma_f32_16x16x32_bf16 a[200:203], a[124:127], a[8:11], a[200:203]
v_exp_f32 v136, v136
v_mfma_f32_16x16x32_bf16 a[200:203], a[96:99], a[16:19], a[200:203]
ds_read_b64_tr_b16 a[116:117], v234 offset:4096
ds_read_b64_tr_b16 a[118:119], v234 offset:6144
v_mfma_f32_16x16x32_bf16 a[204:207], a[120:123], a[4:7], 0
v_exp_f32 v137, v137
)"
R"(v_mfma_f32_16x16x32_bf16 a[204:207], a[124:127], a[12:15], a[204:207]
ds_read_b64_tr_b16 a[120:121], v234 offset:8192
ds_read_b64_tr_b16 a[122:123], v234 offset:10240
v_mfma_f32_16x16x32_bf16 a[204:207], a[96:99], a[20:23], a[204:207]
s_waitcnt lgkmcnt(6)
v_exp_f32 v138, v138
v_mfma_f32_16x16x32_bf16 a[200:203], a[100:103], a[24:27], a[200:203]
ds_read_b64_tr_b16 a[124:125], v234 offset:12288
ds_read_b64_tr_b16 a[126:127], v234 offset:14336
v_mfma_f32_16x16x32_bf16 a[200:203], a[104:107], a[32:35], a[200:203]
v_exp_f32 v139, v139
v_mfma_f32_16x16x32_bf16 a[200:203], a[108:111], a[40:43], a[200:203]
ds_read_b64_tr_b16 a[96:97], v234 offset:16384
ds_read_b64_tr_b16 a[98:99], v234 offset:18432
v_mfma_f32_16x16x32_bf16 a[204:207], a[100:103], a[28:31], a[204:207]
v_cvt_pk_bf16_f32 v160, v128, v129
v_cvt_pk_bf16_f32 v161, v130, v131
v_mfma_f32_16x16x32_bf16 a[204:207], a[104:107], a[36:39], a[204:207]
ds_read_b64_tr_b16 a[100:101], v234 offset:20480
ds_read_b64_tr_b16 a[102:103], v234 offset:22528
v_mfma_f32_16x16x32_bf16 a[204:207], a[108:111], a[44:47], a[204:207]
s_waitcnt lgkmcnt(6)
v_exp_f32 v132, v132
v_mfma_f32_16x16x32_bf16 a[208:211], a[112:115], a[0:3], 0
)"
R"(ds_read_b64_tr_b16 a[104:105], v235 offset:0
ds_read_b64_tr_b16 a[106:107], v235 offset:2048
v_mfma_f32_16x16x32_bf16 a[208:211], a[116:119], a[8:11], a[208:211]
v_exp_f32 v133, v133
v_mfma_f32_16x16x32_bf16 a[208:211], a[120:123], a[16:19], a[208:211]
ds_read_b64_tr_b16 a[108:109], v235 offset:4096
ds_read_b64_tr_b16 a[110:111], v235 offset:6144
v_mfma_f32_16x16x32_bf16 a[212:215], a[112:115], a[4:7], 0
v_exp_f32 v134, v134
v_mfma_f32_16x16x32_bf16 a[212:215], a[116:119], a[12:15], a[212:215]
ds_read_b64_tr_b16 a[112:113], v235 offset:8192
ds_read_b64_tr_b16 a[114:115], v235 offset:10240
v_mfma_f32_16x16x32_bf16 a[212:215], a[120:123], a[20:23], a[212:215]
s_waitcnt lgkmcnt(6)
v_exp_f32 v135, v135
v_mfma_f32_16x16x32_bf16 a[208:211], a[124:127], a[24:27], a[208:211]
s_add_u32 s76, s68, 49152
ds_read_b64_tr_b16 a[116:117], v235 offset:12288
ds_read_b64_tr_b16 a[118:119], v235 offset:14336
v_mfma_f32_16x16x32_bf16 a[208:211], a[96:99], a[32:35], a[208:211]
s_waitcnt vmcnt(18)
s_barrier
v_mfma_f32_16x16x32_bf16 a[208:211], a[100:103], a[40:43], a[208:211]
s_add_u32 m0, s63, 0
)"
R"(ds_read_b64_tr_b16 a[120:121], v235 offset:16384
ds_read_b64_tr_b16 a[122:123], v235 offset:18432
v_mfma_f32_16x16x32_bf16 a[212:215], a[124:127], a[28:31], a[212:215]
buffer_load_dwordx4 %[vV], %[srdV], s76 offen offset:0 nt lds
v_mfma_f32_16x16x32_bf16 a[212:215], a[96:99], a[36:39], a[212:215]
ds_read_b64_tr_b16 a[124:125], v235 offset:20480
ds_read_b64_tr_b16 a[126:127], v235 offset:22528
v_mfma_f32_16x16x32_bf16 a[212:215], a[100:103], a[44:47], a[212:215]
s_waitcnt lgkmcnt(6)
v_add_u32 v232, s49, %[pK]
v_xor_b32 v233, 64, v232
v_mfma_f32_16x16x32_bf16 a[216:219], a[104:107], a[0:3], 0
s_add_u32 m0, s63, 896
ds_read_b128 a[96:99], v232 offset:0
ds_read_b128 a[100:103], v233 offset:0
v_mfma_f32_16x16x32_bf16 a[216:219], a[108:111], a[8:11], a[216:219]
buffer_load_dwordx4 %[vV], %[srdV], s76 offen offset:128 nt lds
v_mfma_f32_16x16x32_bf16 a[216:219], a[112:115], a[16:19], a[216:219]
v_cvt_pk_bf16_f32 v162, v136, v137
v_cvt_pk_bf16_f32 v163, v138, v139
v_mfma_f32_16x16x32_bf16 a[220:223], a[104:107], a[4:7], 0
v_exp_f32 v140, v140
v_mfma_f32_16x16x32_bf16 a[220:223], a[108:111], a[12:15], a[220:223]
s_add_u32 m0, s63, 1792
)"
R"(ds_read_b128 a[104:107], v232 offset:1024
ds_read_b128 a[108:111], v233 offset:1024
v_mfma_f32_16x16x32_bf16 a[220:223], a[112:115], a[20:23], a[220:223]
s_waitcnt lgkmcnt(4)
buffer_load_dwordx4 %[vV], %[srdV], s76 offen offset:256 nt lds
v_mfma_f32_16x16x32_bf16 a[216:219], a[116:119], a[24:27], a[216:219]
v_exp_f32 v141, v141
v_mfma_f32_16x16x32_bf16 a[216:219], a[120:123], a[32:35], a[216:219]
v_exp_f32 v142, v142
v_mfma_f32_16x16x32_bf16 a[216:219], a[124:127], a[40:43], a[216:219]
v_exp_f32 v143, v143
v_mfma_f32_16x16x32_bf16 a[220:223], a[116:119], a[28:31], a[220:223]
s_add_u32 m0, s63, 0
ds_read_b128 a[112:115], v232 offset:2048
ds_read_b128 a[116:119], v233 offset:2048
v_mfma_f32_16x16x32_bf16 a[220:223], a[120:123], a[36:39], a[220:223]
buffer_load_dwordx4 %[vV], %[srdV], s76 offen offset:3072 nt lds
v_mfma_f32_16x16x32_bf16 a[220:223], a[124:127], a[44:47], a[220:223]
s_waitcnt lgkmcnt(3)
v_cvt_pk_bf16_f32 v164, v132, v133
v_cvt_pk_bf16_f32 v165, v134, v135
v_mfma_f32_16x16x32_bf16 v[96:99], a[96:99], v[184:187], v[176:179]
ds_read_b128 a[120:123], v232 offset:6144
ds_read_b128 a[124:127], v233 offset:6144
)"
R"(v_mfma_f32_16x16x32_bf16 v[96:99], a[100:103], v[192:195], v[96:99]
s_add_u32 m0, s63, 896
v_exp_f32 v144, v144
v_mfma_f32_16x16x32_bf16 v[96:99], a[104:107], v[200:203], v[96:99]
buffer_load_dwordx4 %[vV], %[srdV], s76 offen offset:3200 nt lds
v_mfma_f32_16x16x32_bf16 v[100:103], a[96:99], v[188:191], v[180:183]
v_exp_f32 v145, v145
v_mfma_f32_16x16x32_bf16 v[100:103], a[100:103], v[196:199], v[100:103]
s_add_u32 m0, s63, 1792
ds_read_b128 a[96:99], v232 offset:7168
ds_read_b128 a[100:103], v233 offset:7168
v_mfma_f32_16x16x32_bf16 v[100:103], a[104:107], v[204:207], v[100:103]
s_waitcnt lgkmcnt(4)
v_exp_f32 v146, v146
v_mfma_f32_16x16x32_bf16 v[96:99], a[108:111], v[208:211], v[96:99]
buffer_load_dwordx4 %[vV], %[srdV], s76 offen offset:3328 nt lds
v_mfma_f32_16x16x32_bf16 v[96:99], a[112:115], v[216:219], v[96:99]
v_exp_f32 v147, v147
v_mfma_f32_16x16x32_bf16 v[96:99], a[116:119], v[224:227], v[96:99]
v_cvt_pk_bf16_f32 v166, v140, v141
v_cvt_pk_bf16_f32 v167, v142, v143
v_mfma_f32_16x16x32_bf16 v[100:103], a[108:111], v[212:215], v[100:103]
ds_read_b128 a[104:107], v232 offset:8192
ds_read_b128 a[108:111], v233 offset:8192
)"
R"(v_mfma_f32_16x16x32_bf16 v[100:103], a[112:115], v[220:223], v[100:103]
v_exp_f32 v152, v152
v_mfma_f32_16x16x32_bf16 v[100:103], a[116:119], v[228:231], v[100:103]
s_waitcnt lgkmcnt(3)
v_exp_f32 v153, v153
v_mfma_f32_16x16x32_bf16 v[104:107], a[120:123], v[184:187], v[176:179]
ds_read_b128 a[112:115], v232 offset:12288
ds_read_b128 a[116:119], v233 offset:12288
v_mfma_f32_16x16x32_bf16 v[104:107], a[124:127], v[192:195], v[104:107]
v_exp_f32 v154, v154
v_mfma_f32_16x16x32_bf16 v[104:107], a[96:99], v[200:203], v[104:107]
v_exp_f32 v155, v155
v_mfma_f32_16x16x32_bf16 v[108:111], a[120:123], v[188:191], v[180:183]
v_cvt_pk_bf16_f32 v168, v144, v145
v_cvt_pk_bf16_f32 v169, v146, v147
v_mfma_f32_16x16x32_bf16 v[108:111], a[124:127], v[196:199], v[108:111]
ds_read_b128 a[120:123], v232 offset:13312
ds_read_b128 a[124:127], v233 offset:13312
v_mfma_f32_16x16x32_bf16 v[108:111], a[96:99], v[204:207], v[108:111]
s_waitcnt lgkmcnt(4)
v_exp_f32 v148, v148
v_mfma_f32_16x16x32_bf16 v[104:107], a[100:103], v[208:211], v[104:107]
v_exp_f32 v149, v149
v_mfma_f32_16x16x32_bf16 v[104:107], a[104:107], v[216:219], v[104:107]
)"
R"(v_exp_f32 v150, v150
v_mfma_f32_16x16x32_bf16 v[104:107], a[108:111], v[224:227], v[104:107]
v_exp_f32 v151, v151
v_mfma_f32_16x16x32_bf16 v[108:111], a[100:103], v[212:215], v[108:111]
ds_read_b128 a[96:99], v232 offset:14336
ds_read_b128 a[100:103], v233 offset:14336
v_mfma_f32_16x16x32_bf16 v[108:111], a[104:107], v[220:223], v[108:111]
v_cvt_pk_bf16_f32 v170, v152, v153
v_cvt_pk_bf16_f32 v171, v154, v155
v_mfma_f32_16x16x32_bf16 v[108:111], a[108:111], v[228:231], v[108:111]
s_waitcnt lgkmcnt(3)
v_exp_f32 v156, v156
v_mfma_f32_16x16x32_bf16 v[112:115], a[112:115], v[184:187], v[176:179]
ds_read_b128 a[104:107], v232 offset:18432
ds_read_b128 a[108:111], v233 offset:18432
v_mfma_f32_16x16x32_bf16 v[112:115], a[116:119], v[192:195], v[112:115]
v_exp_f32 v157, v157
v_mfma_f32_16x16x32_bf16 v[112:115], a[120:123], v[200:203], v[112:115]
v_exp_f32 v158, v158
v_mfma_f32_16x16x32_bf16 v[116:119], a[112:115], v[188:191], v[180:183]
v_exp_f32 v159, v159
v_mfma_f32_16x16x32_bf16 v[116:119], a[116:119], v[196:199], v[116:119]
ds_read_b128 a[112:115], v232 offset:19456
ds_read_b128 a[116:119], v233 offset:19456
)"
R"(v_mfma_f32_16x16x32_bf16 v[116:119], a[120:123], v[204:207], v[116:119]
s_waitcnt lgkmcnt(4)
v_cvt_pk_bf16_f32 v172, v148, v149
v_cvt_pk_bf16_f32 v173, v150, v151
v_mfma_f32_16x16x32_bf16 v[112:115], a[124:127], v[208:211], v[112:115]
v_cvt_pk_bf16_f32 v174, v156, v157
v_cvt_pk_bf16_f32 v175, v158, v159
v_mfma_f32_16x16x32_bf16 v[112:115], a[96:99], v[216:219], v[112:115]
s_waitcnt vmcnt(18)
s_barrier
v_mfma_f32_16x16x32_bf16 v[112:115], a[100:103], v[224:227], v[112:115]
s_add_u32 s75, s78, 246784
v_mfma_f32_16x16x32_bf16 v[116:119], a[124:127], v[212:215], v[116:119]
s_add_u32 m0, s64, 0
ds_read_b128 a[120:123], v232 offset:20480
ds_read_b128 a[124:127], v233 offset:20480
v_mfma_f32_16x16x32_bf16 v[116:119], a[96:99], v[220:223], v[116:119]
buffer_load_dwordx4 %[vWo], %[srdWo], s75 offen offset:0 lds
v_mfma_f32_16x16x32_bf16 v[116:119], a[100:103], v[228:231], v[116:119]
s_waitcnt lgkmcnt(3)
v_add_u32 v232, s53, %[pV]
v_xor_b32 v233, 32, v232
v_mfma_f32_16x16x32_bf16 v[120:123], a[104:107], v[184:187], v[176:179]
ds_read_b64_tr_b16 a[96:97], v232 offset:0
)"
R"(ds_read_b64_tr_b16 a[98:99], v232 offset:6144
v_mfma_f32_16x16x32_bf16 v[120:123], a[108:111], v[192:195], v[120:123]
v_xor_b32 v234, 64, v232
v_xor_b32 v235, 96, v232
v_mfma_f32_16x16x32_bf16 v[120:123], a[112:115], v[200:203], v[120:123]
s_add_u32 m0, s64, 1024
ds_read_b64_tr_b16 a[100:101], v232 offset:12288
ds_read_b64_tr_b16 a[102:103], v232 offset:18432
v_mfma_f32_16x16x32_bf16 v[124:127], a[104:107], v[188:191], v[180:183]
buffer_load_dwordx4 %[vX1], %[srdWo], s75 offen offset:0 lds
v_mfma_f32_16x16x32_bf16 v[124:127], a[108:111], v[196:199], v[124:127]
ds_read_b64_tr_b16 a[104:105], v233 offset:0
ds_read_b64_tr_b16 a[106:107], v233 offset:6144
v_mfma_f32_16x16x32_bf16 v[124:127], a[112:115], v[204:207], v[124:127]
s_waitcnt lgkmcnt(6)
v_mfma_f32_16x16x32_bf16 v[120:123], a[116:119], v[208:211], v[120:123]
s_add_u32 m0, s64, 2048
ds_read_b64_tr_b16 a[108:109], v233 offset:12288
ds_read_b64_tr_b16 a[110:111], v233 offset:18432
v_mfma_f32_16x16x32_bf16 v[120:123], a[120:123], v[216:219], v[120:123]
buffer_load_dwordx4 %[vW], %[srdWo], s75 offen offset:0 lds
v_mfma_f32_16x16x32_bf16 v[120:123], a[124:127], v[224:227], v[120:123]
ds_read_b64_tr_b16 a[112:113], v234 offset:0
ds_read_b64_tr_b16 a[114:115], v234 offset:6144
)"
R"(v_mfma_f32_16x16x32_bf16 v[124:127], a[116:119], v[212:215], v[124:127]
s_add_u32 s75, s78, 277504
v_mfma_f32_16x16x32_bf16 v[124:127], a[120:123], v[220:223], v[124:127]
s_add_u32 m0, s64, 3072
ds_read_b64_tr_b16 a[116:117], v234 offset:12288
ds_read_b64_tr_b16 a[118:119], v234 offset:18432
v_mfma_f32_16x16x32_bf16 v[124:127], a[124:127], v[228:231], v[124:127]
buffer_load_dwordx4 %[vWo], %[srdWo], s75 offen offset:0 lds
v_mfma_f32_16x16x32_bf16 a[132:135], a[128:131], v[160:163], a[132:135]
v_mfma_f32_16x16x32_bf16 a[132:135], a[128:131], v[168:171], a[132:135]
v_mfma_f32_16x16x32_bf16 a[136:139], a[128:131], v[164:167], a[136:139]
s_add_u32 m0, s64, 4096
v_mfma_f32_16x16x32_bf16 a[136:139], a[128:131], v[172:175], a[136:139]
s_waitcnt lgkmcnt(4)
buffer_load_dwordx4 %[vX1], %[srdWo], s75 offen offset:0 lds
v_mfma_f32_16x16x32_bf16 v[0:3], a[96:99], v[160:163], v[0:3]
ds_read_b64_tr_b16 a[120:121], v235 offset:0
ds_read_b64_tr_b16 a[122:123], v235 offset:6144
v_mfma_f32_16x16x32_bf16 v[0:3], a[100:103], v[168:171], v[0:3]
v_max3_f32 v236, v96, v97, v98
v_max3_f32 v237, v100, v101, v102
v_mfma_f32_16x16x32_bf16 v[4:7], a[96:99], v[164:167], v[4:7]
s_add_u32 m0, s64, 5120
ds_read_b64_tr_b16 a[124:125], v235 offset:12288
)"
R"(ds_read_b64_tr_b16 a[126:127], v235 offset:18432
v_mfma_f32_16x16x32_bf16 v[4:7], a[100:103], v[172:175], v[4:7]
buffer_load_dwordx4 %[vW], %[srdWo], s75 offen offset:0 lds
v_mfma_f32_16x16x32_bf16 v[8:11], a[104:107], v[160:163], v[8:11]
ds_read_b64_tr_b16 a[96:97], v232 offset:1024
ds_read_b64_tr_b16 a[98:99], v232 offset:7168
v_mfma_f32_16x16x32_bf16 v[8:11], a[108:111], v[168:171], v[8:11]
v_max_f32 v236, v236, v99
v_max_f32 v237, v237, v103
v_mfma_f32_16x16x32_bf16 v[12:15], a[104:107], v[164:167], v[12:15]
ds_read_b64_tr_b16 a[100:101], v232 offset:13312
ds_read_b64_tr_b16 a[102:103], v232 offset:19456
v_mfma_f32_16x16x32_bf16 v[12:15], a[108:111], v[172:175], v[12:15]
s_waitcnt lgkmcnt(4)
v_max3_f32 v236, v236, v104, v105
v_max3_f32 v237, v237, v108, v109
v_mfma_f32_16x16x32_bf16 v[16:19], a[112:115], v[160:163], v[16:19]
ds_read_b64_tr_b16 a[104:105], v233 offset:1024
ds_read_b64_tr_b16 a[106:107], v233 offset:7168
v_mfma_f32_16x16x32_bf16 v[16:19], a[116:119], v[168:171], v[16:19]
v_max3_f32 v236, v236, v106, v107
v_max3_f32 v237, v237, v110, v111
v_mfma_f32_16x16x32_bf16 v[20:23], a[112:115], v[164:167], v[20:23]
ds_read_b64_tr_b16 a[108:109], v233 offset:13312
)"
R"(ds_read_b64_tr_b16 a[110:111], v233 offset:19456
v_mfma_f32_16x16x32_bf16 v[20:23], a[116:119], v[172:175], v[20:23]
v_max3_f32 v236, v236, v112, v113
v_max3_f32 v237, v237, v116, v117
v_mfma_f32_16x16x32_bf16 v[24:27], a[120:123], v[160:163], v[24:27]
ds_read_b64_tr_b16 a[112:113], v234 offset:1024
ds_read_b64_tr_b16 a[114:115], v234 offset:7168
v_mfma_f32_16x16x32_bf16 v[24:27], a[124:127], v[168:171], v[24:27]
v_max3_f32 v236, v236, v114, v115
v_max3_f32 v237, v237, v118, v119
v_mfma_f32_16x16x32_bf16 v[28:31], a[120:123], v[164:167], v[28:31]
ds_read_b64_tr_b16 a[116:117], v234 offset:13312
ds_read_b64_tr_b16 a[118:119], v234 offset:19456
v_mfma_f32_16x16x32_bf16 v[28:31], a[124:127], v[172:175], v[28:31]
s_waitcnt lgkmcnt(4)
v_max3_f32 v236, v236, v120, v121
v_max3_f32 v237, v237, v124, v125
v_mfma_f32_16x16x32_bf16 v[32:35], a[96:99], v[160:163], v[32:35]
ds_read_b64_tr_b16 a[120:121], v235 offset:1024
ds_read_b64_tr_b16 a[122:123], v235 offset:7168
v_mfma_f32_16x16x32_bf16 v[32:35], a[100:103], v[168:171], v[32:35]
v_max3_f32 v236, v236, v122, v123
v_max3_f32 v237, v237, v126, v127
v_mfma_f32_16x16x32_bf16 v[36:39], a[96:99], v[164:167], v[36:39]
)"
R"(ds_read_b64_tr_b16 a[124:125], v235 offset:13312
ds_read_b64_tr_b16 a[126:127], v235 offset:19456
v_mfma_f32_16x16x32_bf16 v[36:39], a[100:103], v[172:175], v[36:39]
v_max_f32 v236, v236, v237
v_cmp_lt_f32 vcc, s77, v236
v_mfma_f32_16x16x32_bf16 v[40:43], a[104:107], v[160:163], v[40:43]
ds_read_b64_tr_b16 a[96:97], v232 offset:2048
ds_read_b64_tr_b16 a[98:99], v232 offset:8192
v_mfma_f32_16x16x32_bf16 v[40:43], a[108:111], v[168:171], v[40:43]
v_mfma_f32_16x16x32_bf16 v[44:47], a[104:107], v[164:167], v[44:47]
ds_read_b64_tr_b16 a[100:101], v232 offset:14336
ds_read_b64_tr_b16 a[102:103], v232 offset:20480
v_mfma_f32_16x16x32_bf16 v[44:47], a[108:111], v[172:175], v[44:47]
s_waitcnt lgkmcnt(4)
v_mfma_f32_16x16x32_bf16 v[48:51], a[112:115], v[160:163], v[48:51]
ds_read_b64_tr_b16 a[104:105], v233 offset:2048
ds_read_b64_tr_b16 a[106:107], v233 offset:8192
v_mfma_f32_16x16x32_bf16 v[48:51], a[116:119], v[168:171], v[48:51]
v_mfma_f32_16x16x32_bf16 v[52:55], a[112:115], v[164:167], v[52:55]
ds_read_b64_tr_b16 a[108:109], v233 offset:14336
ds_read_b64_tr_b16 a[110:111], v233 offset:20480
v_mfma_f32_16x16x32_bf16 v[52:55], a[116:119], v[172:175], v[52:55]
v_mfma_f32_16x16x32_bf16 v[56:59], a[120:123], v[160:163], v[56:59]
ds_read_b64_tr_b16 a[112:113], v234 offset:2048
)"
R"(ds_read_b64_tr_b16 a[114:115], v234 offset:8192
v_mfma_f32_16x16x32_bf16 v[56:59], a[124:127], v[168:171], v[56:59]
v_mfma_f32_16x16x32_bf16 v[60:63], a[120:123], v[164:167], v[60:63]
s_add_u32 s75, s67, 98304
ds_read_b64_tr_b16 a[116:117], v234 offset:14336
ds_read_b64_tr_b16 a[118:119], v234 offset:20480
v_mfma_f32_16x16x32_bf16 v[60:63], a[124:127], v[172:175], v[60:63]
s_waitcnt lgkmcnt(4)
s_waitcnt vmcnt(18)
s_barrier
v_mfma_f32_16x16x32_bf16 v[64:67], a[96:99], v[160:163], v[64:67]
s_add_u32 m0, s55, 0
ds_read_b64_tr_b16 a[120:121], v235 offset:2048
ds_read_b64_tr_b16 a[122:123], v235 offset:8192
v_mfma_f32_16x16x32_bf16 v[64:67], a[100:103], v[168:171], v[64:67]
buffer_load_dwordx4 %[vK], %[srdK], s75 offen offset:0 nt lds
v_mfma_f32_16x16x32_bf16 v[68:71], a[96:99], v[164:167], v[68:71]
ds_read_b64_tr_b16 a[124:125], v235 offset:14336
ds_read_b64_tr_b16 a[126:127], v235 offset:20480
v_mfma_f32_16x16x32_bf16 v[68:71], a[100:103], v[172:175], v[68:71]
v_add_u32 v232, s48, %[pWo]
v_xor_b32 v233, 32, v232
v_mfma_f32_16x16x32_bf16 v[72:75], a[104:107], v[160:163], v[72:75]
ds_read_b64_tr_b16 a[96:97], v232 offset:0
)"
R"(ds_read_b64_tr_b16 a[98:99], v232 offset:2048
v_mfma_f32_16x16x32_bf16 v[72:75], a[108:111], v[168:171], v[72:75]
v_xor_b32 v234, 64, v232
v_xor_b32 v235, 96, v232
v_mfma_f32_16x16x32_bf16 v[76:79], a[104:107], v[164:167], v[76:79]
s_add_u32 m0, s55, 896
ds_read_b64_tr_b16 a[100:101], v232 offset:4096
ds_read_b64_tr_b16 a[102:103], v232 offset:6144
v_mfma_f32_16x16x32_bf16 v[76:79], a[108:111], v[172:175], v[76:79]
s_waitcnt lgkmcnt(4)
buffer_load_dwordx4 %[vK], %[srdK], s75 offen offset:128 nt lds
v_mfma_f32_16x16x32_bf16 v[80:83], a[112:115], v[160:163], v[80:83]
ds_read_b64_tr_b16 a[104:105], v232 offset:8192
ds_read_b64_tr_b16 a[106:107], v232 offset:10240
v_mfma_f32_16x16x32_bf16 v[80:83], a[116:119], v[168:171], v[80:83]
v_mfma_f32_16x16x32_bf16 v[84:87], a[112:115], v[164:167], v[84:87]
s_add_u32 m0, s55, 1792
ds_read_b64_tr_b16 a[108:109], v232 offset:12288
ds_read_b64_tr_b16 a[110:111], v232 offset:14336
v_mfma_f32_16x16x32_bf16 v[84:87], a[116:119], v[172:175], v[84:87]
buffer_load_dwordx4 %[vK], %[srdK], s75 offen offset:256 nt lds
v_mfma_f32_16x16x32_bf16 v[88:91], a[120:123], v[160:163], v[88:91]
ds_read_b64_tr_b16 a[112:113], v232 offset:16384
ds_read_b64_tr_b16 a[114:115], v232 offset:18432
)"
R"(v_mfma_f32_16x16x32_bf16 v[88:91], a[124:127], v[168:171], v[88:91]
s_add_u32 s75, s67, 110592
v_mfma_f32_16x16x32_bf16 v[92:95], a[120:123], v[164:167], v[92:95]
s_add_u32 m0, s55, 12288
ds_read_b64_tr_b16 a[116:117], v232 offset:20480
ds_read_b64_tr_b16 a[118:119], v232 offset:22528
v_mfma_f32_16x16x32_bf16 v[92:95], a[124:127], v[172:175], v[92:95]
s_waitcnt lgkmcnt(6)
buffer_load_dwordx4 %[vK], %[srdK], s75 offen offset:0 nt lds
s_cbranch_vccnz RARE11_%=
BACK11_%=:
v_mfma_f32_16x16x32_bf16 a[224:227], a[96:99], a[0:3], 0
ds_read_b64_tr_b16 a[120:121], v233 offset:0
ds_read_b64_tr_b16 a[122:123], v233 offset:2048
v_mfma_f32_16x16x32_bf16 a[224:227], a[100:103], a[8:11], a[224:227]
v_exp_f32 v96, v96
v_mfma_f32_16x16x32_bf16 a[224:227], a[104:107], a[16:19], a[224:227]
s_add_u32 m0, s55, 13184
ds_read_b64_tr_b16 a[124:125], v233 offset:4096
ds_read_b64_tr_b16 a[126:127], v233 offset:6144
v_mfma_f32_16x16x32_bf16 a[228:231], a[96:99], a[4:7], 0
buffer_load_dwordx4 %[vK], %[srdK], s75 offen offset:128 nt lds
v_mfma_f32_16x16x32_bf16 a[228:231], a[100:103], a[12:15], a[228:231]
ds_read_b64_tr_b16 a[96:97], v233 offset:8192
)"
R"(ds_read_b64_tr_b16 a[98:99], v233 offset:10240
v_mfma_f32_16x16x32_bf16 a[228:231], a[104:107], a[20:23], a[228:231]
s_waitcnt lgkmcnt(6)
v_exp_f32 v97, v97
v_mfma_f32_16x16x32_bf16 a[224:227], a[108:111], a[24:27], a[224:227]
s_add_u32 m0, s55, 14080
ds_read_b64_tr_b16 a[100:101], v233 offset:12288
ds_read_b64_tr_b16 a[102:103], v233 offset:14336
v_mfma_f32_16x16x32_bf16 a[224:227], a[112:115], a[32:35], a[224:227]
buffer_load_dwordx4 %[vK], %[srdK], s75 offen offset:256 nt lds
v_mfma_f32_16x16x32_bf16 a[224:227], a[116:119], a[40:43], a[224:227]
ds_read_b64_tr_b16 a[104:105], v233 offset:16384
ds_read_b64_tr_b16 a[106:107], v233 offset:18432
v_mfma_f32_16x16x32_bf16 a[228:231], a[108:111], a[28:31], a[228:231]
v_exp_f32 v98, v98
v_mfma_f32_16x16x32_bf16 a[228:231], a[112:115], a[36:39], a[228:231]
ds_read_b64_tr_b16 a[108:109], v233 offset:20480
ds_read_b64_tr_b16 a[110:111], v233 offset:22528
v_mfma_f32_16x16x32_bf16 a[228:231], a[116:119], a[44:47], a[228:231]
s_waitcnt lgkmcnt(6)
v_exp_f32 v99, v99
v_mfma_f32_16x16x32_bf16 a[232:235], a[120:123], a[0:3], 0
ds_read_b64_tr_b16 a[112:113], v234 offset:0
ds_read_b64_tr_b16 a[114:115], v234 offset:2048
)"
R"(v_mfma_f32_16x16x32_bf16 a[232:235], a[124:127], a[8:11], a[232:235]
v_exp_f32 v104, v104
v_mfma_f32_16x16x32_bf16 a[232:235], a[96:99], a[16:19], a[232:235]
ds_read_b64_tr_b16 a[116:117], v234 offset:4096
ds_read_b64_tr_b16 a[118:119], v234 offset:6144
v_mfma_f32_16x16x32_bf16 a[236:239], a[120:123], a[4:7], 0
v_exp_f32 v105, v105
v_mfma_f32_16x16x32_bf16 a[236:239], a[124:127], a[12:15], a[236:239]
ds_read_b64_tr_b16 a[120:121], v234 offset:8192
ds_read_b64_tr_b16 a[122:123], v234 offset:10240
v_mfma_f32_16x16x32_bf16 a[236:239], a[96:99], a[20:23], a[236:239]
s_waitcnt lgkmcnt(6)
v_exp_f32 v106, v106
v_mfma_f32_16x16x32_bf16 a[232:235], a[100:103], a[24:27], a[232:235]
ds_read_b64_tr_b16 a[124:125], v234 offset:12288
ds_read_b64_tr_b16 a[126:127], v234 offset:14336
v_mfma_f32_16x16x32_bf16 a[232:235], a[104:107], a[32:35], a[232:235]
v_exp_f32 v107, v107
v_mfma_f32_16x16x32_bf16 a[232:235], a[108:111], a[40:43], a[232:235]
ds_read_b64_tr_b16 a[96:97], v234 offset:16384
ds_read_b64_tr_b16 a[98:99], v234 offset:18432
v_mfma_f32_16x16x32_bf16 a[236:239], a[100:103], a[28:31], a[236:239]
v_cvt_pk_bf16_f32 v160, v96, v97
v_cvt_pk_bf16_f32 v161, v98, v99
)"
R"(v_mfma_f32_16x16x32_bf16 a[236:239], a[104:107], a[36:39], a[236:239]
ds_read_b64_tr_b16 a[100:101], v234 offset:20480
ds_read_b64_tr_b16 a[102:103], v234 offset:22528
v_mfma_f32_16x16x32_bf16 a[236:239], a[108:111], a[44:47], a[236:239]
s_waitcnt lgkmcnt(6)
v_exp_f32 v100, v100
v_mfma_f32_16x16x32_bf16 a[240:243], a[112:115], a[0:3], 0
ds_read_b64_tr_b16 a[104:105], v235 offset:0
ds_read_b64_tr_b16 a[106:107], v235 offset:2048
v_mfma_f32_16x16x32_bf16 a[240:243], a[116:119], a[8:11], a[240:243]
v_exp_f32 v101, v101
v_mfma_f32_16x16x32_bf16 a[240:243], a[120:123], a[16:19], a[240:243]
ds_read_b64_tr_b16 a[108:109], v235 offset:4096
ds_read_b64_tr_b16 a[110:111], v235 offset:6144
v_mfma_f32_16x16x32_bf16 a[244:247], a[112:115], a[4:7], 0
v_exp_f32 v102, v102
v_mfma_f32_16x16x32_bf16 a[244:247], a[116:119], a[12:15], a[244:247]
ds_read_b64_tr_b16 a[112:113], v235 offset:8192
ds_read_b64_tr_b16 a[114:115], v235 offset:10240
v_mfma_f32_16x16x32_bf16 a[244:247], a[120:123], a[20:23], a[244:247]
s_waitcnt lgkmcnt(6)
v_exp_f32 v103, v103
v_mfma_f32_16x16x32_bf16 a[240:243], a[124:127], a[24:27], a[240:243]
s_add_u32 s76, s68, 73728
)"
R"(ds_read_b64_tr_b16 a[116:117], v235 offset:12288
ds_read_b64_tr_b16 a[118:119], v235 offset:14336
v_mfma_f32_16x16x32_bf16 a[240:243], a[96:99], a[32:35], a[240:243]
s_waitcnt vmcnt(18)
s_barrier
v_mfma_f32_16x16x32_bf16 a[240:243], a[100:103], a[40:43], a[240:243]
s_add_u32 m0, s65, 0
ds_read_b64_tr_b16 a[120:121], v235 offset:16384
ds_read_b64_tr_b16 a[122:123], v235 offset:18432
v_mfma_f32_16x16x32_bf16 a[244:247], a[124:127], a[28:31], a[244:247]
buffer_load_dwordx4 %[vV], %[srdV], s76 offen offset:0 nt lds
v_mfma_f32_16x16x32_bf16 a[244:247], a[96:99], a[36:39], a[244:247]
ds_read_b64_tr_b16 a[124:125], v235 offset:20480
ds_read_b64_tr_b16 a[126:127], v235 offset:22528
v_mfma_f32_16x16x32_bf16 a[244:247], a[100:103], a[44:47], a[244:247]
s_waitcnt lgkmcnt(6)
v_add_u32 v232, s50, %[pK]
v_xor_b32 v233, 64, v232
v_mfma_f32_16x16x32_bf16 a[248:251], a[104:107], a[0:3], 0
s_add_u32 m0, s65, 896
ds_read_b128 a[96:99], v232 offset:0
ds_read_b128 a[100:103], v233 offset:0
v_mfma_f32_16x16x32_bf16 a[248:251], a[108:111], a[8:11], a[248:251]
buffer_load_dwordx4 %[vV], %[srdV], s76 offen offset:128 nt lds
)"
R"(v_mfma_f32_16x16x32_bf16 a[248:251], a[112:115], a[16:19], a[248:251]
v_cvt_pk_bf16_f32 v162, v104, v105
v_cvt_pk_bf16_f32 v163, v106, v107
v_mfma_f32_16x16x32_bf16 a[252:255], a[104:107], a[4:7], 0
v_exp_f32 v108, v108
v_mfma_f32_16x16x32_bf16 a[252:255], a[108:111], a[12:15], a[252:255]
s_add_u32 m0, s65, 1792
ds_read_b128 a[104:107], v232 offset:1024
ds_read_b128 a[108:111], v233 offset:1024
v_mfma_f32_16x16x32_bf16 a[252:255], a[112:115], a[20:23], a[252:255]
s_waitcnt lgkmcnt(4)
buffer_load_dwordx4 %[vV], %[srdV], s76 offen offset:256 nt lds
v_mfma_f32_16x16x32_bf16 a[248:251], a[116:119], a[24:27], a[248:251]
v_exp_f32 v109, v109
v_mfma_f32_16x16x32_bf16 a[248:251], a[120:123], a[32:35], a[248:251]
v_exp_f32 v110, v110
v_mfma_f32_16x16x32_bf16 a[248:251], a[124:127], a[40:43], a[248:251]
v_exp_f32 v111, v111
v_mfma_f32_16x16x32_bf16 a[252:255], a[116:119], a[28:31], a[252:255]
s_add_u32 m0, s65, 0
ds_read_b128 a[112:115], v232 offset:2048
ds_read_b128 a[116:119], v233 offset:2048
v_mfma_f32_16x16x32_bf16 a[252:255], a[120:123], a[36:39], a[252:255]
buffer_load_dwordx4 %[vV], %[srdV], s76 offen offset:3072 nt lds
)"
R"(v_mfma_f32_16x16x32_bf16 a[252:255], a[124:127], a[44:47], a[252:255]
s_waitcnt lgkmcnt(3)
v_cvt_pk_bf16_f32 v164, v100, v101
v_cvt_pk_bf16_f32 v165, v102, v103
v_mfma_f32_16x16x32_bf16 v[128:131], a[96:99], v[184:187], v[176:179]
ds_read_b128 a[120:123], v232 offset:6144
ds_read_b128 a[124:127], v233 offset:6144
v_mfma_f32_16x16x32_bf16 v[128:131], a[100:103], v[192:195], v[128:131]
s_add_u32 m0, s65, 896
v_exp_f32 v112, v112
v_mfma_f32_16x16x32_bf16 v[128:131], a[104:107], v[200:203], v[128:131]
buffer_load_dwordx4 %[vV], %[srdV], s76 offen offset:3200 nt lds
v_mfma_f32_16x16x32_bf16 v[132:135], a[96:99], v[188:191], v[180:183]
v_exp_f32 v113, v113
v_mfma_f32_16x16x32_bf16 v[132:135], a[100:103], v[196:199], v[132:135]
s_add_u32 m0, s65, 1792
ds_read_b128 a[96:99], v232 offset:7168
ds_read_b128 a[100:103], v233 offset:7168
v_mfma_f32_16x16x32_bf16 v[132:135], a[104:107], v[204:207], v[132:135]
s_waitcnt lgkmcnt(4)
v_exp_f32 v114, v114
v_mfma_f32_16x16x32_bf16 v[128:131], a[108:111], v[208:211], v[128:131]
buffer_load_dwordx4 %[vV], %[srdV], s76 offen offset:3328 nt lds
v_mfma_f32_16x16x32_bf16 v[128:131], a[112:115], v[216:219], v[128:131]
)"
R"(v_exp_f32 v115, v115
v_mfma_f32_16x16x32_bf16 v[128:131], a[116:119], v[224:227], v[128:131]
v_cvt_pk_bf16_f32 v166, v108, v109
v_cvt_pk_bf16_f32 v167, v110, v111
v_mfma_f32_16x16x32_bf16 v[132:135], a[108:111], v[212:215], v[132:135]
ds_read_b128 a[104:107], v232 offset:8192
ds_read_b128 a[108:111], v233 offset:8192
v_mfma_f32_16x16x32_bf16 v[132:135], a[112:115], v[220:223], v[132:135]
v_exp_f32 v120, v120
v_mfma_f32_16x16x32_bf16 v[132:135], a[116:119], v[228:231], v[132:135]
s_waitcnt lgkmcnt(3)
v_exp_f32 v121, v121
v_mfma_f32_16x16x32_bf16 v[136:139], a[120:123], v[184:187], v[176:179]
ds_read_b128 a[112:115], v232 offset:12288
ds_read_b128 a[116:119], v233 offset:12288
v_mfma_f32_16x16x32_bf16 v[136:139], a[124:127], v[192:195], v[136:139]
v_exp_f32 v122, v122
v_mfma_f32_16x16x32_bf16 v[136:139], a[96:99], v[200:203], v[136:139]
v_exp_f32 v123, v123
v_mfma_f32_16x16x32_bf16 v[140:143], a[120:123], v[188:191], v[180:183]
v_cvt_pk_bf16_f32 v168, v112, v113
v_cvt_pk_bf16_f32 v169, v114, v115
v_mfma_f32_16x16x32_bf16 v[140:143], a[124:127], v[196:199], v[140:143]
ds_read_b128 a[120:123], v232 offset:13312
)"
R"(ds_read_b128 a[124:127], v233 offset:13312
v_mfma_f32_16x16x32_bf16 v[140:143], a[96:99], v[204:207], v[140:143]
s_waitcnt lgkmcnt(4)
v_exp_f32 v116, v116
v_mfma_f32_16x16x32_bf16 v[136:139], a[100:103], v[208:211], v[136:139]
v_exp_f32 v117, v117
v_mfma_f32_16x16x32_bf16 v[136:139], a[104:107], v[216:219], v[136:139]
v_exp_f32 v118, v118
v_mfma_f32_16x16x32_bf16 v[136:139], a[108:111], v[224:227], v[136:139]
v_exp_f32 v119, v119
v_mfma_f32_16x16x32_bf16 v[140:143], a[100:103], v[212:215], v[140:143]
ds_read_b128 a[96:99], v232 offset:14336
ds_read_b128 a[100:103], v233 offset:14336
v_mfma_f32_16x16x32_bf16 v[140:143], a[104:107], v[220:223], v[140:143]
v_cvt_pk_bf16_f32 v170, v120, v121
v_cvt_pk_bf16_f32 v171, v122, v123
v_mfma_f32_16x16x32_bf16 v[140:143], a[108:111], v[228:231], v[140:143]
s_waitcnt lgkmcnt(3)
v_exp_f32 v124, v124
v_mfma_f32_16x16x32_bf16 v[144:147], a[112:115], v[184:187], v[176:179]
ds_read_b128 a[104:107], v232 offset:18432
ds_read_b128 a[108:111], v233 offset:18432
v_mfma_f32_16x16x32_bf16 v[144:147], a[116:119], v[192:195], v[144:147]
v_exp_f32 v125, v125
)"
R"(v_mfma_f32_16x16x32_bf16 v[144:147], a[120:123], v[200:203], v[144:147]
v_exp_f32 v126, v126
v_mfma_f32_16x16x32_bf16 v[148:151], a[112:115], v[188:191], v[180:183]
v_exp_f32 v127, v127
v_mfma_f32_16x16x32_bf16 v[148:151], a[116:119], v[196:199], v[148:151]
ds_read_b128 a[112:115], v232 offset:19456
ds_read_b128 a[116:119], v233 offset:19456
v_mfma_f32_16x16x32_bf16 v[148:151], a[120:123], v[204:207], v[148:151]
s_waitcnt lgkmcnt(4)
v_cvt_pk_bf16_f32 v172, v116, v117
v_cvt_pk_bf16_f32 v173, v118, v119
v_mfma_f32_16x16x32_bf16 v[144:147], a[124:127], v[208:211], v[144:147]
v_cvt_pk_bf16_f32 v174, v124, v125
v_cvt_pk_bf16_f32 v175, v126, v127
v_mfma_f32_16x16x32_bf16 v[144:147], a[96:99], v[216:219], v[144:147]
s_waitcnt vmcnt(18)
s_barrier
v_mfma_f32_16x16x32_bf16 v[144:147], a[100:103], v[224:227], v[144:147]
s_add_u32 s75, s78, 246912
v_mfma_f32_16x16x32_bf16 v[148:151], a[124:127], v[212:215], v[148:151]
s_add_u32 m0, s60, 0
ds_read_b128 a[120:123], v232 offset:20480
ds_read_b128 a[124:127], v233 offset:20480
v_mfma_f32_16x16x32_bf16 v[148:151], a[96:99], v[220:223], v[148:151]
)"
R"(buffer_load_dwordx4 %[vWo], %[srdWo], s75 offen offset:0 lds
v_mfma_f32_16x16x32_bf16 v[148:151], a[100:103], v[228:231], v[148:151]
s_waitcnt lgkmcnt(3)
v_add_u32 v232, s51, %[pV]
v_xor_b32 v233, 32, v232
v_mfma_f32_16x16x32_bf16 v[152:155], a[104:107], v[184:187], v[176:179]
ds_read_b64_tr_b16 a[96:97], v232 offset:0
ds_read_b64_tr_b16 a[98:99], v232 offset:6144
v_mfma_f32_16x16x32_bf16 v[152:155], a[108:111], v[192:195], v[152:155]
v_xor_b32 v234, 64, v232
v_xor_b32 v235, 96, v232
v_mfma_f32_16x16x32_bf16 v[152:155], a[112:115], v[200:203], v[152:155]
s_add_u32 m0, s60, 1024
ds_read_b64_tr_b16 a[100:101], v232 offset:12288
ds_read_b64_tr_b16 a[102:103], v232 offset:18432
v_mfma_f32_16x16x32_bf16 v[156:159], a[104:107], v[188:191], v[180:183]
buffer_load_dwordx4 %[vX1], %[srdWo], s75 offen offset:0 lds
v_mfma_f32_16x16x32_bf16 v[156:159], a[108:111], v[196:199], v[156:159]
ds_read_b64_tr_b16 a[104:105], v233 offset:0
ds_read_b64_tr_b16 a[106:107], v233 offset:6144
v_mfma_f32_16x16x32_bf16 v[156:159], a[112:115], v[204:207], v[156:159]
s_waitcnt lgkmcnt(6)
v_mfma_f32_16x16x32_bf16 v[152:155], a[116:119], v[208:211], v[152:155]
s_add_u32 m0, s60, 2048
)"
R"(ds_read_b64_tr_b16 a[108:109], v233 offset:12288
ds_read_b64_tr_b16 a[110:111], v233 offset:18432
v_mfma_f32_16x16x32_bf16 v[152:155], a[120:123], v[216:219], v[152:155]
buffer_load_dwordx4 %[vW], %[srdWo], s75 offen offset:0 lds
v_mfma_f32_16x16x32_bf16 v[152:155], a[124:127], v[224:227], v[152:155]
ds_read_b64_tr_b16 a[112:113], v234 offset:0
ds_read_b64_tr_b16 a[114:115], v234 offset:6144
v_mfma_f32_16x16x32_bf16 v[156:159], a[116:119], v[212:215], v[156:159]
s_add_u32 s75, s78, 277632
v_mfma_f32_16x16x32_bf16 v[156:159], a[120:123], v[220:223], v[156:159]
s_add_u32 m0, s60, 3072
ds_read_b64_tr_b16 a[116:117], v234 offset:12288
ds_read_b64_tr_b16 a[118:119], v234 offset:18432
v_mfma_f32_16x16x32_bf16 v[156:159], a[124:127], v[228:231], v[156:159]
buffer_load_dwordx4 %[vWo], %[srdWo], s75 offen offset:0 lds
v_mfma_f32_16x16x32_bf16 a[132:135], a[128:131], v[160:163], a[132:135]
v_mfma_f32_16x16x32_bf16 a[132:135], a[128:131], v[168:171], a[132:135]
v_mfma_f32_16x16x32_bf16 a[136:139], a[128:131], v[164:167], a[136:139]
s_add_u32 m0, s60, 4096
v_mfma_f32_16x16x32_bf16 a[136:139], a[128:131], v[172:175], a[136:139]
s_waitcnt lgkmcnt(4)
buffer_load_dwordx4 %[vX1], %[srdWo], s75 offen offset:0 lds
v_mfma_f32_16x16x32_bf16 v[0:3], a[96:99], v[160:163], v[0:3]
ds_read_b64_tr_b16 a[120:121], v235 offset:0
)"
R"(ds_read_b64_tr_b16 a[122:123], v235 offset:6144
v_mfma_f32_16x16x32_bf16 v[0:3], a[100:103], v[168:171], v[0:3]
v_max3_f32 v236, v128, v129, v130
v_max3_f32 v237, v132, v133, v134
v_mfma_f32_16x16x32_bf16 v[4:7], a[96:99], v[164:167], v[4:7]
s_add_u32 m0, s60, 5120
ds_read_b64_tr_b16 a[124:125], v235 offset:12288
ds_read_b64_tr_b16 a[126:127], v235 offset:18432
v_mfma_f32_16x16x32_bf16 v[4:7], a[100:103], v[172:175], v[4:7]
buffer_load_dwordx4 %[vW], %[srdWo], s75 offen offset:0 lds
v_mfma_f32_16x16x32_bf16 v[8:11], a[104:107], v[160:163], v[8:11]
ds_read_b64_tr_b16 a[96:97], v232 offset:1024
ds_read_b64_tr_b16 a[98:99], v232 offset:7168
v_mfma_f32_16x16x32_bf16 v[8:11], a[108:111], v[168:171], v[8:11]
v_max_f32 v236, v236, v131
v_max_f32 v237, v237, v135
v_mfma_f32_16x16x32_bf16 v[12:15], a[104:107], v[164:167], v[12:15]
ds_read_b64_tr_b16 a[100:101], v232 offset:13312
ds_read_b64_tr_b16 a[102:103], v232 offset:19456
v_mfma_f32_16x16x32_bf16 v[12:15], a[108:111], v[172:175], v[12:15]
s_waitcnt lgkmcnt(4)
v_max3_f32 v236, v236, v136, v137
v_max3_f32 v237, v237, v140, v141
v_mfma_f32_16x16x32_bf16 v[16:19], a[112:115], v[160:163], v[16:19]
)"
R"(ds_read_b64_tr_b16 a[104:105], v233 offset:1024
ds_read_b64_tr_b16 a[106:107], v233 offset:7168
v_mfma_f32_16x16x32_bf16 v[16:19], a[116:119], v[168:171], v[16:19]
v_max3_f32 v236, v236, v138, v139
v_max3_f32 v237, v237, v142, v143
v_mfma_f32_16x16x32_bf16 v[20:23], a[112:115], v[164:167], v[20:23]
ds_read_b64_tr_b16 a[108:109], v233 offset:13312
ds_read_b64_tr_b16 a[110:111], v233 offset:19456
v_mfma_f32_16x16x32_bf16 v[20:23], a[116:119], v[172:175], v[20:23]
v_max3_f32 v236, v236, v144, v145
v_max3_f32 v237, v237, v148, v149
v_mfma_f32_16x16x32_bf16 v[24:27], a[120:123], v[160:163], v[24:27]
ds_read_b64_tr_b16 a[112:113], v234 offset:1024
ds_read_b64_tr_b16 a[114:115], v234 offset:7168
v_mfma_f32_16x16x32_bf16 v[24:27], a[124:127], v[168:171], v[24:27]
v_max3_f32 v236, v236, v146, v147
v_max3_f32 v237, v237, v150, v151
v_mfma_f32_16x16x32_bf16 v[28:31], a[120:123], v[164:167], v[28:31]
ds_read_b64_tr_b16 a[116:117], v234 offset:13312
ds_read_b64_tr_b16 a[118:119], v234 offset:19456
v_mfma_f32_16x16x32_bf16 v[28:31], a[124:127], v[172:175], v[28:31]
s_waitcnt lgkmcnt(4)
v_max3_f32 v236, v236, v152, v153
v_max3_f32 v237, v237, v156, v157
)"
R"(v_mfma_f32_16x16x32_bf16 v[32:35], a[96:99], v[160:163], v[32:35]
ds_read_b64_tr_b16 a[120:121], v235 offset:1024
ds_read_b64_tr_b16 a[122:123], v235 offset:7168
v_mfma_f32_16x16x32_bf16 v[32:35], a[100:103], v[168:171], v[32:35]
v_max3_f32 v236, v236, v154, v155
v_max3_f32 v237, v237, v158, v159
v_mfma_f32_16x16x32_bf16 v[36:39], a[96:99], v[164:167], v[36:39]
ds_read_b64_tr_b16 a[124:125], v235 offset:13312
ds_read_b64_tr_b16 a[126:127], v235 offset:19456
v_mfma_f32_16x16x32_bf16 v[36:39], a[100:103], v[172:175], v[36:39]
v_max_f32 v236, v236, v237
v_cmp_lt_f32 vcc, s77, v236
v_mfma_f32_16x16x32_bf16 v[40:43], a[104:107], v[160:163], v[40:43]
ds_read_b64_tr_b16 a[96:97], v232 offset:2048
ds_read_b64_tr_b16 a[98:99], v232 offset:8192
v_mfma_f32_16x16x32_bf16 v[40:43], a[108:111], v[168:171], v[40:43]
v_mfma_f32_16x16x32_bf16 v[44:47], a[104:107], v[164:167], v[44:47]
ds_read_b64_tr_b16 a[100:101], v232 offset:14336
ds_read_b64_tr_b16 a[102:103], v232 offset:20480
v_mfma_f32_16x16x32_bf16 v[44:47], a[108:111], v[172:175], v[44:47]
s_waitcnt lgkmcnt(4)
v_mfma_f32_16x16x32_bf16 v[48:51], a[112:115], v[160:163], v[48:51]
ds_read_b64_tr_b16 a[104:105], v233 offset:2048
ds_read_b64_tr_b16 a[106:107], v233 offset:8192
)"
R"(v_mfma_f32_16x16x32_bf16 v[48:51], a[116:119], v[168:171], v[48:51]
v_mfma_f32_16x16x32_bf16 v[52:55], a[112:115], v[164:167], v[52:55]
ds_read_b64_tr_b16 a[108:109], v233 offset:14336
ds_read_b64_tr_b16 a[110:111], v233 offset:20480
v_mfma_f32_16x16x32_bf16 v[52:55], a[116:119], v[172:175], v[52:55]
v_mfma_f32_16x16x32_bf16 v[56:59], a[120:123], v[160:163], v[56:59]
ds_read_b64_tr_b16 a[112:113], v234 offset:2048
ds_read_b64_tr_b16 a[114:115], v234 offset:8192
v_mfma_f32_16x16x32_bf16 v[56:59], a[124:127], v[168:171], v[56:59]
v_mfma_f32_16x16x32_bf16 v[60:63], a[120:123], v[164:167], v[60:63]
s_add_u32 s75, s67, 122880
ds_read_b64_tr_b16 a[116:117], v234 offset:14336
ds_read_b64_tr_b16 a[118:119], v234 offset:20480
v_mfma_f32_16x16x32_bf16 v[60:63], a[124:127], v[172:175], v[60:63]
s_waitcnt lgkmcnt(4)
s_waitcnt vmcnt(18)
s_barrier
v_mfma_f32_16x16x32_bf16 v[64:67], a[96:99], v[160:163], v[64:67]
s_add_u32 m0, s56, 0
ds_read_b64_tr_b16 a[120:121], v235 offset:2048
ds_read_b64_tr_b16 a[122:123], v235 offset:8192
v_mfma_f32_16x16x32_bf16 v[64:67], a[100:103], v[168:171], v[64:67]
buffer_load_dwordx4 %[vK], %[srdK], s75 offen offset:0 nt lds
v_mfma_f32_16x16x32_bf16 v[68:71], a[96:99], v[164:167], v[68:71]
)"
R"(ds_read_b64_tr_b16 a[124:125], v235 offset:14336
ds_read_b64_tr_b16 a[126:127], v235 offset:20480
v_mfma_f32_16x16x32_bf16 v[68:71], a[100:103], v[172:175], v[68:71]
v_add_u32 v232, s52, %[pWo]
v_xor_b32 v233, 32, v232
v_mfma_f32_16x16x32_bf16 v[72:75], a[104:107], v[160:163], v[72:75]
ds_read_b64_tr_b16 a[96:97], v232 offset:0
ds_read_b64_tr_b16 a[98:99], v232 offset:2048
v_mfma_f32_16x16x32_bf16 v[72:75], a[108:111], v[168:171], v[72:75]
v_xor_b32 v234, 64, v232
v_xor_b32 v235, 96, v232
v_mfma_f32_16x16x32_bf16 v[76:79], a[104:107], v[164:167], v[76:79]
s_add_u32 m0, s56, 896
ds_read_b64_tr_b16 a[100:101], v232 offset:4096
ds_read_b64_tr_b16 a[102:103], v232 offset:6144
v_mfma_f32_16x16x32_bf16 v[76:79], a[108:111], v[172:175], v[76:79]
s_waitcnt lgkmcnt(4)
buffer_load_dwordx4 %[vK], %[srdK], s75 offen offset:128 nt lds
v_mfma_f32_16x16x32_bf16 v[80:83], a[112:115], v[160:163], v[80:83]
ds_read_b64_tr_b16 a[104:105], v232 offset:8192
ds_read_b64_tr_b16 a[106:107], v232 offset:10240
v_mfma_f32_16x16x32_bf16 v[80:83], a[116:119], v[168:171], v[80:83]
v_mfma_f32_16x16x32_bf16 v[84:87], a[112:115], v[164:167], v[84:87]
s_add_u32 m0, s56, 1792
)"
R"(ds_read_b64_tr_b16 a[108:109], v232 offset:12288
ds_read_b64_tr_b16 a[110:111], v232 offset:14336
v_mfma_f32_16x16x32_bf16 v[84:87], a[116:119], v[172:175], v[84:87]
buffer_load_dwordx4 %[vK], %[srdK], s75 offen offset:256 nt lds
v_mfma_f32_16x16x32_bf16 v[88:91], a[120:123], v[160:163], v[88:91]
ds_read_b64_tr_b16 a[112:113], v232 offset:16384
ds_read_b64_tr_b16 a[114:115], v232 offset:18432
v_mfma_f32_16x16x32_bf16 v[88:91], a[124:127], v[168:171], v[88:91]
s_add_u32 s75, s67, 135168
v_mfma_f32_16x16x32_bf16 v[92:95], a[120:123], v[164:167], v[92:95]
s_add_u32 m0, s56, 12288
ds_read_b64_tr_b16 a[116:117], v232 offset:20480
ds_read_b64_tr_b16 a[118:119], v232 offset:22528
v_mfma_f32_16x16x32_bf16 v[92:95], a[124:127], v[172:175], v[92:95]
s_waitcnt lgkmcnt(6)
buffer_load_dwordx4 %[vK], %[srdK], s75 offen offset:0 nt lds
s_cbranch_vccnz RARE12_%=
BACK12_%=:
v_mfma_f32_16x16x32_bf16 a[192:195], a[96:99], a[48:51], a[192:195]
ds_read_b64_tr_b16 a[120:121], v233 offset:0
ds_read_b64_tr_b16 a[122:123], v233 offset:2048
v_mfma_f32_16x16x32_bf16 a[192:195], a[100:103], a[56:59], a[192:195]
v_exp_f32 v128, v128
v_mfma_f32_16x16x32_bf16 a[192:195], a[104:107], a[64:67], a[192:195]
)"
R"(s_add_u32 m0, s56, 13184
ds_read_b64_tr_b16 a[124:125], v233 offset:4096
ds_read_b64_tr_b16 a[126:127], v233 offset:6144
v_mfma_f32_16x16x32_bf16 a[196:199], a[96:99], a[52:55], a[196:199]
buffer_load_dwordx4 %[vK], %[srdK], s75 offen offset:128 nt lds
v_mfma_f32_16x16x32_bf16 a[196:199], a[100:103], a[60:63], a[196:199]
ds_read_b64_tr_b16 a[96:97], v233 offset:8192
ds_read_b64_tr_b16 a[98:99], v233 offset:10240
v_mfma_f32_16x16x32_bf16 a[196:199], a[104:107], a[68:71], a[196:199]
s_waitcnt lgkmcnt(6)
v_exp_f32 v129, v129
v_mfma_f32_16x16x32_bf16 a[192:195], a[108:111], a[72:75], a[192:195]
s_add_u32 m0, s56, 14080
ds_read_b64_tr_b16 a[100:101], v233 offset:12288
ds_read_b64_tr_b16 a[102:103], v233 offset:14336
v_mfma_f32_16x16x32_bf16 a[192:195], a[112:115], a[80:83], a[192:195]
buffer_load_dwordx4 %[vK], %[srdK], s75 offen offset:256 nt lds
v_mfma_f32_16x16x32_bf16 a[192:195], a[116:119], a[88:91], a[192:195]
ds_read_b64_tr_b16 a[104:105], v233 offset:16384
ds_read_b64_tr_b16 a[106:107], v233 offset:18432
v_mfma_f32_16x16x32_bf16 a[196:199], a[108:111], a[76:79], a[196:199]
v_exp_f32 v130, v130
v_mfma_f32_16x16x32_bf16 a[196:199], a[112:115], a[84:87], a[196:199]
ds_read_b64_tr_b16 a[108:109], v233 offset:20480
)"
R"(ds_read_b64_tr_b16 a[110:111], v233 offset:22528
v_mfma_f32_16x16x32_bf16 a[196:199], a[116:119], a[92:95], a[196:199]
s_waitcnt lgkmcnt(6)
v_exp_f32 v131, v131
v_mfma_f32_16x16x32_bf16 a[200:203], a[120:123], a[48:51], a[200:203]
ds_read_b64_tr_b16 a[112:113], v234 offset:0
ds_read_b64_tr_b16 a[114:115], v234 offset:2048
v_mfma_f32_16x16x32_bf16 a[200:203], a[124:127], a[56:59], a[200:203]
v_exp_f32 v136, v136
v_mfma_f32_16x16x32_bf16 a[200:203], a[96:99], a[64:67], a[200:203]
ds_read_b64_tr_b16 a[116:117], v234 offset:4096
ds_read_b64_tr_b16 a[118:119], v234 offset:6144
v_mfma_f32_16x16x32_bf16 a[204:207], a[120:123], a[52:55], a[204:207]
v_exp_f32 v137, v137
v_mfma_f32_16x16x32_bf16 a[204:207], a[124:127], a[60:63], a[204:207]
ds_read_b64_tr_b16 a[120:121], v234 offset:8192
ds_read_b64_tr_b16 a[122:123], v234 offset:10240
v_mfma_f32_16x16x32_bf16 a[204:207], a[96:99], a[68:71], a[204:207]
s_waitcnt lgkmcnt(6)
v_exp_f32 v138, v138
v_mfma_f32_16x16x32_bf16 a[200:203], a[100:103], a[72:75], a[200:203]
ds_read_b64_tr_b16 a[124:125], v234 offset:12288
ds_read_b64_tr_b16 a[126:127], v234 offset:14336
v_mfma_f32_16x16x32_bf16 a[200:203], a[104:107], a[80:83], a[200:203]
)"
R"(v_exp_f32 v139, v139
v_mfma_f32_16x16x32_bf16 a[200:203], a[108:111], a[88:91], a[200:203]
ds_read_b64_tr_b16 a[96:97], v234 offset:16384
ds_read_b64_tr_b16 a[98:99], v234 offset:18432
v_mfma_f32_16x16x32_bf16 a[204:207], a[100:103], a[76:79], a[204:207]
v_cvt_pk_bf16_f32 v160, v128, v129
v_cvt_pk_bf16_f32 v161, v130, v131
v_mfma_f32_16x16x32_bf16 a[204:207], a[104:107], a[84:87], a[204:207]
ds_read_b64_tr_b16 a[100:101], v234 offset:20480
ds_read_b64_tr_b16 a[102:103], v234 offset:22528
v_mfma_f32_16x16x32_bf16 a[204:207], a[108:111], a[92:95], a[204:207]
s_waitcnt lgkmcnt(6)
v_exp_f32 v132, v132
v_mfma_f32_16x16x32_bf16 a[208:211], a[112:115], a[48:51], a[208:211]
ds_read_b64_tr_b16 a[104:105], v235 offset:0
ds_read_b64_tr_b16 a[106:107], v235 offset:2048
v_mfma_f32_16x16x32_bf16 a[208:211], a[116:119], a[56:59], a[208:211]
v_exp_f32 v133, v133
v_mfma_f32_16x16x32_bf16 a[208:211], a[120:123], a[64:67], a[208:211]
ds_read_b64_tr_b16 a[108:109], v235 offset:4096
ds_read_b64_tr_b16 a[110:111], v235 offset:6144
v_mfma_f32_16x16x32_bf16 a[212:215], a[112:115], a[52:55], a[212:215]
v_exp_f32 v134, v134
v_mfma_f32_16x16x32_bf16 a[212:215], a[116:119], a[60:63], a[212:215]
)"
R"(ds_read_b64_tr_b16 a[112:113], v235 offset:8192
ds_read_b64_tr_b16 a[114:115], v235 offset:10240
v_mfma_f32_16x16x32_bf16 a[212:215], a[120:123], a[68:71], a[212:215]
s_waitcnt lgkmcnt(6)
v_exp_f32 v135, v135
v_mfma_f32_16x16x32_bf16 a[208:211], a[124:127], a[72:75], a[208:211]
s_add_u32 s76, s68, 98304
ds_read_b64_tr_b16 a[116:117], v235 offset:12288
ds_read_b64_tr_b16 a[118:119], v235 offset:14336
v_mfma_f32_16x16x32_bf16 a[208:211], a[96:99], a[80:83], a[208:211]
s_waitcnt vmcnt(18)
s_barrier
v_mfma_f32_16x16x32_bf16 a[208:211], a[100:103], a[88:91], a[208:211]
s_add_u32 m0, s63, 0
ds_read_b64_tr_b16 a[120:121], v235 offset:16384
ds_read_b64_tr_b16 a[122:123], v235 offset:18432
v_mfma_f32_16x16x32_bf16 a[212:215], a[124:127], a[76:79], a[212:215]
buffer_load_dwordx4 %[vV], %[srdV], s76 offen offset:0 nt lds
v_mfma_f32_16x16x32_bf16 a[212:215], a[96:99], a[84:87], a[212:215]
ds_read_b64_tr_b16 a[124:125], v235 offset:20480
ds_read_b64_tr_b16 a[126:127], v235 offset:22528
v_mfma_f32_16x16x32_bf16 a[212:215], a[100:103], a[92:95], a[212:215]
s_waitcnt lgkmcnt(6)
v_add_u32 v232, s49, %[pK]
)"
R"(v_xor_b32 v233, 64, v232
v_mfma_f32_16x16x32_bf16 a[216:219], a[104:107], a[48:51], a[216:219]
s_add_u32 m0, s63, 896
ds_read_b128 a[96:99], v232 offset:0
ds_read_b128 a[100:103], v233 offset:0
v_mfma_f32_16x16x32_bf16 a[216:219], a[108:111], a[56:59], a[216:219]
buffer_load_dwordx4 %[vV], %[srdV], s76 offen offset:128 nt lds
v_mfma_f32_16x16x32_bf16 a[216:219], a[112:115], a[64:67], a[216:219]
v_cvt_pk_bf16_f32 v162, v136, v137
v_cvt_pk_bf16_f32 v163, v138, v139
v_mfma_f32_16x16x32_bf16 a[220:223], a[104:107], a[52:55], a[220:223]
v_exp_f32 v140, v140
v_mfma_f32_16x16x32_bf16 a[220:223], a[108:111], a[60:63], a[220:223]
s_add_u32 m0, s63, 1792
ds_read_b128 a[104:107], v232 offset:1024
ds_read_b128 a[108:111], v233 offset:1024
v_mfma_f32_16x16x32_bf16 a[220:223], a[112:115], a[68:71], a[220:223]
s_waitcnt lgkmcnt(4)
buffer_load_dwordx4 %[vV], %[srdV], s76 offen offset:256 nt lds
v_mfma_f32_16x16x32_bf16 a[216:219], a[116:119], a[72:75], a[216:219]
v_exp_f32 v141, v141
v_mfma_f32_16x16x32_bf16 a[216:219], a[120:123], a[80:83], a[216:219]
v_exp_f32 v142, v142
v_mfma_f32_16x16x32_bf16 a[216:219], a[124:127], a[88:91], a[216:219]
)"
R"(v_exp_f32 v143, v143
v_mfma_f32_16x16x32_bf16 a[220:223], a[116:119], a[76:79], a[220:223]
s_add_u32 m0, s63, 0
ds_read_b128 a[112:115], v232 offset:2048
ds_read_b128 a[116:119], v233 offset:2048
v_mfma_f32_16x16x32_bf16 a[220:223], a[120:123], a[84:87], a[220:223]
buffer_load_dwordx4 %[vV], %[srdV], s76 offen offset:3072 nt lds
v_mfma_f32_16x16x32_bf16 a[220:223], a[124:127], a[92:95], a[220:223]
s_waitcnt lgkmcnt(3)
v_cvt_pk_bf16_f32 v164, v132, v133
v_cvt_pk_bf16_f32 v165, v134, v135
v_mfma_f32_16x16x32_bf16 v[96:99], a[96:99], v[184:187], v[176:179]
ds_read_b128 a[120:123], v232 offset:6144
ds_read_b128 a[124:127], v233 offset:6144
v_mfma_f32_16x16x32_bf16 v[96:99], a[100:103], v[192:195], v[96:99]
s_add_u32 m0, s63, 896
v_exp_f32 v144, v144
v_mfma_f32_16x16x32_bf16 v[96:99], a[104:107], v[200:203], v[96:99]
buffer_load_dwordx4 %[vV], %[srdV], s76 offen offset:3200 nt lds
v_mfma_f32_16x16x32_bf16 v[100:103], a[96:99], v[188:191], v[180:183]
v_exp_f32 v145, v145
v_mfma_f32_16x16x32_bf16 v[100:103], a[100:103], v[196:199], v[100:103]
s_add_u32 m0, s63, 1792
ds_read_b128 a[96:99], v232 offset:7168
)"
R"(ds_read_b128 a[100:103], v233 offset:7168
v_mfma_f32_16x16x32_bf16 v[100:103], a[104:107], v[204:207], v[100:103]
s_waitcnt lgkmcnt(4)
v_exp_f32 v146, v146
v_mfma_f32_16x16x32_bf16 v[96:99], a[108:111], v[208:211], v[96:99]
buffer_load_dwordx4 %[vV], %[srdV], s76 offen offset:3328 nt lds
v_mfma_f32_16x16x32_bf16 v[96:99], a[112:115], v[216:219], v[96:99]
v_exp_f32 v147, v147
v_mfma_f32_16x16x32_bf16 v[96:99], a[116:119], v[224:227], v[96:99]
v_cvt_pk_bf16_f32 v166, v140, v141
v_cvt_pk_bf16_f32 v167, v142, v143
v_mfma_f32_16x16x32_bf16 v[100:103], a[108:111], v[212:215], v[100:103]
ds_read_b128 a[104:107], v232 offset:8192
ds_read_b128 a[108:111], v233 offset:8192
v_mfma_f32_16x16x32_bf16 v[100:103], a[112:115], v[220:223], v[100:103]
v_exp_f32 v152, v152
v_mfma_f32_16x16x32_bf16 v[100:103], a[116:119], v[228:231], v[100:103]
s_waitcnt lgkmcnt(3)
v_exp_f32 v153, v153
v_mfma_f32_16x16x32_bf16 v[104:107], a[120:123], v[184:187], v[176:179]
ds_read_b128 a[112:115], v232 offset:12288
ds_read_b128 a[116:119], v233 offset:12288
v_mfma_f32_16x16x32_bf16 v[104:107], a[124:127], v[192:195], v[104:107]
v_exp_f32 v154, v154
)"
R"(v_mfma_f32_16x16x32_bf16 v[104:107], a[96:99], v[200:203], v[104:107]
v_exp_f32 v155, v155
v_mfma_f32_16x16x32_bf16 v[108:111], a[120:123], v[188:191], v[180:183]
v_cvt_pk_bf16_f32 v168, v144, v145
v_cvt_pk_bf16_f32 v169, v146, v147
v_mfma_f32_16x16x32_bf16 v[108:111], a[124:127], v[196:199], v[108:111]
ds_read_b128 a[120:123], v232 offset:13312
ds_read_b128 a[124:127], v233 offset:13312
v_mfma_f32_16x16x32_bf16 v[108:111], a[96:99], v[204:207], v[108:111]
s_waitcnt lgkmcnt(4)
v_exp_f32 v148, v148
v_mfma_f32_16x16x32_bf16 v[104:107], a[100:103], v[208:211], v[104:107]
v_exp_f32 v149, v149
v_mfma_f32_16x16x32_bf16 v[104:107], a[104:107], v[216:219], v[104:107]
v_exp_f32 v150, v150
v_mfma_f32_16x16x32_bf16 v[104:107], a[108:111], v[224:227], v[104:107]
v_exp_f32 v151, v151
v_mfma_f32_16x16x32_bf16 v[108:111], a[100:103], v[212:215], v[108:111]
ds_read_b128 a[96:99], v232 offset:14336
ds_read_b128 a[100:103], v233 offset:14336
v_mfma_f32_16x16x32_bf16 v[108:111], a[104:107], v[220:223], v[108:111]
v_cvt_pk_bf16_f32 v170, v152, v153
v_cvt_pk_bf16_f32 v171, v154, v155
v_mfma_f32_16x16x32_bf16 v[108:111], a[108:111], v[228:231], v[108:111]
)"
R"(s_waitcnt lgkmcnt(3)
v_exp_f32 v156, v156
v_mfma_f32_16x16x32_bf16 v[112:115], a[112:115], v[184:187], v[176:179]
ds_read_b128 a[104:107], v232 offset:18432
ds_read_b128 a[108:111], v233 offset:18432
v_mfma_f32_16x16x32_bf16 v[112:115], a[116:119], v[192:195], v[112:115]
v_exp_f32 v157, v157
v_mfma_f32_16x16x32_bf16 v[112:115], a[120:123], v[200:203], v[112:115]
v_exp_f32 v158, v158
v_mfma_f32_16x16x32_bf16 v[116:119], a[112:115], v[188:191], v[180:183]
v_exp_f32 v159, v159
v_mfma_f32_16x16x32_bf16 v[116:119], a[116:119], v[196:199], v[116:119]
ds_read_b128 a[112:115], v232 offset:19456
ds_read_b128 a[116:119], v233 offset:19456
v_mfma_f32_16x16x32_bf16 v[116:119], a[120:123], v[204:207], v[116:119]
s_waitcnt lgkmcnt(4)
v_cvt_pk_bf16_f32 v172, v148, v149
v_cvt_pk_bf16_f32 v173, v150, v151
v_mfma_f32_16x16x32_bf16 v[112:115], a[124:127], v[208:211], v[112:115]
v_cvt_pk_bf16_f32 v174, v156, v157
v_cvt_pk_bf16_f32 v175, v158, v159
v_mfma_f32_16x16x32_bf16 v[112:115], a[96:99], v[216:219], v[112:115]
s_waitcnt vmcnt(18)
s_barrier
)"
R"(v_mfma_f32_16x16x32_bf16 v[112:115], a[100:103], v[224:227], v[112:115]
s_add_u32 s75, s67, 147456
v_mfma_f32_16x16x32_bf16 v[116:119], a[124:127], v[212:215], v[116:119]
s_add_u32 m0, s58, 0
ds_read_b128 a[120:123], v232 offset:20480
ds_read_b128 a[124:127], v233 offset:20480
v_mfma_f32_16x16x32_bf16 v[116:119], a[96:99], v[220:223], v[116:119]
buffer_load_dwordx4 %[vK], %[srdK], s75 offen offset:0 nt lds
v_mfma_f32_16x16x32_bf16 v[116:119], a[100:103], v[228:231], v[116:119]
s_waitcnt lgkmcnt(3)
v_add_u32 v232, s53, %[pV]
v_xor_b32 v233, 32, v232
v_mfma_f32_16x16x32_bf16 v[120:123], a[104:107], v[184:187], v[176:179]
ds_read_b64_tr_b16 a[96:97], v232 offset:0
ds_read_b64_tr_b16 a[98:99], v232 offset:6144
v_mfma_f32_16x16x32_bf16 v[120:123], a[108:111], v[192:195], v[120:123]
v_xor_b32 v234, 64, v232
v_xor_b32 v235, 96, v232
v_mfma_f32_16x16x32_bf16 v[120:123], a[112:115], v[200:203], v[120:123]
s_add_u32 m0, s58, 896
ds_read_b64_tr_b16 a[100:101], v232 offset:12288
ds_read_b64_tr_b16 a[102:103], v232 offset:18432
v_mfma_f32_16x16x32_bf16 v[124:127], a[104:107], v[188:191], v[180:183]
buffer_load_dwordx4 %[vK], %[srdK], s75 offen offset:128 nt lds
)"
R"(v_mfma_f32_16x16x32_bf16 v[124:127], a[108:111], v[196:199], v[124:127]
ds_read_b64_tr_b16 a[104:105], v233 offset:0
ds_read_b64_tr_b16 a[106:107], v233 offset:6144
v_mfma_f32_16x16x32_bf16 v[124:127], a[112:115], v[204:207], v[124:127]
s_waitcnt lgkmcnt(6)
v_mfma_f32_16x16x32_bf16 v[120:123], a[116:119], v[208:211], v[120:123]
s_add_u32 m0, s58, 1792
ds_read_b64_tr_b16 a[108:109], v233 offset:12288
ds_read_b64_tr_b16 a[110:111], v233 offset:18432
v_mfma_f32_16x16x32_bf16 v[120:123], a[120:123], v[216:219], v[120:123]
buffer_load_dwordx4 %[vK], %[srdK], s75 offen offset:256 nt lds
v_mfma_f32_16x16x32_bf16 v[120:123], a[124:127], v[224:227], v[120:123]
ds_read_b64_tr_b16 a[112:113], v234 offset:0
ds_read_b64_tr_b16 a[114:115], v234 offset:6144
v_mfma_f32_16x16x32_bf16 v[124:127], a[116:119], v[212:215], v[124:127]
s_add_u32 s75, s67, 159744
v_mfma_f32_16x16x32_bf16 v[124:127], a[120:123], v[220:223], v[124:127]
s_add_u32 m0, s58, 12288
ds_read_b64_tr_b16 a[116:117], v234 offset:12288
ds_read_b64_tr_b16 a[118:119], v234 offset:18432
v_mfma_f32_16x16x32_bf16 v[124:127], a[124:127], v[228:231], v[124:127]
buffer_load_dwordx4 %[vK], %[srdK], s75 offen offset:0 nt lds
v_mfma_f32_16x16x32_bf16 a[132:135], a[128:131], v[160:163], a[132:135]
v_mfma_f32_16x16x32_bf16 a[132:135], a[128:131], v[168:171], a[132:135]
)"
R"(v_mfma_f32_16x16x32_bf16 a[136:139], a[128:131], v[164:167], a[136:139]
s_add_u32 m0, s58, 13184
v_mfma_f32_16x16x32_bf16 a[136:139], a[128:131], v[172:175], a[136:139]
s_waitcnt lgkmcnt(4)
buffer_load_dwordx4 %[vK], %[srdK], s75 offen offset:128 nt lds
v_mfma_f32_16x16x32_bf16 v[0:3], a[96:99], v[160:163], v[0:3]
ds_read_b64_tr_b16 a[120:121], v235 offset:0
ds_read_b64_tr_b16 a[122:123], v235 offset:6144
v_mfma_f32_16x16x32_bf16 v[0:3], a[100:103], v[168:171], v[0:3]
v_max3_f32 v236, v96, v97, v98
v_max3_f32 v237, v100, v101, v102
v_mfma_f32_16x16x32_bf16 v[4:7], a[96:99], v[164:167], v[4:7]
s_add_u32 m0, s58, 14080
ds_read_b64_tr_b16 a[124:125], v235 offset:12288
ds_read_b64_tr_b16 a[126:127], v235 offset:18432
v_mfma_f32_16x16x32_bf16 v[4:7], a[100:103], v[172:175], v[4:7]
buffer_load_dwordx4 %[vK], %[srdK], s75 offen offset:256 nt lds
v_mfma_f32_16x16x32_bf16 v[8:11], a[104:107], v[160:163], v[8:11]
ds_read_b64_tr_b16 a[96:97], v232 offset:1024
ds_read_b64_tr_b16 a[98:99], v232 offset:7168
v_mfma_f32_16x16x32_bf16 v[8:11], a[108:111], v[168:171], v[8:11]
v_max_f32 v236, v236, v99
v_max_f32 v237, v237, v103
v_mfma_f32_16x16x32_bf16 v[12:15], a[104:107], v[164:167], v[12:15]
)"
R"(ds_read_b64_tr_b16 a[100:101], v232 offset:13312
ds_read_b64_tr_b16 a[102:103], v232 offset:19456
v_mfma_f32_16x16x32_bf16 v[12:15], a[108:111], v[172:175], v[12:15]
s_waitcnt lgkmcnt(4)
v_max3_f32 v236, v236, v104, v105
v_max3_f32 v237, v237, v108, v109
v_mfma_f32_16x16x32_bf16 v[16:19], a[112:115], v[160:163], v[16:19]
ds_read_b64_tr_b16 a[104:105], v233 offset:1024
ds_read_b64_tr_b16 a[106:107], v233 offset:7168
v_mfma_f32_16x16x32_bf16 v[16:19], a[116:119], v[168:171], v[16:19]
v_max3_f32 v236, v236, v106, v107
v_max3_f32 v237, v237, v110, v111
v_mfma_f32_16x16x32_bf16 v[20:23], a[112:115], v[164:167], v[20:23]
ds_read_b64_tr_b16 a[108:109], v233 offset:13312
ds_read_b64_tr_b16 a[110:111], v233 offset:19456
v_mfma_f32_16x16x32_bf16 v[20:23], a[116:119], v[172:175], v[20:23]
v_max3_f32 v236, v236, v112, v113
v_max3_f32 v237, v237, v116, v117
v_mfma_f32_16x16x32_bf16 v[24:27], a[120:123], v[160:163], v[24:27]
ds_read_b64_tr_b16 a[112:113], v234 offset:1024
ds_read_b64_tr_b16 a[114:115], v234 offset:7168
v_mfma_f32_16x16x32_bf16 v[24:27], a[124:127], v[168:171], v[24:27]
v_max3_f32 v236, v236, v114, v115
v_max3_f32 v237, v237, v118, v119
)"
R"(v_mfma_f32_16x16x32_bf16 v[28:31], a[120:123], v[164:167], v[28:31]
ds_read_b64_tr_b16 a[116:117], v234 offset:13312
ds_read_b64_tr_b16 a[118:119], v234 offset:19456
v_mfma_f32_16x16x32_bf16 v[28:31], a[124:127], v[172:175], v[28:31]
s_waitcnt lgkmcnt(4)
v_max3_f32 v236, v236, v120, v121
v_max3_f32 v237, v237, v124, v125
v_mfma_f32_16x16x32_bf16 v[32:35], a[96:99], v[160:163], v[32:35]
ds_read_b64_tr_b16 a[120:121], v235 offset:1024
ds_read_b64_tr_b16 a[122:123], v235 offset:7168
v_mfma_f32_16x16x32_bf16 v[32:35], a[100:103], v[168:171], v[32:35]
v_max3_f32 v236, v236, v122, v123
v_max3_f32 v237, v237, v126, v127
v_mfma_f32_16x16x32_bf16 v[36:39], a[96:99], v[164:167], v[36:39]
ds_read_b64_tr_b16 a[124:125], v235 offset:13312
ds_read_b64_tr_b16 a[126:127], v235 offset:19456
v_mfma_f32_16x16x32_bf16 v[36:39], a[100:103], v[172:175], v[36:39]
v_max_f32 v236, v236, v237
v_cmp_lt_f32 vcc, s77, v236
v_mfma_f32_16x16x32_bf16 v[40:43], a[104:107], v[160:163], v[40:43]
ds_read_b64_tr_b16 a[96:97], v232 offset:2048
ds_read_b64_tr_b16 a[98:99], v232 offset:8192
v_mfma_f32_16x16x32_bf16 v[40:43], a[108:111], v[168:171], v[40:43]
v_mfma_f32_16x16x32_bf16 v[44:47], a[104:107], v[164:167], v[44:47]
)"
R"(ds_read_b64_tr_b16 a[100:101], v232 offset:14336
ds_read_b64_tr_b16 a[102:103], v232 offset:20480
v_mfma_f32_16x16x32_bf16 v[44:47], a[108:111], v[172:175], v[44:47]
s_waitcnt lgkmcnt(4)
v_mfma_f32_16x16x32_bf16 v[48:51], a[112:115], v[160:163], v[48:51]
ds_read_b64_tr_b16 a[104:105], v233 offset:2048
ds_read_b64_tr_b16 a[106:107], v233 offset:8192
v_mfma_f32_16x16x32_bf16 v[48:51], a[116:119], v[168:171], v[48:51]
v_mfma_f32_16x16x32_bf16 v[52:55], a[112:115], v[164:167], v[52:55]
ds_read_b64_tr_b16 a[108:109], v233 offset:14336
ds_read_b64_tr_b16 a[110:111], v233 offset:20480
v_mfma_f32_16x16x32_bf16 v[52:55], a[116:119], v[172:175], v[52:55]
v_mfma_f32_16x16x32_bf16 v[56:59], a[120:123], v[160:163], v[56:59]
ds_read_b64_tr_b16 a[112:113], v234 offset:2048
ds_read_b64_tr_b16 a[114:115], v234 offset:8192
v_mfma_f32_16x16x32_bf16 v[56:59], a[124:127], v[168:171], v[56:59]
v_mfma_f32_16x16x32_bf16 v[60:63], a[120:123], v[164:167], v[60:63]
s_add_u32 s75, s78, 492544
ds_read_b64_tr_b16 a[116:117], v234 offset:14336
ds_read_b64_tr_b16 a[118:119], v234 offset:20480
v_mfma_f32_16x16x32_bf16 v[60:63], a[124:127], v[172:175], v[60:63]
s_waitcnt lgkmcnt(4)
s_waitcnt vmcnt(18)
s_barrier
)"
R"(v_mfma_f32_16x16x32_bf16 v[64:67], a[96:99], v[160:163], v[64:67]
s_add_u32 m0, s61, 0
ds_read_b64_tr_b16 a[120:121], v235 offset:2048
ds_read_b64_tr_b16 a[122:123], v235 offset:8192
v_mfma_f32_16x16x32_bf16 v[64:67], a[100:103], v[168:171], v[64:67]
buffer_load_dwordx4 %[vWo], %[srdWo], s75 offen offset:0 lds
v_mfma_f32_16x16x32_bf16 v[68:71], a[96:99], v[164:167], v[68:71]
ds_read_b64_tr_b16 a[124:125], v235 offset:14336
ds_read_b64_tr_b16 a[126:127], v235 offset:20480
v_mfma_f32_16x16x32_bf16 v[68:71], a[100:103], v[172:175], v[68:71]
v_add_u32 v232, s48, %[pWo]
v_xor_b32 v233, 32, v232
v_mfma_f32_16x16x32_bf16 v[72:75], a[104:107], v[160:163], v[72:75]
ds_read_b64_tr_b16 a[96:97], v232 offset:0
ds_read_b64_tr_b16 a[98:99], v232 offset:2048
v_mfma_f32_16x16x32_bf16 v[72:75], a[108:111], v[168:171], v[72:75]
v_xor_b32 v234, 64, v232
v_xor_b32 v235, 96, v232
v_mfma_f32_16x16x32_bf16 v[76:79], a[104:107], v[164:167], v[76:79]
s_add_u32 m0, s61, 1024
ds_read_b64_tr_b16 a[100:101], v232 offset:4096
ds_read_b64_tr_b16 a[102:103], v232 offset:6144
v_mfma_f32_16x16x32_bf16 v[76:79], a[108:111], v[172:175], v[76:79]
s_waitcnt lgkmcnt(4)
)"
R"(buffer_load_dwordx4 %[vX1], %[srdWo], s75 offen offset:0 lds
v_mfma_f32_16x16x32_bf16 v[80:83], a[112:115], v[160:163], v[80:83]
ds_read_b64_tr_b16 a[104:105], v232 offset:8192
ds_read_b64_tr_b16 a[106:107], v232 offset:10240
v_mfma_f32_16x16x32_bf16 v[80:83], a[116:119], v[168:171], v[80:83]
v_mfma_f32_16x16x32_bf16 v[84:87], a[112:115], v[164:167], v[84:87]
s_add_u32 m0, s61, 2048
ds_read_b64_tr_b16 a[108:109], v232 offset:12288
ds_read_b64_tr_b16 a[110:111], v232 offset:14336
v_mfma_f32_16x16x32_bf16 v[84:87], a[116:119], v[172:175], v[84:87]
buffer_load_dwordx4 %[vW], %[srdWo], s75 offen offset:0 lds
v_mfma_f32_16x16x32_bf16 v[88:91], a[120:123], v[160:163], v[88:91]
ds_read_b64_tr_b16 a[112:113], v232 offset:16384
ds_read_b64_tr_b16 a[114:115], v232 offset:18432
v_mfma_f32_16x16x32_bf16 v[88:91], a[124:127], v[168:171], v[88:91]
s_add_u32 s75, s78, 523264
v_mfma_f32_16x16x32_bf16 v[92:95], a[120:123], v[164:167], v[92:95]
s_add_u32 m0, s61, 3072
ds_read_b64_tr_b16 a[116:117], v232 offset:20480
ds_read_b64_tr_b16 a[118:119], v232 offset:22528
v_mfma_f32_16x16x32_bf16 v[92:95], a[124:127], v[172:175], v[92:95]
s_waitcnt lgkmcnt(6)
buffer_load_dwordx4 %[vWo], %[srdWo], s75 offen offset:0 lds
s_cbranch_vccnz RARE13_%=
)"
R"(BACK13_%=:
v_mfma_f32_16x16x32_bf16 a[224:227], a[96:99], a[48:51], a[224:227]
ds_read_b64_tr_b16 a[120:121], v233 offset:0
ds_read_b64_tr_b16 a[122:123], v233 offset:2048
v_mfma_f32_16x16x32_bf16 a[224:227], a[100:103], a[56:59], a[224:227]
v_exp_f32 v96, v96
v_mfma_f32_16x16x32_bf16 a[224:227], a[104:107], a[64:67], a[224:227]
s_add_u32 m0, s61, 4096
ds_read_b64_tr_b16 a[124:125], v233 offset:4096
ds_read_b64_tr_b16 a[126:127], v233 offset:6144
v_mfma_f32_16x16x32_bf16 a[228:231], a[96:99], a[52:55], a[228:231]
buffer_load_dwordx4 %[vX1], %[srdWo], s75 offen offset:0 lds
v_mfma_f32_16x16x32_bf16 a[228:231], a[100:103], a[60:63], a[228:231]
ds_read_b64_tr_b16 a[96:97], v233 offset:8192
ds_read_b64_tr_b16 a[98:99], v233 offset:10240
v_mfma_f32_16x16x32_bf16 a[228:231], a[104:107], a[68:71], a[228:231]
s_waitcnt lgkmcnt(6)
v_exp_f32 v97, v97
v_mfma_f32_16x16x32_bf16 a[224:227], a[108:111], a[72:75], a[224:227]
s_add_u32 m0, s61, 5120
ds_read_b64_tr_b16 a[100:101], v233 offset:12288
ds_read_b64_tr_b16 a[102:103], v233 offset:14336
v_mfma_f32_16x16x32_bf16 a[224:227], a[112:115], a[80:83], a[224:227]
buffer_load_dwordx4 %[vW], %[srdWo], s75 offen offset:0 lds
)"
R"(v_mfma_f32_16x16x32_bf16 a[224:227], a[116:119], a[88:91], a[224:227]
ds_read_b64_tr_b16 a[104:105], v233 offset:16384
ds_read_b64_tr_b16 a[106:107], v233 offset:18432
v_mfma_f32_16x16x32_bf16 a[228:231], a[108:111], a[76:79], a[228:231]
v_exp_f32 v98, v98
v_mfma_f32_16x16x32_bf16 a[228:231], a[112:115], a[84:87], a[228:231]
ds_read_b64_tr_b16 a[108:109], v233 offset:20480
ds_read_b64_tr_b16 a[110:111], v233 offset:22528
v_mfma_f32_16x16x32_bf16 a[228:231], a[116:119], a[92:95], a[228:231]
s_waitcnt lgkmcnt(6)
v_exp_f32 v99, v99
v_mfma_f32_16x16x32_bf16 a[232:235], a[120:123], a[48:51], a[232:235]
ds_read_b64_tr_b16 a[112:113], v234 offset:0
ds_read_b64_tr_b16 a[114:115], v234 offset:2048
v_mfma_f32_16x16x32_bf16 a[232:235], a[124:127], a[56:59], a[232:235]
v_exp_f32 v104, v104
v_mfma_f32_16x16x32_bf16 a[232:235], a[96:99], a[64:67], a[232:235]
ds_read_b64_tr_b16 a[116:117], v234 offset:4096
ds_read_b64_tr_b16 a[118:119], v234 offset:6144
v_mfma_f32_16x16x32_bf16 a[236:239], a[120:123], a[52:55], a[236:239]
v_exp_f32 v105, v105
v_mfma_f32_16x16x32_bf16 a[236:239], a[124:127], a[60:63], a[236:239]
ds_read_b64_tr_b16 a[120:121], v234 offset:8192
ds_read_b64_tr_b16 a[122:123], v234 offset:10240
)"
R"(v_mfma_f32_16x16x32_bf16 a[236:239], a[96:99], a[68:71], a[236:239]
s_waitcnt lgkmcnt(6)
v_exp_f32 v106, v106
v_mfma_f32_16x16x32_bf16 a[232:235], a[100:103], a[72:75], a[232:235]
ds_read_b64_tr_b16 a[124:125], v234 offset:12288
ds_read_b64_tr_b16 a[126:127], v234 offset:14336
v_mfma_f32_16x16x32_bf16 a[232:235], a[104:107], a[80:83], a[232:235]
v_exp_f32 v107, v107
v_mfma_f32_16x16x32_bf16 a[232:235], a[108:111], a[88:91], a[232:235]
ds_read_b64_tr_b16 a[96:97], v234 offset:16384
ds_read_b64_tr_b16 a[98:99], v234 offset:18432
v_mfma_f32_16x16x32_bf16 a[236:239], a[100:103], a[76:79], a[236:239]
v_cvt_pk_bf16_f32 v160, v96, v97
v_cvt_pk_bf16_f32 v161, v98, v99
v_mfma_f32_16x16x32_bf16 a[236:239], a[104:107], a[84:87], a[236:239]
ds_read_b64_tr_b16 a[100:101], v234 offset:20480
ds_read_b64_tr_b16 a[102:103], v234 offset:22528
v_mfma_f32_16x16x32_bf16 a[236:239], a[108:111], a[92:95], a[236:239]
s_waitcnt lgkmcnt(6)
v_exp_f32 v100, v100
v_mfma_f32_16x16x32_bf16 a[240:243], a[112:115], a[48:51], a[240:243]
ds_read_b64_tr_b16 a[104:105], v235 offset:0
ds_read_b64_tr_b16 a[106:107], v235 offset:2048
v_mfma_f32_16x16x32_bf16 a[240:243], a[116:119], a[56:59], a[240:243]
)"
R"(v_exp_f32 v101, v101
v_mfma_f32_16x16x32_bf16 a[240:243], a[120:123], a[64:67], a[240:243]
ds_read_b64_tr_b16 a[108:109], v235 offset:4096
ds_read_b64_tr_b16 a[110:111], v235 offset:6144
v_mfma_f32_16x16x32_bf16 a[244:247], a[112:115], a[52:55], a[244:247]
v_exp_f32 v102, v102
v_mfma_f32_16x16x32_bf16 a[244:247], a[116:119], a[60:63], a[244:247]
ds_read_b64_tr_b16 a[112:113], v235 offset:8192
ds_read_b64_tr_b16 a[114:115], v235 offset:10240
v_mfma_f32_16x16x32_bf16 a[244:247], a[120:123], a[68:71], a[244:247]
s_waitcnt lgkmcnt(6)
v_exp_f32 v103, v103
v_mfma_f32_16x16x32_bf16 a[240:243], a[124:127], a[72:75], a[240:243]
s_add_u32 s76, s68, 122880
ds_read_b64_tr_b16 a[116:117], v235 offset:12288
ds_read_b64_tr_b16 a[118:119], v235 offset:14336
v_mfma_f32_16x16x32_bf16 a[240:243], a[96:99], a[80:83], a[240:243]
s_waitcnt vmcnt(18)
s_barrier
v_mfma_f32_16x16x32_bf16 a[240:243], a[100:103], a[88:91], a[240:243]
s_add_u32 m0, s65, 0
ds_read_b64_tr_b16 a[120:121], v235 offset:16384
ds_read_b64_tr_b16 a[122:123], v235 offset:18432
v_mfma_f32_16x16x32_bf16 a[244:247], a[124:127], a[76:79], a[244:247]
)"
R"(buffer_load_dwordx4 %[vV], %[srdV], s76 offen offset:0 nt lds
v_mfma_f32_16x16x32_bf16 a[244:247], a[96:99], a[84:87], a[244:247]
ds_read_b64_tr_b16 a[124:125], v235 offset:20480
ds_read_b64_tr_b16 a[126:127], v235 offset:22528
v_mfma_f32_16x16x32_bf16 a[244:247], a[100:103], a[92:95], a[244:247]
s_waitcnt lgkmcnt(6)
v_add_u32 v232, s50, %[pK]
v_xor_b32 v233, 64, v232
v_mfma_f32_16x16x32_bf16 a[248:251], a[104:107], a[48:51], a[248:251]
s_add_u32 m0, s65, 896
ds_read_b128 a[96:99], v232 offset:0
ds_read_b128 a[100:103], v233 offset:0
v_mfma_f32_16x16x32_bf16 a[248:251], a[108:111], a[56:59], a[248:251]
buffer_load_dwordx4 %[vV], %[srdV], s76 offen offset:128 nt lds
v_mfma_f32_16x16x32_bf16 a[248:251], a[112:115], a[64:67], a[248:251]
v_cvt_pk_bf16_f32 v162, v104, v105
v_cvt_pk_bf16_f32 v163, v106, v107
v_mfma_f32_16x16x32_bf16 a[252:255], a[104:107], a[52:55], a[252:255]
v_exp_f32 v108, v108
v_mfma_f32_16x16x32_bf16 a[252:255], a[108:111], a[60:63], a[252:255]
s_add_u32 m0, s65, 1792
ds_read_b128 a[104:107], v232 offset:1024
ds_read_b128 a[108:111], v233 offset:1024
v_mfma_f32_16x16x32_bf16 a[252:255], a[112:115], a[68:71], a[252:255]
)"
R"(s_waitcnt lgkmcnt(4)
buffer_load_dwordx4 %[vV], %[srdV], s76 offen offset:256 nt lds
v_mfma_f32_16x16x32_bf16 a[248:251], a[116:119], a[72:75], a[248:251]
v_exp_f32 v109, v109
v_mfma_f32_16x16x32_bf16 a[248:251], a[120:123], a[80:83], a[248:251]
v_exp_f32 v110, v110
v_mfma_f32_16x16x32_bf16 a[248:251], a[124:127], a[88:91], a[248:251]
v_exp_f32 v111, v111
v_mfma_f32_16x16x32_bf16 a[252:255], a[116:119], a[76:79], a[252:255]
s_add_u32 m0, s65, 0
ds_read_b128 a[112:115], v232 offset:2048
ds_read_b128 a[116:119], v233 offset:2048
v_mfma_f32_16x16x32_bf16 a[252:255], a[120:123], a[84:87], a[252:255]
buffer_load_dwordx4 %[vV], %[srdV], s76 offen offset:3072 nt lds
v_mfma_f32_16x16x32_bf16 a[252:255], a[124:127], a[92:95], a[252:255]
s_waitcnt lgkmcnt(3)
v_cvt_pk_bf16_f32 v164, v100, v101
v_cvt_pk_bf16_f32 v165, v102, v103
v_mfma_f32_16x16x32_bf16 v[128:131], a[96:99], v[184:187], v[176:179]
ds_read_b128 a[120:123], v232 offset:6144
ds_read_b128 a[124:127], v233 offset:6144
v_mfma_f32_16x16x32_bf16 v[128:131], a[100:103], v[192:195], v[128:131]
s_add_u32 m0, s65, 896
v_exp_f32 v112, v112
)"
R"(v_mfma_f32_16x16x32_bf16 v[128:131], a[104:107], v[200:203], v[128:131]
buffer_load_dwordx4 %[vV], %[srdV], s76 offen offset:3200 nt lds
v_mfma_f32_16x16x32_bf16 v[132:135], a[96:99], v[188:191], v[180:183]
v_exp_f32 v113, v113
v_mfma_f32_16x16x32_bf16 v[132:135], a[100:103], v[196:199], v[132:135]
s_add_u32 m0, s65, 1792
ds_read_b128 a[96:99], v232 offset:7168
ds_read_b128 a[100:103], v233 offset:7168
v_mfma_f32_16x16x32_bf16 v[132:135], a[104:107], v[204:207], v[132:135]
s_waitcnt lgkmcnt(4)
v_exp_f32 v114, v114
v_mfma_f32_16x16x32_bf16 v[128:131], a[108:111], v[208:211], v[128:131]
buffer_load_dwordx4 %[vV], %[srdV], s76 offen offset:3328 nt lds
v_mfma_f32_16x16x32_bf16 v[128:131], a[112:115], v[216:219], v[128:131]
v_exp_f32 v115, v115
v_mfma_f32_16x16x32_bf16 v[128:131], a[116:119], v[224:227], v[128:131]
v_cvt_pk_bf16_f32 v166, v108, v109
v_cvt_pk_bf16_f32 v167, v110, v111
v_mfma_f32_16x16x32_bf16 v[132:135], a[108:111], v[212:215], v[132:135]
ds_read_b128 a[104:107], v232 offset:8192
ds_read_b128 a[108:111], v233 offset:8192
v_mfma_f32_16x16x32_bf16 v[132:135], a[112:115], v[220:223], v[132:135]
v_exp_f32 v120, v120
v_mfma_f32_16x16x32_bf16 v[132:135], a[116:119], v[228:231], v[132:135]
)"
R"(s_waitcnt lgkmcnt(3)
v_exp_f32 v121, v121
v_mfma_f32_16x16x32_bf16 v[136:139], a[120:123], v[184:187], v[176:179]
ds_read_b128 a[112:115], v232 offset:12288
ds_read_b128 a[116:119], v233 offset:12288
v_mfma_f32_16x16x32_bf16 v[136:139], a[124:127], v[192:195], v[136:139]
v_exp_f32 v122, v122
v_mfma_f32_16x16x32_bf16 v[136:139], a[96:99], v[200:203], v[136:139]
v_exp_f32 v123, v123
v_mfma_f32_16x16x32_bf16 v[140:143], a[120:123], v[188:191], v[180:183]
v_cvt_pk_bf16_f32 v168, v112, v113
v_cvt_pk_bf16_f32 v169, v114, v115
v_mfma_f32_16x16x32_bf16 v[140:143], a[124:127], v[196:199], v[140:143]
ds_read_b128 a[120:123], v232 offset:13312
ds_read_b128 a[124:127], v233 offset:13312
v_mfma_f32_16x16x32_bf16 v[140:143], a[96:99], v[204:207], v[140:143]
s_waitcnt lgkmcnt(4)
v_exp_f32 v116, v116
v_mfma_f32_16x16x32_bf16 v[136:139], a[100:103], v[208:211], v[136:139]
v_exp_f32 v117, v117
v_mfma_f32_16x16x32_bf16 v[136:139], a[104:107], v[216:219], v[136:139]
v_exp_f32 v118, v118
v_mfma_f32_16x16x32_bf16 v[136:139], a[108:111], v[224:227], v[136:139]
v_exp_f32 v119, v119
)"
R"(v_mfma_f32_16x16x32_bf16 v[140:143], a[100:103], v[212:215], v[140:143]
ds_read_b128 a[96:99], v232 offset:14336
ds_read_b128 a[100:103], v233 offset:14336
v_mfma_f32_16x16x32_bf16 v[140:143], a[104:107], v[220:223], v[140:143]
v_cvt_pk_bf16_f32 v170, v120, v121
v_cvt_pk_bf16_f32 v171, v122, v123
v_mfma_f32_16x16x32_bf16 v[140:143], a[108:111], v[228:231], v[140:143]
s_waitcnt lgkmcnt(3)
v_exp_f32 v124, v124
v_mfma_f32_16x16x32_bf16 v[144:147], a[112:115], v[184:187], v[176:179]
ds_read_b128 a[104:107], v232 offset:18432
ds_read_b128 a[108:111], v233 offset:18432
v_mfma_f32_16x16x32_bf16 v[144:147], a[116:119], v[192:195], v[144:147]
v_exp_f32 v125, v125
v_mfma_f32_16x16x32_bf16 v[144:147], a[120:123], v[200:203], v[144:147]
v_exp_f32 v126, v126
v_mfma_f32_16x16x32_bf16 v[148:151], a[112:115], v[188:191], v[180:183]
v_exp_f32 v127, v127
v_mfma_f32_16x16x32_bf16 v[148:151], a[116:119], v[196:199], v[148:151]
ds_read_b128 a[112:115], v232 offset:19456
ds_read_b128 a[116:119], v233 offset:19456
v_mfma_f32_16x16x32_bf16 v[148:151], a[120:123], v[204:207], v[148:151]
s_waitcnt lgkmcnt(4)
v_cvt_pk_bf16_f32 v172, v116, v117
)"
R"(v_cvt_pk_bf16_f32 v173, v118, v119
v_mfma_f32_16x16x32_bf16 v[144:147], a[124:127], v[208:211], v[144:147]
v_cvt_pk_bf16_f32 v174, v124, v125
v_cvt_pk_bf16_f32 v175, v126, v127
v_mfma_f32_16x16x32_bf16 v[144:147], a[96:99], v[216:219], v[144:147]
s_waitcnt vmcnt(18)
s_barrier
v_mfma_f32_16x16x32_bf16 v[144:147], a[100:103], v[224:227], v[144:147]
s_add_u32 s75, s67, 172032
v_mfma_f32_16x16x32_bf16 v[148:151], a[124:127], v[212:215], v[148:151]
s_add_u32 m0, s54, 0
ds_read_b128 a[120:123], v232 offset:20480
ds_read_b128 a[124:127], v233 offset:20480
v_mfma_f32_16x16x32_bf16 v[148:151], a[96:99], v[220:223], v[148:151]
buffer_load_dwordx4 %[vK], %[srdK], s75 offen offset:0 nt lds
v_mfma_f32_16x16x32_bf16 v[148:151], a[100:103], v[228:231], v[148:151]
s_waitcnt lgkmcnt(3)
v_add_u32 v232, s51, %[pV]
v_xor_b32 v233, 32, v232
v_mfma_f32_16x16x32_bf16 v[152:155], a[104:107], v[184:187], v[176:179]
ds_read_b64_tr_b16 a[96:97], v232 offset:0
ds_read_b64_tr_b16 a[98:99], v232 offset:6144
v_mfma_f32_16x16x32_bf16 v[152:155], a[108:111], v[192:195], v[152:155]
v_xor_b32 v234, 64, v232
)"
R"(v_xor_b32 v235, 96, v232
v_mfma_f32_16x16x32_bf16 v[152:155], a[112:115], v[200:203], v[152:155]
s_add_u32 m0, s54, 896
ds_read_b64_tr_b16 a[100:101], v232 offset:12288
ds_read_b64_tr_b16 a[102:103], v232 offset:18432
v_mfma_f32_16x16x32_bf16 v[156:159], a[104:107], v[188:191], v[180:183]
buffer_load_dwordx4 %[vK], %[srdK], s75 offen offset:128 nt lds
v_mfma_f32_16x16x32_bf16 v[156:159], a[108:111], v[196:199], v[156:159]
ds_read_b64_tr_b16 a[104:105], v233 offset:0
ds_read_b64_tr_b16 a[106:107], v233 offset:6144
v_mfma_f32_16x16x32_bf16 v[156:159], a[112:115], v[204:207], v[156:159]
s_waitcnt lgkmcnt(6)
v_mfma_f32_16x16x32_bf16 v[152:155], a[116:119], v[208:211], v[152:155]
s_add_u32 m0, s54, 1792
ds_read_b64_tr_b16 a[108:109], v233 offset:12288
ds_read_b64_tr_b16 a[110:111], v233 offset:18432
v_mfma_f32_16x16x32_bf16 v[152:155], a[120:123], v[216:219], v[152:155]
buffer_load_dwordx4 %[vK], %[srdK], s75 offen offset:256 nt lds
v_mfma_f32_16x16x32_bf16 v[152:155], a[124:127], v[224:227], v[152:155]
ds_read_b64_tr_b16 a[112:113], v234 offset:0
ds_read_b64_tr_b16 a[114:115], v234 offset:6144
v_mfma_f32_16x16x32_bf16 v[156:159], a[116:119], v[212:215], v[156:159]
s_add_u32 s75, s67, 184320
v_mfma_f32_16x16x32_bf16 v[156:159], a[120:123], v[220:223], v[156:159]
)"
R"(s_add_u32 m0, s54, 12288
ds_read_b64_tr_b16 a[116:117], v234 offset:12288
ds_read_b64_tr_b16 a[118:119], v234 offset:18432
v_mfma_f32_16x16x32_bf16 v[156:159], a[124:127], v[228:231], v[156:159]
buffer_load_dwordx4 %[vK], %[srdK], s75 offen offset:0 nt lds
v_mfma_f32_16x16x32_bf16 a[132:135], a[128:131], v[160:163], a[132:135]
v_mfma_f32_16x16x32_bf16 a[132:135], a[128:131], v[168:171], a[132:135]
v_mfma_f32_16x16x32_bf16 a[136:139], a[128:131], v[164:167], a[136:139]
s_add_u32 m0, s54, 13184
v_mfma_f32_16x16x32_bf16 a[136:139], a[128:131], v[172:175], a[136:139]
s_waitcnt lgkmcnt(4)
buffer_load_dwordx4 %[vK], %[srdK], s75 offen offset:128 nt lds
v_mfma_f32_16x16x32_bf16 v[0:3], a[96:99], v[160:163], v[0:3]
ds_read_b64_tr_b16 a[120:121], v235 offset:0
ds_read_b64_tr_b16 a[122:123], v235 offset:6144
v_mfma_f32_16x16x32_bf16 v[0:3], a[100:103], v[168:171], v[0:3]
v_max3_f32 v236, v128, v129, v130
v_max3_f32 v237, v132, v133, v134
v_mfma_f32_16x16x32_bf16 v[4:7], a[96:99], v[164:167], v[4:7]
s_add_u32 m0, s54, 14080
ds_read_b64_tr_b16 a[124:125], v235 offset:12288
ds_read_b64_tr_b16 a[126:127], v235 offset:18432
v_mfma_f32_16x16x32_bf16 v[4:7], a[100:103], v[172:175], v[4:7]
buffer_load_dwordx4 %[vK], %[srdK], s75 offen offset:256 nt lds
)"
R"(v_mfma_f32_16x16x32_bf16 v[8:11], a[104:107], v[160:163], v[8:11]
ds_read_b64_tr_b16 a[96:97], v232 offset:1024
ds_read_b64_tr_b16 a[98:99], v232 offset:7168
v_mfma_f32_16x16x32_bf16 v[8:11], a[108:111], v[168:171], v[8:11]
v_max_f32 v236, v236, v131
v_max_f32 v237, v237, v135
v_mfma_f32_16x16x32_bf16 v[12:15], a[104:107], v[164:167], v[12:15]
ds_read_b64_tr_b16 a[100:101], v232 offset:13312
ds_read_b64_tr_b16 a[102:103], v232 offset:19456
v_mfma_f32_16x16x32_bf16 v[12:15], a[108:111], v[172:175], v[12:15]
s_waitcnt lgkmcnt(4)
v_max3_f32 v236, v236, v136, v137
v_max3_f32 v237, v237, v140, v141
v_mfma_f32_16x16x32_bf16 v[16:19], a[112:115], v[160:163], v[16:19]
ds_read_b64_tr_b16 a[104:105], v233 offset:1024
ds_read_b64_tr_b16 a[106:107], v233 offset:7168
v_mfma_f32_16x16x32_bf16 v[16:19], a[116:119], v[168:171], v[16:19]
v_max3_f32 v236, v236, v138, v139
v_max3_f32 v237, v237, v142, v143
v_mfma_f32_16x16x32_bf16 v[20:23], a[112:115], v[164:167], v[20:23]
ds_read_b64_tr_b16 a[108:109], v233 offset:13312
ds_read_b64_tr_b16 a[110:111], v233 offset:19456
v_mfma_f32_16x16x32_bf16 v[20:23], a[116:119], v[172:175], v[20:23]
v_max3_f32 v236, v236, v144, v145
)"
R"(v_max3_f32 v237, v237, v148, v149
v_mfma_f32_16x16x32_bf16 v[24:27], a[120:123], v[160:163], v[24:27]
ds_read_b64_tr_b16 a[112:113], v234 offset:1024
ds_read_b64_tr_b16 a[114:115], v234 offset:7168
v_mfma_f32_16x16x32_bf16 v[24:27], a[124:127], v[168:171], v[24:27]
v_max3_f32 v236, v236, v146, v147
v_max3_f32 v237, v237, v150, v151
v_mfma_f32_16x16x32_bf16 v[28:31], a[120:123], v[164:167], v[28:31]
ds_read_b64_tr_b16 a[116:117], v234 offset:13312
ds_read_b64_tr_b16 a[118:119], v234 offset:19456
v_mfma_f32_16x16x32_bf16 v[28:31], a[124:127], v[172:175], v[28:31]
s_waitcnt lgkmcnt(4)
v_max3_f32 v236, v236, v152, v153
v_max3_f32 v237, v237, v156, v157
v_mfma_f32_16x16x32_bf16 v[32:35], a[96:99], v[160:163], v[32:35]
ds_read_b64_tr_b16 a[120:121], v235 offset:1024
ds_read_b64_tr_b16 a[122:123], v235 offset:7168
v_mfma_f32_16x16x32_bf16 v[32:35], a[100:103], v[168:171], v[32:35]
v_max3_f32 v236, v236, v154, v155
v_max3_f32 v237, v237, v158, v159
v_mfma_f32_16x16x32_bf16 v[36:39], a[96:99], v[164:167], v[36:39]
ds_read_b64_tr_b16 a[124:125], v235 offset:13312
ds_read_b64_tr_b16 a[126:127], v235 offset:19456
v_mfma_f32_16x16x32_bf16 v[36:39], a[100:103], v[172:175], v[36:39]
)"
R"(v_max_f32 v236, v236, v237
v_cmp_lt_f32 vcc, s77, v236
v_mfma_f32_16x16x32_bf16 v[40:43], a[104:107], v[160:163], v[40:43]
ds_read_b64_tr_b16 a[96:97], v232 offset:2048
ds_read_b64_tr_b16 a[98:99], v232 offset:8192
v_mfma_f32_16x16x32_bf16 v[40:43], a[108:111], v[168:171], v[40:43]
v_mfma_f32_16x16x32_bf16 v[44:47], a[104:107], v[164:167], v[44:47]
ds_read_b64_tr_b16 a[100:101], v232 offset:14336
ds_read_b64_tr_b16 a[102:103], v232 offset:20480
v_mfma_f32_16x16x32_bf16 v[44:47], a[108:111], v[172:175], v[44:47]
s_waitcnt lgkmcnt(4)
v_mfma_f32_16x16x32_bf16 v[48:51], a[112:115], v[160:163], v[48:51]
ds_read_b64_tr_b16 a[104:105], v233 offset:2048
ds_read_b64_tr_b16 a[106:107], v233 offset:8192
v_mfma_f32_16x16x32_bf16 v[48:51], a[116:119], v[168:171], v[48:51]
v_mfma_f32_16x16x32_bf16 v[52:55], a[112:115], v[164:167], v[52:55]
ds_read_b64_tr_b16 a[108:109], v233 offset:14336
ds_read_b64_tr_b16 a[110:111], v233 offset:20480
v_mfma_f32_16x16x32_bf16 v[52:55], a[116:119], v[172:175], v[52:55]
v_mfma_f32_16x16x32_bf16 v[56:59], a[120:123], v[160:163], v[56:59]
ds_read_b64_tr_b16 a[112:113], v234 offset:2048
ds_read_b64_tr_b16 a[114:115], v234 offset:8192
v_mfma_f32_16x16x32_bf16 v[56:59], a[124:127], v[168:171], v[56:59]
v_mfma_f32_16x16x32_bf16 v[60:63], a[120:123], v[164:167], v[60:63]
)"
R"(s_add_u32 s76, s68, 147456
ds_read_b64_tr_b16 a[116:117], v234 offset:14336
ds_read_b64_tr_b16 a[118:119], v234 offset:20480
v_mfma_f32_16x16x32_bf16 v[60:63], a[124:127], v[172:175], v[60:63]
s_waitcnt lgkmcnt(4)
s_waitcnt vmcnt(18)
s_barrier
v_mfma_f32_16x16x32_bf16 v[64:67], a[96:99], v[160:163], v[64:67]
s_add_u32 m0, s62, 0
ds_read_b64_tr_b16 a[120:121], v235 offset:2048
ds_read_b64_tr_b16 a[122:123], v235 offset:8192
v_mfma_f32_16x16x32_bf16 v[64:67], a[100:103], v[168:171], v[64:67]
buffer_load_dwordx4 %[vV], %[srdV], s76 offen offset:0 nt lds
v_mfma_f32_16x16x32_bf16 v[68:71], a[96:99], v[164:167], v[68:71]
ds_read_b64_tr_b16 a[124:125], v235 offset:14336
ds_read_b64_tr_b16 a[126:127], v235 offset:20480
v_mfma_f32_16x16x32_bf16 v[68:71], a[100:103], v[172:175], v[68:71]
v_add_u32 v232, s52, %[pK]
v_xor_b32 v233, 64, v232
v_mfma_f32_16x16x32_bf16 v[72:75], a[104:107], v[160:163], v[72:75]
s_add_u32 m0, s62, 896
ds_read_b128 a[96:99], v232 offset:0
ds_read_b128 a[100:103], v233 offset:0
v_mfma_f32_16x16x32_bf16 v[72:75], a[108:111], v[168:171], v[72:75]
)"
R"(buffer_load_dwordx4 %[vV], %[srdV], s76 offen offset:128 nt lds
v_mfma_f32_16x16x32_bf16 v[76:79], a[104:107], v[164:167], v[76:79]
v_mfma_f32_16x16x32_bf16 v[76:79], a[108:111], v[172:175], v[76:79]
s_waitcnt lgkmcnt(2)
v_mfma_f32_16x16x32_bf16 v[80:83], a[112:115], v[160:163], v[80:83]
s_add_u32 m0, s62, 1792
ds_read_b128 a[104:107], v232 offset:1024
ds_read_b128 a[108:111], v233 offset:1024
v_mfma_f32_16x16x32_bf16 v[80:83], a[116:119], v[168:171], v[80:83]
buffer_load_dwordx4 %[vV], %[srdV], s76 offen offset:256 nt lds
v_mfma_f32_16x16x32_bf16 v[84:87], a[112:115], v[164:167], v[84:87]
v_mfma_f32_16x16x32_bf16 v[84:87], a[116:119], v[172:175], v[84:87]
v_mfma_f32_16x16x32_bf16 v[88:91], a[120:123], v[160:163], v[88:91]
s_add_u32 m0, s62, 0
ds_read_b128 a[112:115], v232 offset:2048
ds_read_b128 a[116:119], v233 offset:2048
v_mfma_f32_16x16x32_bf16 v[88:91], a[124:127], v[168:171], v[88:91]
buffer_load_dwordx4 %[vV], %[srdV], s76 offen offset:3072 nt lds
v_mfma_f32_16x16x32_bf16 v[92:95], a[120:123], v[164:167], v[92:95]
v_mfma_f32_16x16x32_bf16 v[92:95], a[124:127], v[172:175], v[92:95]
s_waitcnt lgkmcnt(3)
s_cbranch_vccnz RARE14_%=
BACK14_%=:
v_mfma_f32_16x16x32_bf16 v[96:99], a[96:99], v[184:187], v[176:179]
)"
R"(s_add_u32 m0, s62, 896
ds_read_b128 a[120:123], v232 offset:6144
ds_read_b128 a[124:127], v233 offset:6144
v_mfma_f32_16x16x32_bf16 v[96:99], a[100:103], v[192:195], v[96:99]
buffer_load_dwordx4 %[vV], %[srdV], s76 offen offset:3200 nt lds
v_mfma_f32_16x16x32_bf16 v[96:99], a[104:107], v[200:203], v[96:99]
v_exp_f32 v128, v128
v_mfma_f32_16x16x32_bf16 v[100:103], a[96:99], v[188:191], v[180:183]
v_exp_f32 v129, v129
v_mfma_f32_16x16x32_bf16 v[100:103], a[100:103], v[196:199], v[100:103]
s_add_u32 m0, s62, 1792
ds_read_b128 a[96:99], v232 offset:7168
ds_read_b128 a[100:103], v233 offset:7168
v_mfma_f32_16x16x32_bf16 v[100:103], a[104:107], v[204:207], v[100:103]
s_waitcnt lgkmcnt(4)
buffer_load_dwordx4 %[vV], %[srdV], s76 offen offset:3328 nt lds
v_mfma_f32_16x16x32_bf16 v[96:99], a[108:111], v[208:211], v[96:99]
v_exp_f32 v130, v130
v_mfma_f32_16x16x32_bf16 v[96:99], a[112:115], v[216:219], v[96:99]
v_exp_f32 v131, v131
v_mfma_f32_16x16x32_bf16 v[96:99], a[116:119], v[224:227], v[96:99]
v_exp_f32 v136, v136
v_mfma_f32_16x16x32_bf16 v[100:103], a[108:111], v[212:215], v[100:103]
ds_read_b128 a[104:107], v232 offset:8192
)"
R"(ds_read_b128 a[108:111], v233 offset:8192
v_mfma_f32_16x16x32_bf16 v[100:103], a[112:115], v[220:223], v[100:103]
v_exp_f32 v137, v137
v_mfma_f32_16x16x32_bf16 v[100:103], a[116:119], v[228:231], v[100:103]
s_waitcnt lgkmcnt(3)
v_exp_f32 v138, v138
v_mfma_f32_16x16x32_bf16 v[104:107], a[120:123], v[184:187], v[176:179]
ds_read_b128 a[112:115], v232 offset:12288
ds_read_b128 a[116:119], v233 offset:12288
v_mfma_f32_16x16x32_bf16 v[104:107], a[124:127], v[192:195], v[104:107]
v_exp_f32 v139, v139
v_cvt_pk_bf16_f32 v160, v128, v129
v_mfma_f32_16x16x32_bf16 v[104:107], a[96:99], v[200:203], v[104:107]
v_cvt_pk_bf16_f32 v161, v130, v131
v_exp_f32 v132, v132
v_mfma_f32_16x16x32_bf16 v[108:111], a[120:123], v[188:191], v[180:183]
v_exp_f32 v133, v133
v_mfma_f32_16x16x32_bf16 v[108:111], a[124:127], v[196:199], v[108:111]
ds_read_b128 a[120:123], v232 offset:13312
ds_read_b128 a[124:127], v233 offset:13312
v_mfma_f32_16x16x32_bf16 v[108:111], a[96:99], v[204:207], v[108:111]
s_waitcnt lgkmcnt(4)
v_exp_f32 v134, v134
v_mfma_f32_16x16x32_bf16 v[104:107], a[100:103], v[208:211], v[104:107]
)"
R"(v_exp_f32 v135, v135
v_mfma_f32_16x16x32_bf16 v[104:107], a[104:107], v[216:219], v[104:107]
v_cvt_pk_bf16_f32 v162, v136, v137
v_cvt_pk_bf16_f32 v163, v138, v139
v_mfma_f32_16x16x32_bf16 v[104:107], a[108:111], v[224:227], v[104:107]
v_exp_f32 v140, v140
v_mfma_f32_16x16x32_bf16 v[108:111], a[100:103], v[212:215], v[108:111]
ds_read_b128 a[96:99], v232 offset:14336
ds_read_b128 a[100:103], v233 offset:14336
v_mfma_f32_16x16x32_bf16 v[108:111], a[104:107], v[220:223], v[108:111]
v_exp_f32 v141, v141
v_mfma_f32_16x16x32_bf16 v[108:111], a[108:111], v[228:231], v[108:111]
s_waitcnt lgkmcnt(3)
v_exp_f32 v142, v142
v_mfma_f32_16x16x32_bf16 v[112:115], a[112:115], v[184:187], v[176:179]
ds_read_b128 a[104:107], v232 offset:18432
ds_read_b128 a[108:111], v233 offset:18432
v_mfma_f32_16x16x32_bf16 v[112:115], a[116:119], v[192:195], v[112:115]
v_exp_f32 v143, v143
v_mfma_f32_16x16x32_bf16 v[112:115], a[120:123], v[200:203], v[112:115]
v_cvt_pk_bf16_f32 v164, v132, v133
v_cvt_pk_bf16_f32 v165, v134, v135
v_mfma_f32_16x16x32_bf16 v[116:119], a[112:115], v[188:191], v[180:183]
v_exp_f32 v144, v144
)"
R"(v_mfma_f32_16x16x32_bf16 v[116:119], a[116:119], v[196:199], v[116:119]
ds_read_b128 a[112:115], v232 offset:19456
ds_read_b128 a[116:119], v233 offset:19456
v_mfma_f32_16x16x32_bf16 v[116:119], a[120:123], v[204:207], v[116:119]
s_waitcnt lgkmcnt(4)
v_exp_f32 v145, v145
v_mfma_f32_16x16x32_bf16 v[112:115], a[124:127], v[208:211], v[112:115]
v_exp_f32 v146, v146
v_mfma_f32_16x16x32_bf16 v[112:115], a[96:99], v[216:219], v[112:115]
s_waitcnt vmcnt(18)
s_barrier
v_mfma_f32_16x16x32_bf16 v[112:115], a[100:103], v[224:227], v[112:115]
s_add_u32 s75, s78, 492672
v_exp_f32 v147, v147
v_mfma_f32_16x16x32_bf16 v[116:119], a[124:127], v[212:215], v[116:119]
s_add_u32 m0, s63, 0
ds_read_b128 a[120:123], v232 offset:20480
ds_read_b128 a[124:127], v233 offset:20480
v_mfma_f32_16x16x32_bf16 v[116:119], a[96:99], v[220:223], v[116:119]
buffer_load_dwordx4 %[vWo], %[srdWo], s75 offen offset:0 lds
v_mfma_f32_16x16x32_bf16 v[116:119], a[100:103], v[228:231], v[116:119]
s_waitcnt lgkmcnt(3)
v_add_u32 v232, s49, %[pWo]
v_xor_b32 v233, 32, v232
)"
R"(v_mfma_f32_16x16x32_bf16 v[120:123], a[104:107], v[184:187], v[176:179]
ds_read_b64_tr_b16 a[96:97], v232 offset:0
ds_read_b64_tr_b16 a[98:99], v232 offset:2048
v_mfma_f32_16x16x32_bf16 v[120:123], a[108:111], v[192:195], v[120:123]
v_xor_b32 v234, 64, v232
v_xor_b32 v235, 96, v232
v_mfma_f32_16x16x32_bf16 v[120:123], a[112:115], v[200:203], v[120:123]
s_add_u32 m0, s63, 1024
ds_read_b64_tr_b16 a[100:101], v232 offset:4096
ds_read_b64_tr_b16 a[102:103], v232 offset:6144
v_mfma_f32_16x16x32_bf16 v[124:127], a[104:107], v[188:191], v[180:183]
buffer_load_dwordx4 %[vX1], %[srdWo], s75 offen offset:0 lds
v_mfma_f32_16x16x32_bf16 v[124:127], a[108:111], v[196:199], v[124:127]
ds_read_b64_tr_b16 a[104:105], v232 offset:8192
ds_read_b64_tr_b16 a[106:107], v232 offset:10240
v_mfma_f32_16x16x32_bf16 v[124:127], a[112:115], v[204:207], v[124:127]
s_waitcnt lgkmcnt(6)
v_cvt_pk_bf16_f32 v166, v140, v141
v_cvt_pk_bf16_f32 v167, v142, v143
v_mfma_f32_16x16x32_bf16 v[120:123], a[116:119], v[208:211], v[120:123]
s_add_u32 m0, s63, 2048
ds_read_b64_tr_b16 a[108:109], v232 offset:12288
ds_read_b64_tr_b16 a[110:111], v232 offset:14336
v_mfma_f32_16x16x32_bf16 v[120:123], a[120:123], v[216:219], v[120:123]
)"
R"(buffer_load_dwordx4 %[vW], %[srdWo], s75 offen offset:0 lds
v_mfma_f32_16x16x32_bf16 v[120:123], a[124:127], v[224:227], v[120:123]
ds_read_b64_tr_b16 a[112:113], v232 offset:16384
ds_read_b64_tr_b16 a[114:115], v232 offset:18432
v_mfma_f32_16x16x32_bf16 v[124:127], a[116:119], v[212:215], v[124:127]
s_add_u32 s75, s78, 523392
v_exp_f32 v152, v152
v_mfma_f32_16x16x32_bf16 v[124:127], a[120:123], v[220:223], v[124:127]
s_add_u32 m0, s63, 3072
ds_read_b64_tr_b16 a[116:117], v232 offset:20480
ds_read_b64_tr_b16 a[118:119], v232 offset:22528
v_mfma_f32_16x16x32_bf16 v[124:127], a[124:127], v[228:231], v[124:127]
s_waitcnt lgkmcnt(6)
buffer_load_dwordx4 %[vWo], %[srdWo], s75 offen offset:0 lds
v_mfma_f32_16x16x32_bf16 a[192:195], a[96:99], a[140:143], a[192:195]
ds_read_b64_tr_b16 a[120:121], v233 offset:0
ds_read_b64_tr_b16 a[122:123], v233 offset:2048
v_mfma_f32_16x16x32_bf16 a[192:195], a[100:103], a[148:151], a[192:195]
v_exp_f32 v153, v153
v_mfma_f32_16x16x32_bf16 a[192:195], a[104:107], a[156:159], a[192:195]
s_add_u32 m0, s63, 4096
ds_read_b64_tr_b16 a[124:125], v233 offset:4096
ds_read_b64_tr_b16 a[126:127], v233 offset:6144
v_mfma_f32_16x16x32_bf16 a[196:199], a[96:99], a[144:147], a[196:199]
)"
R"(buffer_load_dwordx4 %[vX1], %[srdWo], s75 offen offset:0 lds
v_mfma_f32_16x16x32_bf16 a[196:199], a[100:103], a[152:155], a[196:199]
ds_read_b64_tr_b16 a[96:97], v233 offset:8192
ds_read_b64_tr_b16 a[98:99], v233 offset:10240
v_mfma_f32_16x16x32_bf16 a[196:199], a[104:107], a[160:163], a[196:199]
s_waitcnt lgkmcnt(6)
v_exp_f32 v154, v154
v_mfma_f32_16x16x32_bf16 a[192:195], a[108:111], a[164:167], a[192:195]
s_add_u32 m0, s63, 5120
ds_read_b64_tr_b16 a[100:101], v233 offset:12288
ds_read_b64_tr_b16 a[102:103], v233 offset:14336
v_mfma_f32_16x16x32_bf16 a[192:195], a[112:115], a[172:175], a[192:195]
buffer_load_dwordx4 %[vW], %[srdWo], s75 offen offset:0 lds
v_mfma_f32_16x16x32_bf16 a[192:195], a[116:119], a[180:183], a[192:195]
ds_read_b64_tr_b16 a[104:105], v233 offset:16384
ds_read_b64_tr_b16 a[106:107], v233 offset:18432
v_mfma_f32_16x16x32_bf16 a[196:199], a[108:111], a[168:171], a[196:199]
v_exp_f32 v155, v155
v_mfma_f32_16x16x32_bf16 a[196:199], a[112:115], a[176:179], a[196:199]
ds_read_b64_tr_b16 a[108:109], v233 offset:20480
ds_read_b64_tr_b16 a[110:111], v233 offset:22528
v_mfma_f32_16x16x32_bf16 a[196:199], a[116:119], a[184:187], a[196:199]
s_waitcnt lgkmcnt(6)
v_cvt_pk_bf16_f32 v168, v144, v145
)"
R"(v_cvt_pk_bf16_f32 v169, v146, v147
v_mfma_f32_16x16x32_bf16 a[200:203], a[120:123], a[140:143], a[200:203]
ds_read_b64_tr_b16 a[112:113], v234 offset:0
ds_read_b64_tr_b16 a[114:115], v234 offset:2048
v_mfma_f32_16x16x32_bf16 a[200:203], a[124:127], a[148:151], a[200:203]
v_exp_f32 v148, v148
v_mfma_f32_16x16x32_bf16 a[200:203], a[96:99], a[156:159], a[200:203]
ds_read_b64_tr_b16 a[116:117], v234 offset:4096
ds_read_b64_tr_b16 a[118:119], v234 offset:6144
v_mfma_f32_16x16x32_bf16 a[204:207], a[120:123], a[144:147], a[204:207]
v_exp_f32 v149, v149
v_mfma_f32_16x16x32_bf16 a[204:207], a[124:127], a[152:155], a[204:207]
ds_read_b64_tr_b16 a[120:121], v234 offset:8192
ds_read_b64_tr_b16 a[122:123], v234 offset:10240
v_mfma_f32_16x16x32_bf16 a[204:207], a[96:99], a[160:163], a[204:207]
s_waitcnt lgkmcnt(6)
v_exp_f32 v150, v150
v_mfma_f32_16x16x32_bf16 a[200:203], a[100:103], a[164:167], a[200:203]
ds_read_b64_tr_b16 a[124:125], v234 offset:12288
ds_read_b64_tr_b16 a[126:127], v234 offset:14336
v_mfma_f32_16x16x32_bf16 a[200:203], a[104:107], a[172:175], a[200:203]
v_exp_f32 v151, v151
v_mfma_f32_16x16x32_bf16 a[200:203], a[108:111], a[180:183], a[200:203]
ds_read_b64_tr_b16 a[96:97], v234 offset:16384
)"
R"(ds_read_b64_tr_b16 a[98:99], v234 offset:18432
v_mfma_f32_16x16x32_bf16 a[204:207], a[100:103], a[168:171], a[204:207]
v_cvt_pk_bf16_f32 v170, v152, v153
v_cvt_pk_bf16_f32 v171, v154, v155
v_mfma_f32_16x16x32_bf16 a[204:207], a[104:107], a[176:179], a[204:207]
ds_read_b64_tr_b16 a[100:101], v234 offset:20480
ds_read_b64_tr_b16 a[102:103], v234 offset:22528
v_mfma_f32_16x16x32_bf16 a[204:207], a[108:111], a[184:187], a[204:207]
s_waitcnt lgkmcnt(6)
v_exp_f32 v156, v156
v_mfma_f32_16x16x32_bf16 a[208:211], a[112:115], a[140:143], a[208:211]
ds_read_b64_tr_b16 a[104:105], v235 offset:0
ds_read_b64_tr_b16 a[106:107], v235 offset:2048
v_mfma_f32_16x16x32_bf16 a[208:211], a[116:119], a[148:151], a[208:211]
v_exp_f32 v157, v157
v_mfma_f32_16x16x32_bf16 a[208:211], a[120:123], a[156:159], a[208:211]
ds_read_b64_tr_b16 a[108:109], v235 offset:4096
ds_read_b64_tr_b16 a[110:111], v235 offset:6144
v_mfma_f32_16x16x32_bf16 a[212:215], a[112:115], a[144:147], a[212:215]
v_exp_f32 v158, v158
v_mfma_f32_16x16x32_bf16 a[212:215], a[116:119], a[152:155], a[212:215]
ds_read_b64_tr_b16 a[112:113], v235 offset:8192
ds_read_b64_tr_b16 a[114:115], v235 offset:10240
v_mfma_f32_16x16x32_bf16 a[212:215], a[120:123], a[160:163], a[212:215]
)"
R"(s_waitcnt lgkmcnt(6)
v_exp_f32 v159, v159
v_mfma_f32_16x16x32_bf16 a[208:211], a[124:127], a[164:167], a[208:211]
s_add_u32 s76, s68, 172032
ds_read_b64_tr_b16 a[116:117], v235 offset:12288
ds_read_b64_tr_b16 a[118:119], v235 offset:14336
v_mfma_f32_16x16x32_bf16 a[208:211], a[96:99], a[172:175], a[208:211]
s_waitcnt vmcnt(18)
s_barrier
v_mfma_f32_16x16x32_bf16 a[208:211], a[100:103], a[180:183], a[208:211]
s_add_u32 m0, s64, 0
ds_read_b64_tr_b16 a[120:121], v235 offset:16384
ds_read_b64_tr_b16 a[122:123], v235 offset:18432
v_mfma_f32_16x16x32_bf16 a[212:215], a[124:127], a[168:171], a[212:215]
buffer_load_dwordx4 %[vV], %[srdV], s76 offen offset:0 nt lds
v_mfma_f32_16x16x32_bf16 a[212:215], a[96:99], a[176:179], a[212:215]
ds_read_b64_tr_b16 a[124:125], v235 offset:20480
ds_read_b64_tr_b16 a[126:127], v235 offset:22528
v_mfma_f32_16x16x32_bf16 a[212:215], a[100:103], a[184:187], a[212:215]
s_waitcnt lgkmcnt(6)
v_add_u32 v232, s53, %[pV]
v_xor_b32 v233, 32, v232
v_mfma_f32_16x16x32_bf16 a[216:219], a[104:107], a[140:143], a[216:219]
ds_read_b64_tr_b16 a[96:97], v232 offset:0
)"
R"(ds_read_b64_tr_b16 a[98:99], v232 offset:6144
v_mfma_f32_16x16x32_bf16 a[216:219], a[108:111], a[148:151], a[216:219]
v_xor_b32 v234, 64, v232
v_xor_b32 v235, 96, v232
v_mfma_f32_16x16x32_bf16 a[216:219], a[112:115], a[156:159], a[216:219]
s_add_u32 m0, s64, 896
ds_read_b64_tr_b16 a[100:101], v232 offset:12288
ds_read_b64_tr_b16 a[102:103], v232 offset:18432
v_mfma_f32_16x16x32_bf16 a[220:223], a[104:107], a[144:147], a[220:223]
buffer_load_dwordx4 %[vV], %[srdV], s76 offen offset:128 nt lds
v_mfma_f32_16x16x32_bf16 a[220:223], a[108:111], a[152:155], a[220:223]
ds_read_b64_tr_b16 a[104:105], v233 offset:0
ds_read_b64_tr_b16 a[106:107], v233 offset:6144
v_mfma_f32_16x16x32_bf16 a[220:223], a[112:115], a[160:163], a[220:223]
s_waitcnt lgkmcnt(6)
v_cvt_pk_bf16_f32 v172, v148, v149
v_cvt_pk_bf16_f32 v173, v150, v151
v_mfma_f32_16x16x32_bf16 a[216:219], a[116:119], a[164:167], a[216:219]
s_add_u32 m0, s64, 1792
ds_read_b64_tr_b16 a[108:109], v233 offset:12288
ds_read_b64_tr_b16 a[110:111], v233 offset:18432
v_mfma_f32_16x16x32_bf16 a[216:219], a[120:123], a[172:175], a[216:219]
buffer_load_dwordx4 %[vV], %[srdV], s76 offen offset:256 nt lds
v_mfma_f32_16x16x32_bf16 a[216:219], a[124:127], a[180:183], a[216:219]
)"
R"(ds_read_b64_tr_b16 a[112:113], v234 offset:0
ds_read_b64_tr_b16 a[114:115], v234 offset:6144
v_mfma_f32_16x16x32_bf16 a[220:223], a[116:119], a[168:171], a[220:223]
v_cvt_pk_bf16_f32 v174, v156, v157
v_cvt_pk_bf16_f32 v175, v158, v159
v_mfma_f32_16x16x32_bf16 a[220:223], a[120:123], a[176:179], a[220:223]
s_add_u32 m0, s64, 0
ds_read_b64_tr_b16 a[116:117], v234 offset:12288
ds_read_b64_tr_b16 a[118:119], v234 offset:18432
v_mfma_f32_16x16x32_bf16 a[220:223], a[124:127], a[184:187], a[220:223]
buffer_load_dwordx4 %[vV], %[srdV], s76 offen offset:3072 nt lds
v_mfma_f32_16x16x32_bf16 a[132:135], a[128:131], v[160:163], a[132:135]
v_max3_f32 v236, v96, v97, v98
v_max3_f32 v237, v100, v101, v102
v_mfma_f32_16x16x32_bf16 a[132:135], a[128:131], v[168:171], a[132:135]
v_max_f32 v236, v236, v99
v_max_f32 v237, v237, v103
v_mfma_f32_16x16x32_bf16 a[136:139], a[128:131], v[164:167], a[136:139]
s_add_u32 m0, s64, 896
v_max3_f32 v236, v236, v104, v105
v_max3_f32 v237, v237, v108, v109
v_mfma_f32_16x16x32_bf16 a[136:139], a[128:131], v[172:175], a[136:139]
s_waitcnt lgkmcnt(4)
buffer_load_dwordx4 %[vV], %[srdV], s76 offen offset:3200 nt lds
)"
R"(v_mfma_f32_16x16x32_bf16 v[0:3], a[96:99], v[160:163], v[0:3]
ds_read_b64_tr_b16 a[120:121], v235 offset:0
ds_read_b64_tr_b16 a[122:123], v235 offset:6144
v_mfma_f32_16x16x32_bf16 v[0:3], a[100:103], v[168:171], v[0:3]
v_max3_f32 v236, v236, v106, v107
v_max3_f32 v237, v237, v110, v111
v_mfma_f32_16x16x32_bf16 v[4:7], a[96:99], v[164:167], v[4:7]
s_add_u32 m0, s64, 1792
ds_read_b64_tr_b16 a[124:125], v235 offset:12288
ds_read_b64_tr_b16 a[126:127], v235 offset:18432
v_mfma_f32_16x16x32_bf16 v[4:7], a[100:103], v[172:175], v[4:7]
buffer_load_dwordx4 %[vV], %[srdV], s76 offen offset:3328 nt lds
v_mfma_f32_16x16x32_bf16 v[8:11], a[104:107], v[160:163], v[8:11]
ds_read_b64_tr_b16 a[96:97], v232 offset:1024
ds_read_b64_tr_b16 a[98:99], v232 offset:7168
v_mfma_f32_16x16x32_bf16 v[8:11], a[108:111], v[168:171], v[8:11]
v_max3_f32 v236, v236, v112, v113
v_max3_f32 v237, v237, v116, v117
v_mfma_f32_16x16x32_bf16 v[12:15], a[104:107], v[164:167], v[12:15]
ds_read_b64_tr_b16 a[100:101], v232 offset:13312
ds_read_b64_tr_b16 a[102:103], v232 offset:19456
v_mfma_f32_16x16x32_bf16 v[12:15], a[108:111], v[172:175], v[12:15]
s_waitcnt lgkmcnt(4)
v_max3_f32 v236, v236, v114, v115
)"
R"(v_max3_f32 v237, v237, v118, v119
v_mfma_f32_16x16x32_bf16 v[16:19], a[112:115], v[160:163], v[16:19]
ds_read_b64_tr_b16 a[104:105], v233 offset:1024
ds_read_b64_tr_b16 a[106:107], v233 offset:7168
v_mfma_f32_16x16x32_bf16 v[16:19], a[116:119], v[168:171], v[16:19]
v_max3_f32 v236, v236, v120, v121
v_max3_f32 v237, v237, v124, v125
v_mfma_f32_16x16x32_bf16 v[20:23], a[112:115], v[164:167], v[20:23]
ds_read_b64_tr_b16 a[108:109], v233 offset:13312
ds_read_b64_tr_b16 a[110:111], v233 offset:19456
v_mfma_f32_16x16x32_bf16 v[20:23], a[116:119], v[172:175], v[20:23]
v_max3_f32 v236, v236, v122, v123
v_max3_f32 v237, v237, v126, v127
v_mfma_f32_16x16x32_bf16 v[24:27], a[120:123], v[160:163], v[24:27]
ds_read_b64_tr_b16 a[112:113], v234 offset:1024
ds_read_b64_tr_b16 a[114:115], v234 offset:7168
v_mfma_f32_16x16x32_bf16 v[24:27], a[124:127], v[168:171], v[24:27]
v_max_f32 v236, v236, v237
v_cmp_lt_f32 vcc, s77, v236
v_mfma_f32_16x16x32_bf16 v[28:31], a[120:123], v[164:167], v[28:31]
ds_read_b64_tr_b16 a[116:117], v234 offset:13312
ds_read_b64_tr_b16 a[118:119], v234 offset:19456
v_mfma_f32_16x16x32_bf16 v[28:31], a[124:127], v[172:175], v[28:31]
s_waitcnt lgkmcnt(4)
)"
R"(v_mfma_f32_16x16x32_bf16 v[32:35], a[96:99], v[160:163], v[32:35]
ds_read_b64_tr_b16 a[120:121], v235 offset:1024
ds_read_b64_tr_b16 a[122:123], v235 offset:7168
v_mfma_f32_16x16x32_bf16 v[32:35], a[100:103], v[168:171], v[32:35]
v_mfma_f32_16x16x32_bf16 v[36:39], a[96:99], v[164:167], v[36:39]
ds_read_b64_tr_b16 a[124:125], v235 offset:13312
ds_read_b64_tr_b16 a[126:127], v235 offset:19456
v_mfma_f32_16x16x32_bf16 v[36:39], a[100:103], v[172:175], v[36:39]
v_mfma_f32_16x16x32_bf16 v[40:43], a[104:107], v[160:163], v[40:43]
ds_read_b64_tr_b16 a[96:97], v232 offset:2048
ds_read_b64_tr_b16 a[98:99], v232 offset:8192
v_mfma_f32_16x16x32_bf16 v[40:43], a[108:111], v[168:171], v[40:43]
v_mfma_f32_16x16x32_bf16 v[44:47], a[104:107], v[164:167], v[44:47]
ds_read_b64_tr_b16 a[100:101], v232 offset:14336
ds_read_b64_tr_b16 a[102:103], v232 offset:20480
v_mfma_f32_16x16x32_bf16 v[44:47], a[108:111], v[172:175], v[44:47]
s_waitcnt lgkmcnt(4)
v_mfma_f32_16x16x32_bf16 v[48:51], a[112:115], v[160:163], v[48:51]
ds_read_b64_tr_b16 a[104:105], v233 offset:2048
ds_read_b64_tr_b16 a[106:107], v233 offset:8192
v_mfma_f32_16x16x32_bf16 v[48:51], a[116:119], v[168:171], v[48:51]
v_mfma_f32_16x16x32_bf16 v[52:55], a[112:115], v[164:167], v[52:55]
ds_read_b64_tr_b16 a[108:109], v233 offset:14336
ds_read_b64_tr_b16 a[110:111], v233 offset:20480
)"
R"(v_mfma_f32_16x16x32_bf16 v[52:55], a[116:119], v[172:175], v[52:55]
v_mfma_f32_16x16x32_bf16 v[56:59], a[120:123], v[160:163], v[56:59]
ds_read_b64_tr_b16 a[112:113], v234 offset:2048
ds_read_b64_tr_b16 a[114:115], v234 offset:8192
v_mfma_f32_16x16x32_bf16 v[56:59], a[124:127], v[168:171], v[56:59]
v_mfma_f32_16x16x32_bf16 v[60:63], a[120:123], v[164:167], v[60:63]
s_add_u32 s75, s78, 738304
ds_read_b64_tr_b16 a[116:117], v234 offset:14336
ds_read_b64_tr_b16 a[118:119], v234 offset:20480
v_mfma_f32_16x16x32_bf16 v[60:63], a[124:127], v[172:175], v[60:63]
s_waitcnt lgkmcnt(4)
s_waitcnt vmcnt(18)
s_barrier
v_mfma_f32_16x16x32_bf16 v[64:67], a[96:99], v[160:163], v[64:67]
s_add_u32 m0, s61, 0
ds_read_b64_tr_b16 a[120:121], v235 offset:2048
ds_read_b64_tr_b16 a[122:123], v235 offset:8192
v_mfma_f32_16x16x32_bf16 v[64:67], a[100:103], v[168:171], v[64:67]
buffer_load_dwordx4 %[vWo], %[srdWo], s75 offen offset:0 lds
v_mfma_f32_16x16x32_bf16 v[68:71], a[96:99], v[164:167], v[68:71]
ds_read_b64_tr_b16 a[124:125], v235 offset:14336
ds_read_b64_tr_b16 a[126:127], v235 offset:20480
v_mfma_f32_16x16x32_bf16 v[68:71], a[100:103], v[172:175], v[68:71]
v_add_u32 v232, s48, %[pK]
)"
R"(v_xor_b32 v233, 64, v232
v_mfma_f32_16x16x32_bf16 v[72:75], a[104:107], v[160:163], v[72:75]
s_add_u32 m0, s61, 1024
ds_read_b128 a[96:99], v232 offset:0
ds_read_b128 a[100:103], v233 offset:0
v_mfma_f32_16x16x32_bf16 v[72:75], a[108:111], v[168:171], v[72:75]
buffer_load_dwordx4 %[vX1], %[srdWo], s75 offen offset:0 lds
v_mfma_f32_16x16x32_bf16 v[76:79], a[104:107], v[164:167], v[76:79]
v_mfma_f32_16x16x32_bf16 v[76:79], a[108:111], v[172:175], v[76:79]
s_waitcnt lgkmcnt(2)
v_mfma_f32_16x16x32_bf16 v[80:83], a[112:115], v[160:163], v[80:83]
s_add_u32 m0, s61, 2048
ds_read_b128 a[104:107], v232 offset:1024
ds_read_b128 a[108:111], v233 offset:1024
v_mfma_f32_16x16x32_bf16 v[80:83], a[116:119], v[168:171], v[80:83]
buffer_load_dwordx4 %[vW], %[srdWo], s75 offen offset:0 lds
v_mfma_f32_16x16x32_bf16 v[84:87], a[112:115], v[164:167], v[84:87]
v_mfma_f32_16x16x32_bf16 v[84:87], a[116:119], v[172:175], v[84:87]
s_add_u32 s75, s78, 769024
v_mfma_f32_16x16x32_bf16 v[88:91], a[120:123], v[160:163], v[88:91]
s_add_u32 m0, s61, 3072
ds_read_b128 a[112:115], v232 offset:2048
ds_read_b128 a[116:119], v233 offset:2048
v_mfma_f32_16x16x32_bf16 v[88:91], a[124:127], v[168:171], v[88:91]
)"
R"(buffer_load_dwordx4 %[vWo], %[srdWo], s75 offen offset:0 lds
v_mfma_f32_16x16x32_bf16 v[92:95], a[120:123], v[164:167], v[92:95]
v_mfma_f32_16x16x32_bf16 v[92:95], a[124:127], v[172:175], v[92:95]
s_waitcnt lgkmcnt(3)
s_cbranch_vccnz RARE15_%=
BACK15_%=:
v_mfma_f32_16x16x32_bf16 v[128:131], a[96:99], v[184:187], v[176:179]
s_add_u32 m0, s61, 4096
ds_read_b128 a[120:123], v232 offset:6144
ds_read_b128 a[124:127], v233 offset:6144
v_exp_f32 v96, v96
v_mfma_f32_16x16x32_bf16 v[128:131], a[100:103], v[192:195], v[128:131]
buffer_load_dwordx4 %[vX1], %[srdWo], s75 offen offset:0 lds
v_exp_f32 v97, v97
v_mfma_f32_16x16x32_bf16 v[128:131], a[104:107], v[200:203], v[128:131]
v_exp_f32 v98, v98
v_exp_f32 v99, v99
v_mfma_f32_16x16x32_bf16 v[132:135], a[96:99], v[188:191], v[180:183]
v_exp_f32 v104, v104
v_exp_f32 v105, v105
v_mfma_f32_16x16x32_bf16 v[132:135], a[100:103], v[196:199], v[132:135]
s_add_u32 m0, s61, 5120
ds_read_b128 a[96:99], v232 offset:7168
ds_read_b128 a[100:103], v233 offset:7168
)"
R"(v_exp_f32 v106, v106
v_mfma_f32_16x16x32_bf16 v[132:135], a[104:107], v[204:207], v[132:135]
s_waitcnt lgkmcnt(4)
buffer_load_dwordx4 %[vW], %[srdWo], s75 offen offset:0 lds
v_mfma_f32_16x16x32_bf16 v[128:131], a[108:111], v[208:211], v[128:131]
v_exp_f32 v107, v107
v_cvt_pk_bf16_f32 v160, v96, v97
v_mfma_f32_16x16x32_bf16 v[128:131], a[112:115], v[216:219], v[128:131]
v_cvt_pk_bf16_f32 v161, v98, v99
v_exp_f32 v100, v100
v_mfma_f32_16x16x32_bf16 v[128:131], a[116:119], v[224:227], v[128:131]
v_exp_f32 v101, v101
v_exp_f32 v102, v102
v_mfma_f32_16x16x32_bf16 v[132:135], a[108:111], v[212:215], v[132:135]
ds_read_b128 a[104:107], v232 offset:8192
ds_read_b128 a[108:111], v233 offset:8192
v_mfma_f32_16x16x32_bf16 v[132:135], a[112:115], v[220:223], v[132:135]
v_exp_f32 v103, v103
v_cvt_pk_bf16_f32 v162, v104, v105
v_mfma_f32_16x16x32_bf16 v[132:135], a[116:119], v[228:231], v[132:135]
s_waitcnt lgkmcnt(3)
v_cvt_pk_bf16_f32 v163, v106, v107
v_exp_f32 v108, v108
v_mfma_f32_16x16x32_bf16 v[136:139], a[120:123], v[184:187], v[176:179]
)"
R"(ds_read_b128 a[112:115], v232 offset:12288
ds_read_b128 a[116:119], v233 offset:12288
v_exp_f32 v109, v109
v_mfma_f32_16x16x32_bf16 v[136:139], a[124:127], v[192:195], v[136:139]
v_exp_f32 v110, v110
v_mfma_f32_16x16x32_bf16 v[136:139], a[96:99], v[200:203], v[136:139]
v_exp_f32 v111, v111
v_cvt_pk_bf16_f32 v164, v100, v101
v_mfma_f32_16x16x32_bf16 v[140:143], a[120:123], v[188:191], v[180:183]
v_cvt_pk_bf16_f32 v165, v102, v103
v_exp_f32 v112, v112
v_mfma_f32_16x16x32_bf16 v[140:143], a[124:127], v[196:199], v[140:143]
ds_read_b128 a[120:123], v232 offset:13312
ds_read_b128 a[124:127], v233 offset:13312
v_exp_f32 v113, v113
v_mfma_f32_16x16x32_bf16 v[140:143], a[96:99], v[204:207], v[140:143]
s_waitcnt lgkmcnt(4)
v_exp_f32 v114, v114
v_mfma_f32_16x16x32_bf16 v[136:139], a[100:103], v[208:211], v[136:139]
v_exp_f32 v115, v115
v_cvt_pk_bf16_f32 v166, v108, v109
v_mfma_f32_16x16x32_bf16 v[136:139], a[104:107], v[216:219], v[136:139]
v_cvt_pk_bf16_f32 v167, v110, v111
v_exp_f32 v120, v120
)"
R"(v_mfma_f32_16x16x32_bf16 v[136:139], a[108:111], v[224:227], v[136:139]
v_exp_f32 v121, v121
v_exp_f32 v122, v122
v_mfma_f32_16x16x32_bf16 v[140:143], a[100:103], v[212:215], v[140:143]
ds_read_b128 a[96:99], v232 offset:14336
ds_read_b128 a[100:103], v233 offset:14336
v_mfma_f32_16x16x32_bf16 v[140:143], a[104:107], v[220:223], v[140:143]
v_exp_f32 v123, v123
v_cvt_pk_bf16_f32 v168, v112, v113
v_mfma_f32_16x16x32_bf16 v[140:143], a[108:111], v[228:231], v[140:143]
s_waitcnt lgkmcnt(3)
v_cvt_pk_bf16_f32 v169, v114, v115
v_exp_f32 v116, v116
v_mfma_f32_16x16x32_bf16 v[144:147], a[112:115], v[184:187], v[176:179]
ds_read_b128 a[104:107], v232 offset:18432
ds_read_b128 a[108:111], v233 offset:18432
v_mfma_f32_16x16x32_bf16 v[144:147], a[116:119], v[192:195], v[144:147]
v_exp_f32 v117, v117
v_mfma_f32_16x16x32_bf16 v[144:147], a[120:123], v[200:203], v[144:147]
v_exp_f32 v118, v118
v_mfma_f32_16x16x32_bf16 v[148:151], a[112:115], v[188:191], v[180:183]
v_exp_f32 v119, v119
v_cvt_pk_bf16_f32 v170, v120, v121
v_mfma_f32_16x16x32_bf16 v[148:151], a[116:119], v[196:199], v[148:151]
)"
R"(ds_read_b128 a[112:115], v232 offset:19456
ds_read_b128 a[116:119], v233 offset:19456
v_cvt_pk_bf16_f32 v171, v122, v123
v_mfma_f32_16x16x32_bf16 v[148:151], a[120:123], v[204:207], v[148:151]
s_waitcnt lgkmcnt(4)
v_exp_f32 v124, v124
v_mfma_f32_16x16x32_bf16 v[144:147], a[124:127], v[208:211], v[144:147]
v_exp_f32 v125, v125
v_mfma_f32_16x16x32_bf16 v[144:147], a[96:99], v[216:219], v[144:147]
s_waitcnt vmcnt(18)
s_barrier
v_mfma_f32_16x16x32_bf16 v[144:147], a[100:103], v[224:227], v[144:147]
s_add_u32 s75, s78, 738432
v_exp_f32 v126, v126
v_mfma_f32_16x16x32_bf16 v[148:151], a[124:127], v[212:215], v[148:151]
s_add_u32 m0, s65, 0
ds_read_b128 a[120:123], v232 offset:20480
ds_read_b128 a[124:127], v233 offset:20480
v_mfma_f32_16x16x32_bf16 v[148:151], a[96:99], v[220:223], v[148:151]
buffer_load_dwordx4 %[vWo], %[srdWo], s75 offen offset:0 lds
v_mfma_f32_16x16x32_bf16 v[148:151], a[100:103], v[228:231], v[148:151]
s_waitcnt lgkmcnt(3)
v_add_u32 v232, s50, %[pV]
v_xor_b32 v233, 32, v232
)"
R"(v_mfma_f32_16x16x32_bf16 v[152:155], a[104:107], v[184:187], v[176:179]
ds_read_b64_tr_b16 a[96:97], v232 offset:0
ds_read_b64_tr_b16 a[98:99], v232 offset:6144
v_mfma_f32_16x16x32_bf16 v[152:155], a[108:111], v[192:195], v[152:155]
v_xor_b32 v234, 64, v232
v_xor_b32 v235, 96, v232
v_mfma_f32_16x16x32_bf16 v[152:155], a[112:115], v[200:203], v[152:155]
s_add_u32 m0, s65, 1024
ds_read_b64_tr_b16 a[100:101], v232 offset:12288
ds_read_b64_tr_b16 a[102:103], v232 offset:18432
v_mfma_f32_16x16x32_bf16 v[156:159], a[104:107], v[188:191], v[180:183]
buffer_load_dwordx4 %[vX1], %[srdWo], s75 offen offset:0 lds
v_mfma_f32_16x16x32_bf16 v[156:159], a[108:111], v[196:199], v[156:159]
ds_read_b64_tr_b16 a[104:105], v233 offset:0
ds_read_b64_tr_b16 a[106:107], v233 offset:6144
v_mfma_f32_16x16x32_bf16 v[156:159], a[112:115], v[204:207], v[156:159]
s_waitcnt lgkmcnt(6)
v_exp_f32 v127, v127
v_cvt_pk_bf16_f32 v172, v116, v117
v_mfma_f32_16x16x32_bf16 v[152:155], a[116:119], v[208:211], v[152:155]
s_add_u32 m0, s65, 2048
ds_read_b64_tr_b16 a[108:109], v233 offset:12288
ds_read_b64_tr_b16 a[110:111], v233 offset:18432
v_cvt_pk_bf16_f32 v173, v118, v119
)"
R"(v_mfma_f32_16x16x32_bf16 v[152:155], a[120:123], v[216:219], v[152:155]
buffer_load_dwordx4 %[vW], %[srdWo], s75 offen offset:0 lds
v_mfma_f32_16x16x32_bf16 v[152:155], a[124:127], v[224:227], v[152:155]
ds_read_b64_tr_b16 a[112:113], v234 offset:0
ds_read_b64_tr_b16 a[114:115], v234 offset:6144
v_mfma_f32_16x16x32_bf16 v[156:159], a[116:119], v[212:215], v[156:159]
s_add_u32 s75, s78, 769152
v_cvt_pk_bf16_f32 v174, v124, v125
v_cvt_pk_bf16_f32 v175, v126, v127
v_mfma_f32_16x16x32_bf16 v[156:159], a[120:123], v[220:223], v[156:159]
s_add_u32 m0, s65, 3072
ds_read_b64_tr_b16 a[116:117], v234 offset:12288
ds_read_b64_tr_b16 a[118:119], v234 offset:18432
v_mfma_f32_16x16x32_bf16 v[156:159], a[124:127], v[228:231], v[156:159]
buffer_load_dwordx4 %[vWo], %[srdWo], s75 offen offset:0 lds
v_mfma_f32_16x16x32_bf16 a[132:135], a[128:131], v[160:163], a[132:135]
v_mfma_f32_16x16x32_bf16 a[132:135], a[128:131], v[168:171], a[132:135]
v_mfma_f32_16x16x32_bf16 a[136:139], a[128:131], v[164:167], a[136:139]
s_add_u32 m0, s65, 4096
v_mfma_f32_16x16x32_bf16 a[136:139], a[128:131], v[172:175], a[136:139]
s_waitcnt lgkmcnt(4)
buffer_load_dwordx4 %[vX1], %[srdWo], s75 offen offset:0 lds
v_mfma_f32_16x16x32_bf16 v[0:3], a[96:99], v[160:163], v[0:3]
ds_read_b64_tr_b16 a[120:121], v235 offset:0
)"
R"(ds_read_b64_tr_b16 a[122:123], v235 offset:6144
v_mfma_f32_16x16x32_bf16 v[0:3], a[100:103], v[168:171], v[0:3]
v_max3_f32 v236, v128, v129, v130
v_max3_f32 v237, v132, v133, v134
v_mfma_f32_16x16x32_bf16 v[4:7], a[96:99], v[164:167], v[4:7]
s_add_u32 m0, s65, 5120
ds_read_b64_tr_b16 a[124:125], v235 offset:12288
ds_read_b64_tr_b16 a[126:127], v235 offset:18432
v_mfma_f32_16x16x32_bf16 v[4:7], a[100:103], v[172:175], v[4:7]
buffer_load_dwordx4 %[vW], %[srdWo], s75 offen offset:0 lds
v_mfma_f32_16x16x32_bf16 v[8:11], a[104:107], v[160:163], v[8:11]
ds_read_b64_tr_b16 a[96:97], v232 offset:1024
ds_read_b64_tr_b16 a[98:99], v232 offset:7168
v_mfma_f32_16x16x32_bf16 v[8:11], a[108:111], v[168:171], v[8:11]
s_add_u32 s47, s46, 0
v_max_f32 v236, v236, v131
v_max_f32 v237, v237, v135
v_mfma_f32_16x16x32_bf16 v[12:15], a[104:107], v[164:167], v[12:15]
s_mov_b32 m0, s84
ds_read_b64_tr_b16 a[100:101], v232 offset:13312
ds_read_b64_tr_b16 a[102:103], v232 offset:19456
v_mfma_f32_16x16x32_bf16 v[12:15], a[108:111], v[172:175], v[12:15]
s_waitcnt lgkmcnt(4)
buffer_load_dword %[vX0], %[srdWo], s47 offen offset:0 lds
)"
R"(v_mfma_f32_16x16x32_bf16 v[16:19], a[112:115], v[160:163], v[16:19]
ds_read_b64_tr_b16 a[104:105], v233 offset:1024
ds_read_b64_tr_b16 a[106:107], v233 offset:7168
v_mfma_f32_16x16x32_bf16 v[16:19], a[116:119], v[168:171], v[16:19]
s_add_u32 s47, s46, 5120
v_max3_f32 v236, v236, v136, v137
v_max3_f32 v237, v237, v140, v141
v_mfma_f32_16x16x32_bf16 v[20:23], a[112:115], v[164:167], v[20:23]
s_mov_b32 m0, s84
ds_read_b64_tr_b16 a[108:109], v233 offset:13312
ds_read_b64_tr_b16 a[110:111], v233 offset:19456
v_mfma_f32_16x16x32_bf16 v[20:23], a[116:119], v[172:175], v[20:23]
buffer_load_dword %[vX0], %[srdWo], s47 offen offset:0 lds
v_mfma_f32_16x16x32_bf16 v[24:27], a[120:123], v[160:163], v[24:27]
ds_read_b64_tr_b16 a[112:113], v234 offset:1024
ds_read_b64_tr_b16 a[114:115], v234 offset:7168
v_mfma_f32_16x16x32_bf16 v[24:27], a[124:127], v[168:171], v[24:27]
v_max3_f32 v236, v236, v138, v139
v_max3_f32 v237, v237, v142, v143
v_mfma_f32_16x16x32_bf16 v[28:31], a[120:123], v[164:167], v[28:31]
ds_read_b64_tr_b16 a[116:117], v234 offset:13312
ds_read_b64_tr_b16 a[118:119], v234 offset:19456
v_mfma_f32_16x16x32_bf16 v[28:31], a[124:127], v[172:175], v[28:31]
s_waitcnt lgkmcnt(4)
)"
R"(v_max3_f32 v236, v236, v144, v145
v_max3_f32 v237, v237, v148, v149
v_mfma_f32_16x16x32_bf16 v[32:35], a[96:99], v[160:163], v[32:35]
ds_read_b64_tr_b16 a[120:121], v235 offset:1024
ds_read_b64_tr_b16 a[122:123], v235 offset:7168
v_mfma_f32_16x16x32_bf16 v[32:35], a[100:103], v[168:171], v[32:35]
v_max3_f32 v236, v236, v146, v147
v_max3_f32 v237, v237, v150, v151
v_mfma_f32_16x16x32_bf16 v[36:39], a[96:99], v[164:167], v[36:39]
ds_read_b64_tr_b16 a[124:125], v235 offset:13312
ds_read_b64_tr_b16 a[126:127], v235 offset:19456
v_mfma_f32_16x16x32_bf16 v[36:39], a[100:103], v[172:175], v[36:39]
v_max3_f32 v236, v236, v152, v153
v_max3_f32 v237, v237, v156, v157
v_mfma_f32_16x16x32_bf16 v[40:43], a[104:107], v[160:163], v[40:43]
ds_read_b64_tr_b16 a[96:97], v232 offset:2048
ds_read_b64_tr_b16 a[98:99], v232 offset:8192
v_mfma_f32_16x16x32_bf16 v[40:43], a[108:111], v[168:171], v[40:43]
v_max3_f32 v236, v236, v154, v155
v_max3_f32 v237, v237, v158, v159
v_mfma_f32_16x16x32_bf16 v[44:47], a[104:107], v[164:167], v[44:47]
ds_read_b64_tr_b16 a[100:101], v232 offset:14336
ds_read_b64_tr_b16 a[102:103], v232 offset:20480
v_mfma_f32_16x16x32_bf16 v[44:47], a[108:111], v[172:175], v[44:47]
)"
R"(s_waitcnt lgkmcnt(4)
v_max_f32 v236, v236, v237
v_cmp_lt_f32 vcc, s77, v236
v_mfma_f32_16x16x32_bf16 v[48:51], a[112:115], v[160:163], v[48:51]
ds_read_b64_tr_b16 a[104:105], v233 offset:2048
ds_read_b64_tr_b16 a[106:107], v233 offset:8192
v_mfma_f32_16x16x32_bf16 v[48:51], a[116:119], v[168:171], v[48:51]
v_mfma_f32_16x16x32_bf16 v[52:55], a[112:115], v[164:167], v[52:55]
ds_read_b64_tr_b16 a[108:109], v233 offset:14336
ds_read_b64_tr_b16 a[110:111], v233 offset:20480
v_mfma_f32_16x16x32_bf16 v[52:55], a[116:119], v[172:175], v[52:55]
v_mfma_f32_16x16x32_bf16 v[56:59], a[120:123], v[160:163], v[56:59]
ds_read_b64_tr_b16 a[112:113], v234 offset:2048
ds_read_b64_tr_b16 a[114:115], v234 offset:8192
v_mfma_f32_16x16x32_bf16 v[56:59], a[124:127], v[168:171], v[56:59]
v_mfma_f32_16x16x32_bf16 v[60:63], a[120:123], v[164:167], v[60:63]
s_add_u32 s47, s46, 10240
ds_read_b64_tr_b16 a[116:117], v234 offset:14336
ds_read_b64_tr_b16 a[118:119], v234 offset:20480
v_mfma_f32_16x16x32_bf16 v[60:63], a[124:127], v[172:175], v[60:63]
s_waitcnt lgkmcnt(4)
s_waitcnt vmcnt(20)
s_barrier
v_mfma_f32_16x16x32_bf16 v[64:67], a[96:99], v[160:163], v[64:67]
)"
R"(s_mov_b32 m0, s84
ds_read_b64_tr_b16 a[120:121], v235 offset:2048
ds_read_b64_tr_b16 a[122:123], v235 offset:8192
v_mfma_f32_16x16x32_bf16 v[64:67], a[100:103], v[168:171], v[64:67]
buffer_load_dword %[vX0], %[srdWo], s47 offen offset:0 lds
v_mfma_f32_16x16x32_bf16 v[68:71], a[96:99], v[164:167], v[68:71]
ds_read_b64_tr_b16 a[124:125], v235 offset:14336
ds_read_b64_tr_b16 a[126:127], v235 offset:20480
v_mfma_f32_16x16x32_bf16 v[68:71], a[100:103], v[172:175], v[68:71]
v_add_u32 v232, s51, %[pWo]
v_xor_b32 v233, 32, v232
v_mfma_f32_16x16x32_bf16 v[72:75], a[104:107], v[160:163], v[72:75]
ds_read_b64_tr_b16 a[96:97], v232 offset:0
ds_read_b64_tr_b16 a[98:99], v232 offset:2048
v_mfma_f32_16x16x32_bf16 v[72:75], a[108:111], v[168:171], v[72:75]
v_xor_b32 v234, 64, v232
v_xor_b32 v235, 96, v232
v_mfma_f32_16x16x32_bf16 v[76:79], a[104:107], v[164:167], v[76:79]
ds_read_b64_tr_b16 a[100:101], v232 offset:4096
ds_read_b64_tr_b16 a[102:103], v232 offset:6144
v_mfma_f32_16x16x32_bf16 v[76:79], a[108:111], v[172:175], v[76:79]
s_waitcnt lgkmcnt(4)
v_mfma_f32_16x16x32_bf16 v[80:83], a[112:115], v[160:163], v[80:83]
ds_read_b64_tr_b16 a[104:105], v232 offset:8192
)"
R"(ds_read_b64_tr_b16 a[106:107], v232 offset:10240
v_mfma_f32_16x16x32_bf16 v[80:83], a[116:119], v[168:171], v[80:83]
v_mfma_f32_16x16x32_bf16 v[84:87], a[112:115], v[164:167], v[84:87]
ds_read_b64_tr_b16 a[108:109], v232 offset:12288
ds_read_b64_tr_b16 a[110:111], v232 offset:14336
v_mfma_f32_16x16x32_bf16 v[84:87], a[116:119], v[172:175], v[84:87]
v_mfma_f32_16x16x32_bf16 v[88:91], a[120:123], v[160:163], v[88:91]
ds_read_b64_tr_b16 a[112:113], v232 offset:16384
ds_read_b64_tr_b16 a[114:115], v232 offset:18432
v_mfma_f32_16x16x32_bf16 v[88:91], a[124:127], v[168:171], v[88:91]
v_mfma_f32_16x16x32_bf16 v[92:95], a[120:123], v[164:167], v[92:95]
ds_read_b64_tr_b16 a[116:117], v232 offset:20480
ds_read_b64_tr_b16 a[118:119], v232 offset:22528
v_mfma_f32_16x16x32_bf16 v[92:95], a[124:127], v[172:175], v[92:95]
s_waitcnt lgkmcnt(6)
s_cbranch_vccnz RARE16_%=
BACK16_%=:
v_mfma_f32_16x16x32_bf16 a[224:227], a[96:99], a[140:143], a[224:227]
ds_read_b64_tr_b16 a[120:121], v233 offset:0
ds_read_b64_tr_b16 a[122:123], v233 offset:2048
v_exp_f32 v128, v128
v_mfma_f32_16x16x32_bf16 a[224:227], a[100:103], a[148:151], a[224:227]
v_exp_f32 v129, v129
v_exp_f32 v130, v130
)"
R"(v_mfma_f32_16x16x32_bf16 a[224:227], a[104:107], a[156:159], a[224:227]
ds_read_b64_tr_b16 a[124:125], v233 offset:4096
ds_read_b64_tr_b16 a[126:127], v233 offset:6144
v_exp_f32 v131, v131
v_mfma_f32_16x16x32_bf16 a[228:231], a[96:99], a[144:147], a[228:231]
v_exp_f32 v136, v136
v_exp_f32 v137, v137
v_mfma_f32_16x16x32_bf16 a[228:231], a[100:103], a[152:155], a[228:231]
ds_read_b64_tr_b16 a[96:97], v233 offset:8192
ds_read_b64_tr_b16 a[98:99], v233 offset:10240
v_exp_f32 v138, v138
v_mfma_f32_16x16x32_bf16 a[228:231], a[104:107], a[160:163], a[228:231]
s_waitcnt lgkmcnt(6)
v_exp_f32 v139, v139
v_cvt_pk_bf16_f32 v160, v128, v129
v_mfma_f32_16x16x32_bf16 a[224:227], a[108:111], a[164:167], a[224:227]
ds_read_b64_tr_b16 a[100:101], v233 offset:12288
ds_read_b64_tr_b16 a[102:103], v233 offset:14336
v_cvt_pk_bf16_f32 v161, v130, v131
v_mfma_f32_16x16x32_bf16 a[224:227], a[112:115], a[172:175], a[224:227]
v_exp_f32 v132, v132
v_exp_f32 v133, v133
v_mfma_f32_16x16x32_bf16 a[224:227], a[116:119], a[180:183], a[224:227]
ds_read_b64_tr_b16 a[104:105], v233 offset:16384
)"
R"(ds_read_b64_tr_b16 a[106:107], v233 offset:18432
v_exp_f32 v134, v134
v_mfma_f32_16x16x32_bf16 a[228:231], a[108:111], a[168:171], a[228:231]
v_exp_f32 v135, v135
v_cvt_pk_bf16_f32 v162, v136, v137
v_mfma_f32_16x16x32_bf16 a[228:231], a[112:115], a[176:179], a[228:231]
ds_read_b64_tr_b16 a[108:109], v233 offset:20480
ds_read_b64_tr_b16 a[110:111], v233 offset:22528
v_cvt_pk_bf16_f32 v163, v138, v139
v_mfma_f32_16x16x32_bf16 a[228:231], a[116:119], a[184:187], a[228:231]
s_waitcnt lgkmcnt(6)
v_exp_f32 v140, v140
v_exp_f32 v141, v141
v_mfma_f32_16x16x32_bf16 a[232:235], a[120:123], a[140:143], a[232:235]
ds_read_b64_tr_b16 a[112:113], v234 offset:0
ds_read_b64_tr_b16 a[114:115], v234 offset:2048
v_exp_f32 v142, v142
v_mfma_f32_16x16x32_bf16 a[232:235], a[124:127], a[148:151], a[232:235]
v_exp_f32 v143, v143
v_cvt_pk_bf16_f32 v164, v132, v133
v_mfma_f32_16x16x32_bf16 a[232:235], a[96:99], a[156:159], a[232:235]
ds_read_b64_tr_b16 a[116:117], v234 offset:4096
ds_read_b64_tr_b16 a[118:119], v234 offset:6144
v_cvt_pk_bf16_f32 v165, v134, v135
)"
R"(v_mfma_f32_16x16x32_bf16 a[236:239], a[120:123], a[144:147], a[236:239]
v_exp_f32 v144, v144
v_exp_f32 v145, v145
v_mfma_f32_16x16x32_bf16 a[236:239], a[124:127], a[152:155], a[236:239]
ds_read_b64_tr_b16 a[120:121], v234 offset:8192
ds_read_b64_tr_b16 a[122:123], v234 offset:10240
v_exp_f32 v146, v146
v_mfma_f32_16x16x32_bf16 a[236:239], a[96:99], a[160:163], a[236:239]
s_waitcnt lgkmcnt(6)
v_exp_f32 v147, v147
v_cvt_pk_bf16_f32 v166, v140, v141
v_mfma_f32_16x16x32_bf16 a[232:235], a[100:103], a[164:167], a[232:235]
ds_read_b64_tr_b16 a[124:125], v234 offset:12288
ds_read_b64_tr_b16 a[126:127], v234 offset:14336
v_cvt_pk_bf16_f32 v167, v142, v143
v_mfma_f32_16x16x32_bf16 a[232:235], a[104:107], a[172:175], a[232:235]
v_exp_f32 v152, v152
v_exp_f32 v153, v153
v_mfma_f32_16x16x32_bf16 a[232:235], a[108:111], a[180:183], a[232:235]
ds_read_b64_tr_b16 a[96:97], v234 offset:16384
ds_read_b64_tr_b16 a[98:99], v234 offset:18432
v_exp_f32 v154, v154
v_mfma_f32_16x16x32_bf16 a[236:239], a[100:103], a[168:171], a[236:239]
v_exp_f32 v155, v155
)"
R"(v_cvt_pk_bf16_f32 v168, v144, v145
v_mfma_f32_16x16x32_bf16 a[236:239], a[104:107], a[176:179], a[236:239]
ds_read_b64_tr_b16 a[100:101], v234 offset:20480
ds_read_b64_tr_b16 a[102:103], v234 offset:22528
v_cvt_pk_bf16_f32 v169, v146, v147
v_mfma_f32_16x16x32_bf16 a[236:239], a[108:111], a[184:187], a[236:239]
s_waitcnt lgkmcnt(6)
v_exp_f32 v148, v148
v_mfma_f32_16x16x32_bf16 a[240:243], a[112:115], a[140:143], a[240:243]
ds_read_b64_tr_b16 a[104:105], v235 offset:0
ds_read_b64_tr_b16 a[106:107], v235 offset:2048
v_mfma_f32_16x16x32_bf16 a[240:243], a[116:119], a[148:151], a[240:243]
v_exp_f32 v149, v149
v_mfma_f32_16x16x32_bf16 a[240:243], a[120:123], a[156:159], a[240:243]
ds_read_b64_tr_b16 a[108:109], v235 offset:4096
ds_read_b64_tr_b16 a[110:111], v235 offset:6144
v_mfma_f32_16x16x32_bf16 a[244:247], a[112:115], a[144:147], a[244:247]
v_exp_f32 v150, v150
v_mfma_f32_16x16x32_bf16 a[244:247], a[116:119], a[152:155], a[244:247]
ds_read_b64_tr_b16 a[112:113], v235 offset:8192
ds_read_b64_tr_b16 a[114:115], v235 offset:10240
v_mfma_f32_16x16x32_bf16 a[244:247], a[120:123], a[160:163], a[244:247]
s_waitcnt lgkmcnt(6)
v_exp_f32 v151, v151
)"
R"(v_cvt_pk_bf16_f32 v170, v152, v153
v_mfma_f32_16x16x32_bf16 a[240:243], a[124:127], a[164:167], a[240:243]
ds_read_b64_tr_b16 a[116:117], v235 offset:12288
ds_read_b64_tr_b16 a[118:119], v235 offset:14336
v_cvt_pk_bf16_f32 v171, v154, v155
v_mfma_f32_16x16x32_bf16 a[240:243], a[96:99], a[172:175], a[240:243]
s_waitcnt vmcnt(15)
s_barrier
v_mfma_f32_16x16x32_bf16 a[240:243], a[100:103], a[180:183], a[240:243]
ds_read_b64_tr_b16 a[120:121], v235 offset:16384
ds_read_b64_tr_b16 a[122:123], v235 offset:18432
v_mfma_f32_16x16x32_bf16 a[244:247], a[124:127], a[168:171], a[244:247]
v_exp_f32 v156, v156
v_mfma_f32_16x16x32_bf16 a[244:247], a[96:99], a[176:179], a[244:247]
ds_read_b64_tr_b16 a[124:125], v235 offset:20480
ds_read_b64_tr_b16 a[126:127], v235 offset:22528
v_mfma_f32_16x16x32_bf16 a[244:247], a[100:103], a[184:187], a[244:247]
s_waitcnt lgkmcnt(6)
v_add_u32 v232, s52, %[pV]
v_xor_b32 v233, 32, v232
v_mfma_f32_16x16x32_bf16 a[248:251], a[104:107], a[140:143], a[248:251]
ds_read_b64_tr_b16 a[96:97], v232 offset:0
ds_read_b64_tr_b16 a[98:99], v232 offset:6144
v_mfma_f32_16x16x32_bf16 a[248:251], a[108:111], a[148:151], a[248:251]
)"
R"(v_xor_b32 v234, 64, v232
v_xor_b32 v235, 96, v232
v_mfma_f32_16x16x32_bf16 a[248:251], a[112:115], a[156:159], a[248:251]
ds_read_b64_tr_b16 a[100:101], v232 offset:12288
ds_read_b64_tr_b16 a[102:103], v232 offset:18432
v_mfma_f32_16x16x32_bf16 a[252:255], a[104:107], a[144:147], a[252:255]
v_exp_f32 v157, v157
v_mfma_f32_16x16x32_bf16 a[252:255], a[108:111], a[152:155], a[252:255]
ds_read_b64_tr_b16 a[104:105], v233 offset:0
ds_read_b64_tr_b16 a[106:107], v233 offset:6144
v_mfma_f32_16x16x32_bf16 a[252:255], a[112:115], a[160:163], a[252:255]
s_waitcnt lgkmcnt(6)
v_exp_f32 v158, v158
v_mfma_f32_16x16x32_bf16 a[248:251], a[116:119], a[164:167], a[248:251]
ds_read_b64_tr_b16 a[108:109], v233 offset:12288
ds_read_b64_tr_b16 a[110:111], v233 offset:18432
v_mfma_f32_16x16x32_bf16 a[248:251], a[120:123], a[172:175], a[248:251]
v_exp_f32 v159, v159
v_cvt_pk_bf16_f32 v172, v148, v149
v_mfma_f32_16x16x32_bf16 a[248:251], a[124:127], a[180:183], a[248:251]
ds_read_b64_tr_b16 a[112:113], v234 offset:0
ds_read_b64_tr_b16 a[114:115], v234 offset:6144
v_cvt_pk_bf16_f32 v173, v150, v151
v_mfma_f32_16x16x32_bf16 a[252:255], a[116:119], a[168:171], a[252:255]
)"
R"(v_cvt_pk_bf16_f32 v174, v156, v157
v_cvt_pk_bf16_f32 v175, v158, v159
v_mfma_f32_16x16x32_bf16 a[252:255], a[120:123], a[176:179], a[252:255]
ds_read_b64_tr_b16 a[116:117], v234 offset:12288
ds_read_b64_tr_b16 a[118:119], v234 offset:18432
v_mfma_f32_16x16x32_bf16 a[252:255], a[124:127], a[184:187], a[252:255]
v_mfma_f32_16x16x32_bf16 a[132:135], a[128:131], v[160:163], a[132:135]
v_mfma_f32_16x16x32_bf16 a[132:135], a[128:131], v[168:171], a[132:135]
v_mfma_f32_16x16x32_bf16 a[136:139], a[128:131], v[164:167], a[136:139]
v_mfma_f32_16x16x32_bf16 a[136:139], a[128:131], v[172:175], a[136:139]
s_waitcnt lgkmcnt(4)
v_mfma_f32_16x16x32_bf16 v[0:3], a[96:99], v[160:163], v[0:3]
ds_read_b64_tr_b16 a[120:121], v235 offset:0
ds_read_b64_tr_b16 a[122:123], v235 offset:6144
v_mfma_f32_16x16x32_bf16 v[0:3], a[100:103], v[168:171], v[0:3]
v_mfma_f32_16x16x32_bf16 v[4:7], a[96:99], v[164:167], v[4:7]
ds_read_b64_tr_b16 a[124:125], v235 offset:12288
ds_read_b64_tr_b16 a[126:127], v235 offset:18432
v_mfma_f32_16x16x32_bf16 v[4:7], a[100:103], v[172:175], v[4:7]
v_mfma_f32_16x16x32_bf16 v[8:11], a[104:107], v[160:163], v[8:11]
ds_read_b64_tr_b16 a[96:97], v232 offset:1024
ds_read_b64_tr_b16 a[98:99], v232 offset:7168
v_mfma_f32_16x16x32_bf16 v[8:11], a[108:111], v[168:171], v[8:11]
v_mfma_f32_16x16x32_bf16 v[12:15], a[104:107], v[164:167], v[12:15]
)"
R"(ds_read_b64_tr_b16 a[100:101], v232 offset:13312
ds_read_b64_tr_b16 a[102:103], v232 offset:19456
v_mfma_f32_16x16x32_bf16 v[12:15], a[108:111], v[172:175], v[12:15]
s_waitcnt lgkmcnt(4)
v_mfma_f32_16x16x32_bf16 v[16:19], a[112:115], v[160:163], v[16:19]
ds_read_b64_tr_b16 a[104:105], v233 offset:1024
ds_read_b64_tr_b16 a[106:107], v233 offset:7168
v_mfma_f32_16x16x32_bf16 v[16:19], a[116:119], v[168:171], v[16:19]
v_mfma_f32_16x16x32_bf16 v[20:23], a[112:115], v[164:167], v[20:23]
ds_read_b64_tr_b16 a[108:109], v233 offset:13312
ds_read_b64_tr_b16 a[110:111], v233 offset:19456
v_mfma_f32_16x16x32_bf16 v[20:23], a[116:119], v[172:175], v[20:23]
v_mfma_f32_16x16x32_bf16 v[24:27], a[120:123], v[160:163], v[24:27]
ds_read_b64_tr_b16 a[112:113], v234 offset:1024
ds_read_b64_tr_b16 a[114:115], v234 offset:7168
v_mfma_f32_16x16x32_bf16 v[24:27], a[124:127], v[168:171], v[24:27]
v_accvgpr_read_b32 v238, a132
v_accvgpr_read_b32 v240, a136
v_mfma_f32_16x16x32_bf16 v[28:31], a[120:123], v[164:167], v[28:31]
ds_read_b64_tr_b16 a[116:117], v234 offset:13312
ds_read_b64_tr_b16 a[118:119], v234 offset:19456
v_mfma_f32_16x16x32_bf16 v[28:31], a[124:127], v[172:175], v[28:31]
s_waitcnt lgkmcnt(4)
v_rcp_f32 v238, v238
)"
R"(v_mfma_f32_16x16x32_bf16 v[32:35], a[96:99], v[160:163], v[32:35]
ds_read_b64_tr_b16 a[120:121], v235 offset:1024
ds_read_b64_tr_b16 a[122:123], v235 offset:7168
v_mfma_f32_16x16x32_bf16 v[32:35], a[100:103], v[168:171], v[32:35]
v_rcp_f32 v240, v240
v_mfma_f32_16x16x32_bf16 v[36:39], a[96:99], v[164:167], v[36:39]
ds_read_b64_tr_b16 a[124:125], v235 offset:13312
ds_read_b64_tr_b16 a[126:127], v235 offset:19456
v_mfma_f32_16x16x32_bf16 v[36:39], a[100:103], v[172:175], v[36:39]
v_mfma_f32_16x16x32_bf16 v[40:43], a[104:107], v[160:163], v[40:43]
ds_read_b64_tr_b16 a[96:97], v232 offset:2048
ds_read_b64_tr_b16 a[98:99], v232 offset:8192
v_mfma_f32_16x16x32_bf16 v[40:43], a[108:111], v[168:171], v[40:43]
v_mfma_f32_16x16x32_bf16 v[44:47], a[104:107], v[164:167], v[44:47]
ds_read_b64_tr_b16 a[100:101], v232 offset:14336
ds_read_b64_tr_b16 a[102:103], v232 offset:20480
v_mfma_f32_16x16x32_bf16 v[44:47], a[108:111], v[172:175], v[44:47]
s_waitcnt lgkmcnt(4)
v_mfma_f32_16x16x32_bf16 v[48:51], a[112:115], v[160:163], v[48:51]
ds_read_b64_tr_b16 a[104:105], v233 offset:2048
ds_read_b64_tr_b16 a[106:107], v233 offset:8192
v_mfma_f32_16x16x32_bf16 v[48:51], a[116:119], v[168:171], v[48:51]
v_mfma_f32_16x16x32_bf16 v[52:55], a[112:115], v[164:167], v[52:55]
ds_read_b64_tr_b16 a[108:109], v233 offset:14336
)"
R"(ds_read_b64_tr_b16 a[110:111], v233 offset:20480
v_mfma_f32_16x16x32_bf16 v[52:55], a[116:119], v[172:175], v[52:55]
v_mfma_f32_16x16x32_bf16 v[56:59], a[120:123], v[160:163], v[56:59]
ds_read_b64_tr_b16 a[112:113], v234 offset:2048
ds_read_b64_tr_b16 a[114:115], v234 offset:8192
v_mfma_f32_16x16x32_bf16 v[56:59], a[124:127], v[168:171], v[56:59]
v_mfma_f32_16x16x32_bf16 v[60:63], a[120:123], v[164:167], v[60:63]
s_add_u32 s75, s66, 0
ds_read_b64_tr_b16 a[116:117], v234 offset:14336
ds_read_b64_tr_b16 a[118:119], v234 offset:20480
v_mfma_f32_16x16x32_bf16 v[60:63], a[124:127], v[172:175], v[60:63]
s_waitcnt lgkmcnt(4)
s_waitcnt vmcnt(9)
s_barrier
v_mfma_f32_16x16x32_bf16 v[64:67], a[96:99], v[160:163], v[64:67]
s_add_u32 m0, s60, 0
ds_read_b64_tr_b16 a[120:121], v235 offset:2048
ds_read_b64_tr_b16 a[122:123], v235 offset:8192
v_mfma_f32_16x16x32_bf16 v[64:67], a[100:103], v[168:171], v[64:67]
buffer_load_dwordx4 %[vWo], %[srdWo], s75 offen offset:0 lds
v_mfma_f32_16x16x32_bf16 v[68:71], a[96:99], v[164:167], v[68:71]
ds_read_b64_tr_b16 a[124:125], v235 offset:14336
ds_read_b64_tr_b16 a[126:127], v235 offset:20480
v_mfma_f32_16x16x32_bf16 v[68:71], a[100:103], v[172:175], v[68:71]
)"
R"(v_add_u32 v232, s49, %[pWo]
v_xor_b32 v233, 32, v232
v_mfma_f32_16x16x32_bf16 v[72:75], a[104:107], v[160:163], v[72:75]
ds_read_b64_tr_b16 a[96:97], v232 offset:0
ds_read_b64_tr_b16 a[98:99], v232 offset:2048
v_mfma_f32_16x16x32_bf16 v[72:75], a[108:111], v[168:171], v[72:75]
v_xor_b32 v234, 64, v232
v_xor_b32 v235, 96, v232
v_mfma_f32_16x16x32_bf16 v[76:79], a[104:107], v[164:167], v[76:79]
s_add_u32 m0, s60, 1024
ds_read_b64_tr_b16 a[100:101], v233 offset:0
ds_read_b64_tr_b16 a[102:103], v233 offset:2048
v_mfma_f32_16x16x32_bf16 v[76:79], a[108:111], v[172:175], v[76:79]
s_waitcnt lgkmcnt(4)
buffer_load_dwordx4 %[vX1], %[srdWo], s75 offen offset:0 lds
v_mfma_f32_16x16x32_bf16 v[80:83], a[112:115], v[160:163], v[80:83]
ds_read_b64_tr_b16 a[104:105], v234 offset:0
ds_read_b64_tr_b16 a[106:107], v234 offset:2048
v_mfma_f32_16x16x32_bf16 v[80:83], a[116:119], v[168:171], v[80:83]
v_mfma_f32_16x16x32_bf16 v[84:87], a[112:115], v[164:167], v[84:87]
s_add_u32 m0, s60, 2048
ds_read_b64_tr_b16 a[108:109], v235 offset:0
ds_read_b64_tr_b16 a[110:111], v235 offset:2048
v_mfma_f32_16x16x32_bf16 v[84:87], a[116:119], v[172:175], v[84:87]
)"
R"(buffer_load_dwordx4 %[vW], %[srdWo], s75 offen offset:0 lds
v_mfma_f32_16x16x32_bf16 v[88:91], a[120:123], v[160:163], v[88:91]
ds_read_b64_tr_b16 a[112:113], v232 offset:4096
ds_read_b64_tr_b16 a[114:115], v232 offset:6144
v_mfma_f32_16x16x32_bf16 v[88:91], a[124:127], v[168:171], v[88:91]
s_add_u32 s75, s66, 30720
v_mfma_f32_16x16x32_bf16 v[92:95], a[120:123], v[164:167], v[92:95]
s_add_u32 m0, s60, 3072
ds_read_b64_tr_b16 a[116:117], v233 offset:4096
ds_read_b64_tr_b16 a[118:119], v233 offset:6144
v_mfma_f32_16x16x32_bf16 v[92:95], a[124:127], v[172:175], v[92:95]
s_waitcnt lgkmcnt(4)
buffer_load_dwordx4 %[vWo], %[srdWo], s75 offen offset:0 lds
v_pk_mul_f32 v[0:1], v[0:1], v[238:239] op_sel_hi:[1,0]
v_pk_mul_f32 v[2:3], v[2:3], v[238:239] op_sel_hi:[1,0]
v_pk_mul_f32 v[4:5], v[4:5], v[240:241] op_sel_hi:[1,0]
v_pk_mul_f32 v[6:7], v[6:7], v[240:241] op_sel_hi:[1,0]
v_pk_mul_f32 v[8:9], v[8:9], v[238:239] op_sel_hi:[1,0]
v_pk_mul_f32 v[10:11], v[10:11], v[238:239] op_sel_hi:[1,0]
v_pk_mul_f32 v[12:13], v[12:13], v[240:241] op_sel_hi:[1,0]
v_pk_mul_f32 v[14:15], v[14:15], v[240:241] op_sel_hi:[1,0]
v_pk_mul_f32 v[16:17], v[16:17], v[238:239] op_sel_hi:[1,0]
v_pk_mul_f32 v[18:19], v[18:19], v[238:239] op_sel_hi:[1,0]
v_pk_mul_f32 v[20:21], v[20:21], v[240:241] op_sel_hi:[1,0]
)"
R"(v_pk_mul_f32 v[22:23], v[22:23], v[240:241] op_sel_hi:[1,0]
v_pk_mul_f32 v[24:25], v[24:25], v[238:239] op_sel_hi:[1,0]
v_pk_mul_f32 v[26:27], v[26:27], v[238:239] op_sel_hi:[1,0]
v_pk_mul_f32 v[28:29], v[28:29], v[240:241] op_sel_hi:[1,0]
v_pk_mul_f32 v[30:31], v[30:31], v[240:241] op_sel_hi:[1,0]
v_pk_mul_f32 v[32:33], v[32:33], v[238:239] op_sel_hi:[1,0]
v_pk_mul_f32 v[34:35], v[34:35], v[238:239] op_sel_hi:[1,0]
v_pk_mul_f32 v[36:37], v[36:37], v[240:241] op_sel_hi:[1,0]
v_pk_mul_f32 v[38:39], v[38:39], v[240:241] op_sel_hi:[1,0]
v_pk_mul_f32 v[40:41], v[40:41], v[238:239] op_sel_hi:[1,0]
v_pk_mul_f32 v[42:43], v[42:43], v[238:239] op_sel_hi:[1,0]
v_pk_mul_f32 v[44:45], v[44:45], v[240:241] op_sel_hi:[1,0]
v_pk_mul_f32 v[46:47], v[46:47], v[240:241] op_sel_hi:[1,0]
v_pk_mul_f32 v[48:49], v[48:49], v[238:239] op_sel_hi:[1,0]
v_pk_mul_f32 v[50:51], v[50:51], v[238:239] op_sel_hi:[1,0]
v_pk_mul_f32 v[52:53], v[52:53], v[240:241] op_sel_hi:[1,0]
v_pk_mul_f32 v[54:55], v[54:55], v[240:241] op_sel_hi:[1,0]
v_pk_mul_f32 v[56:57], v[56:57], v[238:239] op_sel_hi:[1,0]
v_pk_mul_f32 v[58:59], v[58:59], v[238:239] op_sel_hi:[1,0]
v_pk_mul_f32 v[60:61], v[60:61], v[240:241] op_sel_hi:[1,0]
v_pk_mul_f32 v[62:63], v[62:63], v[240:241] op_sel_hi:[1,0]
v_pk_mul_f32 v[64:65], v[64:65], v[238:239] op_sel_hi:[1,0]
v_pk_mul_f32 v[66:67], v[66:67], v[238:239] op_sel_hi:[1,0]
v_pk_mul_f32 v[68:69], v[68:69], v[240:241] op_sel_hi:[1,0]
)"
R"(v_pk_mul_f32 v[70:71], v[70:71], v[240:241] op_sel_hi:[1,0]
v_pk_mul_f32 v[72:73], v[72:73], v[238:239] op_sel_hi:[1,0]
v_pk_mul_f32 v[74:75], v[74:75], v[238:239] op_sel_hi:[1,0]
v_pk_mul_f32 v[76:77], v[76:77], v[240:241] op_sel_hi:[1,0]
v_pk_mul_f32 v[78:79], v[78:79], v[240:241] op_sel_hi:[1,0]
v_pk_mul_f32 v[80:81], v[80:81], v[238:239] op_sel_hi:[1,0]
v_pk_mul_f32 v[82:83], v[82:83], v[238:239] op_sel_hi:[1,0]
v_pk_mul_f32 v[84:85], v[84:85], v[240:241] op_sel_hi:[1,0]
v_pk_mul_f32 v[86:87], v[86:87], v[240:241] op_sel_hi:[1,0]
v_pk_mul_f32 v[88:89], v[88:89], v[238:239] op_sel_hi:[1,0]
v_pk_mul_f32 v[90:91], v[90:91], v[238:239] op_sel_hi:[1,0]
v_pk_mul_f32 v[92:93], v[92:93], v[240:241] op_sel_hi:[1,0]
v_pk_mul_f32 v[94:95], v[94:95], v[240:241] op_sel_hi:[1,0]
v_cvt_pk_bf16_f32 v128, v0, v1
v_cvt_pk_bf16_f32 v129, v2, v3
v_cvt_pk_bf16_f32 v132, v4, v5
v_cvt_pk_bf16_f32 v133, v6, v7
v_cvt_pk_bf16_f32 v130, v8, v9
v_cvt_pk_bf16_f32 v131, v10, v11
v_cvt_pk_bf16_f32 v134, v12, v13
v_cvt_pk_bf16_f32 v135, v14, v15
v_mfma_f32_16x16x32_bf16 a[192:195], a[96:99], v[128:131], a[192:195]
s_mov_b32 s87, 262144
ds_read_b64_tr_b16 a[120:121], v234 offset:4096
)"
R"(ds_read_b64_tr_b16 a[122:123], v234 offset:6144
v_cvt_pk_bf16_f32 v136, v16, v17
v_mfma_f32_16x16x32_bf16 a[196:199], a[96:99], v[132:135], a[196:199]
s_lshl_b32 s70, %[hgi], 10
v_cvt_pk_bf16_f32 v137, v18, v19
v_cvt_pk_bf16_f32 v140, v20, v21
v_cvt_pk_bf16_f32 v141, v22, v23
v_mfma_f32_16x16x32_bf16 a[200:203], a[100:103], v[128:131], a[200:203]
s_add_u32 m0, s60, 4096
ds_read_b64_tr_b16 a[124:125], v235 offset:4096
ds_read_b64_tr_b16 a[126:127], v235 offset:6144
v_cvt_pk_bf16_f32 v138, v24, v25
v_mfma_f32_16x16x32_bf16 a[204:207], a[100:103], v[132:135], a[204:207]
s_add_u32 s47, %[hgi], 8
buffer_load_dwordx4 %[vX1], %[srdWo], s75 offen offset:0 lds
v_cvt_pk_bf16_f32 v139, v26, v27
v_bfe_u32 v227, %[vP], 4, 4
v_mfma_f32_16x16x32_bf16 a[208:211], a[104:107], v[128:131], a[208:211]
ds_read_b64_tr_b16 a[96:97], v232 offset:8192
ds_read_b64_tr_b16 a[98:99], v232 offset:10240
v_lshlrev_b32 v238, 2, v227
v_mfma_f32_16x16x32_bf16 a[212:215], a[104:107], v[132:135], a[212:215]
v_cvt_pk_bf16_f32 v142, v28, v29
v_cvt_pk_bf16_f32 v143, v30, v31
)"
R"(v_add3_u32 v230, v227, %[hgi], 1
v_mfma_f32_16x16x32_bf16 a[216:219], a[108:111], v[128:131], a[216:219]
s_add_u32 m0, s60, 5120
ds_read_b64_tr_b16 a[100:101], v233 offset:8192
ds_read_b64_tr_b16 a[102:103], v233 offset:10240
v_cmp_gt_u32 vcc, 8, v227
v_mfma_f32_16x16x32_bf16 a[220:223], a[108:111], v[132:135], a[220:223]
s_waitcnt lgkmcnt(4)
buffer_load_dwordx4 %[vW], %[srdWo], s75 offen offset:0 lds
v_cndmask_b32 v230, v227, v230, vcc
v_mfma_f32_16x16x32_bf16 a[192:195], a[112:115], v[136:139], a[192:195]
ds_read_b64_tr_b16 a[104:105], v234 offset:8192
ds_read_b64_tr_b16 a[106:107], v234 offset:10240
v_cvt_pk_bf16_f32 v144, v32, v33
v_mov_b32 v231, s47
v_mfma_f32_16x16x32_bf16 a[196:199], a[112:115], v[140:143], a[196:199]
v_cvt_pk_bf16_f32 v145, v34, v35
v_cvt_pk_bf16_f32 v148, v36, v37
v_cvt_pk_bf16_f32 v149, v38, v39
v_cndmask_b32 v231, v231, v227, vcc
v_mfma_f32_16x16x32_bf16 a[200:203], a[116:119], v[136:139], a[200:203]
ds_read_b64_tr_b16 a[108:109], v235 offset:8192
ds_read_b64_tr_b16 a[110:111], v235 offset:10240
v_and_b32 v230, 7, v230
)"
R"(v_mfma_f32_16x16x32_bf16 a[204:207], a[116:119], v[140:143], a[204:207]
v_cvt_pk_bf16_f32 v146, v40, v41
v_cvt_pk_bf16_f32 v147, v42, v43
v_lshlrev_b32 v230, 10, v230
v_mfma_f32_16x16x32_bf16 a[208:211], a[120:123], v[136:139], a[208:211]
ds_read_b64_tr_b16 a[112:113], v232 offset:12288
ds_read_b64_tr_b16 a[114:115], v232 offset:14336
v_lshl_add_u32 %[pW], v231, 2, v230
v_mfma_f32_16x16x32_bf16 a[212:215], a[120:123], v[140:143], a[212:215]
v_cvt_pk_bf16_f32 v150, v44, v45
v_cvt_pk_bf16_f32 v151, v46, v47
v_lshl_add_u32 v230, v231, 3, v230
v_mfma_f32_16x16x32_bf16 a[216:219], a[124:127], v[136:139], a[216:219]
ds_read_b64_tr_b16 a[116:117], v233 offset:12288
ds_read_b64_tr_b16 a[118:119], v233 offset:14336
v_lshlrev_b32 v231, 3, v227
v_mfma_f32_16x16x32_bf16 a[220:223], a[124:127], v[140:143], a[220:223]
s_waitcnt lgkmcnt(4)
buffer_load_dwordx2 v[190:191], v230, %[srdC], 0 offen offset:128 sc1
v_mfma_f32_16x16x32_bf16 a[192:195], a[96:99], v[144:147], a[192:195]
ds_read_b64_tr_b16 a[120:121], v234 offset:12288
ds_read_b64_tr_b16 a[122:123], v234 offset:14336
v_cvt_pk_bf16_f32 v152, v48, v49
v_mfma_f32_16x16x32_bf16 a[196:199], a[96:99], v[148:151], a[196:199]
)"
R"(v_cvt_pk_bf16_f32 v153, v50, v51
v_cvt_pk_bf16_f32 v156, v52, v53
v_cvt_pk_bf16_f32 v157, v54, v55
v_mfma_f32_16x16x32_bf16 a[200:203], a[100:103], v[144:147], a[200:203]
ds_read_b64_tr_b16 a[124:125], v235 offset:12288
ds_read_b64_tr_b16 a[126:127], v235 offset:14336
v_mfma_f32_16x16x32_bf16 a[204:207], a[100:103], v[148:151], a[204:207]
v_cvt_pk_bf16_f32 v154, v56, v57
v_cvt_pk_bf16_f32 v155, v58, v59
v_mfma_f32_16x16x32_bf16 a[208:211], a[104:107], v[144:147], a[208:211]
ds_read_b64_tr_b16 a[96:97], v232 offset:16384
ds_read_b64_tr_b16 a[98:99], v232 offset:18432
v_mfma_f32_16x16x32_bf16 a[212:215], a[104:107], v[148:151], a[212:215]
v_cvt_pk_bf16_f32 v158, v60, v61
v_cvt_pk_bf16_f32 v159, v62, v63
v_mfma_f32_16x16x32_bf16 a[216:219], a[108:111], v[144:147], a[216:219]
ds_read_b64_tr_b16 a[100:101], v233 offset:16384
ds_read_b64_tr_b16 a[102:103], v233 offset:18432
v_mfma_f32_16x16x32_bf16 a[220:223], a[108:111], v[148:151], a[220:223]
s_waitcnt lgkmcnt(4)
buffer_load_dwordx2 v[224:225], v231, %[srdC], s70 offen offset:128 sc1
v_mov_b32 v231, 0x80000000
v_mfma_f32_16x16x32_bf16 a[192:195], a[112:115], v[152:155], a[192:195]
ds_read_b64_tr_b16 a[104:105], v234 offset:16384
)"
R"(ds_read_b64_tr_b16 a[106:107], v234 offset:18432
v_cvt_pk_bf16_f32 v160, v64, v65
v_cmp_eq_u32 vcc, 7, v227
v_mfma_f32_16x16x32_bf16 a[196:199], a[112:115], v[156:159], a[196:199]
v_cvt_pk_bf16_f32 v161, v66, v67
v_cvt_pk_bf16_f32 v164, v68, v69
v_cvt_pk_bf16_f32 v165, v70, v71
v_cndmask_b32 %[pW], %[pW], v231, vcc
v_mfma_f32_16x16x32_bf16 a[200:203], a[116:119], v[152:155], a[200:203]
ds_read_b64_tr_b16 a[108:109], v235 offset:16384
ds_read_b64_tr_b16 a[110:111], v235 offset:18432
v_mfma_f32_16x16x32_bf16 a[204:207], a[116:119], v[156:159], a[204:207]
v_cvt_pk_bf16_f32 v162, v72, v73
v_cvt_pk_bf16_f32 v163, v74, v75
v_mfma_f32_16x16x32_bf16 a[208:211], a[120:123], v[152:155], a[208:211]
ds_read_b64_tr_b16 a[112:113], v232 offset:20480
ds_read_b64_tr_b16 a[114:115], v232 offset:22528
v_mfma_f32_16x16x32_bf16 a[212:215], a[120:123], v[156:159], a[212:215]
v_cvt_pk_bf16_f32 v166, v76, v77
v_cvt_pk_bf16_f32 v167, v78, v79
v_mfma_f32_16x16x32_bf16 a[216:219], a[124:127], v[152:155], a[216:219]
s_add_u32 s75, s66, 245760
ds_read_b64_tr_b16 a[116:117], v233 offset:20480
ds_read_b64_tr_b16 a[118:119], v233 offset:22528
)"
R"(v_mfma_f32_16x16x32_bf16 a[220:223], a[124:127], v[156:159], a[220:223]
s_waitcnt lgkmcnt(4)
s_waitcnt vmcnt(11)
s_barrier
v_mfma_f32_16x16x32_bf16 a[192:195], a[96:99], v[160:163], a[192:195]
s_add_u32 m0, s62, 0
ds_read_b64_tr_b16 a[120:121], v234 offset:20480
ds_read_b64_tr_b16 a[122:123], v234 offset:22528
v_cvt_pk_bf16_f32 v168, v80, v81
v_cvt_pk_bf16_f32 v169, v82, v83
v_mfma_f32_16x16x32_bf16 a[196:199], a[96:99], v[164:167], a[196:199]
buffer_load_dwordx4 %[vWo], %[srdWo], s75 offen offset:0 lds
v_cvt_pk_bf16_f32 v172, v84, v85
v_cvt_pk_bf16_f32 v173, v86, v87
v_mfma_f32_16x16x32_bf16 a[200:203], a[100:103], v[160:163], a[200:203]
ds_read_b64_tr_b16 a[124:125], v235 offset:20480
ds_read_b64_tr_b16 a[126:127], v235 offset:22528
v_cvt_pk_bf16_f32 v170, v88, v89
v_mfma_f32_16x16x32_bf16 a[204:207], a[100:103], v[164:167], a[204:207]
v_add_u32 v232, s53, %[pWo]
v_xor_b32 v233, 32, v232
v_cvt_pk_bf16_f32 v171, v90, v91
v_mfma_f32_16x16x32_bf16 a[208:211], a[104:107], v[160:163], a[208:211]
ds_read_b64_tr_b16 a[96:97], v232 offset:0
)"
R"(ds_read_b64_tr_b16 a[98:99], v232 offset:2048
v_cvt_pk_bf16_f32 v174, v92, v93
v_mfma_f32_16x16x32_bf16 a[212:215], a[104:107], v[164:167], a[212:215]
v_xor_b32 v234, 64, v232
v_xor_b32 v235, 96, v232
v_cvt_pk_bf16_f32 v175, v94, v95
v_mfma_f32_16x16x32_bf16 a[216:219], a[108:111], v[160:163], a[216:219]
s_add_u32 m0, s62, 1024
ds_read_b64_tr_b16 a[100:101], v232 offset:4096
ds_read_b64_tr_b16 a[102:103], v232 offset:6144
v_mfma_f32_16x16x32_bf16 a[220:223], a[108:111], v[164:167], a[220:223]
s_waitcnt lgkmcnt(4)
buffer_load_dwordx4 %[vX1], %[srdWo], s75 offen offset:0 lds
v_mfma_f32_16x16x32_bf16 a[192:195], a[112:115], v[168:171], a[192:195]
ds_read_b64_tr_b16 a[104:105], v232 offset:8192
ds_read_b64_tr_b16 a[106:107], v232 offset:10240
v_mfma_f32_16x16x32_bf16 a[196:199], a[112:115], v[172:175], a[196:199]
v_mfma_f32_16x16x32_bf16 a[200:203], a[116:119], v[168:171], a[200:203]
s_add_u32 m0, s62, 2048
ds_read_b64_tr_b16 a[108:109], v232 offset:12288
ds_read_b64_tr_b16 a[110:111], v232 offset:14336
v_mfma_f32_16x16x32_bf16 a[204:207], a[116:119], v[172:175], a[204:207]
buffer_load_dwordx4 %[vW], %[srdWo], s75 offen offset:0 lds
v_lshrrev_b32 v240, 10, %[vP]
)"
R"(v_lshlrev_b32 v240, 5, v240
v_mfma_f32_16x16x32_bf16 a[208:211], a[120:123], v[168:171], a[208:211]
ds_read_b64_tr_b16 a[112:113], v232 offset:16384
ds_read_b64_tr_b16 a[114:115], v232 offset:18432
v_bfe_u32 v236, %[vP], 4, 4
v_add_u32 v240, v240, v236
v_mfma_f32_16x16x32_bf16 a[212:215], a[120:123], v[172:175], a[212:215]
s_add_u32 s75, s66, 276480
v_mul_u32_u24 v240, 2560, v240
v_bfe_u32 v236, %[vP], 8, 1
v_lshl_add_u32 v240, v236, 5, v240
v_bfe_u32 v236, %[vP], 9, 1
v_mfma_f32_16x16x32_bf16 a[216:219], a[124:127], v[168:171], a[216:219]
s_add_u32 m0, s62, 3072
ds_read_b64_tr_b16 a[116:117], v232 offset:20480
ds_read_b64_tr_b16 a[118:119], v232 offset:22528
v_lshl_add_u32 v240, v236, 4, v240
v_bfe_u32 v236, %[vP], 4, 1
v_mfma_f32_16x16x32_bf16 a[220:223], a[124:127], v[172:175], a[220:223]
s_waitcnt lgkmcnt(6)
buffer_load_dwordx4 %[vWo], %[srdWo], s75 offen offset:0 lds
v_lshlrev_b32 v236, 4, v236
v_mov_b32 v237, 0x3f80
v_mfma_f32_16x16x32_bf16 a[224:227], a[96:99], v[128:131], a[224:227]
)"
R"(ds_read_b64_tr_b16 a[120:121], v233 offset:0
ds_read_b64_tr_b16 a[122:123], v233 offset:2048
v_lshlrev_b32 v237, v236, v237
v_mfma_f32_16x16x32_bf16 a[224:227], a[100:103], v[136:139], a[224:227]
v_bfe_u32 v236, %[vP], 6, 2
v_bfe_u32 v184, %[vP], 8, 2
v_cmp_eq_u32 vcc, v236, v184
v_mfma_f32_16x16x32_bf16 a[224:227], a[104:107], v[144:147], a[224:227]
s_add_u32 m0, s62, 4096
ds_read_b64_tr_b16 a[124:125], v233 offset:4096
ds_read_b64_tr_b16 a[126:127], v233 offset:6144
v_cndmask_b32 v237, 0, v237, vcc
v_mfma_f32_16x16x32_bf16 a[228:231], a[96:99], v[132:135], a[228:231]
buffer_load_dwordx4 %[vX1], %[srdWo], s75 offen offset:0 lds
v_bfe_u32 v236, %[vP], 5, 1
v_mfma_f32_16x16x32_bf16 a[228:231], a[100:103], v[140:143], a[228:231]
ds_read_b64_tr_b16 a[96:97], v233 offset:8192
ds_read_b64_tr_b16 a[98:99], v233 offset:10240
v_cmp_eq_u32 vcc, 0, v236
v_mfma_f32_16x16x32_bf16 a[228:231], a[104:107], v[148:151], a[228:231]
s_waitcnt lgkmcnt(6)
v_cndmask_b32 v186, 0, v237, vcc
v_cndmask_b32 v187, v237, 0, vcc
v_mov_b32 v184, 0
)"
R"(v_mfma_f32_16x16x32_bf16 a[224:227], a[108:111], v[152:155], a[224:227]
s_add_u32 m0, s62, 5120
ds_read_b64_tr_b16 a[100:101], v233 offset:12288
ds_read_b64_tr_b16 a[102:103], v233 offset:14336
v_mov_b32 v185, 0
v_mfma_f32_16x16x32_bf16 a[224:227], a[112:115], v[160:163], a[224:227]
s_lshl_b32 s85, %[hgi], 12
buffer_load_dwordx4 %[vW], %[srdWo], s75 offen offset:0 lds
v_accvgpr_read_b32 v176, a192
v_accvgpr_read_b32 v177, a193
v_mov_b32 v188, 0
v_mfma_f32_16x16x32_bf16 a[224:227], a[116:119], v[168:171], a[224:227]
s_mov_b32 s73, 0
ds_read_b64_tr_b16 a[104:105], v233 offset:16384
ds_read_b64_tr_b16 a[106:107], v233 offset:18432
v_accvgpr_read_b32 v178, a194
v_accvgpr_read_b32 v179, a195
v_mov_b32 v189, 0
v_mfma_f32_16x16x32_bf16 a[228:231], a[108:111], v[156:159], a[228:231]
s_mov_b32 s72, 0
v_cvt_pk_bf16_f32 v96, v176, v177
v_cvt_pk_bf16_f32 v97, v178, v179
v_accvgpr_read_b32 v180, a200
v_mfma_f32_16x16x32_bf16 a[228:231], a[112:115], v[164:167], a[228:231]
)"
R"(s_mov_b32 s67, 0x80000000
ds_read_b64_tr_b16 a[108:109], v233 offset:20480
ds_read_b64_tr_b16 a[110:111], v233 offset:22528
v_accvgpr_read_b32 v181, a201
v_mfma_f32_16x16x32_bf16 a[228:231], a[116:119], v[172:175], a[228:231]
s_waitcnt lgkmcnt(6)
v_accvgpr_read_b32 v182, a202
v_accvgpr_read_b32 v183, a203
v_cvt_pk_bf16_f32 v98, v180, v181
v_mfma_f32_16x16x32_bf16 a[232:235], a[120:123], v[128:131], a[232:235]
s_add_u32 s79, s85, 917504
ds_read_b64_tr_b16 a[112:113], v234 offset:0
ds_read_b64_tr_b16 a[114:115], v234 offset:2048
v_cvt_pk_bf16_f32 v99, v182, v183
v_mfma_f32_16x16x32_bf16 a[232:235], a[124:127], v[136:139], a[232:235]
s_mov_b32 s68, 7
buffer_store_dwordx4 v[96:99], %[vP], %[srdG], s79 offen offset:0 sc1
v_mfma_f32_16x16x32_bf16 a[232:235], a[96:99], v[144:147], a[232:235]
s_mov_b32 s69, 0x80000000
ds_read_b64_tr_b16 a[116:117], v234 offset:4096
ds_read_b64_tr_b16 a[118:119], v234 offset:6144
v_accvgpr_read_b32 v176, a196
v_mfma_f32_16x16x32_bf16 a[236:239], a[120:123], v[132:135], a[236:239]
s_mov_b32 s71, 0
)"
R"(v_accvgpr_read_b32 v177, a197
v_accvgpr_read_b32 v178, a198
v_accvgpr_read_b32 v179, a199
v_bfe_u32 v236, %[vP], 4, 6
v_add_u32 v237, %[hgi], v236
v_mfma_f32_16x16x32_bf16 a[236:239], a[124:127], v[140:143], a[236:239]
ds_read_b64_tr_b16 a[120:121], v234 offset:8192
ds_read_b64_tr_b16 a[122:123], v234 offset:10240
v_cvt_pk_bf16_f32 v100, v176, v177
v_and_b32 v237, 7, v237
v_mul_u32_u24 v226, 114688, v237
v_mfma_f32_16x16x32_bf16 a[236:239], a[96:99], v[148:151], a[236:239]
s_waitcnt lgkmcnt(6)
v_cvt_pk_bf16_f32 v101, v178, v179
v_accvgpr_read_b32 v180, a204
v_accvgpr_read_b32 v181, a205
v_lshl_add_u32 v226, v236, 14, v226
v_subrev_u32 v226, 16384, v226
v_mfma_f32_16x16x32_bf16 a[232:235], a[100:103], v[152:155], a[232:235]
ds_read_b64_tr_b16 a[124:125], v234 offset:12288
ds_read_b64_tr_b16 a[126:127], v234 offset:14336
v_accvgpr_read_b32 v182, a206
v_lshlrev_b32 v228, 14, v236
v_add_u32 v228, s45, v228
)"
R"(v_mfma_f32_16x16x32_bf16 a[232:235], a[104:107], v[160:163], a[232:235]
s_add_u32 s79, s85, 950272
v_accvgpr_read_b32 v183, a207
v_cvt_pk_bf16_f32 v102, v180, v181
v_cvt_pk_bf16_f32 v103, v182, v183
v_subrev_u32 v228, 49152, v228
v_add_u32 v237, 2, v237
v_mfma_f32_16x16x32_bf16 a[232:235], a[108:111], v[168:171], a[232:235]
ds_read_b64_tr_b16 a[96:97], v234 offset:16384
ds_read_b64_tr_b16 a[98:99], v234 offset:18432
v_and_b32 v237, 7, v237
v_lshlrev_b32 v237, 7, v237
v_mfma_f32_16x16x32_bf16 a[236:239], a[100:103], v[156:159], a[236:239]
buffer_store_dwordx4 v[100:103], %[vP], %[srdG], s79 offen offset:0 sc1
v_add_u32 v229, s78, v237
v_lshlrev_b32 v231, v236, 1
v_mfma_f32_16x16x32_bf16 a[236:239], a[104:107], v[164:167], a[236:239]
ds_read_b64_tr_b16 a[100:101], v234 offset:20480
ds_read_b64_tr_b16 a[102:103], v234 offset:22528
v_accvgpr_read_b32 v176, a208
v_lshrrev_b32 v231, 3, v231
v_mov_b32 v237, 0x80000000
v_mfma_f32_16x16x32_bf16 a[236:239], a[108:111], v[172:175], a[236:239]
s_waitcnt lgkmcnt(6)
)"
R"(v_accvgpr_read_b32 v177, a209
v_accvgpr_read_b32 v178, a210
v_accvgpr_read_b32 v179, a211
v_cmp_gt_u32 vcc, 3, v236
s_nop 1
v_cndmask_b32 v228, v228, v237, vcc
v_mfma_f32_16x16x32_bf16 a[240:243], a[112:115], v[128:131], a[240:243]
ds_read_b64_tr_b16 a[104:105], v235 offset:0
ds_read_b64_tr_b16 a[106:107], v235 offset:2048
v_cvt_pk_bf16_f32 v104, v176, v177
v_mov_b32 v64, 0
v_mov_b32 v65, 0
v_mfma_f32_16x16x32_bf16 a[240:243], a[116:119], v[136:139], a[240:243]
v_cvt_pk_bf16_f32 v105, v178, v179
v_accvgpr_read_b32 v180, a216
v_accvgpr_read_b32 v181, a217
v_mov_b32 v66, 0
v_mov_b32 v67, 0
v_mfma_f32_16x16x32_bf16 a[240:243], a[120:123], v[144:147], a[240:243]
ds_read_b64_tr_b16 a[108:109], v235 offset:4096
ds_read_b64_tr_b16 a[110:111], v235 offset:6144
v_accvgpr_read_b32 v182, a218
v_mov_b32 v68, 0
v_mov_b32 v69, 0
)"
R"(v_mfma_f32_16x16x32_bf16 a[244:247], a[112:115], v[132:135], a[244:247]
s_add_u32 s79, s85, 983040
v_accvgpr_read_b32 v183, a219
v_cvt_pk_bf16_f32 v106, v180, v181
v_cvt_pk_bf16_f32 v107, v182, v183
v_mov_b32 v70, 0
v_mov_b32 v71, 0
v_mfma_f32_16x16x32_bf16 a[244:247], a[116:119], v[140:143], a[244:247]
ds_read_b64_tr_b16 a[112:113], v235 offset:8192
ds_read_b64_tr_b16 a[114:115], v235 offset:10240
v_mov_b32 v72, 0
v_mov_b32 v73, 0
v_mfma_f32_16x16x32_bf16 a[244:247], a[120:123], v[148:151], a[244:247]
s_waitcnt lgkmcnt(6)
buffer_store_dwordx4 v[104:107], %[vP], %[srdG], s79 offen offset:0 sc1
v_mov_b32 v74, 0
v_mov_b32 v75, 0
v_mfma_f32_16x16x32_bf16 a[240:243], a[124:127], v[152:155], a[240:243]
s_add_u32 s75, s66, 491520
ds_read_b64_tr_b16 a[116:117], v235 offset:12288
ds_read_b64_tr_b16 a[118:119], v235 offset:14336
v_accvgpr_read_b32 v176, a212
v_mov_b32 v76, 0
v_mov_b32 v77, 0
)"
R"(v_mfma_f32_16x16x32_bf16 a[240:243], a[96:99], v[160:163], a[240:243]
s_waitcnt vmcnt(9)
s_barrier
v_accvgpr_read_b32 v177, a213
v_mov_b32 v78, 0
v_mov_b32 v79, 0
v_mfma_f32_16x16x32_bf16 a[240:243], a[100:103], v[168:171], a[240:243]
s_add_u32 m0, s63, 0
ds_read_b64_tr_b16 a[120:121], v235 offset:16384
ds_read_b64_tr_b16 a[122:123], v235 offset:18432
v_accvgpr_read_b32 v178, a214
v_mov_b32 v80, 0
v_mfma_f32_16x16x32_bf16 a[244:247], a[124:127], v[156:159], a[244:247]
buffer_load_dwordx4 %[vWo], %[srdWo], s75 offen offset:0 lds
v_accvgpr_read_b32 v179, a215
v_mov_b32 v81, 0
v_mfma_f32_16x16x32_bf16 a[244:247], a[96:99], v[164:167], a[244:247]
ds_read_b64_tr_b16 a[124:125], v235 offset:20480
ds_read_b64_tr_b16 a[126:127], v235 offset:22528
v_cvt_pk_bf16_f32 v108, v176, v177
v_mov_b32 v82, 0
v_mfma_f32_16x16x32_bf16 a[244:247], a[100:103], v[172:175], a[244:247]
s_waitcnt lgkmcnt(6)
v_add_u32 v232, s48, %[pWo]
)"
R"(v_xor_b32 v233, 32, v232
v_cvt_pk_bf16_f32 v109, v178, v179
v_mov_b32 v83, 0
v_mfma_f32_16x16x32_bf16 a[248:251], a[104:107], v[128:131], a[248:251]
ds_read_b64_tr_b16 a[96:97], v232 offset:0
ds_read_b64_tr_b16 a[98:99], v232 offset:2048
v_mov_b32 v84, 0
v_mfma_f32_16x16x32_bf16 a[248:251], a[108:111], v[136:139], a[248:251]
v_xor_b32 v234, 64, v232
v_xor_b32 v235, 96, v232
v_mov_b32 v85, 0
v_mfma_f32_16x16x32_bf16 a[248:251], a[112:115], v[144:147], a[248:251]
s_add_u32 m0, s63, 1024
ds_read_b64_tr_b16 a[100:101], v232 offset:4096
ds_read_b64_tr_b16 a[102:103], v232 offset:6144
v_mov_b32 v86, 0
v_mfma_f32_16x16x32_bf16 a[252:255], a[104:107], v[132:135], a[252:255]
buffer_load_dwordx4 %[vX1], %[srdWo], s75 offen offset:0 lds
v_mov_b32 v87, 0
v_mfma_f32_16x16x32_bf16 a[252:255], a[108:111], v[140:143], a[252:255]
ds_read_b64_tr_b16 a[104:105], v232 offset:8192
ds_read_b64_tr_b16 a[106:107], v232 offset:10240
v_mov_b32 v88, 0
v_mfma_f32_16x16x32_bf16 a[252:255], a[112:115], v[148:151], a[252:255]
)"
R"(s_waitcnt lgkmcnt(6)
v_accvgpr_read_b32 v180, a220
v_accvgpr_read_b32 v181, a221
v_mov_b32 v89, 0
v_mfma_f32_16x16x32_bf16 a[248:251], a[116:119], v[152:155], a[248:251]
ds_read_b64_tr_b16 a[108:109], v232 offset:12288
ds_read_b64_tr_b16 a[110:111], v232 offset:14336
v_mov_b32 v90, 0
v_mfma_f32_16x16x32_bf16 a[248:251], a[120:123], v[160:163], a[248:251]
v_accvgpr_read_b32 v182, a222
v_accvgpr_read_b32 v183, a223
v_mov_b32 v91, 0
v_mfma_f32_16x16x32_bf16 a[248:251], a[124:127], v[168:171], a[248:251]
ds_read_b64_tr_b16 a[112:113], v232 offset:16384
ds_read_b64_tr_b16 a[114:115], v232 offset:18432
v_mov_b32 v92, 0
v_mfma_f32_16x16x32_bf16 a[252:255], a[116:119], v[156:159], a[252:255]
s_add_u32 s79, s85, 1015808
v_cvt_pk_bf16_f32 v110, v180, v181
v_cvt_pk_bf16_f32 v111, v182, v183
v_mov_b32 v93, 0
v_mfma_f32_16x16x32_bf16 a[252:255], a[120:123], v[164:167], a[252:255]
ds_read_b64_tr_b16 a[116:117], v232 offset:20480
ds_read_b64_tr_b16 a[118:119], v232 offset:22528
)"
R"(v_mov_b32 v94, 0
v_mfma_f32_16x16x32_bf16 a[252:255], a[124:127], v[172:175], a[252:255]
buffer_store_dwordx4 v[108:111], %[vP], %[srdG], s79 offen offset:0 sc1
v_mov_b32 v95, 0
v_cmp_eq_u32 vcc, %[tag], v191
s_nop 1
s_cmp_eq_u64 vcc, -1
s_cbranch_scc0 SPINM_%=
SPBM_%=:
v_accvgpr_read_b32 v176, a224
v_accvgpr_read_b32 v177, a225
v_accvgpr_read_b32 v178, a226
v_accvgpr_read_b32 v179, a227
v_cvt_pk_bf16_f32 v96, v176, v177
v_cvt_pk_bf16_f32 v97, v178, v179
v_accvgpr_read_b32 v180, a232
v_accvgpr_read_b32 v181, a233
v_accvgpr_read_b32 v182, a234
v_accvgpr_read_b32 v183, a235
v_cvt_pk_bf16_f32 v98, v180, v181
v_cvt_pk_bf16_f32 v99, v182, v183
s_add_u32 s79, s85, 1048576
buffer_store_dwordx4 v[96:99], %[vP], %[srdG], s79 offen offset:0 sc1
v_accvgpr_read_b32 v176, a228
)"
R"(v_accvgpr_read_b32 v177, a229
v_accvgpr_read_b32 v178, a230
v_accvgpr_read_b32 v179, a231
v_cvt_pk_bf16_f32 v100, v176, v177
v_cvt_pk_bf16_f32 v101, v178, v179
v_accvgpr_read_b32 v180, a236
v_accvgpr_read_b32 v181, a237
v_accvgpr_read_b32 v182, a238
v_accvgpr_read_b32 v183, a239
v_cvt_pk_bf16_f32 v102, v180, v181
v_cvt_pk_bf16_f32 v103, v182, v183
s_add_u32 s79, s85, 1081344
buffer_store_dwordx4 v[100:103], %[vP], %[srdG], s79 offen offset:0 sc1
v_accvgpr_read_b32 v176, a240
v_accvgpr_read_b32 v177, a241
v_accvgpr_read_b32 v178, a242
v_accvgpr_read_b32 v179, a243
v_cvt_pk_bf16_f32 v104, v176, v177
v_cvt_pk_bf16_f32 v105, v178, v179
v_accvgpr_read_b32 v180, a248
v_accvgpr_read_b32 v181, a249
v_accvgpr_read_b32 v182, a250
v_accvgpr_read_b32 v183, a251
v_cvt_pk_bf16_f32 v106, v180, v181
)"
R"(v_cvt_pk_bf16_f32 v107, v182, v183
s_add_u32 s79, s85, 1114112
buffer_store_dwordx4 v[104:107], %[vP], %[srdG], s79 offen offset:0 sc1
v_accvgpr_read_b32 v176, a244
v_accvgpr_read_b32 v177, a245
v_accvgpr_read_b32 v178, a246
v_accvgpr_read_b32 v179, a247
v_cvt_pk_bf16_f32 v108, v176, v177
v_cvt_pk_bf16_f32 v109, v178, v179
v_accvgpr_read_b32 v180, a252
v_accvgpr_read_b32 v181, a253
v_accvgpr_read_b32 v182, a254
v_accvgpr_read_b32 v183, a255
v_cvt_pk_bf16_f32 v110, v180, v181
v_cvt_pk_bf16_f32 v111, v182, v183
s_add_u32 s79, s85, 1146880
buffer_store_dwordx4 v[108:111], %[vP], %[srdG], s79 offen offset:0 sc1
OPL_%=:
s_waitcnt lgkmcnt(6)
v_mfma_f32_16x16x32_bf16 v[0:3], a[96:99], a[0:3], 0
ds_read_b64_tr_b16 a[120:121], v233 offset:0
ds_read_b64_tr_b16 a[122:123], v233 offset:2048
v_mfma_f32_16x16x32_bf16 v[0:3], a[100:103], a[8:11], v[0:3]
v_mfma_f32_16x16x32_bf16 v[0:3], a[104:107], a[16:19], v[0:3]
)"
R"(s_add_u32 m0, s63, 2048
ds_read_b64_tr_b16 a[124:125], v233 offset:4096
ds_read_b64_tr_b16 a[126:127], v233 offset:6144
v_mfma_f32_16x16x32_bf16 v[4:7], a[96:99], a[4:7], 0
buffer_load_dwordx4 %[vW], %[srdWo], s75 offen offset:0 lds
v_mfma_f32_16x16x32_bf16 v[4:7], a[100:103], a[12:15], v[4:7]
s_add_u32 s75, s66, 522240
ds_read_b64_tr_b16 a[96:97], v233 offset:8192
ds_read_b64_tr_b16 a[98:99], v233 offset:10240
v_mfma_f32_16x16x32_bf16 v[4:7], a[104:107], a[20:23], v[4:7]
s_waitcnt lgkmcnt(6)
v_mfma_f32_16x16x32_bf16 v[0:3], a[108:111], a[24:27], v[0:3]
s_add_u32 m0, s63, 3072
ds_read_b64_tr_b16 a[100:101], v233 offset:12288
ds_read_b64_tr_b16 a[102:103], v233 offset:14336
v_mfma_f32_16x16x32_bf16 v[0:3], a[112:115], a[32:35], v[0:3]
buffer_load_dwordx4 %[vWo], %[srdWo], s75 offen offset:0 lds
v_mfma_f32_16x16x32_bf16 v[0:3], a[116:119], a[40:43], v[0:3]
ds_read_b64_tr_b16 a[104:105], v233 offset:16384
ds_read_b64_tr_b16 a[106:107], v233 offset:18432
v_mfma_f32_16x16x32_bf16 v[4:7], a[108:111], a[28:31], v[4:7]
v_cvt_pk_bf16_f32 v96, v32, v33
v_cvt_pk_bf16_f32 v97, v34, v35
v_mfma_f32_16x16x32_bf16 v[4:7], a[112:115], a[36:39], v[4:7]
)"
R"(s_add_u32 m0, s63, 4096
ds_read_b64_tr_b16 a[108:109], v233 offset:20480
ds_read_b64_tr_b16 a[110:111], v233 offset:22528
v_mfma_f32_16x16x32_bf16 v[4:7], a[116:119], a[44:47], v[4:7]
s_waitcnt lgkmcnt(6)
buffer_load_dwordx4 %[vX1], %[srdWo], s75 offen offset:0 lds
v_mfma_f32_16x16x32_bf16 v[8:11], a[120:123], a[0:3], 0
ds_read_b64_tr_b16 a[112:113], v234 offset:0
ds_read_b64_tr_b16 a[114:115], v234 offset:2048
v_mfma_f32_16x16x32_bf16 v[8:11], a[124:127], a[8:11], v[8:11]
s_add_u32 s79, s67, 0
v_cvt_pk_bf16_f32 v98, v40, v41
v_cvt_pk_bf16_f32 v99, v42, v43
v_mfma_f32_16x16x32_bf16 v[8:11], a[96:99], a[16:19], v[8:11]
s_add_u32 m0, s63, 5120
ds_read_b64_tr_b16 a[116:117], v234 offset:4096
ds_read_b64_tr_b16 a[118:119], v234 offset:6144
v_mfma_f32_16x16x32_bf16 v[12:15], a[120:123], a[4:7], 0
buffer_load_dwordx4 %[vW], %[srdWo], s75 offen offset:0 lds
v_mfma_f32_16x16x32_bf16 v[12:15], a[124:127], a[12:15], v[12:15]
ds_read_b64_tr_b16 a[120:121], v234 offset:8192
ds_read_b64_tr_b16 a[122:123], v234 offset:10240
v_mfma_f32_16x16x32_bf16 v[12:15], a[96:99], a[20:23], v[12:15]
s_waitcnt lgkmcnt(6)
)"
R"(buffer_store_dwordx4 v[96:99], %[vP], %[srdG], s79 offen offset:0 sc1
v_mfma_f32_16x16x32_bf16 v[8:11], a[100:103], a[24:27], v[8:11]
ds_read_b64_tr_b16 a[124:125], v234 offset:12288
ds_read_b64_tr_b16 a[126:127], v234 offset:14336
v_mfma_f32_16x16x32_bf16 v[8:11], a[104:107], a[32:35], v[8:11]
v_cvt_pk_bf16_f32 v100, v36, v37
v_cvt_pk_bf16_f32 v101, v38, v39
v_mfma_f32_16x16x32_bf16 v[8:11], a[108:111], a[40:43], v[8:11]
ds_read_b64_tr_b16 a[96:97], v234 offset:16384
ds_read_b64_tr_b16 a[98:99], v234 offset:18432
v_mfma_f32_16x16x32_bf16 v[12:15], a[100:103], a[28:31], v[12:15]
s_add_u32 s79, s67, 4096
v_cvt_pk_bf16_f32 v102, v44, v45
v_cvt_pk_bf16_f32 v103, v46, v47
v_mfma_f32_16x16x32_bf16 v[12:15], a[104:107], a[36:39], v[12:15]
ds_read_b64_tr_b16 a[100:101], v234 offset:20480
ds_read_b64_tr_b16 a[102:103], v234 offset:22528
v_mfma_f32_16x16x32_bf16 v[12:15], a[108:111], a[44:47], v[12:15]
s_waitcnt lgkmcnt(6)
buffer_store_dwordx4 v[100:103], %[vP], %[srdG], s79 offen offset:0 sc1
v_mfma_f32_16x16x32_bf16 v[16:19], a[112:115], a[0:3], 0
ds_read_b64_tr_b16 a[104:105], v235 offset:0
ds_read_b64_tr_b16 a[106:107], v235 offset:2048
v_mfma_f32_16x16x32_bf16 v[16:19], a[116:119], a[8:11], v[16:19]
)"
R"(v_cvt_pk_bf16_f32 v104, v48, v49
v_cvt_pk_bf16_f32 v105, v50, v51
v_mfma_f32_16x16x32_bf16 v[16:19], a[120:123], a[16:19], v[16:19]
ds_read_b64_tr_b16 a[108:109], v235 offset:4096
ds_read_b64_tr_b16 a[110:111], v235 offset:6144
v_mfma_f32_16x16x32_bf16 v[20:23], a[112:115], a[4:7], 0
s_add_u32 s79, s67, 8192
v_cvt_pk_bf16_f32 v106, v56, v57
v_cvt_pk_bf16_f32 v107, v58, v59
v_mfma_f32_16x16x32_bf16 v[20:23], a[116:119], a[12:15], v[20:23]
ds_read_b64_tr_b16 a[112:113], v235 offset:8192
ds_read_b64_tr_b16 a[114:115], v235 offset:10240
v_mfma_f32_16x16x32_bf16 v[20:23], a[120:123], a[20:23], v[20:23]
s_waitcnt lgkmcnt(6)
buffer_store_dwordx4 v[104:107], %[vP], %[srdG], s79 offen offset:0 sc1
v_mfma_f32_16x16x32_bf16 v[16:19], a[124:127], a[24:27], v[16:19]
s_add_u32 s75, s66, 737280
ds_read_b64_tr_b16 a[116:117], v235 offset:12288
ds_read_b64_tr_b16 a[118:119], v235 offset:14336
v_mfma_f32_16x16x32_bf16 v[16:19], a[96:99], a[32:35], v[16:19]
s_waitcnt vmcnt(8)
s_barrier
v_cmp_eq_u32 vcc, v239, v224
s_nop 1
)"
R"(s_and_b32 s47, vcc_lo, s71
s_cmp_eq_u32 s47, s71
s_cbranch_scc0 SPINL0_%=
SPBL0_%=:
v_mfma_f32_16x16x32_bf16 v[16:19], a[100:103], a[40:43], v[16:19]
s_add_u32 m0, s64, 0
ds_read_b64_tr_b16 a[120:121], v235 offset:16384
ds_read_b64_tr_b16 a[122:123], v235 offset:18432
v_mfma_f32_16x16x32_bf16 v[20:23], a[124:127], a[28:31], v[20:23]
s_add_u32 s76, s69, 0
buffer_load_dwordx4 %[vWo], %[srdWo], s75 offen offset:0 lds
v_mfma_f32_16x16x32_bf16 v[20:23], a[96:99], a[36:39], v[20:23]
ds_read_b64_tr_b16 a[124:125], v235 offset:20480
ds_read_b64_tr_b16 a[126:127], v235 offset:22528
v_mfma_f32_16x16x32_bf16 v[20:23], a[100:103], a[44:47], v[20:23]
s_waitcnt lgkmcnt(6)
v_add_u32 v232, s50, %[pWo]
v_xor_b32 v233, 32, v232
v_mfma_f32_16x16x32_bf16 v[24:27], a[104:107], a[0:3], 0
ds_read_b64_tr_b16 a[96:97], v232 offset:0
ds_read_b64_tr_b16 a[98:99], v232 offset:2048
v_mfma_f32_16x16x32_bf16 v[24:27], a[108:111], a[8:11], v[24:27]
v_xor_b32 v234, 64, v232
v_xor_b32 v235, 96, v232
)"
R"(v_mfma_f32_16x16x32_bf16 v[24:27], a[112:115], a[16:19], v[24:27]
s_add_u32 m0, s64, 1024
ds_read_b64_tr_b16 a[100:101], v232 offset:4096
ds_read_b64_tr_b16 a[102:103], v232 offset:6144
v_mfma_f32_16x16x32_bf16 v[28:31], a[104:107], a[4:7], 0
buffer_load_dwordx4 %[vX1], %[srdWo], s75 offen offset:0 lds
v_mfma_f32_16x16x32_bf16 v[28:31], a[108:111], a[12:15], v[28:31]
ds_read_b64_tr_b16 a[104:105], v232 offset:8192
ds_read_b64_tr_b16 a[106:107], v232 offset:10240
v_mfma_f32_16x16x32_bf16 v[28:31], a[112:115], a[20:23], v[28:31]
s_waitcnt lgkmcnt(6)
v_cvt_pk_bf16_f32 v108, v52, v53
v_cvt_pk_bf16_f32 v109, v54, v55
v_mfma_f32_16x16x32_bf16 v[24:27], a[116:119], a[24:27], v[24:27]
s_add_u32 m0, s64, 2048
ds_read_b64_tr_b16 a[108:109], v232 offset:12288
ds_read_b64_tr_b16 a[110:111], v232 offset:14336
v_mfma_f32_16x16x32_bf16 v[24:27], a[120:123], a[32:35], v[24:27]
buffer_load_dwordx4 %[vW], %[srdWo], s75 offen offset:0 lds
v_mfma_f32_16x16x32_bf16 v[24:27], a[124:127], a[40:43], v[24:27]
ds_read_b64_tr_b16 a[112:113], v232 offset:16384
ds_read_b64_tr_b16 a[114:115], v232 offset:18432
v_mfma_f32_16x16x32_bf16 v[28:31], a[116:119], a[28:31], v[28:31]
s_add_u32 s75, s66, 768000
)"
R"(v_cvt_pk_bf16_f32 v110, v60, v61
v_cvt_pk_bf16_f32 v111, v62, v63
v_mfma_f32_16x16x32_bf16 v[28:31], a[120:123], a[36:39], v[28:31]
s_add_u32 m0, s64, 3072
ds_read_b64_tr_b16 a[116:117], v232 offset:20480
ds_read_b64_tr_b16 a[118:119], v232 offset:22528
v_mfma_f32_16x16x32_bf16 v[28:31], a[124:127], a[44:47], v[28:31]
s_waitcnt lgkmcnt(6)
buffer_load_dwordx4 %[vWo], %[srdWo], s75 offen offset:0 lds
v_mfma_f32_16x16x32_bf16 v[0:3], a[96:99], a[48:51], v[0:3]
s_add_u32 s79, s67, 12288
ds_read_b64_tr_b16 a[120:121], v233 offset:0
ds_read_b64_tr_b16 a[122:123], v233 offset:2048
v_mfma_f32_16x16x32_bf16 v[0:3], a[100:103], a[56:59], v[0:3]
buffer_store_dwordx4 v[108:111], %[vP], %[srdG], s79 offen offset:0 sc1
v_mfma_f32_16x16x32_bf16 v[0:3], a[104:107], a[64:67], v[0:3]
s_add_u32 m0, s64, 4096
ds_read_b64_tr_b16 a[124:125], v233 offset:4096
ds_read_b64_tr_b16 a[126:127], v233 offset:6144
v_mfma_f32_16x16x32_bf16 v[4:7], a[96:99], a[52:55], v[4:7]
buffer_load_dwordx4 %[vX1], %[srdWo], s75 offen offset:0 lds
v_mfma_f32_16x16x32_bf16 v[4:7], a[100:103], a[60:63], v[4:7]
ds_read_b64_tr_b16 a[96:97], v233 offset:8192
ds_read_b64_tr_b16 a[98:99], v233 offset:10240
)"
R"(v_mfma_f32_16x16x32_bf16 v[4:7], a[104:107], a[68:71], v[4:7]
s_waitcnt lgkmcnt(6)
buffer_load_dwordx4 v[112:115], %[vP], %[srdG], s76 offen offset:0 sc1
v_mfma_f32_16x16x32_bf16 v[0:3], a[108:111], a[72:75], v[0:3]
s_add_u32 m0, s64, 5120
ds_read_b64_tr_b16 a[100:101], v233 offset:12288
ds_read_b64_tr_b16 a[102:103], v233 offset:14336
v_mfma_f32_16x16x32_bf16 v[0:3], a[112:115], a[80:83], v[0:3]
s_add_u32 s76, s69, 4096
buffer_load_dwordx4 %[vW], %[srdWo], s75 offen offset:0 lds
v_mfma_f32_16x16x32_bf16 v[0:3], a[116:119], a[88:91], v[0:3]
ds_read_b64_tr_b16 a[104:105], v233 offset:16384
ds_read_b64_tr_b16 a[106:107], v233 offset:18432
v_mfma_f32_16x16x32_bf16 v[4:7], a[108:111], a[76:79], v[4:7]
buffer_load_dwordx4 v[116:119], %[vP], %[srdG], s76 offen offset:0 sc1
v_mfma_f32_16x16x32_bf16 v[4:7], a[112:115], a[84:87], v[4:7]
s_add_u32 s76, s69, 8192
ds_read_b64_tr_b16 a[108:109], v233 offset:20480
ds_read_b64_tr_b16 a[110:111], v233 offset:22528
v_mfma_f32_16x16x32_bf16 v[4:7], a[116:119], a[92:95], v[4:7]
s_waitcnt lgkmcnt(6)
buffer_load_dwordx4 v[120:123], %[vP], %[srdG], s76 offen offset:0 sc1
v_mfma_f32_16x16x32_bf16 v[8:11], a[120:123], a[48:51], v[8:11]
s_add_u32 s76, s69, 12288
)"
R"(ds_read_b64_tr_b16 a[112:113], v234 offset:0
ds_read_b64_tr_b16 a[114:115], v234 offset:2048
v_mfma_f32_16x16x32_bf16 v[8:11], a[124:127], a[56:59], v[8:11]
buffer_load_dwordx4 v[124:127], %[vP], %[srdG], s76 offen offset:0 sc1
v_mfma_f32_16x16x32_bf16 v[8:11], a[96:99], a[64:67], v[8:11]
ds_read_b64_tr_b16 a[116:117], v234 offset:4096
ds_read_b64_tr_b16 a[118:119], v234 offset:6144
v_mfma_f32_16x16x32_bf16 v[12:15], a[120:123], a[52:55], v[12:15]
v_mfma_f32_16x16x32_bf16 v[12:15], a[124:127], a[60:63], v[12:15]
ds_read_b64_tr_b16 a[120:121], v234 offset:8192
ds_read_b64_tr_b16 a[122:123], v234 offset:10240
v_mfma_f32_16x16x32_bf16 v[12:15], a[96:99], a[68:71], v[12:15]
s_waitcnt lgkmcnt(6)
v_mfma_f32_16x16x32_bf16 v[8:11], a[100:103], a[72:75], v[8:11]
ds_read_b64_tr_b16 a[124:125], v234 offset:12288
ds_read_b64_tr_b16 a[126:127], v234 offset:14336
v_mfma_f32_16x16x32_bf16 v[8:11], a[104:107], a[80:83], v[8:11]
v_mfma_f32_16x16x32_bf16 v[8:11], a[108:111], a[88:91], v[8:11]
ds_read_b64_tr_b16 a[96:97], v234 offset:16384
ds_read_b64_tr_b16 a[98:99], v234 offset:18432
v_mfma_f32_16x16x32_bf16 v[12:15], a[100:103], a[76:79], v[12:15]
v_mfma_f32_16x16x32_bf16 v[12:15], a[104:107], a[84:87], v[12:15]
ds_read_b64_tr_b16 a[100:101], v234 offset:20480
ds_read_b64_tr_b16 a[102:103], v234 offset:22528
)"
R"(v_mfma_f32_16x16x32_bf16 v[12:15], a[108:111], a[92:95], v[12:15]
s_waitcnt lgkmcnt(6)
v_mfma_f32_16x16x32_bf16 v[16:19], a[112:115], a[48:51], v[16:19]
ds_read_b64_tr_b16 a[104:105], v235 offset:0
ds_read_b64_tr_b16 a[106:107], v235 offset:2048
v_mfma_f32_16x16x32_bf16 v[16:19], a[116:119], a[56:59], v[16:19]
v_mfma_f32_16x16x32_bf16 v[16:19], a[120:123], a[64:67], v[16:19]
ds_read_b64_tr_b16 a[108:109], v235 offset:4096
ds_read_b64_tr_b16 a[110:111], v235 offset:6144
v_mfma_f32_16x16x32_bf16 v[20:23], a[112:115], a[52:55], v[20:23]
v_mfma_f32_16x16x32_bf16 v[20:23], a[116:119], a[60:63], v[20:23]
ds_read_b64_tr_b16 a[112:113], v235 offset:8192
ds_read_b64_tr_b16 a[114:115], v235 offset:10240
v_mfma_f32_16x16x32_bf16 v[20:23], a[120:123], a[68:71], v[20:23]
s_waitcnt lgkmcnt(6)
v_readlane_b32 s66, v229, s73
s_nop 1
v_mfma_f32_16x16x32_bf16 v[16:19], a[124:127], a[72:75], v[16:19]
s_add_u32 s75, s66, 0
ds_read_b64_tr_b16 a[116:117], v235 offset:12288
ds_read_b64_tr_b16 a[118:119], v235 offset:14336
v_mfma_f32_16x16x32_bf16 v[16:19], a[96:99], a[80:83], v[16:19]
s_waitcnt vmcnt(14)
s_barrier
)"
R"(v_mfma_f32_16x16x32_bf16 v[16:19], a[100:103], a[88:91], v[16:19]
s_add_u32 m0, s60, 0
ds_read_b64_tr_b16 a[120:121], v235 offset:16384
ds_read_b64_tr_b16 a[122:123], v235 offset:18432
v_mfma_f32_16x16x32_bf16 v[20:23], a[124:127], a[76:79], v[20:23]
buffer_load_dwordx4 %[vWo], %[srdWo], s75 offen offset:0 lds
v_mfma_f32_16x16x32_bf16 v[20:23], a[96:99], a[84:87], v[20:23]
ds_read_b64_tr_b16 a[124:125], v235 offset:20480
ds_read_b64_tr_b16 a[126:127], v235 offset:22528
v_mfma_f32_16x16x32_bf16 v[20:23], a[100:103], a[92:95], v[20:23]
s_waitcnt lgkmcnt(6)
v_add_u32 v232, s51, %[pWo]
v_xor_b32 v233, 32, v232
v_mfma_f32_16x16x32_bf16 v[24:27], a[104:107], a[48:51], v[24:27]
ds_read_b64_tr_b16 a[96:97], v232 offset:0
ds_read_b64_tr_b16 a[98:99], v232 offset:2048
v_mfma_f32_16x16x32_bf16 v[24:27], a[108:111], a[56:59], v[24:27]
v_xor_b32 v234, 64, v232
v_xor_b32 v235, 96, v232
v_mfma_f32_16x16x32_bf16 v[24:27], a[112:115], a[64:67], v[24:27]
s_add_u32 m0, s60, 1024
ds_read_b64_tr_b16 a[100:101], v232 offset:4096
ds_read_b64_tr_b16 a[102:103], v232 offset:6144
v_mfma_f32_16x16x32_bf16 v[28:31], a[104:107], a[52:55], v[28:31]
)"
R"(buffer_load_dwordx4 %[vX1], %[srdWo], s75 offen offset:0 lds
v_mfma_f32_16x16x32_bf16 v[28:31], a[108:111], a[60:63], v[28:31]
ds_read_b64_tr_b16 a[104:105], v232 offset:8192
ds_read_b64_tr_b16 a[106:107], v232 offset:10240
v_mfma_f32_16x16x32_bf16 v[28:31], a[112:115], a[68:71], v[28:31]
s_waitcnt lgkmcnt(6)
v_mfma_f32_16x16x32_bf16 v[24:27], a[116:119], a[72:75], v[24:27]
s_add_u32 m0, s60, 2048
ds_read_b64_tr_b16 a[108:109], v232 offset:12288
ds_read_b64_tr_b16 a[110:111], v232 offset:14336
v_mfma_f32_16x16x32_bf16 v[24:27], a[120:123], a[80:83], v[24:27]
buffer_load_dwordx4 %[vW], %[srdWo], s75 offen offset:0 lds
v_mfma_f32_16x16x32_bf16 v[24:27], a[124:127], a[88:91], v[24:27]
ds_read_b64_tr_b16 a[112:113], v232 offset:16384
ds_read_b64_tr_b16 a[114:115], v232 offset:18432
v_mfma_f32_16x16x32_bf16 v[28:31], a[116:119], a[76:79], v[28:31]
s_add_u32 s75, s66, 30720
v_mfma_f32_16x16x32_bf16 v[28:31], a[120:123], a[84:87], v[28:31]
s_add_u32 m0, s60, 3072
ds_read_b64_tr_b16 a[116:117], v232 offset:20480
ds_read_b64_tr_b16 a[118:119], v232 offset:22528
v_mfma_f32_16x16x32_bf16 v[28:31], a[124:127], a[92:95], v[28:31]
s_waitcnt lgkmcnt(6)
buffer_load_dwordx4 %[vWo], %[srdWo], s75 offen offset:0 lds
)"
R"(v_mfma_f32_16x16x32_bf16 v[0:3], a[96:99], a[140:143], v[0:3]
ds_read_b64_tr_b16 a[120:121], v233 offset:0
ds_read_b64_tr_b16 a[122:123], v233 offset:2048
v_mfma_f32_16x16x32_bf16 v[0:3], a[100:103], a[148:151], v[0:3]
v_mfma_f32_16x16x32_bf16 v[0:3], a[104:107], a[156:159], v[0:3]
s_add_u32 m0, s60, 4096
ds_read_b64_tr_b16 a[124:125], v233 offset:4096
ds_read_b64_tr_b16 a[126:127], v233 offset:6144
v_mfma_f32_16x16x32_bf16 v[4:7], a[96:99], a[144:147], v[4:7]
buffer_load_dwordx4 %[vX1], %[srdWo], s75 offen offset:0 lds
v_mfma_f32_16x16x32_bf16 v[4:7], a[100:103], a[152:155], v[4:7]
s_add_u32 s73, s73, 1
ds_read_b64_tr_b16 a[96:97], v233 offset:8192
ds_read_b64_tr_b16 a[98:99], v233 offset:10240
v_mfma_f32_16x16x32_bf16 v[4:7], a[104:107], a[160:163], v[4:7]
s_waitcnt lgkmcnt(6)
v_readlane_b32 s67, v226, s73
v_readlane_b32 s69, v228, s73
v_mfma_f32_16x16x32_bf16 v[0:3], a[108:111], a[164:167], v[0:3]
s_add_u32 m0, s60, 5120
ds_read_b64_tr_b16 a[100:101], v233 offset:12288
ds_read_b64_tr_b16 a[102:103], v233 offset:14336
v_mfma_f32_16x16x32_bf16 v[0:3], a[112:115], a[172:175], v[0:3]
buffer_load_dwordx4 %[vW], %[srdWo], s75 offen offset:0 lds
)"
R"(v_mfma_f32_16x16x32_bf16 v[0:3], a[116:119], a[180:183], v[0:3]
ds_read_b64_tr_b16 a[104:105], v233 offset:16384
ds_read_b64_tr_b16 a[106:107], v233 offset:18432
v_mfma_f32_16x16x32_bf16 v[4:7], a[108:111], a[168:171], v[4:7]
v_readlane_b32 s71, v231, s73
v_mfma_f32_16x16x32_bf16 v[4:7], a[112:115], a[176:179], v[4:7]
ds_read_b64_tr_b16 a[108:109], v233 offset:20480
ds_read_b64_tr_b16 a[110:111], v233 offset:22528
v_mfma_f32_16x16x32_bf16 v[4:7], a[116:119], a[184:187], v[4:7]
s_waitcnt lgkmcnt(6)
v_mfma_f32_16x16x32_bf16 v[8:11], a[120:123], a[140:143], v[8:11]
ds_read_b64_tr_b16 a[112:113], v234 offset:0
ds_read_b64_tr_b16 a[114:115], v234 offset:2048
v_mfma_f32_16x16x32_bf16 v[8:11], a[124:127], a[148:151], v[8:11]
v_mfma_f32_16x16x32_bf16 v[8:11], a[96:99], a[156:159], v[8:11]
ds_read_b64_tr_b16 a[116:117], v234 offset:4096
ds_read_b64_tr_b16 a[118:119], v234 offset:6144
v_mfma_f32_16x16x32_bf16 v[12:15], a[120:123], a[144:147], v[12:15]
v_mfma_f32_16x16x32_bf16 v[12:15], a[124:127], a[152:155], v[12:15]
ds_read_b64_tr_b16 a[120:121], v234 offset:8192
ds_read_b64_tr_b16 a[122:123], v234 offset:10240
v_mfma_f32_16x16x32_bf16 v[12:15], a[96:99], a[160:163], v[12:15]
s_waitcnt lgkmcnt(6)
v_mfma_f32_16x16x32_bf16 v[8:11], a[100:103], a[164:167], v[8:11]
)"
R"(ds_read_b64_tr_b16 a[124:125], v234 offset:12288
ds_read_b64_tr_b16 a[126:127], v234 offset:14336
v_mfma_f32_16x16x32_bf16 v[8:11], a[104:107], a[172:175], v[8:11]
v_mfma_f32_16x16x32_bf16 v[8:11], a[108:111], a[180:183], v[8:11]
ds_read_b64_tr_b16 a[96:97], v234 offset:16384
ds_read_b64_tr_b16 a[98:99], v234 offset:18432
v_mfma_f32_16x16x32_bf16 v[12:15], a[100:103], a[168:171], v[12:15]
v_mfma_f32_16x16x32_bf16 v[12:15], a[104:107], a[176:179], v[12:15]
ds_read_b64_tr_b16 a[100:101], v234 offset:20480
ds_read_b64_tr_b16 a[102:103], v234 offset:22528
v_mfma_f32_16x16x32_bf16 v[12:15], a[108:111], a[184:187], v[12:15]
s_waitcnt lgkmcnt(6)
v_mfma_f32_16x16x32_bf16 v[16:19], a[112:115], a[140:143], v[16:19]
ds_read_b64_tr_b16 a[104:105], v235 offset:0
ds_read_b64_tr_b16 a[106:107], v235 offset:2048
v_mfma_f32_16x16x32_bf16 v[16:19], a[116:119], a[148:151], v[16:19]
v_mfma_f32_16x16x32_bf16 v[16:19], a[120:123], a[156:159], v[16:19]
ds_read_b64_tr_b16 a[108:109], v235 offset:4096
ds_read_b64_tr_b16 a[110:111], v235 offset:6144
v_mfma_f32_16x16x32_bf16 v[20:23], a[112:115], a[144:147], v[20:23]
v_mfma_f32_16x16x32_bf16 v[20:23], a[116:119], a[152:155], v[20:23]
ds_read_b64_tr_b16 a[112:113], v235 offset:8192
ds_read_b64_tr_b16 a[114:115], v235 offset:10240
v_mfma_f32_16x16x32_bf16 v[20:23], a[120:123], a[160:163], v[20:23]
)"
R"(s_waitcnt lgkmcnt(6)
v_mfma_f32_16x16x32_bf16 v[16:19], a[124:127], a[164:167], v[16:19]
s_add_u32 s75, s66, 245760
ds_read_b64_tr_b16 a[116:117], v235 offset:12288
ds_read_b64_tr_b16 a[118:119], v235 offset:14336
v_mfma_f32_16x16x32_bf16 v[16:19], a[96:99], a[172:175], v[16:19]
s_waitcnt vmcnt(9)
s_barrier
v_mfma_f32_16x16x32_bf16 v[16:19], a[100:103], a[180:183], v[16:19]
s_add_u32 m0, s62, 0
ds_read_b64_tr_b16 a[120:121], v235 offset:16384
ds_read_b64_tr_b16 a[122:123], v235 offset:18432
v_mfma_f32_16x16x32_bf16 v[20:23], a[124:127], a[168:171], v[20:23]
buffer_load_dwordx4 %[vWo], %[srdWo], s75 offen offset:0 lds
v_mfma_f32_16x16x32_bf16 v[20:23], a[96:99], a[176:179], v[20:23]
ds_read_b64_tr_b16 a[124:125], v235 offset:20480
ds_read_b64_tr_b16 a[126:127], v235 offset:22528
v_mfma_f32_16x16x32_bf16 v[20:23], a[100:103], a[184:187], v[20:23]
s_waitcnt lgkmcnt(6)
v_add_u32 v232, s52, %[pWo]
v_xor_b32 v233, 32, v232
v_mfma_f32_16x16x32_bf16 v[24:27], a[104:107], a[140:143], v[24:27]
ds_read_b64_tr_b16 a[96:97], v232 offset:0
ds_read_b64_tr_b16 a[98:99], v232 offset:2048
)"
R"(v_mfma_f32_16x16x32_bf16 v[24:27], a[108:111], a[148:151], v[24:27]
v_xor_b32 v234, 64, v232
v_xor_b32 v235, 96, v232
v_mfma_f32_16x16x32_bf16 v[24:27], a[112:115], a[156:159], v[24:27]
s_add_u32 m0, s62, 1024
ds_read_b64_tr_b16 a[100:101], v232 offset:4096
ds_read_b64_tr_b16 a[102:103], v232 offset:6144
v_mfma_f32_16x16x32_bf16 v[28:31], a[104:107], a[144:147], v[28:31]
buffer_load_dwordx4 %[vX1], %[srdWo], s75 offen offset:0 lds
v_mfma_f32_16x16x32_bf16 v[28:31], a[108:111], a[152:155], v[28:31]
ds_read_b64_tr_b16 a[104:105], v232 offset:8192
ds_read_b64_tr_b16 a[106:107], v232 offset:10240
v_mfma_f32_16x16x32_bf16 v[28:31], a[112:115], a[160:163], v[28:31]
s_waitcnt lgkmcnt(6)
v_mfma_f32_16x16x32_bf16 v[24:27], a[116:119], a[164:167], v[24:27]
s_add_u32 m0, s62, 2048
ds_read_b64_tr_b16 a[108:109], v232 offset:12288
ds_read_b64_tr_b16 a[110:111], v232 offset:14336
v_mfma_f32_16x16x32_bf16 v[24:27], a[120:123], a[172:175], v[24:27]
buffer_load_dwordx4 %[vW], %[srdWo], s75 offen offset:0 lds
v_mfma_f32_16x16x32_bf16 v[24:27], a[124:127], a[180:183], v[24:27]
ds_read_b64_tr_b16 a[112:113], v232 offset:16384
ds_read_b64_tr_b16 a[114:115], v232 offset:18432
v_mfma_f32_16x16x32_bf16 v[28:31], a[116:119], a[168:171], v[28:31]
)"
R"(s_add_u32 s75, s66, 276480
v_mfma_f32_16x16x32_bf16 v[28:31], a[120:123], a[176:179], v[28:31]
s_add_u32 m0, s62, 3072
ds_read_b64_tr_b16 a[116:117], v232 offset:20480
ds_read_b64_tr_b16 a[118:119], v232 offset:22528
v_mfma_f32_16x16x32_bf16 v[28:31], a[124:127], a[184:187], v[28:31]
s_waitcnt lgkmcnt(6)
buffer_load_dwordx4 %[vWo], %[srdWo], s75 offen offset:0 lds
v_mfma_f32_16x16x32_bf16 v[0:3], a[96:99], v[128:131], v[0:3]
ds_read_b64_tr_b16 a[120:121], v233 offset:0
ds_read_b64_tr_b16 a[122:123], v233 offset:2048
v_mfma_f32_16x16x32_bf16 v[0:3], a[100:103], v[136:139], v[0:3]
v_mfma_f32_16x16x32_bf16 v[0:3], a[104:107], v[144:147], v[0:3]
s_add_u32 m0, s62, 4096
ds_read_b64_tr_b16 a[124:125], v233 offset:4096
ds_read_b64_tr_b16 a[126:127], v233 offset:6144
v_mfma_f32_16x16x32_bf16 v[4:7], a[96:99], v[132:135], v[4:7]
buffer_load_dwordx4 %[vX1], %[srdWo], s75 offen offset:0 lds
v_mfma_f32_16x16x32_bf16 v[4:7], a[100:103], v[140:143], v[4:7]
ds_read_b64_tr_b16 a[96:97], v233 offset:8192
ds_read_b64_tr_b16 a[98:99], v233 offset:10240
v_mfma_f32_16x16x32_bf16 v[4:7], a[104:107], v[148:151], v[4:7]
s_waitcnt lgkmcnt(6)
v_mfma_f32_16x16x32_bf16 v[0:3], a[108:111], v[152:155], v[0:3]
)"
R"(s_add_u32 m0, s62, 5120
ds_read_b64_tr_b16 a[100:101], v233 offset:12288
ds_read_b64_tr_b16 a[102:103], v233 offset:14336
v_mfma_f32_16x16x32_bf16 v[0:3], a[112:115], v[160:163], v[0:3]
buffer_load_dwordx4 %[vW], %[srdWo], s75 offen offset:0 lds
v_mfma_f32_16x16x32_bf16 v[0:3], a[116:119], v[168:171], v[0:3]
ds_read_b64_tr_b16 a[104:105], v233 offset:16384
ds_read_b64_tr_b16 a[106:107], v233 offset:18432
v_mfma_f32_16x16x32_bf16 v[4:7], a[108:111], v[156:159], v[4:7]
v_mfma_f32_16x16x32_bf16 v[4:7], a[112:115], v[164:167], v[4:7]
ds_read_b64_tr_b16 a[108:109], v233 offset:20480
ds_read_b64_tr_b16 a[110:111], v233 offset:22528
v_mfma_f32_16x16x32_bf16 v[4:7], a[116:119], v[172:175], v[4:7]
s_waitcnt lgkmcnt(6)
v_mfma_f32_16x16x32_bf16 v[8:11], a[120:123], v[128:131], v[8:11]
ds_read_b64_tr_b16 a[112:113], v234 offset:0
ds_read_b64_tr_b16 a[114:115], v234 offset:2048
v_mfma_f32_16x16x32_bf16 v[8:11], a[124:127], v[136:139], v[8:11]
v_mfma_f32_16x16x32_bf16 v[8:11], a[96:99], v[144:147], v[8:11]
ds_read_b64_tr_b16 a[116:117], v234 offset:4096
ds_read_b64_tr_b16 a[118:119], v234 offset:6144
v_mfma_f32_16x16x32_bf16 v[12:15], a[120:123], v[132:135], v[12:15]
v_mfma_f32_16x16x32_bf16 v[12:15], a[124:127], v[140:143], v[12:15]
ds_read_b64_tr_b16 a[120:121], v234 offset:8192
)"
R"(ds_read_b64_tr_b16 a[122:123], v234 offset:10240
v_mfma_f32_16x16x32_bf16 v[12:15], a[96:99], v[148:151], v[12:15]
s_waitcnt lgkmcnt(6)
v_mfma_f32_16x16x32_bf16 v[8:11], a[100:103], v[152:155], v[8:11]
ds_read_b64_tr_b16 a[124:125], v234 offset:12288
ds_read_b64_tr_b16 a[126:127], v234 offset:14336
v_mfma_f32_16x16x32_bf16 v[8:11], a[104:107], v[160:163], v[8:11]
v_mfma_f32_16x16x32_bf16 v[8:11], a[108:111], v[168:171], v[8:11]
ds_read_b64_tr_b16 a[96:97], v234 offset:16384
ds_read_b64_tr_b16 a[98:99], v234 offset:18432
v_mfma_f32_16x16x32_bf16 v[12:15], a[100:103], v[156:159], v[12:15]
v_mfma_f32_16x16x32_bf16 v[12:15], a[104:107], v[164:167], v[12:15]
ds_read_b64_tr_b16 a[100:101], v234 offset:20480
ds_read_b64_tr_b16 a[102:103], v234 offset:22528
v_mfma_f32_16x16x32_bf16 v[12:15], a[108:111], v[172:175], v[12:15]
s_waitcnt lgkmcnt(6)
v_mfma_f32_16x16x32_bf16 v[16:19], a[112:115], v[128:131], v[16:19]
ds_read_b64_tr_b16 a[104:105], v235 offset:0
ds_read_b64_tr_b16 a[106:107], v235 offset:2048
v_mfma_f32_16x16x32_bf16 v[16:19], a[116:119], v[136:139], v[16:19]
v_mfma_f32_16x16x32_bf16 v[16:19], a[120:123], v[144:147], v[16:19]
ds_read_b64_tr_b16 a[108:109], v235 offset:4096
ds_read_b64_tr_b16 a[110:111], v235 offset:6144
v_mfma_f32_16x16x32_bf16 v[20:23], a[112:115], v[132:135], v[20:23]
)"
R"(v_mfma_f32_16x16x32_bf16 v[20:23], a[116:119], v[140:143], v[20:23]
ds_read_b64_tr_b16 a[112:113], v235 offset:8192
ds_read_b64_tr_b16 a[114:115], v235 offset:10240
v_mfma_f32_16x16x32_bf16 v[20:23], a[120:123], v[148:151], v[20:23]
s_waitcnt lgkmcnt(6)
v_mfma_f32_16x16x32_bf16 v[16:19], a[124:127], v[152:155], v[16:19]
s_add_u32 s75, s66, 491520
ds_read_b64_tr_b16 a[116:117], v235 offset:12288
ds_read_b64_tr_b16 a[118:119], v235 offset:14336
v_mfma_f32_16x16x32_bf16 v[16:19], a[96:99], v[160:163], v[16:19]
s_waitcnt vmcnt(0)
s_barrier
s_lshl_b64 exec, 1, s68
buffer_store_dword v190, %[pW], %[srdC], 0 offen sc1
s_mov_b64 exec, -1
v_mfma_f32_16x16x32_bf16 v[16:19], a[100:103], v[168:171], v[16:19]
s_add_u32 m0, s63, 0
ds_read_b64_tr_b16 a[120:121], v235 offset:16384
ds_read_b64_tr_b16 a[122:123], v235 offset:18432
v_mfma_f32_16x16x32_bf16 v[64:67], v[186:189], v[112:115], v[64:67]
v_mfma_f32_16x16x32_bf16 v[20:23], a[124:127], v[156:159], v[20:23]
buffer_load_dwordx4 %[vWo], %[srdWo], s75 offen offset:0 lds
s_mov_b64 exec, 0xff00
buffer_store_dword v190, %[pW], %[srdC], s72 offen sc1
)"
R"(s_mov_b64 exec, -1
s_mov_b32 s72, 0x80000000
v_mfma_f32_16x16x32_bf16 v[72:75], v[184:187], v[112:115], v[72:75]
v_mfma_f32_16x16x32_bf16 v[20:23], a[96:99], v[164:167], v[20:23]
s_sub_u32 s68, s73, 1
ds_read_b64_tr_b16 a[124:125], v235 offset:20480
ds_read_b64_tr_b16 a[126:127], v235 offset:22528
buffer_load_dword v239, v238, %[srdC], s70 offen sc1
v_mfma_f32_16x16x32_bf16 v[68:71], v[186:189], v[116:119], v[68:71]
v_mfma_f32_16x16x32_bf16 v[20:23], a[100:103], v[172:175], v[20:23]
s_waitcnt lgkmcnt(6)
v_add_u32 v232, s48, %[pWo]
v_xor_b32 v233, 32, v232
v_mfma_f32_16x16x32_bf16 v[76:79], v[184:187], v[116:119], v[76:79]
v_mfma_f32_16x16x32_bf16 v[24:27], a[104:107], v[128:131], v[24:27]
ds_read_b64_tr_b16 a[96:97], v232 offset:0
ds_read_b64_tr_b16 a[98:99], v232 offset:2048
v_mfma_f32_16x16x32_bf16 v[80:83], v[186:189], v[120:123], v[80:83]
v_mfma_f32_16x16x32_bf16 v[24:27], a[108:111], v[136:139], v[24:27]
v_xor_b32 v234, 64, v232
v_xor_b32 v235, 96, v232
v_mfma_f32_16x16x32_bf16 v[88:91], v[184:187], v[120:123], v[88:91]
v_mfma_f32_16x16x32_bf16 v[24:27], a[112:115], v[144:147], v[24:27]
s_add_u32 m0, s63, 1024
)"
R"(ds_read_b64_tr_b16 a[100:101], v232 offset:4096
ds_read_b64_tr_b16 a[102:103], v232 offset:6144
v_mfma_f32_16x16x32_bf16 v[28:31], a[104:107], v[132:135], v[28:31]
buffer_load_dwordx4 %[vX1], %[srdWo], s75 offen offset:0 lds
v_mfma_f32_16x16x32_bf16 v[28:31], a[108:111], v[140:143], v[28:31]
ds_read_b64_tr_b16 a[104:105], v232 offset:8192
ds_read_b64_tr_b16 a[106:107], v232 offset:10240
v_mfma_f32_16x16x32_bf16 v[28:31], a[112:115], v[148:151], v[28:31]
s_waitcnt lgkmcnt(6)
v_mfma_f32_16x16x32_bf16 v[84:87], v[186:189], v[124:127], v[84:87]
v_mfma_f32_16x16x32_bf16 v[24:27], a[116:119], v[152:155], v[24:27]
s_add_u32 m0, s63, 2048
ds_read_b64_tr_b16 a[108:109], v232 offset:12288
ds_read_b64_tr_b16 a[110:111], v232 offset:14336
v_mfma_f32_16x16x32_bf16 v[24:27], a[120:123], v[160:163], v[24:27]
buffer_load_dwordx4 %[vW], %[srdWo], s75 offen offset:0 lds
v_mfma_f32_16x16x32_bf16 v[24:27], a[124:127], v[168:171], v[24:27]
ds_read_b64_tr_b16 a[112:113], v232 offset:16384
ds_read_b64_tr_b16 a[114:115], v232 offset:18432
v_mfma_f32_16x16x32_bf16 v[28:31], a[116:119], v[156:159], v[28:31]
s_add_u32 s75, s66, 522240
v_mfma_f32_16x16x32_bf16 v[92:95], v[184:187], v[124:127], v[92:95]
v_mfma_f32_16x16x32_bf16 v[28:31], a[120:123], v[164:167], v[28:31]
s_add_u32 m0, s63, 3072
)"
R"(ds_read_b64_tr_b16 a[116:117], v232 offset:20480
ds_read_b64_tr_b16 a[118:119], v232 offset:22528
v_mfma_f32_16x16x32_bf16 v[28:31], a[124:127], v[172:175], v[28:31]
s_waitcnt lgkmcnt(6)
buffer_load_dwordx4 %[vWo], %[srdWo], s75 offen offset:0 lds
v_mfma_f32_16x16x32_bf16 v[32:35], a[96:99], a[0:3], 0
ds_read_b64_tr_b16 a[120:121], v233 offset:0
ds_read_b64_tr_b16 a[122:123], v233 offset:2048
v_mfma_f32_16x16x32_bf16 v[32:35], a[100:103], a[8:11], v[32:35]
v_mfma_f32_16x16x32_bf16 v[32:35], a[104:107], a[16:19], v[32:35]
s_add_u32 m0, s63, 4096
ds_read_b64_tr_b16 a[124:125], v233 offset:4096
ds_read_b64_tr_b16 a[126:127], v233 offset:6144
v_mfma_f32_16x16x32_bf16 v[36:39], a[96:99], a[4:7], 0
buffer_load_dwordx4 %[vX1], %[srdWo], s75 offen offset:0 lds
v_mfma_f32_16x16x32_bf16 v[36:39], a[100:103], a[12:15], v[36:39]
ds_read_b64_tr_b16 a[96:97], v233 offset:8192
ds_read_b64_tr_b16 a[98:99], v233 offset:10240
v_mfma_f32_16x16x32_bf16 v[36:39], a[104:107], a[20:23], v[36:39]
s_waitcnt lgkmcnt(6)
v_mfma_f32_16x16x32_bf16 v[32:35], a[108:111], a[24:27], v[32:35]
s_add_u32 m0, s63, 5120
ds_read_b64_tr_b16 a[100:101], v233 offset:12288
ds_read_b64_tr_b16 a[102:103], v233 offset:14336
)"
R"(v_mfma_f32_16x16x32_bf16 v[32:35], a[112:115], a[32:35], v[32:35]
buffer_load_dwordx4 %[vW], %[srdWo], s75 offen offset:0 lds
v_mfma_f32_16x16x32_bf16 v[32:35], a[116:119], a[40:43], v[32:35]
ds_read_b64_tr_b16 a[104:105], v233 offset:16384
ds_read_b64_tr_b16 a[106:107], v233 offset:18432
v_mfma_f32_16x16x32_bf16 v[36:39], a[108:111], a[28:31], v[36:39]
v_cvt_pk_bf16_f32 v96, v0, v1
v_cvt_pk_bf16_f32 v97, v2, v3
v_mfma_f32_16x16x32_bf16 v[36:39], a[112:115], a[36:39], v[36:39]
ds_read_b64_tr_b16 a[108:109], v233 offset:20480
ds_read_b64_tr_b16 a[110:111], v233 offset:22528
v_mfma_f32_16x16x32_bf16 v[36:39], a[116:119], a[44:47], v[36:39]
s_waitcnt lgkmcnt(6)
v_cvt_pk_bf16_f32 v98, v8, v9
v_cvt_pk_bf16_f32 v99, v10, v11
v_mfma_f32_16x16x32_bf16 v[40:43], a[120:123], a[0:3], 0
s_add_u32 s79, s67, 0
ds_read_b64_tr_b16 a[112:113], v234 offset:0
ds_read_b64_tr_b16 a[114:115], v234 offset:2048
v_mfma_f32_16x16x32_bf16 v[40:43], a[124:127], a[8:11], v[40:43]
buffer_store_dwordx4 v[96:99], %[vP], %[srdG], s79 offen offset:0 sc1
v_mfma_f32_16x16x32_bf16 v[40:43], a[96:99], a[16:19], v[40:43]
ds_read_b64_tr_b16 a[116:117], v234 offset:4096
ds_read_b64_tr_b16 a[118:119], v234 offset:6144
)"
R"(v_mfma_f32_16x16x32_bf16 v[44:47], a[120:123], a[4:7], 0
v_cvt_pk_bf16_f32 v100, v4, v5
v_cvt_pk_bf16_f32 v101, v6, v7
v_mfma_f32_16x16x32_bf16 v[44:47], a[124:127], a[12:15], v[44:47]
ds_read_b64_tr_b16 a[120:121], v234 offset:8192
ds_read_b64_tr_b16 a[122:123], v234 offset:10240
v_mfma_f32_16x16x32_bf16 v[44:47], a[96:99], a[20:23], v[44:47]
s_waitcnt lgkmcnt(6)
v_cvt_pk_bf16_f32 v102, v12, v13
v_cvt_pk_bf16_f32 v103, v14, v15
v_mfma_f32_16x16x32_bf16 v[40:43], a[100:103], a[24:27], v[40:43]
s_add_u32 s79, s67, 4096
ds_read_b64_tr_b16 a[124:125], v234 offset:12288
ds_read_b64_tr_b16 a[126:127], v234 offset:14336
v_mfma_f32_16x16x32_bf16 v[40:43], a[104:107], a[32:35], v[40:43]
buffer_store_dwordx4 v[100:103], %[vP], %[srdG], s79 offen offset:0 sc1
v_mfma_f32_16x16x32_bf16 v[40:43], a[108:111], a[40:43], v[40:43]
ds_read_b64_tr_b16 a[96:97], v234 offset:16384
ds_read_b64_tr_b16 a[98:99], v234 offset:18432
v_mfma_f32_16x16x32_bf16 v[44:47], a[100:103], a[28:31], v[44:47]
v_cvt_pk_bf16_f32 v104, v16, v17
v_cvt_pk_bf16_f32 v105, v18, v19
v_mfma_f32_16x16x32_bf16 v[44:47], a[104:107], a[36:39], v[44:47]
ds_read_b64_tr_b16 a[100:101], v234 offset:20480
)"
R"(ds_read_b64_tr_b16 a[102:103], v234 offset:22528
v_mfma_f32_16x16x32_bf16 v[44:47], a[108:111], a[44:47], v[44:47]
s_waitcnt lgkmcnt(6)
v_cvt_pk_bf16_f32 v106, v24, v25
v_cvt_pk_bf16_f32 v107, v26, v27
v_mfma_f32_16x16x32_bf16 v[48:51], a[112:115], a[0:3], 0
s_add_u32 s79, s67, 8192
ds_read_b64_tr_b16 a[104:105], v235 offset:0
ds_read_b64_tr_b16 a[106:107], v235 offset:2048
v_mfma_f32_16x16x32_bf16 v[48:51], a[116:119], a[8:11], v[48:51]
buffer_store_dwordx4 v[104:107], %[vP], %[srdG], s79 offen offset:0 sc1
v_mfma_f32_16x16x32_bf16 v[48:51], a[120:123], a[16:19], v[48:51]
ds_read_b64_tr_b16 a[108:109], v235 offset:4096
ds_read_b64_tr_b16 a[110:111], v235 offset:6144
v_mfma_f32_16x16x32_bf16 v[52:55], a[112:115], a[4:7], 0
v_cvt_pk_bf16_f32 v108, v20, v21
v_cvt_pk_bf16_f32 v109, v22, v23
v_mfma_f32_16x16x32_bf16 v[52:55], a[116:119], a[12:15], v[52:55]
ds_read_b64_tr_b16 a[112:113], v235 offset:8192
ds_read_b64_tr_b16 a[114:115], v235 offset:10240
v_mfma_f32_16x16x32_bf16 v[52:55], a[120:123], a[20:23], v[52:55]
s_waitcnt lgkmcnt(6)
v_cvt_pk_bf16_f32 v110, v28, v29
v_cvt_pk_bf16_f32 v111, v30, v31
)"
R"(v_mfma_f32_16x16x32_bf16 v[48:51], a[124:127], a[24:27], v[48:51]
s_add_u32 s75, s66, 737280
ds_read_b64_tr_b16 a[116:117], v235 offset:12288
ds_read_b64_tr_b16 a[118:119], v235 offset:14336
v_mfma_f32_16x16x32_bf16 v[48:51], a[96:99], a[32:35], v[48:51]
s_waitcnt vmcnt(8)
s_barrier
v_cmp_eq_u32 vcc, v239, v224
s_nop 1
s_and_b32 s47, vcc_lo, s71
s_cmp_eq_u32 s47, s71
s_cbranch_scc0 SPINL1_%=
SPBL1_%=:
v_mfma_f32_16x16x32_bf16 v[48:51], a[100:103], a[40:43], v[48:51]
s_add_u32 m0, s64, 0
ds_read_b64_tr_b16 a[120:121], v235 offset:16384
ds_read_b64_tr_b16 a[122:123], v235 offset:18432
v_mfma_f32_16x16x32_bf16 v[52:55], a[124:127], a[28:31], v[52:55]
s_add_u32 s79, s67, 12288
buffer_load_dwordx4 %[vWo], %[srdWo], s75 offen offset:0 lds
v_mfma_f32_16x16x32_bf16 v[52:55], a[96:99], a[36:39], v[52:55]
s_add_u32 s76, s69, 0
ds_read_b64_tr_b16 a[124:125], v235 offset:20480
ds_read_b64_tr_b16 a[126:127], v235 offset:22528
)"
R"(v_mfma_f32_16x16x32_bf16 v[52:55], a[100:103], a[44:47], v[52:55]
s_waitcnt lgkmcnt(6)
v_add_u32 v232, s50, %[pWo]
v_xor_b32 v233, 32, v232
v_mfma_f32_16x16x32_bf16 v[56:59], a[104:107], a[0:3], 0
ds_read_b64_tr_b16 a[96:97], v232 offset:0
ds_read_b64_tr_b16 a[98:99], v232 offset:2048
v_mfma_f32_16x16x32_bf16 v[56:59], a[108:111], a[8:11], v[56:59]
v_xor_b32 v234, 64, v232
v_xor_b32 v235, 96, v232
v_mfma_f32_16x16x32_bf16 v[56:59], a[112:115], a[16:19], v[56:59]
s_add_u32 m0, s64, 1024
ds_read_b64_tr_b16 a[100:101], v232 offset:4096
ds_read_b64_tr_b16 a[102:103], v232 offset:6144
v_mfma_f32_16x16x32_bf16 v[60:63], a[104:107], a[4:7], 0
buffer_load_dwordx4 %[vX1], %[srdWo], s75 offen offset:0 lds
v_mfma_f32_16x16x32_bf16 v[60:63], a[108:111], a[12:15], v[60:63]
ds_read_b64_tr_b16 a[104:105], v232 offset:8192
ds_read_b64_tr_b16 a[106:107], v232 offset:10240
v_mfma_f32_16x16x32_bf16 v[60:63], a[112:115], a[20:23], v[60:63]
s_waitcnt lgkmcnt(6)
buffer_store_dwordx4 v[108:111], %[vP], %[srdG], s79 offen offset:0 sc1
v_mfma_f32_16x16x32_bf16 v[56:59], a[116:119], a[24:27], v[56:59]
s_add_u32 m0, s64, 2048
)"
R"(ds_read_b64_tr_b16 a[108:109], v232 offset:12288
ds_read_b64_tr_b16 a[110:111], v232 offset:14336
v_mfma_f32_16x16x32_bf16 v[56:59], a[120:123], a[32:35], v[56:59]
buffer_load_dwordx4 %[vW], %[srdWo], s75 offen offset:0 lds
v_mfma_f32_16x16x32_bf16 v[56:59], a[124:127], a[40:43], v[56:59]
ds_read_b64_tr_b16 a[112:113], v232 offset:16384
ds_read_b64_tr_b16 a[114:115], v232 offset:18432
v_mfma_f32_16x16x32_bf16 v[60:63], a[116:119], a[28:31], v[60:63]
s_add_u32 s75, s66, 768000
buffer_load_dwordx4 v[112:115], %[vP], %[srdG], s76 offen offset:0 sc1
v_mfma_f32_16x16x32_bf16 v[60:63], a[120:123], a[36:39], v[60:63]
s_add_u32 m0, s64, 3072
ds_read_b64_tr_b16 a[116:117], v232 offset:20480
ds_read_b64_tr_b16 a[118:119], v232 offset:22528
v_mfma_f32_16x16x32_bf16 v[60:63], a[124:127], a[44:47], v[60:63]
s_waitcnt lgkmcnt(6)
buffer_load_dwordx4 %[vWo], %[srdWo], s75 offen offset:0 lds
v_mfma_f32_16x16x32_bf16 v[32:35], a[96:99], a[48:51], v[32:35]
s_add_u32 s76, s69, 4096
ds_read_b64_tr_b16 a[120:121], v233 offset:0
ds_read_b64_tr_b16 a[122:123], v233 offset:2048
v_mfma_f32_16x16x32_bf16 v[32:35], a[100:103], a[56:59], v[32:35]
buffer_load_dwordx4 v[116:119], %[vP], %[srdG], s76 offen offset:0 sc1
v_mfma_f32_16x16x32_bf16 v[32:35], a[104:107], a[64:67], v[32:35]
)"
R"(s_add_u32 m0, s64, 4096
ds_read_b64_tr_b16 a[124:125], v233 offset:4096
ds_read_b64_tr_b16 a[126:127], v233 offset:6144
v_mfma_f32_16x16x32_bf16 v[36:39], a[96:99], a[52:55], v[36:39]
s_add_u32 s76, s69, 8192
buffer_load_dwordx4 %[vX1], %[srdWo], s75 offen offset:0 lds
v_mfma_f32_16x16x32_bf16 v[36:39], a[100:103], a[60:63], v[36:39]
ds_read_b64_tr_b16 a[96:97], v233 offset:8192
ds_read_b64_tr_b16 a[98:99], v233 offset:10240
v_mfma_f32_16x16x32_bf16 v[36:39], a[104:107], a[68:71], v[36:39]
s_waitcnt lgkmcnt(6)
buffer_load_dwordx4 v[120:123], %[vP], %[srdG], s76 offen offset:0 sc1
v_mfma_f32_16x16x32_bf16 v[32:35], a[108:111], a[72:75], v[32:35]
s_add_u32 m0, s64, 5120
ds_read_b64_tr_b16 a[100:101], v233 offset:12288
ds_read_b64_tr_b16 a[102:103], v233 offset:14336
v_mfma_f32_16x16x32_bf16 v[32:35], a[112:115], a[80:83], v[32:35]
s_add_u32 s76, s69, 12288
buffer_load_dwordx4 %[vW], %[srdWo], s75 offen offset:0 lds
v_mfma_f32_16x16x32_bf16 v[32:35], a[116:119], a[88:91], v[32:35]
ds_read_b64_tr_b16 a[104:105], v233 offset:16384
ds_read_b64_tr_b16 a[106:107], v233 offset:18432
v_mfma_f32_16x16x32_bf16 v[36:39], a[108:111], a[76:79], v[36:39]
buffer_load_dwordx4 v[124:127], %[vP], %[srdG], s76 offen offset:0 sc1
)"
R"(v_mfma_f32_16x16x32_bf16 v[36:39], a[112:115], a[84:87], v[36:39]
ds_read_b64_tr_b16 a[108:109], v233 offset:20480
ds_read_b64_tr_b16 a[110:111], v233 offset:22528
v_mfma_f32_16x16x32_bf16 v[36:39], a[116:119], a[92:95], v[36:39]
s_waitcnt lgkmcnt(6)
v_mfma_f32_16x16x32_bf16 v[40:43], a[120:123], a[48:51], v[40:43]
ds_read_b64_tr_b16 a[112:113], v234 offset:0
ds_read_b64_tr_b16 a[114:115], v234 offset:2048
v_mfma_f32_16x16x32_bf16 v[40:43], a[124:127], a[56:59], v[40:43]
v_mfma_f32_16x16x32_bf16 v[40:43], a[96:99], a[64:67], v[40:43]
ds_read_b64_tr_b16 a[116:117], v234 offset:4096
ds_read_b64_tr_b16 a[118:119], v234 offset:6144
v_mfma_f32_16x16x32_bf16 v[44:47], a[120:123], a[52:55], v[44:47]
v_mfma_f32_16x16x32_bf16 v[44:47], a[124:127], a[60:63], v[44:47]
ds_read_b64_tr_b16 a[120:121], v234 offset:8192
ds_read_b64_tr_b16 a[122:123], v234 offset:10240
v_mfma_f32_16x16x32_bf16 v[44:47], a[96:99], a[68:71], v[44:47]
s_waitcnt lgkmcnt(6)
v_mfma_f32_16x16x32_bf16 v[40:43], a[100:103], a[72:75], v[40:43]
ds_read_b64_tr_b16 a[124:125], v234 offset:12288
ds_read_b64_tr_b16 a[126:127], v234 offset:14336
v_mfma_f32_16x16x32_bf16 v[40:43], a[104:107], a[80:83], v[40:43]
v_mfma_f32_16x16x32_bf16 v[40:43], a[108:111], a[88:91], v[40:43]
ds_read_b64_tr_b16 a[96:97], v234 offset:16384
)"
R"(ds_read_b64_tr_b16 a[98:99], v234 offset:18432
v_mfma_f32_16x16x32_bf16 v[44:47], a[100:103], a[76:79], v[44:47]
v_mfma_f32_16x16x32_bf16 v[44:47], a[104:107], a[84:87], v[44:47]
ds_read_b64_tr_b16 a[100:101], v234 offset:20480
ds_read_b64_tr_b16 a[102:103], v234 offset:22528
v_mfma_f32_16x16x32_bf16 v[44:47], a[108:111], a[92:95], v[44:47]
s_waitcnt lgkmcnt(6)
v_mfma_f32_16x16x32_bf16 v[48:51], a[112:115], a[48:51], v[48:51]
ds_read_b64_tr_b16 a[104:105], v235 offset:0
ds_read_b64_tr_b16 a[106:107], v235 offset:2048
v_mfma_f32_16x16x32_bf16 v[48:51], a[116:119], a[56:59], v[48:51]
v_mfma_f32_16x16x32_bf16 v[48:51], a[120:123], a[64:67], v[48:51]
ds_read_b64_tr_b16 a[108:109], v235 offset:4096
ds_read_b64_tr_b16 a[110:111], v235 offset:6144
v_mfma_f32_16x16x32_bf16 v[52:55], a[112:115], a[52:55], v[52:55]
v_mfma_f32_16x16x32_bf16 v[52:55], a[116:119], a[60:63], v[52:55]
ds_read_b64_tr_b16 a[112:113], v235 offset:8192
ds_read_b64_tr_b16 a[114:115], v235 offset:10240
v_mfma_f32_16x16x32_bf16 v[52:55], a[120:123], a[68:71], v[52:55]
s_waitcnt lgkmcnt(6)
v_readlane_b32 s66, v229, s73
s_nop 1
v_mfma_f32_16x16x32_bf16 v[48:51], a[124:127], a[72:75], v[48:51]
s_add_u32 s75, s66, 0
)"
R"(ds_read_b64_tr_b16 a[116:117], v235 offset:12288
ds_read_b64_tr_b16 a[118:119], v235 offset:14336
v_mfma_f32_16x16x32_bf16 v[48:51], a[96:99], a[80:83], v[48:51]
s_waitcnt vmcnt(14)
s_barrier
v_mfma_f32_16x16x32_bf16 v[48:51], a[100:103], a[88:91], v[48:51]
s_add_u32 m0, s60, 0
ds_read_b64_tr_b16 a[120:121], v235 offset:16384
ds_read_b64_tr_b16 a[122:123], v235 offset:18432
v_mfma_f32_16x16x32_bf16 v[52:55], a[124:127], a[76:79], v[52:55]
buffer_load_dwordx4 %[vWo], %[srdWo], s75 offen offset:0 lds
v_mfma_f32_16x16x32_bf16 v[52:55], a[96:99], a[84:87], v[52:55]
ds_read_b64_tr_b16 a[124:125], v235 offset:20480
ds_read_b64_tr_b16 a[126:127], v235 offset:22528
v_mfma_f32_16x16x32_bf16 v[52:55], a[100:103], a[92:95], v[52:55]
s_waitcnt lgkmcnt(6)
v_add_u32 v232, s51, %[pWo]
v_xor_b32 v233, 32, v232
v_mfma_f32_16x16x32_bf16 v[56:59], a[104:107], a[48:51], v[56:59]
ds_read_b64_tr_b16 a[96:97], v232 offset:0
ds_read_b64_tr_b16 a[98:99], v232 offset:2048
v_mfma_f32_16x16x32_bf16 v[56:59], a[108:111], a[56:59], v[56:59]
v_xor_b32 v234, 64, v232
v_xor_b32 v235, 96, v232
)"
R"(v_mfma_f32_16x16x32_bf16 v[56:59], a[112:115], a[64:67], v[56:59]
s_add_u32 m0, s60, 1024
ds_read_b64_tr_b16 a[100:101], v232 offset:4096
ds_read_b64_tr_b16 a[102:103], v232 offset:6144
v_mfma_f32_16x16x32_bf16 v[60:63], a[104:107], a[52:55], v[60:63]
buffer_load_dwordx4 %[vX1], %[srdWo], s75 offen offset:0 lds
v_mfma_f32_16x16x32_bf16 v[60:63], a[108:111], a[60:63], v[60:63]
ds_read_b64_tr_b16 a[104:105], v232 offset:8192
ds_read_b64_tr_b16 a[106:107], v232 offset:10240
v_mfma_f32_16x16x32_bf16 v[60:63], a[112:115], a[68:71], v[60:63]
s_waitcnt lgkmcnt(6)
v_mfma_f32_16x16x32_bf16 v[56:59], a[116:119], a[72:75], v[56:59]
s_add_u32 m0, s60, 2048
ds_read_b64_tr_b16 a[108:109], v232 offset:12288
ds_read_b64_tr_b16 a[110:111], v232 offset:14336
v_mfma_f32_16x16x32_bf16 v[56:59], a[120:123], a[80:83], v[56:59]
buffer_load_dwordx4 %[vW], %[srdWo], s75 offen offset:0 lds
v_mfma_f32_16x16x32_bf16 v[56:59], a[124:127], a[88:91], v[56:59]
ds_read_b64_tr_b16 a[112:113], v232 offset:16384
ds_read_b64_tr_b16 a[114:115], v232 offset:18432
v_mfma_f32_16x16x32_bf16 v[60:63], a[116:119], a[76:79], v[60:63]
s_add_u32 s75, s66, 30720
v_mfma_f32_16x16x32_bf16 v[60:63], a[120:123], a[84:87], v[60:63]
s_add_u32 m0, s60, 3072
)"
R"(ds_read_b64_tr_b16 a[116:117], v232 offset:20480
ds_read_b64_tr_b16 a[118:119], v232 offset:22528
v_mfma_f32_16x16x32_bf16 v[60:63], a[124:127], a[92:95], v[60:63]
s_waitcnt lgkmcnt(6)
buffer_load_dwordx4 %[vWo], %[srdWo], s75 offen offset:0 lds
v_mfma_f32_16x16x32_bf16 v[32:35], a[96:99], a[140:143], v[32:35]
ds_read_b64_tr_b16 a[120:121], v233 offset:0
ds_read_b64_tr_b16 a[122:123], v233 offset:2048
v_mfma_f32_16x16x32_bf16 v[32:35], a[100:103], a[148:151], v[32:35]
v_mfma_f32_16x16x32_bf16 v[32:35], a[104:107], a[156:159], v[32:35]
s_add_u32 m0, s60, 4096
ds_read_b64_tr_b16 a[124:125], v233 offset:4096
ds_read_b64_tr_b16 a[126:127], v233 offset:6144
v_mfma_f32_16x16x32_bf16 v[36:39], a[96:99], a[144:147], v[36:39]
buffer_load_dwordx4 %[vX1], %[srdWo], s75 offen offset:0 lds
v_mfma_f32_16x16x32_bf16 v[36:39], a[100:103], a[152:155], v[36:39]
s_add_u32 s73, s73, 1
ds_read_b64_tr_b16 a[96:97], v233 offset:8192
ds_read_b64_tr_b16 a[98:99], v233 offset:10240
v_mfma_f32_16x16x32_bf16 v[36:39], a[104:107], a[160:163], v[36:39]
s_waitcnt lgkmcnt(6)
v_readlane_b32 s67, v226, s73
v_readlane_b32 s69, v228, s73
v_mfma_f32_16x16x32_bf16 v[32:35], a[108:111], a[164:167], v[32:35]
)"
R"(s_add_u32 m0, s60, 5120
ds_read_b64_tr_b16 a[100:101], v233 offset:12288
ds_read_b64_tr_b16 a[102:103], v233 offset:14336
v_mfma_f32_16x16x32_bf16 v[32:35], a[112:115], a[172:175], v[32:35]
buffer_load_dwordx4 %[vW], %[srdWo], s75 offen offset:0 lds
v_mfma_f32_16x16x32_bf16 v[32:35], a[116:119], a[180:183], v[32:35]
ds_read_b64_tr_b16 a[104:105], v233 offset:16384
ds_read_b64_tr_b16 a[106:107], v233 offset:18432
v_mfma_f32_16x16x32_bf16 v[36:39], a[108:111], a[168:171], v[36:39]
v_readlane_b32 s71, v231, s73
v_mfma_f32_16x16x32_bf16 v[36:39], a[112:115], a[176:179], v[36:39]
ds_read_b64_tr_b16 a[108:109], v233 offset:20480
ds_read_b64_tr_b16 a[110:111], v233 offset:22528
v_mfma_f32_16x16x32_bf16 v[36:39], a[116:119], a[184:187], v[36:39]
s_waitcnt lgkmcnt(6)
v_mfma_f32_16x16x32_bf16 v[40:43], a[120:123], a[140:143], v[40:43]
ds_read_b64_tr_b16 a[112:113], v234 offset:0
ds_read_b64_tr_b16 a[114:115], v234 offset:2048
v_mfma_f32_16x16x32_bf16 v[40:43], a[124:127], a[148:151], v[40:43]
v_mfma_f32_16x16x32_bf16 v[40:43], a[96:99], a[156:159], v[40:43]
ds_read_b64_tr_b16 a[116:117], v234 offset:4096
ds_read_b64_tr_b16 a[118:119], v234 offset:6144
v_mfma_f32_16x16x32_bf16 v[44:47], a[120:123], a[144:147], v[44:47]
v_mfma_f32_16x16x32_bf16 v[44:47], a[124:127], a[152:155], v[44:47]
)"
R"(ds_read_b64_tr_b16 a[120:121], v234 offset:8192
ds_read_b64_tr_b16 a[122:123], v234 offset:10240
v_mfma_f32_16x16x32_bf16 v[44:47], a[96:99], a[160:163], v[44:47]
s_waitcnt lgkmcnt(6)
v_mfma_f32_16x16x32_bf16 v[40:43], a[100:103], a[164:167], v[40:43]
ds_read_b64_tr_b16 a[124:125], v234 offset:12288
ds_read_b64_tr_b16 a[126:127], v234 offset:14336
v_mfma_f32_16x16x32_bf16 v[40:43], a[104:107], a[172:175], v[40:43]
v_mfma_f32_16x16x32_bf16 v[40:43], a[108:111], a[180:183], v[40:43]
ds_read_b64_tr_b16 a[96:97], v234 offset:16384
ds_read_b64_tr_b16 a[98:99], v234 offset:18432
v_mfma_f32_16x16x32_bf16 v[44:47], a[100:103], a[168:171], v[44:47]
v_mfma_f32_16x16x32_bf16 v[44:47], a[104:107], a[176:179], v[44:47]
ds_read_b64_tr_b16 a[100:101], v234 offset:20480
ds_read_b64_tr_b16 a[102:103], v234 offset:22528
v_mfma_f32_16x16x32_bf16 v[44:47], a[108:111], a[184:187], v[44:47]
s_waitcnt lgkmcnt(6)
v_mfma_f32_16x16x32_bf16 v[48:51], a[112:115], a[140:143], v[48:51]
ds_read_b64_tr_b16 a[104:105], v235 offset:0
ds_read_b64_tr_b16 a[106:107], v235 offset:2048
v_mfma_f32_16x16x32_bf16 v[48:51], a[116:119], a[148:151], v[48:51]
v_mfma_f32_16x16x32_bf16 v[48:51], a[120:123], a[156:159], v[48:51]
ds_read_b64_tr_b16 a[108:109], v235 offset:4096
ds_read_b64_tr_b16 a[110:111], v235 offset:6144
)"
R"(v_mfma_f32_16x16x32_bf16 v[52:55], a[112:115], a[144:147], v[52:55]
v_mfma_f32_16x16x32_bf16 v[52:55], a[116:119], a[152:155], v[52:55]
ds_read_b64_tr_b16 a[112:113], v235 offset:8192
ds_read_b64_tr_b16 a[114:115], v235 offset:10240
v_mfma_f32_16x16x32_bf16 v[52:55], a[120:123], a[160:163], v[52:55]
s_waitcnt lgkmcnt(6)
v_mfma_f32_16x16x32_bf16 v[48:51], a[124:127], a[164:167], v[48:51]
s_add_u32 s75, s66, 245760
ds_read_b64_tr_b16 a[116:117], v235 offset:12288
ds_read_b64_tr_b16 a[118:119], v235 offset:14336
v_mfma_f32_16x16x32_bf16 v[48:51], a[96:99], a[172:175], v[48:51]
s_waitcnt vmcnt(7)
s_barrier
v_mfma_f32_16x16x32_bf16 v[48:51], a[100:103], a[180:183], v[48:51]
s_add_u32 m0, s62, 0
ds_read_b64_tr_b16 a[120:121], v235 offset:16384
ds_read_b64_tr_b16 a[122:123], v235 offset:18432
v_mfma_f32_16x16x32_bf16 v[52:55], a[124:127], a[168:171], v[52:55]
buffer_load_dwordx4 %[vWo], %[srdWo], s75 offen offset:0 lds
v_mfma_f32_16x16x32_bf16 v[52:55], a[96:99], a[176:179], v[52:55]
ds_read_b64_tr_b16 a[124:125], v235 offset:20480
ds_read_b64_tr_b16 a[126:127], v235 offset:22528
v_mfma_f32_16x16x32_bf16 v[52:55], a[100:103], a[184:187], v[52:55]
s_waitcnt lgkmcnt(6)
)"
R"(v_add_u32 v232, s52, %[pWo]
v_xor_b32 v233, 32, v232
v_mfma_f32_16x16x32_bf16 v[56:59], a[104:107], a[140:143], v[56:59]
ds_read_b64_tr_b16 a[96:97], v232 offset:0
ds_read_b64_tr_b16 a[98:99], v232 offset:2048
v_mfma_f32_16x16x32_bf16 v[56:59], a[108:111], a[148:151], v[56:59]
v_xor_b32 v234, 64, v232
v_xor_b32 v235, 96, v232
v_mfma_f32_16x16x32_bf16 v[56:59], a[112:115], a[156:159], v[56:59]
s_add_u32 m0, s62, 1024
ds_read_b64_tr_b16 a[100:101], v232 offset:4096
ds_read_b64_tr_b16 a[102:103], v232 offset:6144
v_mfma_f32_16x16x32_bf16 v[60:63], a[104:107], a[144:147], v[60:63]
buffer_load_dwordx4 %[vX1], %[srdWo], s75 offen offset:0 lds
v_mfma_f32_16x16x32_bf16 v[60:63], a[108:111], a[152:155], v[60:63]
ds_read_b64_tr_b16 a[104:105], v232 offset:8192
ds_read_b64_tr_b16 a[106:107], v232 offset:10240
v_mfma_f32_16x16x32_bf16 v[60:63], a[112:115], a[160:163], v[60:63]
s_waitcnt lgkmcnt(6)
v_mfma_f32_16x16x32_bf16 v[56:59], a[116:119], a[164:167], v[56:59]
s_add_u32 m0, s62, 2048
ds_read_b64_tr_b16 a[108:109], v232 offset:12288
ds_read_b64_tr_b16 a[110:111], v232 offset:14336
v_mfma_f32_16x16x32_bf16 v[56:59], a[120:123], a[172:175], v[56:59]
)"
R"(buffer_load_dwordx4 %[vW], %[srdWo], s75 offen offset:0 lds
v_mfma_f32_16x16x32_bf16 v[56:59], a[124:127], a[180:183], v[56:59]
ds_read_b64_tr_b16 a[112:113], v232 offset:16384
ds_read_b64_tr_b16 a[114:115], v232 offset:18432
v_mfma_f32_16x16x32_bf16 v[60:63], a[116:119], a[168:171], v[60:63]
s_add_u32 s75, s66, 276480
v_mfma_f32_16x16x32_bf16 v[60:63], a[120:123], a[176:179], v[60:63]
s_add_u32 m0, s62, 3072
ds_read_b64_tr_b16 a[116:117], v232 offset:20480
ds_read_b64_tr_b16 a[118:119], v232 offset:22528
v_mfma_f32_16x16x32_bf16 v[60:63], a[124:127], a[184:187], v[60:63]
s_waitcnt lgkmcnt(6)
buffer_load_dwordx4 %[vWo], %[srdWo], s75 offen offset:0 lds
v_mfma_f32_16x16x32_bf16 v[32:35], a[96:99], v[128:131], v[32:35]
ds_read_b64_tr_b16 a[120:121], v233 offset:0
ds_read_b64_tr_b16 a[122:123], v233 offset:2048
v_mfma_f32_16x16x32_bf16 v[32:35], a[100:103], v[136:139], v[32:35]
v_mfma_f32_16x16x32_bf16 v[32:35], a[104:107], v[144:147], v[32:35]
s_add_u32 m0, s62, 4096
ds_read_b64_tr_b16 a[124:125], v233 offset:4096
ds_read_b64_tr_b16 a[126:127], v233 offset:6144
v_mfma_f32_16x16x32_bf16 v[36:39], a[96:99], v[132:135], v[36:39]
buffer_load_dwordx4 %[vX1], %[srdWo], s75 offen offset:0 lds
v_mfma_f32_16x16x32_bf16 v[36:39], a[100:103], v[140:143], v[36:39]
)"
R"(ds_read_b64_tr_b16 a[96:97], v233 offset:8192
ds_read_b64_tr_b16 a[98:99], v233 offset:10240
v_mfma_f32_16x16x32_bf16 v[36:39], a[104:107], v[148:151], v[36:39]
s_waitcnt lgkmcnt(6)
v_mfma_f32_16x16x32_bf16 v[32:35], a[108:111], v[152:155], v[32:35]
s_add_u32 m0, s62, 5120
ds_read_b64_tr_b16 a[100:101], v233 offset:12288
ds_read_b64_tr_b16 a[102:103], v233 offset:14336
v_mfma_f32_16x16x32_bf16 v[32:35], a[112:115], v[160:163], v[32:35]
buffer_load_dwordx4 %[vW], %[srdWo], s75 offen offset:0 lds
v_mfma_f32_16x16x32_bf16 v[32:35], a[116:119], v[168:171], v[32:35]
ds_read_b64_tr_b16 a[104:105], v233 offset:16384
ds_read_b64_tr_b16 a[106:107], v233 offset:18432
v_mfma_f32_16x16x32_bf16 v[36:39], a[108:111], v[156:159], v[36:39]
v_mfma_f32_16x16x32_bf16 v[36:39], a[112:115], v[164:167], v[36:39]
ds_read_b64_tr_b16 a[108:109], v233 offset:20480
ds_read_b64_tr_b16 a[110:111], v233 offset:22528
v_mfma_f32_16x16x32_bf16 v[36:39], a[116:119], v[172:175], v[36:39]
s_waitcnt lgkmcnt(6)
v_mfma_f32_16x16x32_bf16 v[40:43], a[120:123], v[128:131], v[40:43]
ds_read_b64_tr_b16 a[112:113], v234 offset:0
ds_read_b64_tr_b16 a[114:115], v234 offset:2048
v_mfma_f32_16x16x32_bf16 v[40:43], a[124:127], v[136:139], v[40:43]
v_mfma_f32_16x16x32_bf16 v[40:43], a[96:99], v[144:147], v[40:43]
)"
R"(ds_read_b64_tr_b16 a[116:117], v234 offset:4096
ds_read_b64_tr_b16 a[118:119], v234 offset:6144
v_mfma_f32_16x16x32_bf16 v[44:47], a[120:123], v[132:135], v[44:47]
v_mfma_f32_16x16x32_bf16 v[44:47], a[124:127], v[140:143], v[44:47]
ds_read_b64_tr_b16 a[120:121], v234 offset:8192
ds_read_b64_tr_b16 a[122:123], v234 offset:10240
v_mfma_f32_16x16x32_bf16 v[44:47], a[96:99], v[148:151], v[44:47]
s_waitcnt lgkmcnt(6)
v_mfma_f32_16x16x32_bf16 v[40:43], a[100:103], v[152:155], v[40:43]
ds_read_b64_tr_b16 a[124:125], v234 offset:12288
ds_read_b64_tr_b16 a[126:127], v234 offset:14336
v_mfma_f32_16x16x32_bf16 v[40:43], a[104:107], v[160:163], v[40:43]
v_mfma_f32_16x16x32_bf16 v[40:43], a[108:111], v[168:171], v[40:43]
ds_read_b64_tr_b16 a[96:97], v234 offset:16384
ds_read_b64_tr_b16 a[98:99], v234 offset:18432
v_mfma_f32_16x16x32_bf16 v[44:47], a[100:103], v[156:159], v[44:47]
v_mfma_f32_16x16x32_bf16 v[44:47], a[104:107], v[164:167], v[44:47]
ds_read_b64_tr_b16 a[100:101], v234 offset:20480
ds_read_b64_tr_b16 a[102:103], v234 offset:22528
v_mfma_f32_16x16x32_bf16 v[44:47], a[108:111], v[172:175], v[44:47]
s_waitcnt lgkmcnt(6)
v_mfma_f32_16x16x32_bf16 v[48:51], a[112:115], v[128:131], v[48:51]
ds_read_b64_tr_b16 a[104:105], v235 offset:0
ds_read_b64_tr_b16 a[106:107], v235 offset:2048
)"
R"(v_mfma_f32_16x16x32_bf16 v[48:51], a[116:119], v[136:139], v[48:51]
v_mfma_f32_16x16x32_bf16 v[48:51], a[120:123], v[144:147], v[48:51]
ds_read_b64_tr_b16 a[108:109], v235 offset:4096
ds_read_b64_tr_b16 a[110:111], v235 offset:6144
v_mfma_f32_16x16x32_bf16 v[52:55], a[112:115], v[132:135], v[52:55]
v_mfma_f32_16x16x32_bf16 v[52:55], a[116:119], v[140:143], v[52:55]
ds_read_b64_tr_b16 a[112:113], v235 offset:8192
ds_read_b64_tr_b16 a[114:115], v235 offset:10240
v_mfma_f32_16x16x32_bf16 v[52:55], a[120:123], v[148:151], v[52:55]
s_waitcnt lgkmcnt(6)
v_mfma_f32_16x16x32_bf16 v[48:51], a[124:127], v[152:155], v[48:51]
s_add_u32 s75, s66, 491520
ds_read_b64_tr_b16 a[116:117], v235 offset:12288
ds_read_b64_tr_b16 a[118:119], v235 offset:14336
v_mfma_f32_16x16x32_bf16 v[48:51], a[96:99], v[160:163], v[48:51]
s_waitcnt vmcnt(0)
s_barrier
s_lshl_b64 exec, 1, s68
buffer_store_dword v190, %[pW], %[srdC], 0 offen sc1
s_mov_b64 exec, -1
v_mfma_f32_16x16x32_bf16 v[48:51], a[100:103], v[168:171], v[48:51]
s_add_u32 m0, s63, 0
ds_read_b64_tr_b16 a[120:121], v235 offset:16384
ds_read_b64_tr_b16 a[122:123], v235 offset:18432
)"
R"(v_mfma_f32_16x16x32_bf16 v[64:67], v[186:189], v[112:115], v[64:67]
v_mfma_f32_16x16x32_bf16 v[52:55], a[124:127], v[156:159], v[52:55]
buffer_load_dwordx4 %[vWo], %[srdWo], s75 offen offset:0 lds
v_mfma_f32_16x16x32_bf16 v[72:75], v[184:187], v[112:115], v[72:75]
v_mfma_f32_16x16x32_bf16 v[52:55], a[96:99], v[164:167], v[52:55]
s_sub_u32 s68, s73, 1
ds_read_b64_tr_b16 a[124:125], v235 offset:20480
ds_read_b64_tr_b16 a[126:127], v235 offset:22528
buffer_load_dword v239, v238, %[srdC], s70 offen sc1
v_mfma_f32_16x16x32_bf16 v[68:71], v[186:189], v[116:119], v[68:71]
v_mfma_f32_16x16x32_bf16 v[52:55], a[100:103], v[172:175], v[52:55]
s_waitcnt lgkmcnt(6)
v_add_u32 v232, s48, %[pWo]
v_xor_b32 v233, 32, v232
v_mfma_f32_16x16x32_bf16 v[76:79], v[184:187], v[116:119], v[76:79]
v_mfma_f32_16x16x32_bf16 v[56:59], a[104:107], v[128:131], v[56:59]
ds_read_b64_tr_b16 a[96:97], v232 offset:0
ds_read_b64_tr_b16 a[98:99], v232 offset:2048
v_mfma_f32_16x16x32_bf16 v[56:59], a[108:111], v[136:139], v[56:59]
v_xor_b32 v234, 64, v232
v_xor_b32 v235, 96, v232
v_mfma_f32_16x16x32_bf16 v[56:59], a[112:115], v[144:147], v[56:59]
s_add_u32 m0, s63, 1024
ds_read_b64_tr_b16 a[100:101], v232 offset:4096
)"
R"(ds_read_b64_tr_b16 a[102:103], v232 offset:6144
v_mfma_f32_16x16x32_bf16 v[60:63], a[104:107], v[132:135], v[60:63]
buffer_load_dwordx4 %[vX1], %[srdWo], s75 offen offset:0 lds
v_mfma_f32_16x16x32_bf16 v[60:63], a[108:111], v[140:143], v[60:63]
ds_read_b64_tr_b16 a[104:105], v232 offset:8192
ds_read_b64_tr_b16 a[106:107], v232 offset:10240
v_mfma_f32_16x16x32_bf16 v[60:63], a[112:115], v[148:151], v[60:63]
s_waitcnt lgkmcnt(6)
v_mfma_f32_16x16x32_bf16 v[80:83], v[186:189], v[120:123], v[80:83]
v_mfma_f32_16x16x32_bf16 v[56:59], a[116:119], v[152:155], v[56:59]
ds_read_b64_tr_b16 a[108:109], v232 offset:12288
ds_read_b64_tr_b16 a[110:111], v232 offset:14336
v_mfma_f32_16x16x32_bf16 v[56:59], a[120:123], v[160:163], v[56:59]
v_mfma_f32_16x16x32_bf16 v[88:91], v[184:187], v[120:123], v[88:91]
v_mfma_f32_16x16x32_bf16 v[56:59], a[124:127], v[168:171], v[56:59]
ds_read_b64_tr_b16 a[112:113], v232 offset:16384
ds_read_b64_tr_b16 a[114:115], v232 offset:18432
v_mfma_f32_16x16x32_bf16 v[60:63], a[116:119], v[156:159], v[60:63]
v_mfma_f32_16x16x32_bf16 v[84:87], v[186:189], v[124:127], v[84:87]
v_mfma_f32_16x16x32_bf16 v[60:63], a[120:123], v[164:167], v[60:63]
ds_read_b64_tr_b16 a[116:117], v232 offset:20480
ds_read_b64_tr_b16 a[118:119], v232 offset:22528
v_mfma_f32_16x16x32_bf16 v[60:63], a[124:127], v[172:175], v[60:63]
v_mfma_f32_16x16x32_bf16 v[92:95], v[184:187], v[124:127], v[92:95]
)"
R"(s_cmp_lt_u32 s73, 8
s_cbranch_scc1 OPL_%=
s_lshl_b32 s85, %[hgi], 15
s_waitcnt vmcnt(0)
v_cmp_eq_u32 vcc, v239, v224
s_nop 1
s_and_b32 s47, vcc_lo, 0xff20
s_cmp_eq_u32 s47, 0xff20
s_cbranch_scc0 SPINT5_%=
SPBT5_%=:
s_add_u32 s79, s45, 81920
buffer_load_dwordx4 v[112:115], %[vP], %[srdG], s79 offen offset:0 sc1
s_add_u32 s79, s45, 86016
buffer_load_dwordx4 v[116:119], %[vP], %[srdG], s79 offen offset:0 sc1
s_add_u32 s79, s45, 90112
buffer_load_dwordx4 v[120:123], %[vP], %[srdG], s79 offen offset:0 sc1
s_add_u32 s79, s45, 94208
buffer_load_dwordx4 v[124:127], %[vP], %[srdG], s79 offen offset:0 sc1
s_add_u32 s79, s85, 917504
buffer_load_dwordx4 v[192:195], %[vP], %[srdG], s79 offen offset:0 sc1
s_add_u32 s79, s85, 921600
buffer_load_dwordx4 v[196:199], %[vP], %[srdG], s79 offen offset:0 sc1
s_add_u32 s79, s85, 925696
buffer_load_dwordx4 v[200:203], %[vP], %[srdG], s79 offen offset:0 sc1
)"
R"(s_add_u32 s79, s85, 929792
buffer_load_dwordx4 v[204:207], %[vP], %[srdG], s79 offen offset:0 sc1
s_add_u32 s79, s85, 933888
buffer_load_dwordx4 v[208:211], %[vP], %[srdG], s79 offen offset:0 sc1
s_add_u32 s79, s85, 937984
buffer_load_dwordx4 v[212:215], %[vP], %[srdG], s79 offen offset:0 sc1
s_add_u32 s79, s85, 942080
buffer_load_dwordx4 v[216:219], %[vP], %[srdG], s79 offen offset:0 sc1
s_add_u32 s79, s85, 946176
buffer_load_dwordx4 v[220:223], %[vP], %[srdG], s79 offen offset:0 sc1
buffer_load_dword v239, v238, %[srdC], s70 offen sc1
v_pk_add_f32 v[64:65], v[64:65], v[32:33]
v_pk_add_f32 v[66:67], v[66:67], v[34:35]
v_pk_add_f32 v[68:69], v[68:69], v[36:37]
v_pk_add_f32 v[70:71], v[70:71], v[38:39]
v_pk_add_f32 v[72:73], v[72:73], v[40:41]
v_pk_add_f32 v[74:75], v[74:75], v[42:43]
v_pk_add_f32 v[76:77], v[76:77], v[44:45]
v_pk_add_f32 v[78:79], v[78:79], v[46:47]
v_pk_add_f32 v[80:81], v[80:81], v[48:49]
v_pk_add_f32 v[82:83], v[82:83], v[50:51]
v_pk_add_f32 v[84:85], v[84:85], v[52:53]
v_pk_add_f32 v[86:87], v[86:87], v[54:55]
v_pk_add_f32 v[88:89], v[88:89], v[56:57]
)"
R"(v_pk_add_f32 v[90:91], v[90:91], v[58:59]
v_pk_add_f32 v[92:93], v[92:93], v[60:61]
v_pk_add_f32 v[94:95], v[94:95], v[62:63]
s_and_b32 s86, %[hgi], 1
s_mul_i32 s86, s86, 40960
s_lshr_b32 s79, %[hgi], 1
s_lshl_b32 s79, s79, 6
s_add_u32 s86, s86, s79
s_waitcnt vmcnt(0)
v_cmp_eq_u32 vcc, v239, v224
s_nop 1
s_and_b32 s47, vcc_lo, 0x40
s_cmp_eq_u32 s47, 0x40
s_cbranch_scc0 SPINT6_%=
SPBT6_%=:
s_add_u32 s79, s45, 98304
buffer_load_dwordx4 v[96:99], %[vP], %[srdG], s79 offen offset:0 sc1
s_add_u32 s79, s45, 102400
buffer_load_dwordx4 v[100:103], %[vP], %[srdG], s79 offen offset:0 sc1
s_add_u32 s79, s45, 106496
buffer_load_dwordx4 v[104:107], %[vP], %[srdG], s79 offen offset:0 sc1
s_add_u32 s79, s45, 110592
buffer_load_dwordx4 v[108:111], %[vP], %[srdG], s79 offen offset:0 sc1
v_mfma_f32_16x16x32_bf16 v[64:67], v[186:189], v[112:115], v[64:67]
)"
R"(v_mfma_f32_16x16x32_bf16 v[72:75], v[184:187], v[112:115], v[72:75]
v_mfma_f32_16x16x32_bf16 v[68:71], v[186:189], v[116:119], v[68:71]
v_mfma_f32_16x16x32_bf16 v[76:79], v[184:187], v[116:119], v[76:79]
v_mfma_f32_16x16x32_bf16 v[80:83], v[186:189], v[120:123], v[80:83]
v_mfma_f32_16x16x32_bf16 v[88:91], v[184:187], v[120:123], v[88:91]
v_mfma_f32_16x16x32_bf16 v[84:87], v[186:189], v[124:127], v[84:87]
v_mfma_f32_16x16x32_bf16 v[92:95], v[184:187], v[124:127], v[92:95]
v_mfma_f32_16x16x32_bf16 v[0:3], v[186:189], v[192:195], 0
v_mfma_f32_16x16x32_bf16 v[0:3], v[186:189], v[196:199], v[0:3]
v_mfma_f32_16x16x32_bf16 v[0:3], v[186:189], v[200:203], v[0:3]
v_mfma_f32_16x16x32_bf16 v[0:3], v[186:189], v[204:207], v[0:3]
v_mfma_f32_16x16x32_bf16 v[0:3], v[186:189], v[208:211], v[0:3]
v_mfma_f32_16x16x32_bf16 v[0:3], v[186:189], v[212:215], v[0:3]
v_mfma_f32_16x16x32_bf16 v[0:3], v[186:189], v[216:219], v[0:3]
v_mfma_f32_16x16x32_bf16 v[0:3], v[186:189], v[220:223], v[0:3]
v_mfma_f32_16x16x32_bf16 v[4:7], v[184:187], v[192:195], 0
v_mfma_f32_16x16x32_bf16 v[4:7], v[184:187], v[196:199], v[4:7]
v_mfma_f32_16x16x32_bf16 v[4:7], v[184:187], v[200:203], v[4:7]
v_mfma_f32_16x16x32_bf16 v[4:7], v[184:187], v[204:207], v[4:7]
v_mfma_f32_16x16x32_bf16 v[4:7], v[184:187], v[208:211], v[4:7]
v_mfma_f32_16x16x32_bf16 v[4:7], v[184:187], v[212:215], v[4:7]
v_mfma_f32_16x16x32_bf16 v[4:7], v[184:187], v[216:219], v[4:7]
v_mfma_f32_16x16x32_bf16 v[4:7], v[184:187], v[220:223], v[4:7]
s_waitcnt vmcnt(0)
)"
R"(v_mfma_f32_16x16x32_bf16 v[64:67], v[186:189], v[96:99], v[64:67]
v_mfma_f32_16x16x32_bf16 v[72:75], v[184:187], v[96:99], v[72:75]
v_mfma_f32_16x16x32_bf16 v[68:71], v[186:189], v[100:103], v[68:71]
v_mfma_f32_16x16x32_bf16 v[76:79], v[184:187], v[100:103], v[76:79]
v_mfma_f32_16x16x32_bf16 v[80:83], v[186:189], v[104:107], v[80:83]
v_mfma_f32_16x16x32_bf16 v[88:91], v[184:187], v[104:107], v[88:91]
v_mfma_f32_16x16x32_bf16 v[84:87], v[186:189], v[108:111], v[84:87]
v_mfma_f32_16x16x32_bf16 v[92:95], v[184:187], v[108:111], v[92:95]
v_cvt_pk_bf16_f32 v176, v0, v1
v_cvt_pk_bf16_f32 v177, v2, v3
v_cvt_pk_bf16_f32 v178, v4, v5
v_cvt_pk_bf16_f32 v179, v6, v7
s_nop 0
v_permlane16_swap_b32 v176, v178
v_permlane16_swap_b32 v177, v179
buffer_store_dwordx4 v[176:179], v240, %[srdO], s86 offen offset:1024
s_lshl_b32 s86, %[hgi], 7
s_add_u32 s46, s86, 40960
v_cvt_pk_bf16_f32 v192, v64, v65
v_cvt_pk_bf16_f32 v193, v66, v67
v_cvt_pk_bf16_f32 v194, v72, v73
v_cvt_pk_bf16_f32 v195, v74, v75
s_nop 0
v_permlane16_swap_b32 v192, v194
)"
R"(v_permlane16_swap_b32 v193, v195
buffer_store_dwordx4 v[192:195], v240, %[srdO], s86 offen offset:0
v_cvt_pk_bf16_f32 v196, v68, v69
v_cvt_pk_bf16_f32 v197, v70, v71
v_cvt_pk_bf16_f32 v198, v76, v77
v_cvt_pk_bf16_f32 v199, v78, v79
s_nop 0
v_permlane16_swap_b32 v196, v198
v_permlane16_swap_b32 v197, v199
buffer_store_dwordx4 v[196:199], v240, %[srdO], s46 offen offset:0
v_cvt_pk_bf16_f32 v200, v80, v81
v_cvt_pk_bf16_f32 v201, v82, v83
v_cvt_pk_bf16_f32 v202, v88, v89
v_cvt_pk_bf16_f32 v203, v90, v91
s_nop 0
v_permlane16_swap_b32 v200, v202
v_permlane16_swap_b32 v201, v203
buffer_store_dwordx4 v[200:203], v240, %[srdO], s86 offen offset:64
v_cvt_pk_bf16_f32 v204, v84, v85
v_cvt_pk_bf16_f32 v205, v86, v87
v_cvt_pk_bf16_f32 v206, v92, v93
v_cvt_pk_bf16_f32 v207, v94, v95
s_nop 0
v_permlane16_swap_b32 v204, v206
)"
R"(v_permlane16_swap_b32 v205, v207
buffer_store_dwordx4 v[204:207], v240, %[srdO], s46 offen offset:64
s_waitcnt vmcnt(0)
s_branch END_%=
RARE1_%=:
s_nop 7
v_max3_f32 v238, v96, v97, v98
v_max3_f32 v238, v238, v99, v104
v_max3_f32 v238, v238, v105, v106
v_max3_f32 v238, v238, v107, v112
v_max3_f32 v238, v238, v113, v114
v_max3_f32 v238, v238, v115, v120
v_max3_f32 v238, v238, v121, v122
v_max_f32 v238, v238, v123
v_mov_b32 v239, v238
s_nop 1
v_permlane16_swap_b32 v238, v239
v_max_f32 v238, v238, v239
v_mov_b32 v239, v238
s_nop 1
v_permlane32_swap_b32 v238, v239
v_max_f32 v238, v238, v239
v_max_f32 v238, 0, v238
v_sub_f32 v176, v176, v238
)"
R"(v_sub_f32 v177, v177, v238
v_sub_f32 v178, v178, v238
v_sub_f32 v179, v179, v238
v_sub_f32 v96, v96, v238
v_sub_f32 v97, v97, v238
v_sub_f32 v98, v98, v238
v_sub_f32 v99, v99, v238
v_sub_f32 v104, v104, v238
v_sub_f32 v105, v105, v238
v_sub_f32 v106, v106, v238
v_sub_f32 v107, v107, v238
v_sub_f32 v112, v112, v238
v_sub_f32 v113, v113, v238
v_sub_f32 v114, v114, v238
v_sub_f32 v115, v115, v238
v_sub_f32 v120, v120, v238
v_sub_f32 v121, v121, v238
v_sub_f32 v122, v122, v238
v_sub_f32 v123, v123, v238
v_max3_f32 v238, v100, v101, v102
v_max3_f32 v238, v238, v103, v108
v_max3_f32 v238, v238, v109, v110
v_max3_f32 v238, v238, v111, v116
v_max3_f32 v238, v238, v117, v118
)"
R"(v_max3_f32 v238, v238, v119, v124
v_max3_f32 v238, v238, v125, v126
v_max_f32 v238, v238, v127
v_mov_b32 v239, v238
s_nop 1
v_permlane16_swap_b32 v238, v239
v_max_f32 v238, v238, v239
v_mov_b32 v239, v238
s_nop 1
v_permlane32_swap_b32 v238, v239
v_max_f32 v238, v238, v239
v_max_f32 v238, 0, v238
v_sub_f32 v180, v180, v238
v_sub_f32 v181, v181, v238
v_sub_f32 v182, v182, v238
v_sub_f32 v183, v183, v238
v_sub_f32 v100, v100, v238
v_sub_f32 v101, v101, v238
v_sub_f32 v102, v102, v238
v_sub_f32 v103, v103, v238
v_sub_f32 v108, v108, v238
v_sub_f32 v109, v109, v238
v_sub_f32 v110, v110, v238
v_sub_f32 v111, v111, v238
)"
R"(v_sub_f32 v116, v116, v238
v_sub_f32 v117, v117, v238
v_sub_f32 v118, v118, v238
v_sub_f32 v119, v119, v238
v_sub_f32 v124, v124, v238
v_sub_f32 v125, v125, v238
v_sub_f32 v126, v126, v238
v_sub_f32 v127, v127, v238
s_nop 1
s_branch BACK1_%=
RARE2_%=:
s_nop 7
v_max3_f32 v238, v128, v129, v130
v_max3_f32 v238, v238, v131, v136
v_max3_f32 v238, v238, v137, v138
v_max3_f32 v238, v238, v139, v144
v_max3_f32 v238, v238, v145, v146
v_max3_f32 v238, v238, v147, v152
v_max3_f32 v238, v238, v153, v154
v_max_f32 v238, v238, v155
v_mov_b32 v239, v238
s_nop 1
v_permlane16_swap_b32 v238, v239
v_max_f32 v238, v238, v239
)"
R"(v_mov_b32 v239, v238
s_nop 1
v_permlane32_swap_b32 v238, v239
v_max_f32 v238, v238, v239
v_max_f32 v238, 0, v238
v_sub_f32 v176, v176, v238
v_sub_f32 v177, v177, v238
v_sub_f32 v178, v178, v238
v_sub_f32 v179, v179, v238
v_sub_f32 v128, v128, v238
v_sub_f32 v129, v129, v238
v_sub_f32 v130, v130, v238
v_sub_f32 v131, v131, v238
v_sub_f32 v136, v136, v238
v_sub_f32 v137, v137, v238
v_sub_f32 v138, v138, v238
v_sub_f32 v139, v139, v238
v_sub_f32 v144, v144, v238
v_sub_f32 v145, v145, v238
v_sub_f32 v146, v146, v238
v_sub_f32 v147, v147, v238
v_sub_f32 v152, v152, v238
v_sub_f32 v153, v153, v238
v_sub_f32 v154, v154, v238
)"
R"(v_sub_f32 v155, v155, v238
v_exp_f32 v240, -v238
s_nop 0
v_mul_f32 v0, v0, v240
v_mul_f32 v1, v1, v240
v_mul_f32 v2, v2, v240
v_mul_f32 v3, v3, v240
v_mul_f32 v8, v8, v240
v_mul_f32 v9, v9, v240
v_mul_f32 v10, v10, v240
v_mul_f32 v11, v11, v240
v_mul_f32 v16, v16, v240
v_mul_f32 v17, v17, v240
v_mul_f32 v18, v18, v240
v_mul_f32 v19, v19, v240
v_mul_f32 v24, v24, v240
v_mul_f32 v25, v25, v240
v_mul_f32 v26, v26, v240
v_mul_f32 v27, v27, v240
v_mul_f32 v32, v32, v240
v_mul_f32 v33, v33, v240
v_mul_f32 v34, v34, v240
v_mul_f32 v35, v35, v240
v_mul_f32 v40, v40, v240
)"
R"(v_mul_f32 v41, v41, v240
v_mul_f32 v42, v42, v240
v_mul_f32 v43, v43, v240
v_mul_f32 v48, v48, v240
v_mul_f32 v49, v49, v240
v_mul_f32 v50, v50, v240
v_mul_f32 v51, v51, v240
v_mul_f32 v56, v56, v240
v_mul_f32 v57, v57, v240
v_mul_f32 v58, v58, v240
v_mul_f32 v59, v59, v240
v_mul_f32 v64, v64, v240
v_mul_f32 v65, v65, v240
v_mul_f32 v66, v66, v240
v_mul_f32 v67, v67, v240
v_mul_f32 v72, v72, v240
v_mul_f32 v73, v73, v240
v_mul_f32 v74, v74, v240
v_mul_f32 v75, v75, v240
v_mul_f32 v80, v80, v240
v_mul_f32 v81, v81, v240
v_mul_f32 v82, v82, v240
v_mul_f32 v83, v83, v240
v_mul_f32 v88, v88, v240
)"
R"(v_mul_f32 v89, v89, v240
v_mul_f32 v90, v90, v240
v_mul_f32 v91, v91, v240
v_accvgpr_read_b32 v239, a132
v_mul_f32 v239, v239, v240
v_accvgpr_write_b32 a132, v239
v_max3_f32 v238, v132, v133, v134
v_max3_f32 v238, v238, v135, v140
v_max3_f32 v238, v238, v141, v142
v_max3_f32 v238, v238, v143, v148
v_max3_f32 v238, v238, v149, v150
v_max3_f32 v238, v238, v151, v156
v_max3_f32 v238, v238, v157, v158
v_max_f32 v238, v238, v159
v_mov_b32 v239, v238
s_nop 1
v_permlane16_swap_b32 v238, v239
v_max_f32 v238, v238, v239
v_mov_b32 v239, v238
s_nop 1
v_permlane32_swap_b32 v238, v239
v_max_f32 v238, v238, v239
v_max_f32 v238, 0, v238
v_sub_f32 v180, v180, v238
)"
R"(v_sub_f32 v181, v181, v238
v_sub_f32 v182, v182, v238
v_sub_f32 v183, v183, v238
v_sub_f32 v132, v132, v238
v_sub_f32 v133, v133, v238
v_sub_f32 v134, v134, v238
v_sub_f32 v135, v135, v238
v_sub_f32 v140, v140, v238
v_sub_f32 v141, v141, v238
v_sub_f32 v142, v142, v238
v_sub_f32 v143, v143, v238
v_sub_f32 v148, v148, v238
v_sub_f32 v149, v149, v238
v_sub_f32 v150, v150, v238
v_sub_f32 v151, v151, v238
v_sub_f32 v156, v156, v238
v_sub_f32 v157, v157, v238
v_sub_f32 v158, v158, v238
v_sub_f32 v159, v159, v238
v_exp_f32 v240, -v238
s_nop 0
v_mul_f32 v4, v4, v240
v_mul_f32 v5, v5, v240
v_mul_f32 v6, v6, v240
)"
R"(v_mul_f32 v7, v7, v240
v_mul_f32 v12, v12, v240
v_mul_f32 v13, v13, v240
v_mul_f32 v14, v14, v240
v_mul_f32 v15, v15, v240
v_mul_f32 v20, v20, v240
v_mul_f32 v21, v21, v240
v_mul_f32 v22, v22, v240
v_mul_f32 v23, v23, v240
v_mul_f32 v28, v28, v240
v_mul_f32 v29, v29, v240
v_mul_f32 v30, v30, v240
v_mul_f32 v31, v31, v240
v_mul_f32 v36, v36, v240
v_mul_f32 v37, v37, v240
v_mul_f32 v38, v38, v240
v_mul_f32 v39, v39, v240
v_mul_f32 v44, v44, v240
v_mul_f32 v45, v45, v240
v_mul_f32 v46, v46, v240
v_mul_f32 v47, v47, v240
v_mul_f32 v52, v52, v240
v_mul_f32 v53, v53, v240
v_mul_f32 v54, v54, v240
)"
R"(v_mul_f32 v55, v55, v240
v_mul_f32 v60, v60, v240
v_mul_f32 v61, v61, v240
v_mul_f32 v62, v62, v240
v_mul_f32 v63, v63, v240
v_mul_f32 v68, v68, v240
v_mul_f32 v69, v69, v240
v_mul_f32 v70, v70, v240
v_mul_f32 v71, v71, v240
v_mul_f32 v76, v76, v240
v_mul_f32 v77, v77, v240
v_mul_f32 v78, v78, v240
v_mul_f32 v79, v79, v240
v_mul_f32 v84, v84, v240
v_mul_f32 v85, v85, v240
v_mul_f32 v86, v86, v240
v_mul_f32 v87, v87, v240
v_mul_f32 v92, v92, v240
v_mul_f32 v93, v93, v240
v_mul_f32 v94, v94, v240
v_mul_f32 v95, v95, v240
v_accvgpr_read_b32 v239, a136
v_mul_f32 v239, v239, v240
v_accvgpr_write_b32 a136, v239
)"
R"(s_nop 1
s_branch BACK2_%=
RARE3_%=:
s_nop 7
v_max3_f32 v238, v96, v97, v98
v_max3_f32 v238, v238, v99, v104
v_max3_f32 v238, v238, v105, v106
v_max3_f32 v238, v238, v107, v112
v_max3_f32 v238, v238, v113, v114
v_max3_f32 v238, v238, v115, v120
v_max3_f32 v238, v238, v121, v122
v_max_f32 v238, v238, v123
v_mov_b32 v239, v238
s_nop 1
v_permlane16_swap_b32 v238, v239
v_max_f32 v238, v238, v239
v_mov_b32 v239, v238
s_nop 1
v_permlane32_swap_b32 v238, v239
v_max_f32 v238, v238, v239
v_max_f32 v238, 0, v238
v_sub_f32 v176, v176, v238
v_sub_f32 v177, v177, v238
v_sub_f32 v178, v178, v238
)"
R"(v_sub_f32 v179, v179, v238
v_sub_f32 v96, v96, v238
v_sub_f32 v97, v97, v238
v_sub_f32 v98, v98, v238
v_sub_f32 v99, v99, v238
v_sub_f32 v104, v104, v238
v_sub_f32 v105, v105, v238
v_sub_f32 v106, v106, v238
v_sub_f32 v107, v107, v238
v_sub_f32 v112, v112, v238
v_sub_f32 v113, v113, v238
v_sub_f32 v114, v114, v238
v_sub_f32 v115, v115, v238
v_sub_f32 v120, v120, v238
v_sub_f32 v121, v121, v238
v_sub_f32 v122, v122, v238
v_sub_f32 v123, v123, v238
v_exp_f32 v240, -v238
s_nop 0
v_mul_f32 v0, v0, v240
v_mul_f32 v1, v1, v240
v_mul_f32 v2, v2, v240
v_mul_f32 v3, v3, v240
v_mul_f32 v8, v8, v240
)"
R"(v_mul_f32 v9, v9, v240
v_mul_f32 v10, v10, v240
v_mul_f32 v11, v11, v240
v_mul_f32 v16, v16, v240
v_mul_f32 v17, v17, v240
v_mul_f32 v18, v18, v240
v_mul_f32 v19, v19, v240
v_mul_f32 v24, v24, v240
v_mul_f32 v25, v25, v240
v_mul_f32 v26, v26, v240
v_mul_f32 v27, v27, v240
v_mul_f32 v32, v32, v240
v_mul_f32 v33, v33, v240
v_mul_f32 v34, v34, v240
v_mul_f32 v35, v35, v240
v_mul_f32 v40, v40, v240
v_mul_f32 v41, v41, v240
v_mul_f32 v42, v42, v240
v_mul_f32 v43, v43, v240
v_mul_f32 v48, v48, v240
v_mul_f32 v49, v49, v240
v_mul_f32 v50, v50, v240
v_mul_f32 v51, v51, v240
v_mul_f32 v56, v56, v240
)"
R"(v_mul_f32 v57, v57, v240
v_mul_f32 v58, v58, v240
v_mul_f32 v59, v59, v240
v_mul_f32 v64, v64, v240
v_mul_f32 v65, v65, v240
v_mul_f32 v66, v66, v240
v_mul_f32 v67, v67, v240
v_mul_f32 v72, v72, v240
v_mul_f32 v73, v73, v240
v_mul_f32 v74, v74, v240
v_mul_f32 v75, v75, v240
v_mul_f32 v80, v80, v240
v_mul_f32 v81, v81, v240
v_mul_f32 v82, v82, v240
v_mul_f32 v83, v83, v240
v_mul_f32 v88, v88, v240
v_mul_f32 v89, v89, v240
v_mul_f32 v90, v90, v240
v_mul_f32 v91, v91, v240
v_accvgpr_read_b32 v239, a132
v_mul_f32 v239, v239, v240
v_accvgpr_write_b32 a132, v239
v_max3_f32 v238, v100, v101, v102
v_max3_f32 v238, v238, v103, v108
)"
R"(v_max3_f32 v238, v238, v109, v110
v_max3_f32 v238, v238, v111, v116
v_max3_f32 v238, v238, v117, v118
v_max3_f32 v238, v238, v119, v124
v_max3_f32 v238, v238, v125, v126
v_max_f32 v238, v238, v127
v_mov_b32 v239, v238
s_nop 1
v_permlane16_swap_b32 v238, v239
v_max_f32 v238, v238, v239
v_mov_b32 v239, v238
s_nop 1
v_permlane32_swap_b32 v238, v239
v_max_f32 v238, v238, v239
v_max_f32 v238, 0, v238
v_sub_f32 v180, v180, v238
v_sub_f32 v181, v181, v238
v_sub_f32 v182, v182, v238
v_sub_f32 v183, v183, v238
v_sub_f32 v100, v100, v238
v_sub_f32 v101, v101, v238
v_sub_f32 v102, v102, v238
v_sub_f32 v103, v103, v238
v_sub_f32 v108, v108, v238
)"
R"(v_sub_f32 v109, v109, v238
v_sub_f32 v110, v110, v238
v_sub_f32 v111, v111, v238
v_sub_f32 v116, v116, v238
v_sub_f32 v117, v117, v238
v_sub_f32 v118, v118, v238
v_sub_f32 v119, v119, v238
v_sub_f32 v124, v124, v238
v_sub_f32 v125, v125, v238
v_sub_f32 v126, v126, v238
v_sub_f32 v127, v127, v238
v_exp_f32 v240, -v238
s_nop 0
v_mul_f32 v4, v4, v240
v_mul_f32 v5, v5, v240
v_mul_f32 v6, v6, v240
v_mul_f32 v7, v7, v240
v_mul_f32 v12, v12, v240
v_mul_f32 v13, v13, v240
v_mul_f32 v14, v14, v240
v_mul_f32 v15, v15, v240
v_mul_f32 v20, v20, v240
v_mul_f32 v21, v21, v240
v_mul_f32 v22, v22, v240
)"
R"(v_mul_f32 v23, v23, v240
v_mul_f32 v28, v28, v240
v_mul_f32 v29, v29, v240
v_mul_f32 v30, v30, v240
v_mul_f32 v31, v31, v240
v_mul_f32 v36, v36, v240
v_mul_f32 v37, v37, v240
v_mul_f32 v38, v38, v240
v_mul_f32 v39, v39, v240
v_mul_f32 v44, v44, v240
v_mul_f32 v45, v45, v240
v_mul_f32 v46, v46, v240
v_mul_f32 v47, v47, v240
v_mul_f32 v52, v52, v240
v_mul_f32 v53, v53, v240
v_mul_f32 v54, v54, v240
v_mul_f32 v55, v55, v240
v_mul_f32 v60, v60, v240
v_mul_f32 v61, v61, v240
v_mul_f32 v62, v62, v240
v_mul_f32 v63, v63, v240
v_mul_f32 v68, v68, v240
v_mul_f32 v69, v69, v240
v_mul_f32 v70, v70, v240
)"
R"(v_mul_f32 v71, v71, v240
v_mul_f32 v76, v76, v240
v_mul_f32 v77, v77, v240
v_mul_f32 v78, v78, v240
v_mul_f32 v79, v79, v240
v_mul_f32 v84, v84, v240
v_mul_f32 v85, v85, v240
v_mul_f32 v86, v86, v240
v_mul_f32 v87, v87, v240
v_mul_f32 v92, v92, v240
v_mul_f32 v93, v93, v240
v_mul_f32 v94, v94, v240
v_mul_f32 v95, v95, v240
v_accvgpr_read_b32 v239, a136
v_mul_f32 v239, v239, v240
v_accvgpr_write_b32 a136, v239
s_nop 1
s_branch BACK3_%=
RARE4_%=:
s_nop 7
v_max3_f32 v238, v128, v129, v130
v_max3_f32 v238, v238, v131, v136
v_max3_f32 v238, v238, v137, v138
v_max3_f32 v238, v238, v139, v144
)"
R"(v_max3_f32 v238, v238, v145, v146
v_max3_f32 v238, v238, v147, v152
v_max3_f32 v238, v238, v153, v154
v_max_f32 v238, v238, v155
v_mov_b32 v239, v238
s_nop 1
v_permlane16_swap_b32 v238, v239
v_max_f32 v238, v238, v239
v_mov_b32 v239, v238
s_nop 1
v_permlane32_swap_b32 v238, v239
v_max_f32 v238, v238, v239
v_max_f32 v238, 0, v238
v_sub_f32 v176, v176, v238
v_sub_f32 v177, v177, v238
v_sub_f32 v178, v178, v238
v_sub_f32 v179, v179, v238
v_sub_f32 v128, v128, v238
v_sub_f32 v129, v129, v238
v_sub_f32 v130, v130, v238
v_sub_f32 v131, v131, v238
v_sub_f32 v136, v136, v238
v_sub_f32 v137, v137, v238
v_sub_f32 v138, v138, v238
)"
R"(v_sub_f32 v139, v139, v238
v_sub_f32 v144, v144, v238
v_sub_f32 v145, v145, v238
v_sub_f32 v146, v146, v238
v_sub_f32 v147, v147, v238
v_sub_f32 v152, v152, v238
v_sub_f32 v153, v153, v238
v_sub_f32 v154, v154, v238
v_sub_f32 v155, v155, v238
v_exp_f32 v240, -v238
s_nop 0
v_mul_f32 v0, v0, v240
v_mul_f32 v1, v1, v240
v_mul_f32 v2, v2, v240
v_mul_f32 v3, v3, v240
v_mul_f32 v8, v8, v240
v_mul_f32 v9, v9, v240
v_mul_f32 v10, v10, v240
v_mul_f32 v11, v11, v240
v_mul_f32 v16, v16, v240
v_mul_f32 v17, v17, v240
v_mul_f32 v18, v18, v240
v_mul_f32 v19, v19, v240
v_mul_f32 v24, v24, v240
)"
R"(v_mul_f32 v25, v25, v240
v_mul_f32 v26, v26, v240
v_mul_f32 v27, v27, v240
v_mul_f32 v32, v32, v240
v_mul_f32 v33, v33, v240
v_mul_f32 v34, v34, v240
v_mul_f32 v35, v35, v240
v_mul_f32 v40, v40, v240
v_mul_f32 v41, v41, v240
v_mul_f32 v42, v42, v240
v_mul_f32 v43, v43, v240
v_mul_f32 v48, v48, v240
v_mul_f32 v49, v49, v240
v_mul_f32 v50, v50, v240
v_mul_f32 v51, v51, v240
v_mul_f32 v56, v56, v240
v_mul_f32 v57, v57, v240
v_mul_f32 v58, v58, v240
v_mul_f32 v59, v59, v240
v_mul_f32 v64, v64, v240
v_mul_f32 v65, v65, v240
v_mul_f32 v66, v66, v240
v_mul_f32 v67, v67, v240
v_mul_f32 v72, v72, v240
)"
R"(v_mul_f32 v73, v73, v240
v_mul_f32 v74, v74, v240
v_mul_f32 v75, v75, v240
v_mul_f32 v80, v80, v240
v_mul_f32 v81, v81, v240
v_mul_f32 v82, v82, v240
v_mul_f32 v83, v83, v240
v_mul_f32 v88, v88, v240
v_mul_f32 v89, v89, v240
v_mul_f32 v90, v90, v240
v_mul_f32 v91, v91, v240
v_accvgpr_read_b32 v239, a132
v_mul_f32 v239, v239, v240
v_accvgpr_write_b32 a132, v239
v_max3_f32 v238, v132, v133, v134
v_max3_f32 v238, v238, v135, v140
v_max3_f32 v238, v238, v141, v142
v_max3_f32 v238, v238, v143, v148
v_max3_f32 v238, v238, v149, v150
v_max3_f32 v238, v238, v151, v156
v_max3_f32 v238, v238, v157, v158
v_max_f32 v238, v238, v159
v_mov_b32 v239, v238
s_nop 1
)"
R"(v_permlane16_swap_b32 v238, v239
v_max_f32 v238, v238, v239
v_mov_b32 v239, v238
s_nop 1
v_permlane32_swap_b32 v238, v239
v_max_f32 v238, v238, v239
v_max_f32 v238, 0, v238
v_sub_f32 v180, v180, v238
v_sub_f32 v181, v181, v238
v_sub_f32 v182, v182, v238
v_sub_f32 v183, v183, v238
v_sub_f32 v132, v132, v238
v_sub_f32 v133, v133, v238
v_sub_f32 v134, v134, v238
v_sub_f32 v135, v135, v238
v_sub_f32 v140, v140, v238
v_sub_f32 v141, v141, v238
v_sub_f32 v142, v142, v238
v_sub_f32 v143, v143, v238
v_sub_f32 v148, v148, v238
v_sub_f32 v149, v149, v238
v_sub_f32 v150, v150, v238
v_sub_f32 v151, v151, v238
v_sub_f32 v156, v156, v238
)"
R"(v_sub_f32 v157, v157, v238
v_sub_f32 v158, v158, v238
v_sub_f32 v159, v159, v238
v_exp_f32 v240, -v238
s_nop 0
v_mul_f32 v4, v4, v240
v_mul_f32 v5, v5, v240
v_mul_f32 v6, v6, v240
v_mul_f32 v7, v7, v240
v_mul_f32 v12, v12, v240
v_mul_f32 v13, v13, v240
v_mul_f32 v14, v14, v240
v_mul_f32 v15, v15, v240
v_mul_f32 v20, v20, v240
v_mul_f32 v21, v21, v240
v_mul_f32 v22, v22, v240
v_mul_f32 v23, v23, v240
v_mul_f32 v28, v28, v240
v_mul_f32 v29, v29, v240
v_mul_f32 v30, v30, v240
v_mul_f32 v31, v31, v240
v_mul_f32 v36, v36, v240
v_mul_f32 v37, v37, v240
v_mul_f32 v38, v38, v240
)"
R"(v_mul_f32 v39, v39, v240
v_mul_f32 v44, v44, v240
v_mul_f32 v45, v45, v240
v_mul_f32 v46, v46, v240
v_mul_f32 v47, v47, v240
v_mul_f32 v52, v52, v240
v_mul_f32 v53, v53, v240
v_mul_f32 v54, v54, v240
v_mul_f32 v55, v55, v240
v_mul_f32 v60, v60, v240
v_mul_f32 v61, v61, v240
v_mul_f32 v62, v62, v240
v_mul_f32 v63, v63, v240
v_mul_f32 v68, v68, v240
v_mul_f32 v69, v69, v240
v_mul_f32 v70, v70, v240
v_mul_f32 v71, v71, v240
v_mul_f32 v76, v76, v240
v_mul_f32 v77, v77, v240
v_mul_f32 v78, v78, v240
v_mul_f32 v79, v79, v240
v_mul_f32 v84, v84, v240
v_mul_f32 v85, v85, v240
v_mul_f32 v86, v86, v240
)"
R"(v_mul_f32 v87, v87, v240
v_mul_f32 v92, v92, v240
v_mul_f32 v93, v93, v240
v_mul_f32 v94, v94, v240
v_mul_f32 v95, v95, v240
v_accvgpr_read_b32 v239, a136
v_mul_f32 v239, v239, v240
v_accvgpr_write_b32 a136, v239
s_nop 1
s_branch BACK4_%=
RARE5_%=:
s_nop 7
v_max3_f32 v238, v96, v97, v98
v_max3_f32 v238, v238, v99, v104
v_max3_f32 v238, v238, v105, v106
v_max3_f32 v238, v238, v107, v112
v_max3_f32 v238, v238, v113, v114
v_max3_f32 v238, v238, v115, v120
v_max3_f32 v238, v238, v121, v122
v_max_f32 v238, v238, v123
v_mov_b32 v239, v238
s_nop 1
v_permlane16_swap_b32 v238, v239
v_max_f32 v238, v238, v239
)"
R"(v_mov_b32 v239, v238
s_nop 1
v_permlane32_swap_b32 v238, v239
v_max_f32 v238, v238, v239
v_max_f32 v238, 0, v238
v_sub_f32 v176, v176, v238
v_sub_f32 v177, v177, v238
v_sub_f32 v178, v178, v238
v_sub_f32 v179, v179, v238
v_sub_f32 v96, v96, v238
v_sub_f32 v97, v97, v238
v_sub_f32 v98, v98, v238
v_sub_f32 v99, v99, v238
v_sub_f32 v104, v104, v238
v_sub_f32 v105, v105, v238
v_sub_f32 v106, v106, v238
v_sub_f32 v107, v107, v238
v_sub_f32 v112, v112, v238
v_sub_f32 v113, v113, v238
v_sub_f32 v114, v114, v238
v_sub_f32 v115, v115, v238
v_sub_f32 v120, v120, v238
v_sub_f32 v121, v121, v238
v_sub_f32 v122, v122, v238
)"
R"(v_sub_f32 v123, v123, v238
v_exp_f32 v240, -v238
s_nop 0
v_mul_f32 v0, v0, v240
v_mul_f32 v1, v1, v240
v_mul_f32 v2, v2, v240
v_mul_f32 v3, v3, v240
v_mul_f32 v8, v8, v240
v_mul_f32 v9, v9, v240
v_mul_f32 v10, v10, v240
v_mul_f32 v11, v11, v240
v_mul_f32 v16, v16, v240
v_mul_f32 v17, v17, v240
v_mul_f32 v18, v18, v240
v_mul_f32 v19, v19, v240
v_mul_f32 v24, v24, v240
v_mul_f32 v25, v25, v240
v_mul_f32 v26, v26, v240
v_mul_f32 v27, v27, v240
v_mul_f32 v32, v32, v240
v_mul_f32 v33, v33, v240
v_mul_f32 v34, v34, v240
v_mul_f32 v35, v35, v240
v_mul_f32 v40, v40, v240
)"
R"(v_mul_f32 v41, v41, v240
v_mul_f32 v42, v42, v240
v_mul_f32 v43, v43, v240
v_mul_f32 v48, v48, v240
v_mul_f32 v49, v49, v240
v_mul_f32 v50, v50, v240
v_mul_f32 v51, v51, v240
v_mul_f32 v56, v56, v240
v_mul_f32 v57, v57, v240
v_mul_f32 v58, v58, v240
v_mul_f32 v59, v59, v240
v_mul_f32 v64, v64, v240
v_mul_f32 v65, v65, v240
v_mul_f32 v66, v66, v240
v_mul_f32 v67, v67, v240
v_mul_f32 v72, v72, v240
v_mul_f32 v73, v73, v240
v_mul_f32 v74, v74, v240
v_mul_f32 v75, v75, v240
v_mul_f32 v80, v80, v240
v_mul_f32 v81, v81, v240
v_mul_f32 v82, v82, v240
v_mul_f32 v83, v83, v240
v_mul_f32 v88, v88, v240
)"
R"(v_mul_f32 v89, v89, v240
v_mul_f32 v90, v90, v240
v_mul_f32 v91, v91, v240
v_accvgpr_read_b32 v239, a132
v_mul_f32 v239, v239, v240
v_accvgpr_write_b32 a132, v239
v_max3_f32 v238, v100, v101, v102
v_max3_f32 v238, v238, v103, v108
v_max3_f32 v238, v238, v109, v110
v_max3_f32 v238, v238, v111, v116
v_max3_f32 v238, v238, v117, v118
v_max3_f32 v238, v238, v119, v124
v_max3_f32 v238, v238, v125, v126
v_max_f32 v238, v238, v127
v_mov_b32 v239, v238
s_nop 1
v_permlane16_swap_b32 v238, v239
v_max_f32 v238, v238, v239
v_mov_b32 v239, v238
s_nop 1
v_permlane32_swap_b32 v238, v239
v_max_f32 v238, v238, v239
v_max_f32 v238, 0, v238
v_sub_f32 v180, v180, v238
)"
R"(v_sub_f32 v181, v181, v238
v_sub_f32 v182, v182, v238
v_sub_f32 v183, v183, v238
v_sub_f32 v100, v100, v238
v_sub_f32 v101, v101, v238
v_sub_f32 v102, v102, v238
v_sub_f32 v103, v103, v238
v_sub_f32 v108, v108, v238
v_sub_f32 v109, v109, v238
v_sub_f32 v110, v110, v238
v_sub_f32 v111, v111, v238
v_sub_f32 v116, v116, v238
v_sub_f32 v117, v117, v238
v_sub_f32 v118, v118, v238
v_sub_f32 v119, v119, v238
v_sub_f32 v124, v124, v238
v_sub_f32 v125, v125, v238
v_sub_f32 v126, v126, v238
v_sub_f32 v127, v127, v238
v_exp_f32 v240, -v238
s_nop 0
v_mul_f32 v4, v4, v240
v_mul_f32 v5, v5, v240
v_mul_f32 v6, v6, v240
)"
R"(v_mul_f32 v7, v7, v240
v_mul_f32 v12, v12, v240
v_mul_f32 v13, v13, v240
v_mul_f32 v14, v14, v240
v_mul_f32 v15, v15, v240
v_mul_f32 v20, v20, v240
v_mul_f32 v21, v21, v240
v_mul_f32 v22, v22, v240
v_mul_f32 v23, v23, v240
v_mul_f32 v28, v28, v240
v_mul_f32 v29, v29, v240
v_mul_f32 v30, v30, v240
v_mul_f32 v31, v31, v240
v_mul_f32 v36, v36, v240
v_mul_f32 v37, v37, v240
v_mul_f32 v38, v38, v240
v_mul_f32 v39, v39, v240
v_mul_f32 v44, v44, v240
v_mul_f32 v45, v45, v240
v_mul_f32 v46, v46, v240
v_mul_f32 v47, v47, v240
v_mul_f32 v52, v52, v240
v_mul_f32 v53, v53, v240
v_mul_f32 v54, v54, v240
)"
R"(v_mul_f32 v55, v55, v240
v_mul_f32 v60, v60, v240
v_mul_f32 v61, v61, v240
v_mul_f32 v62, v62, v240
v_mul_f32 v63, v63, v240
v_mul_f32 v68, v68, v240
v_mul_f32 v69, v69, v240
v_mul_f32 v70, v70, v240
v_mul_f32 v71, v71, v240
v_mul_f32 v76, v76, v240
v_mul_f32 v77, v77, v240
v_mul_f32 v78, v78, v240
v_mul_f32 v79, v79, v240
v_mul_f32 v84, v84, v240
v_mul_f32 v85, v85, v240
v_mul_f32 v86, v86, v240
v_mul_f32 v87, v87, v240
v_mul_f32 v92, v92, v240
v_mul_f32 v93, v93, v240
v_mul_f32 v94, v94, v240
v_mul_f32 v95, v95, v240
v_accvgpr_read_b32 v239, a136
v_mul_f32 v239, v239, v240
v_accvgpr_write_b32 a136, v239
)"
R"(s_nop 1
s_branch BACK5_%=
RARE6_%=:
s_nop 7
v_max3_f32 v238, v128, v129, v130
v_max3_f32 v238, v238, v131, v136
v_max3_f32 v238, v238, v137, v138
v_max3_f32 v238, v238, v139, v144
v_max3_f32 v238, v238, v145, v146
v_max3_f32 v238, v238, v147, v152
v_max3_f32 v238, v238, v153, v154
v_max_f32 v238, v238, v155
v_mov_b32 v239, v238
s_nop 1
v_permlane16_swap_b32 v238, v239
v_max_f32 v238, v238, v239
v_mov_b32 v239, v238
s_nop 1
v_permlane32_swap_b32 v238, v239
v_max_f32 v238, v238, v239
v_max_f32 v238, 0, v238
v_sub_f32 v176, v176, v238
v_sub_f32 v177, v177, v238
v_sub_f32 v178, v178, v238
)"
R"(v_sub_f32 v179, v179, v238
v_sub_f32 v128, v128, v238
v_sub_f32 v129, v129, v238
v_sub_f32 v130, v130, v238
v_sub_f32 v131, v131, v238
v_sub_f32 v136, v136, v238
v_sub_f32 v137, v137, v238
v_sub_f32 v138, v138, v238
v_sub_f32 v139, v139, v238
v_sub_f32 v144, v144, v238
v_sub_f32 v145, v145, v238
v_sub_f32 v146, v146, v238
v_sub_f32 v147, v147, v238
v_sub_f32 v152, v152, v238
v_sub_f32 v153, v153, v238
v_sub_f32 v154, v154, v238
v_sub_f32 v155, v155, v238
v_exp_f32 v240, -v238
s_nop 0
v_mul_f32 v0, v0, v240
v_mul_f32 v1, v1, v240
v_mul_f32 v2, v2, v240
v_mul_f32 v3, v3, v240
v_mul_f32 v8, v8, v240
)"
R"(v_mul_f32 v9, v9, v240
v_mul_f32 v10, v10, v240
v_mul_f32 v11, v11, v240
v_mul_f32 v16, v16, v240
v_mul_f32 v17, v17, v240
v_mul_f32 v18, v18, v240
v_mul_f32 v19, v19, v240
v_mul_f32 v24, v24, v240
v_mul_f32 v25, v25, v240
v_mul_f32 v26, v26, v240
v_mul_f32 v27, v27, v240
v_mul_f32 v32, v32, v240
v_mul_f32 v33, v33, v240
v_mul_f32 v34, v34, v240
v_mul_f32 v35, v35, v240
v_mul_f32 v40, v40, v240
v_mul_f32 v41, v41, v240
v_mul_f32 v42, v42, v240
v_mul_f32 v43, v43, v240
v_mul_f32 v48, v48, v240
v_mul_f32 v49, v49, v240
v_mul_f32 v50, v50, v240
v_mul_f32 v51, v51, v240
v_mul_f32 v56, v56, v240
)"
R"(v_mul_f32 v57, v57, v240
v_mul_f32 v58, v58, v240
v_mul_f32 v59, v59, v240
v_mul_f32 v64, v64, v240
v_mul_f32 v65, v65, v240
v_mul_f32 v66, v66, v240
v_mul_f32 v67, v67, v240
v_mul_f32 v72, v72, v240
v_mul_f32 v73, v73, v240
v_mul_f32 v74, v74, v240
v_mul_f32 v75, v75, v240
v_mul_f32 v80, v80, v240
v_mul_f32 v81, v81, v240
v_mul_f32 v82, v82, v240
v_mul_f32 v83, v83, v240
v_mul_f32 v88, v88, v240
v_mul_f32 v89, v89, v240
v_mul_f32 v90, v90, v240
v_mul_f32 v91, v91, v240
v_accvgpr_read_b32 v239, a132
v_mul_f32 v239, v239, v240
v_accvgpr_write_b32 a132, v239
v_max3_f32 v238, v132, v133, v134
v_max3_f32 v238, v238, v135, v140
)"
R"(v_max3_f32 v238, v238, v141, v142
v_max3_f32 v238, v238, v143, v148
v_max3_f32 v238, v238, v149, v150
v_max3_f32 v238, v238, v151, v156
v_max3_f32 v238, v238, v157, v158
v_max_f32 v238, v238, v159
v_mov_b32 v239, v238
s_nop 1
v_permlane16_swap_b32 v238, v239
v_max_f32 v238, v238, v239
v_mov_b32 v239, v238
s_nop 1
v_permlane32_swap_b32 v238, v239
v_max_f32 v238, v238, v239
v_max_f32 v238, 0, v238
v_sub_f32 v180, v180, v238
v_sub_f32 v181, v181, v238
v_sub_f32 v182, v182, v238
v_sub_f32 v183, v183, v238
v_sub_f32 v132, v132, v238
v_sub_f32 v133, v133, v238
v_sub_f32 v134, v134, v238
v_sub_f32 v135, v135, v238
v_sub_f32 v140, v140, v238
)"
R"(v_sub_f32 v141, v141, v238
v_sub_f32 v142, v142, v238
v_sub_f32 v143, v143, v238
v_sub_f32 v148, v148, v238
v_sub_f32 v149, v149, v238
v_sub_f32 v150, v150, v238
v_sub_f32 v151, v151, v238
v_sub_f32 v156, v156, v238
v_sub_f32 v157, v157, v238
v_sub_f32 v158, v158, v238
v_sub_f32 v159, v159, v238
v_exp_f32 v240, -v238
s_nop 0
v_mul_f32 v4, v4, v240
v_mul_f32 v5, v5, v240
v_mul_f32 v6, v6, v240
v_mul_f32 v7, v7, v240
v_mul_f32 v12, v12, v240
v_mul_f32 v13, v13, v240
v_mul_f32 v14, v14, v240
v_mul_f32 v15, v15, v240
v_mul_f32 v20, v20, v240
v_mul_f32 v21, v21, v240
v_mul_f32 v22, v22, v240
)"
R"(v_mul_f32 v23, v23, v240
v_mul_f32 v28, v28, v240
v_mul_f32 v29, v29, v240
v_mul_f32 v30, v30, v240
v_mul_f32 v31, v31, v240
v_mul_f32 v36, v36, v240
v_mul_f32 v37, v37, v240
v_mul_f32 v38, v38, v240
v_mul_f32 v39, v39, v240
v_mul_f32 v44, v44, v240
v_mul_f32 v45, v45, v240
v_mul_f32 v46, v46, v240
v_mul_f32 v47, v47, v240
v_mul_f32 v52, v52, v240
v_mul_f32 v53, v53, v240
v_mul_f32 v54, v54, v240
v_mul_f32 v55, v55, v240
v_mul_f32 v60, v60, v240
v_mul_f32 v61, v61, v240
v_mul_f32 v62, v62, v240
v_mul_f32 v63, v63, v240
v_mul_f32 v68, v68, v240
v_mul_f32 v69, v69, v240
v_mul_f32 v70, v70, v240
)"
R"(v_mul_f32 v71, v71, v240
v_mul_f32 v76, v76, v240
v_mul_f32 v77, v77, v240
v_mul_f32 v78, v78, v240
v_mul_f32 v79, v79, v240
v_mul_f32 v84, v84, v240
v_mul_f32 v85, v85, v240
v_mul_f32 v86, v86, v240
v_mul_f32 v87, v87, v240
v_mul_f32 v92, v92, v240
v_mul_f32 v93, v93, v240
v_mul_f32 v94, v94, v240
v_mul_f32 v95, v95, v240
v_accvgpr_read_b32 v239, a136
v_mul_f32 v239, v239, v240
v_accvgpr_write_b32 a136, v239
s_nop 1
s_branch BACK6_%=
RARE7_%=:
s_nop 7
v_max3_f32 v238, v96, v97, v98
v_max3_f32 v238, v238, v99, v104
v_max3_f32 v238, v238, v105, v106
v_max3_f32 v238, v238, v107, v112
)"
R"(v_max3_f32 v238, v238, v113, v114
v_max3_f32 v238, v238, v115, v120
v_max3_f32 v238, v238, v121, v122
v_max_f32 v238, v238, v123
v_mov_b32 v239, v238
s_nop 1
v_permlane16_swap_b32 v238, v239
v_max_f32 v238, v238, v239
v_mov_b32 v239, v238
s_nop 1
v_permlane32_swap_b32 v238, v239
v_max_f32 v238, v238, v239
v_max_f32 v238, 0, v238
v_sub_f32 v176, v176, v238
v_sub_f32 v177, v177, v238
v_sub_f32 v178, v178, v238
v_sub_f32 v179, v179, v238
v_sub_f32 v96, v96, v238
v_sub_f32 v97, v97, v238
v_sub_f32 v98, v98, v238
v_sub_f32 v99, v99, v238
v_sub_f32 v104, v104, v238
v_sub_f32 v105, v105, v238
v_sub_f32 v106, v106, v238
)"
R"(v_sub_f32 v107, v107, v238
v_sub_f32 v112, v112, v238
v_sub_f32 v113, v113, v238
v_sub_f32 v114, v114, v238
v_sub_f32 v115, v115, v238
v_sub_f32 v120, v120, v238
v_sub_f32 v121, v121, v238
v_sub_f32 v122, v122, v238
v_sub_f32 v123, v123, v238
v_exp_f32 v240, -v238
s_nop 0
v_mul_f32 v0, v0, v240
v_mul_f32 v1, v1, v240
v_mul_f32 v2, v2, v240
v_mul_f32 v3, v3, v240
v_mul_f32 v8, v8, v240
v_mul_f32 v9, v9, v240
v_mul_f32 v10, v10, v240
v_mul_f32 v11, v11, v240
v_mul_f32 v16, v16, v240
v_mul_f32 v17, v17, v240
v_mul_f32 v18, v18, v240
v_mul_f32 v19, v19, v240
v_mul_f32 v24, v24, v240
)"
R"(v_mul_f32 v25, v25, v240
v_mul_f32 v26, v26, v240
v_mul_f32 v27, v27, v240
v_mul_f32 v32, v32, v240
v_mul_f32 v33, v33, v240
v_mul_f32 v34, v34, v240
v_mul_f32 v35, v35, v240
v_mul_f32 v40, v40, v240
v_mul_f32 v41, v41, v240
v_mul_f32 v42, v42, v240
v_mul_f32 v43, v43, v240
v_mul_f32 v48, v48, v240
v_mul_f32 v49, v49, v240
v_mul_f32 v50, v50, v240
v_mul_f32 v51, v51, v240
v_mul_f32 v56, v56, v240
v_mul_f32 v57, v57, v240
v_mul_f32 v58, v58, v240
v_mul_f32 v59, v59, v240
v_mul_f32 v64, v64, v240
v_mul_f32 v65, v65, v240
v_mul_f32 v66, v66, v240
v_mul_f32 v67, v67, v240
v_mul_f32 v72, v72, v240
)"
R"(v_mul_f32 v73, v73, v240
v_mul_f32 v74, v74, v240
v_mul_f32 v75, v75, v240
v_mul_f32 v80, v80, v240
v_mul_f32 v81, v81, v240
v_mul_f32 v82, v82, v240
v_mul_f32 v83, v83, v240
v_mul_f32 v88, v88, v240
v_mul_f32 v89, v89, v240
v_mul_f32 v90, v90, v240
v_mul_f32 v91, v91, v240
v_accvgpr_read_b32 v239, a132
v_mul_f32 v239, v239, v240
v_accvgpr_write_b32 a132, v239
v_max3_f32 v238, v100, v101, v102
v_max3_f32 v238, v238, v103, v108
v_max3_f32 v238, v238, v109, v110
v_max3_f32 v238, v238, v111, v116
v_max3_f32 v238, v238, v117, v118
v_max3_f32 v238, v238, v119, v124
v_max3_f32 v238, v238, v125, v126
v_max_f32 v238, v238, v127
v_mov_b32 v239, v238
s_nop 1
)"
R"(v_permlane16_swap_b32 v238, v239
v_max_f32 v238, v238, v239
v_mov_b32 v239, v238
s_nop 1
v_permlane32_swap_b32 v238, v239
v_max_f32 v238, v238, v239
v_max_f32 v238, 0, v238
v_sub_f32 v180, v180, v238
v_sub_f32 v181, v181, v238
v_sub_f32 v182, v182, v238
v_sub_f32 v183, v183, v238
v_sub_f32 v100, v100, v238
v_sub_f32 v101, v101, v238
v_sub_f32 v102, v102, v238
v_sub_f32 v103, v103, v238
v_sub_f32 v108, v108, v238
v_sub_f32 v109, v109, v238
v_sub_f32 v110, v110, v238
v_sub_f32 v111, v111, v238
v_sub_f32 v116, v116, v238
v_sub_f32 v117, v117, v238
v_sub_f32 v118, v118, v238
v_sub_f32 v119, v119, v238
v_sub_f32 v124, v124, v238
)"
R"(v_sub_f32 v125, v125, v238
v_sub_f32 v126, v126, v238
v_sub_f32 v127, v127, v238
v_exp_f32 v240, -v238
s_nop 0
v_mul_f32 v4, v4, v240
v_mul_f32 v5, v5, v240
v_mul_f32 v6, v6, v240
v_mul_f32 v7, v7, v240
v_mul_f32 v12, v12, v240
v_mul_f32 v13, v13, v240
v_mul_f32 v14, v14, v240
v_mul_f32 v15, v15, v240
v_mul_f32 v20, v20, v240
v_mul_f32 v21, v21, v240
v_mul_f32 v22, v22, v240
v_mul_f32 v23, v23, v240
v_mul_f32 v28, v28, v240
v_mul_f32 v29, v29, v240
v_mul_f32 v30, v30, v240
v_mul_f32 v31, v31, v240
v_mul_f32 v36, v36, v240
v_mul_f32 v37, v37, v240
v_mul_f32 v38, v38, v240
)"
R"(v_mul_f32 v39, v39, v240
v_mul_f32 v44, v44, v240
v_mul_f32 v45, v45, v240
v_mul_f32 v46, v46, v240
v_mul_f32 v47, v47, v240
v_mul_f32 v52, v52, v240
v_mul_f32 v53, v53, v240
v_mul_f32 v54, v54, v240
v_mul_f32 v55, v55, v240
v_mul_f32 v60, v60, v240
v_mul_f32 v61, v61, v240
v_mul_f32 v62, v62, v240
v_mul_f32 v63, v63, v240
v_mul_f32 v68, v68, v240
v_mul_f32 v69, v69, v240
v_mul_f32 v70, v70, v240
v_mul_f32 v71, v71, v240
v_mul_f32 v76, v76, v240
v_mul_f32 v77, v77, v240
v_mul_f32 v78, v78, v240
v_mul_f32 v79, v79, v240
v_mul_f32 v84, v84, v240
v_mul_f32 v85, v85, v240
v_mul_f32 v86, v86, v240
)"
R"(v_mul_f32 v87, v87, v240
v_mul_f32 v92, v92, v240
v_mul_f32 v93, v93, v240
v_mul_f32 v94, v94, v240
v_mul_f32 v95, v95, v240
v_accvgpr_read_b32 v239, a136
v_mul_f32 v239, v239, v240
v_accvgpr_write_b32 a136, v239
s_nop 1
s_branch BACK7_%=
RARE8_%=:
s_nop 7
v_max3_f32 v238, v128, v129, v130
v_max3_f32 v238, v238, v131, v136
v_max3_f32 v238, v238, v137, v138
v_max3_f32 v238, v238, v139, v144
v_max3_f32 v238, v238, v145, v146
v_max3_f32 v238, v238, v147, v152
v_max3_f32 v238, v238, v153, v154
v_max_f32 v238, v238, v155
v_mov_b32 v239, v238
s_nop 1
v_permlane16_swap_b32 v238, v239
v_max_f32 v238, v238, v239
)"
R"(v_mov_b32 v239, v238
s_nop 1
v_permlane32_swap_b32 v238, v239
v_max_f32 v238, v238, v239
v_max_f32 v238, 0, v238
v_sub_f32 v176, v176, v238
v_sub_f32 v177, v177, v238
v_sub_f32 v178, v178, v238
v_sub_f32 v179, v179, v238
v_sub_f32 v128, v128, v238
v_sub_f32 v129, v129, v238
v_sub_f32 v130, v130, v238
v_sub_f32 v131, v131, v238
v_sub_f32 v136, v136, v238
v_sub_f32 v137, v137, v238
v_sub_f32 v138, v138, v238
v_sub_f32 v139, v139, v238
v_sub_f32 v144, v144, v238
v_sub_f32 v145, v145, v238
v_sub_f32 v146, v146, v238
v_sub_f32 v147, v147, v238
v_sub_f32 v152, v152, v238
v_sub_f32 v153, v153, v238
v_sub_f32 v154, v154, v238
)"
R"(v_sub_f32 v155, v155, v238
v_exp_f32 v240, -v238
s_nop 0
v_mul_f32 v0, v0, v240
v_mul_f32 v1, v1, v240
v_mul_f32 v2, v2, v240
v_mul_f32 v3, v3, v240
v_mul_f32 v8, v8, v240
v_mul_f32 v9, v9, v240
v_mul_f32 v10, v10, v240
v_mul_f32 v11, v11, v240
v_mul_f32 v16, v16, v240
v_mul_f32 v17, v17, v240
v_mul_f32 v18, v18, v240
v_mul_f32 v19, v19, v240
v_mul_f32 v24, v24, v240
v_mul_f32 v25, v25, v240
v_mul_f32 v26, v26, v240
v_mul_f32 v27, v27, v240
v_mul_f32 v32, v32, v240
v_mul_f32 v33, v33, v240
v_mul_f32 v34, v34, v240
v_mul_f32 v35, v35, v240
v_mul_f32 v40, v40, v240
)"
R"(v_mul_f32 v41, v41, v240
v_mul_f32 v42, v42, v240
v_mul_f32 v43, v43, v240
v_mul_f32 v48, v48, v240
v_mul_f32 v49, v49, v240
v_mul_f32 v50, v50, v240
v_mul_f32 v51, v51, v240
v_mul_f32 v56, v56, v240
v_mul_f32 v57, v57, v240
v_mul_f32 v58, v58, v240
v_mul_f32 v59, v59, v240
v_mul_f32 v64, v64, v240
v_mul_f32 v65, v65, v240
v_mul_f32 v66, v66, v240
v_mul_f32 v67, v67, v240
v_mul_f32 v72, v72, v240
v_mul_f32 v73, v73, v240
v_mul_f32 v74, v74, v240
v_mul_f32 v75, v75, v240
v_mul_f32 v80, v80, v240
v_mul_f32 v81, v81, v240
v_mul_f32 v82, v82, v240
v_mul_f32 v83, v83, v240
v_mul_f32 v88, v88, v240
)"
R"(v_mul_f32 v89, v89, v240
v_mul_f32 v90, v90, v240
v_mul_f32 v91, v91, v240
v_accvgpr_read_b32 v239, a132
v_mul_f32 v239, v239, v240
v_accvgpr_write_b32 a132, v239
v_max3_f32 v238, v132, v133, v134
v_max3_f32 v238, v238, v135, v140
v_max3_f32 v238, v238, v141, v142
v_max3_f32 v238, v238, v143, v148
v_max3_f32 v238, v238, v149, v150
v_max3_f32 v238, v238, v151, v156
v_max3_f32 v238, v238, v157, v158
v_max_f32 v238, v238, v159
v_mov_b32 v239, v238
s_nop 1
v_permlane16_swap_b32 v238, v239
v_max_f32 v238, v238, v239
v_mov_b32 v239, v238
s_nop 1
v_permlane32_swap_b32 v238, v239
v_max_f32 v238, v238, v239
v_max_f32 v238, 0, v238
v_sub_f32 v180, v180, v238
)"
R"(v_sub_f32 v181, v181, v238
v_sub_f32 v182, v182, v238
v_sub_f32 v183, v183, v238
v_sub_f32 v132, v132, v238
v_sub_f32 v133, v133, v238
v_sub_f32 v134, v134, v238
v_sub_f32 v135, v135, v238
v_sub_f32 v140, v140, v238
v_sub_f32 v141, v141, v238
v_sub_f32 v142, v142, v238
v_sub_f32 v143, v143, v238
v_sub_f32 v148, v148, v238
v_sub_f32 v149, v149, v238
v_sub_f32 v150, v150, v238
v_sub_f32 v151, v151, v238
v_sub_f32 v156, v156, v238
v_sub_f32 v157, v157, v238
v_sub_f32 v158, v158, v238
v_sub_f32 v159, v159, v238
v_exp_f32 v240, -v238
s_nop 0
v_mul_f32 v4, v4, v240
v_mul_f32 v5, v5, v240
v_mul_f32 v6, v6, v240
)"
R"(v_mul_f32 v7, v7, v240
v_mul_f32 v12, v12, v240
v_mul_f32 v13, v13, v240
v_mul_f32 v14, v14, v240
v_mul_f32 v15, v15, v240
v_mul_f32 v20, v20, v240
v_mul_f32 v21, v21, v240
v_mul_f32 v22, v22, v240
v_mul_f32 v23, v23, v240
v_mul_f32 v28, v28, v240
v_mul_f32 v29, v29, v240
v_mul_f32 v30, v30, v240
v_mul_f32 v31, v31, v240
v_mul_f32 v36, v36, v240
v_mul_f32 v37, v37, v240
v_mul_f32 v38, v38, v240
v_mul_f32 v39, v39, v240
v_mul_f32 v44, v44, v240
v_mul_f32 v45, v45, v240
v_mul_f32 v46, v46, v240
v_mul_f32 v47, v47, v240
v_mul_f32 v52, v52, v240
v_mul_f32 v53, v53, v240
v_mul_f32 v54, v54, v240
)"
R"(v_mul_f32 v55, v55, v240
v_mul_f32 v60, v60, v240
v_mul_f32 v61, v61, v240
v_mul_f32 v62, v62, v240
v_mul_f32 v63, v63, v240
v_mul_f32 v68, v68, v240
v_mul_f32 v69, v69, v240
v_mul_f32 v70, v70, v240
v_mul_f32 v71, v71, v240
v_mul_f32 v76, v76, v240
v_mul_f32 v77, v77, v240
v_mul_f32 v78, v78, v240
v_mul_f32 v79, v79, v240
v_mul_f32 v84, v84, v240
v_mul_f32 v85, v85, v240
v_mul_f32 v86, v86, v240
v_mul_f32 v87, v87, v240
v_mul_f32 v92, v92, v240
v_mul_f32 v93, v93, v240
v_mul_f32 v94, v94, v240
v_mul_f32 v95, v95, v240
v_accvgpr_read_b32 v239, a136
v_mul_f32 v239, v239, v240
v_accvgpr_write_b32 a136, v239
)"
R"(s_nop 1
s_branch BACK8_%=
RARE9_%=:
s_nop 7
v_max3_f32 v238, v96, v97, v98
v_max3_f32 v238, v238, v99, v104
v_max3_f32 v238, v238, v105, v106
v_max3_f32 v238, v238, v107, v112
v_max3_f32 v238, v238, v113, v114
v_max3_f32 v238, v238, v115, v120
v_max3_f32 v238, v238, v121, v122
v_max_f32 v238, v238, v123
v_mov_b32 v239, v238
s_nop 1
v_permlane16_swap_b32 v238, v239
v_max_f32 v238, v238, v239
v_mov_b32 v239, v238
s_nop 1
v_permlane32_swap_b32 v238, v239
v_max_f32 v238, v238, v239
v_max_f32 v238, 0, v238
v_sub_f32 v176, v176, v238
v_sub_f32 v177, v177, v238
v_sub_f32 v178, v178, v238
)"
R"(v_sub_f32 v179, v179, v238
v_sub_f32 v96, v96, v238
v_sub_f32 v97, v97, v238
v_sub_f32 v98, v98, v238
v_sub_f32 v99, v99, v238
v_sub_f32 v104, v104, v238
v_sub_f32 v105, v105, v238
v_sub_f32 v106, v106, v238
v_sub_f32 v107, v107, v238
v_sub_f32 v112, v112, v238
v_sub_f32 v113, v113, v238
v_sub_f32 v114, v114, v238
v_sub_f32 v115, v115, v238
v_sub_f32 v120, v120, v238
v_sub_f32 v121, v121, v238
v_sub_f32 v122, v122, v238
v_sub_f32 v123, v123, v238
v_max3_f32 v238, v100, v101, v102
v_max3_f32 v238, v238, v103, v108
v_max3_f32 v238, v238, v109, v110
v_max3_f32 v238, v238, v111, v116
v_max3_f32 v238, v238, v117, v118
v_max3_f32 v238, v238, v119, v124
v_max3_f32 v238, v238, v125, v126
)"
R"(v_max_f32 v238, v238, v127
v_mov_b32 v239, v238
s_nop 1
v_permlane16_swap_b32 v238, v239
v_max_f32 v238, v238, v239
v_mov_b32 v239, v238
s_nop 1
v_permlane32_swap_b32 v238, v239
v_max_f32 v238, v238, v239
v_max_f32 v238, 0, v238
v_sub_f32 v180, v180, v238
v_sub_f32 v181, v181, v238
v_sub_f32 v182, v182, v238
v_sub_f32 v183, v183, v238
v_sub_f32 v100, v100, v238
v_sub_f32 v101, v101, v238
v_sub_f32 v102, v102, v238
v_sub_f32 v103, v103, v238
v_sub_f32 v108, v108, v238
v_sub_f32 v109, v109, v238
v_sub_f32 v110, v110, v238
v_sub_f32 v111, v111, v238
v_sub_f32 v116, v116, v238
v_sub_f32 v117, v117, v238
)"
R"(v_sub_f32 v118, v118, v238
v_sub_f32 v119, v119, v238
v_sub_f32 v124, v124, v238
v_sub_f32 v125, v125, v238
v_sub_f32 v126, v126, v238
v_sub_f32 v127, v127, v238
s_nop 1
s_branch BACK9_%=
RARE10_%=:
s_nop 7
v_max3_f32 v238, v128, v129, v130
v_max3_f32 v238, v238, v131, v136
v_max3_f32 v238, v238, v137, v138
v_max3_f32 v238, v238, v139, v144
v_max3_f32 v238, v238, v145, v146
v_max3_f32 v238, v238, v147, v152
v_max3_f32 v238, v238, v153, v154
v_max_f32 v238, v238, v155
v_mov_b32 v239, v238
s_nop 1
v_permlane16_swap_b32 v238, v239
v_max_f32 v238, v238, v239
v_mov_b32 v239, v238
s_nop 1
)"
R"(v_permlane32_swap_b32 v238, v239
v_max_f32 v238, v238, v239
v_max_f32 v238, 0, v238
v_sub_f32 v176, v176, v238
v_sub_f32 v177, v177, v238
v_sub_f32 v178, v178, v238
v_sub_f32 v179, v179, v238
v_sub_f32 v128, v128, v238
v_sub_f32 v129, v129, v238
v_sub_f32 v130, v130, v238
v_sub_f32 v131, v131, v238
v_sub_f32 v136, v136, v238
v_sub_f32 v137, v137, v238
v_sub_f32 v138, v138, v238
v_sub_f32 v139, v139, v238
v_sub_f32 v144, v144, v238
v_sub_f32 v145, v145, v238
v_sub_f32 v146, v146, v238
v_sub_f32 v147, v147, v238
v_sub_f32 v152, v152, v238
v_sub_f32 v153, v153, v238
v_sub_f32 v154, v154, v238
v_sub_f32 v155, v155, v238
v_exp_f32 v240, -v238
)"
R"(s_nop 0
v_mul_f32 v0, v0, v240
v_mul_f32 v1, v1, v240
v_mul_f32 v2, v2, v240
v_mul_f32 v3, v3, v240
v_mul_f32 v8, v8, v240
v_mul_f32 v9, v9, v240
v_mul_f32 v10, v10, v240
v_mul_f32 v11, v11, v240
v_mul_f32 v16, v16, v240
v_mul_f32 v17, v17, v240
v_mul_f32 v18, v18, v240
v_mul_f32 v19, v19, v240
v_mul_f32 v24, v24, v240
v_mul_f32 v25, v25, v240
v_mul_f32 v26, v26, v240
v_mul_f32 v27, v27, v240
v_mul_f32 v32, v32, v240
v_mul_f32 v33, v33, v240
v_mul_f32 v34, v34, v240
v_mul_f32 v35, v35, v240
v_mul_f32 v40, v40, v240
v_mul_f32 v41, v41, v240
v_mul_f32 v42, v42, v240
)"
R"(v_mul_f32 v43, v43, v240
v_mul_f32 v48, v48, v240
v_mul_f32 v49, v49, v240
v_mul_f32 v50, v50, v240
v_mul_f32 v51, v51, v240
v_mul_f32 v56, v56, v240
v_mul_f32 v57, v57, v240
v_mul_f32 v58, v58, v240
v_mul_f32 v59, v59, v240
v_mul_f32 v64, v64, v240
v_mul_f32 v65, v65, v240
v_mul_f32 v66, v66, v240
v_mul_f32 v67, v67, v240
v_mul_f32 v72, v72, v240
v_mul_f32 v73, v73, v240
v_mul_f32 v74, v74, v240
v_mul_f32 v75, v75, v240
v_mul_f32 v80, v80, v240
v_mul_f32 v81, v81, v240
v_mul_f32 v82, v82, v240
v_mul_f32 v83, v83, v240
v_mul_f32 v88, v88, v240
v_mul_f32 v89, v89, v240
v_mul_f32 v90, v90, v240
)"
R"(v_mul_f32 v91, v91, v240
v_accvgpr_read_b32 v239, a132
v_mul_f32 v239, v239, v240
v_accvgpr_write_b32 a132, v239
v_max3_f32 v238, v132, v133, v134
v_max3_f32 v238, v238, v135, v140
v_max3_f32 v238, v238, v141, v142
v_max3_f32 v238, v238, v143, v148
v_max3_f32 v238, v238, v149, v150
v_max3_f32 v238, v238, v151, v156
v_max3_f32 v238, v238, v157, v158
v_max_f32 v238, v238, v159
v_mov_b32 v239, v238
s_nop 1
v_permlane16_swap_b32 v238, v239
v_max_f32 v238, v238, v239
v_mov_b32 v239, v238
s_nop 1
v_permlane32_swap_b32 v238, v239
v_max_f32 v238, v238, v239
v_max_f32 v238, 0, v238
v_sub_f32 v180, v180, v238
v_sub_f32 v181, v181, v238
v_sub_f32 v182, v182, v238
)"
R"(v_sub_f32 v183, v183, v238
v_sub_f32 v132, v132, v238
v_sub_f32 v133, v133, v238
v_sub_f32 v134, v134, v238
v_sub_f32 v135, v135, v238
v_sub_f32 v140, v140, v238
v_sub_f32 v141, v141, v238
v_sub_f32 v142, v142, v238
v_sub_f32 v143, v143, v238
v_sub_f32 v148, v148, v238
v_sub_f32 v149, v149, v238
v_sub_f32 v150, v150, v238
v_sub_f32 v151, v151, v238
v_sub_f32 v156, v156, v238
v_sub_f32 v157, v157, v238
v_sub_f32 v158, v158, v238
v_sub_f32 v159, v159, v238
v_exp_f32 v240, -v238
s_nop 0
v_mul_f32 v4, v4, v240
v_mul_f32 v5, v5, v240
v_mul_f32 v6, v6, v240
v_mul_f32 v7, v7, v240
v_mul_f32 v12, v12, v240
)"
R"(v_mul_f32 v13, v13, v240
v_mul_f32 v14, v14, v240
v_mul_f32 v15, v15, v240
v_mul_f32 v20, v20, v240
v_mul_f32 v21, v21, v240
v_mul_f32 v22, v22, v240
v_mul_f32 v23, v23, v240
v_mul_f32 v28, v28, v240
v_mul_f32 v29, v29, v240
v_mul_f32 v30, v30, v240
v_mul_f32 v31, v31, v240
v_mul_f32 v36, v36, v240
v_mul_f32 v37, v37, v240
v_mul_f32 v38, v38, v240
v_mul_f32 v39, v39, v240
v_mul_f32 v44, v44, v240
v_mul_f32 v45, v45, v240
v_mul_f32 v46, v46, v240
v_mul_f32 v47, v47, v240
v_mul_f32 v52, v52, v240
v_mul_f32 v53, v53, v240
v_mul_f32 v54, v54, v240
v_mul_f32 v55, v55, v240
v_mul_f32 v60, v60, v240
)"
R"(v_mul_f32 v61, v61, v240
v_mul_f32 v62, v62, v240
v_mul_f32 v63, v63, v240
v_mul_f32 v68, v68, v240
v_mul_f32 v69, v69, v240
v_mul_f32 v70, v70, v240
v_mul_f32 v71, v71, v240
v_mul_f32 v76, v76, v240
v_mul_f32 v77, v77, v240
v_mul_f32 v78, v78, v240
v_mul_f32 v79, v79, v240
v_mul_f32 v84, v84, v240
v_mul_f32 v85, v85, v240
v_mul_f32 v86, v86, v240
v_mul_f32 v87, v87, v240
v_mul_f32 v92, v92, v240
v_mul_f32 v93, v93, v240
v_mul_f32 v94, v94, v240
v_mul_f32 v95, v95, v240
v_accvgpr_read_b32 v239, a136
v_mul_f32 v239, v239, v240
v_accvgpr_write_b32 a136, v239
s_nop 1
s_branch BACK10_%=
)"
R"(RARE11_%=:
s_nop 7
v_max3_f32 v238, v96, v97, v98
v_max3_f32 v238, v238, v99, v104
v_max3_f32 v238, v238, v105, v106
v_max3_f32 v238, v238, v107, v112
v_max3_f32 v238, v238, v113, v114
v_max3_f32 v238, v238, v115, v120
v_max3_f32 v238, v238, v121, v122
v_max_f32 v238, v238, v123
v_mov_b32 v239, v238
s_nop 1
v_permlane16_swap_b32 v238, v239
v_max_f32 v238, v238, v239
v_mov_b32 v239, v238
s_nop 1
v_permlane32_swap_b32 v238, v239
v_max_f32 v238, v238, v239
v_max_f32 v238, 0, v238
v_sub_f32 v176, v176, v238
v_sub_f32 v177, v177, v238
v_sub_f32 v178, v178, v238
v_sub_f32 v179, v179, v238
v_sub_f32 v96, v96, v238
)"
R"(v_sub_f32 v97, v97, v238
v_sub_f32 v98, v98, v238
v_sub_f32 v99, v99, v238
v_sub_f32 v104, v104, v238
v_sub_f32 v105, v105, v238
v_sub_f32 v106, v106, v238
v_sub_f32 v107, v107, v238
v_sub_f32 v112, v112, v238
v_sub_f32 v113, v113, v238
v_sub_f32 v114, v114, v238
v_sub_f32 v115, v115, v238
v_sub_f32 v120, v120, v238
v_sub_f32 v121, v121, v238
v_sub_f32 v122, v122, v238
v_sub_f32 v123, v123, v238
v_exp_f32 v240, -v238
s_nop 0
v_mul_f32 v0, v0, v240
v_mul_f32 v1, v1, v240
v_mul_f32 v2, v2, v240
v_mul_f32 v3, v3, v240
v_mul_f32 v8, v8, v240
v_mul_f32 v9, v9, v240
v_mul_f32 v10, v10, v240
)"
R"(v_mul_f32 v11, v11, v240
v_mul_f32 v16, v16, v240
v_mul_f32 v17, v17, v240
v_mul_f32 v18, v18, v240
v_mul_f32 v19, v19, v240
v_mul_f32 v24, v24, v240
v_mul_f32 v25, v25, v240
v_mul_f32 v26, v26, v240
v_mul_f32 v27, v27, v240
v_mul_f32 v32, v32, v240
v_mul_f32 v33, v33, v240
v_mul_f32 v34, v34, v240
v_mul_f32 v35, v35, v240
v_mul_f32 v40, v40, v240
v_mul_f32 v41, v41, v240
v_mul_f32 v42, v42, v240
v_mul_f32 v43, v43, v240
v_mul_f32 v48, v48, v240
v_mul_f32 v49, v49, v240
v_mul_f32 v50, v50, v240
v_mul_f32 v51, v51, v240
v_mul_f32 v56, v56, v240
v_mul_f32 v57, v57, v240
v_mul_f32 v58, v58, v240
)"
R"(v_mul_f32 v59, v59, v240
v_mul_f32 v64, v64, v240
v_mul_f32 v65, v65, v240
v_mul_f32 v66, v66, v240
v_mul_f32 v67, v67, v240
v_mul_f32 v72, v72, v240
v_mul_f32 v73, v73, v240
v_mul_f32 v74, v74, v240
v_mul_f32 v75, v75, v240
v_mul_f32 v80, v80, v240
v_mul_f32 v81, v81, v240
v_mul_f32 v82, v82, v240
v_mul_f32 v83, v83, v240
v_mul_f32 v88, v88, v240
v_mul_f32 v89, v89, v240
v_mul_f32 v90, v90, v240
v_mul_f32 v91, v91, v240
v_accvgpr_read_b32 v239, a132
v_mul_f32 v239, v239, v240
v_accvgpr_write_b32 a132, v239
v_max3_f32 v238, v100, v101, v102
v_max3_f32 v238, v238, v103, v108
v_max3_f32 v238, v238, v109, v110
v_max3_f32 v238, v238, v111, v116
)"
R"(v_max3_f32 v238, v238, v117, v118
v_max3_f32 v238, v238, v119, v124
v_max3_f32 v238, v238, v125, v126
v_max_f32 v238, v238, v127
v_mov_b32 v239, v238
s_nop 1
v_permlane16_swap_b32 v238, v239
v_max_f32 v238, v238, v239
v_mov_b32 v239, v238
s_nop 1
v_permlane32_swap_b32 v238, v239
v_max_f32 v238, v238, v239
v_max_f32 v238, 0, v238
v_sub_f32 v180, v180, v238
v_sub_f32 v181, v181, v238
v_sub_f32 v182, v182, v238
v_sub_f32 v183, v183, v238
v_sub_f32 v100, v100, v238
v_sub_f32 v101, v101, v238
v_sub_f32 v102, v102, v238
v_sub_f32 v103, v103, v238
v_sub_f32 v108, v108, v238
v_sub_f32 v109, v109, v238
v_sub_f32 v110, v110, v238
)"
R"(v_sub_f32 v111, v111, v238
v_sub_f32 v116, v116, v238
v_sub_f32 v117, v117, v238
v_sub_f32 v118, v118, v238
v_sub_f32 v119, v119, v238
v_sub_f32 v124, v124, v238
v_sub_f32 v125, v125, v238
v_sub_f32 v126, v126, v238
v_sub_f32 v127, v127, v238
v_exp_f32 v240, -v238
s_nop 0
v_mul_f32 v4, v4, v240
v_mul_f32 v5, v5, v240
v_mul_f32 v6, v6, v240
v_mul_f32 v7, v7, v240
v_mul_f32 v12, v12, v240
v_mul_f32 v13, v13, v240
v_mul_f32 v14, v14, v240
v_mul_f32 v15, v15, v240
v_mul_f32 v20, v20, v240
v_mul_f32 v21, v21, v240
v_mul_f32 v22, v22, v240
v_mul_f32 v23, v23, v240
v_mul_f32 v28, v28, v240
)"
R"(v_mul_f32 v29, v29, v240
v_mul_f32 v30, v30, v240
v_mul_f32 v31, v31, v240
v_mul_f32 v36, v36, v240
v_mul_f32 v37, v37, v240
v_mul_f32 v38, v38, v240
v_mul_f32 v39, v39, v240
v_mul_f32 v44, v44, v240
v_mul_f32 v45, v45, v240
v_mul_f32 v46, v46, v240
v_mul_f32 v47, v47, v240
v_mul_f32 v52, v52, v240
v_mul_f32 v53, v53, v240
v_mul_f32 v54, v54, v240
v_mul_f32 v55, v55, v240
v_mul_f32 v60, v60, v240
v_mul_f32 v61, v61, v240
v_mul_f32 v62, v62, v240
v_mul_f32 v63, v63, v240
v_mul_f32 v68, v68, v240
v_mul_f32 v69, v69, v240
v_mul_f32 v70, v70, v240
v_mul_f32 v71, v71, v240
v_mul_f32 v76, v76, v240
)"
R"(v_mul_f32 v77, v77, v240
v_mul_f32 v78, v78, v240
v_mul_f32 v79, v79, v240
v_mul_f32 v84, v84, v240
v_mul_f32 v85, v85, v240
v_mul_f32 v86, v86, v240
v_mul_f32 v87, v87, v240
v_mul_f32 v92, v92, v240
v_mul_f32 v93, v93, v240
v_mul_f32 v94, v94, v240
v_mul_f32 v95, v95, v240
v_accvgpr_read_b32 v239, a136
v_mul_f32 v239, v239, v240
v_accvgpr_write_b32 a136, v239
s_nop 1
s_branch BACK11_%=
RARE12_%=:
s_nop 7
v_max3_f32 v238, v128, v129, v130
v_max3_f32 v238, v238, v131, v136
v_max3_f32 v238, v238, v137, v138
v_max3_f32 v238, v238, v139, v144
v_max3_f32 v238, v238, v145, v146
v_max3_f32 v238, v238, v147, v152
)"
R"(v_max3_f32 v238, v238, v153, v154
v_max_f32 v238, v238, v155
v_mov_b32 v239, v238
s_nop 1
v_permlane16_swap_b32 v238, v239
v_max_f32 v238, v238, v239
v_mov_b32 v239, v238
s_nop 1
v_permlane32_swap_b32 v238, v239
v_max_f32 v238, v238, v239
v_max_f32 v238, 0, v238
v_sub_f32 v176, v176, v238
v_sub_f32 v177, v177, v238
v_sub_f32 v178, v178, v238
v_sub_f32 v179, v179, v238
v_sub_f32 v128, v128, v238
v_sub_f32 v129, v129, v238
v_sub_f32 v130, v130, v238
v_sub_f32 v131, v131, v238
v_sub_f32 v136, v136, v238
v_sub_f32 v137, v137, v238
v_sub_f32 v138, v138, v238
v_sub_f32 v139, v139, v238
v_sub_f32 v144, v144, v238
)"
R"(v_sub_f32 v145, v145, v238
v_sub_f32 v146, v146, v238
v_sub_f32 v147, v147, v238
v_sub_f32 v152, v152, v238
v_sub_f32 v153, v153, v238
v_sub_f32 v154, v154, v238
v_sub_f32 v155, v155, v238
v_exp_f32 v240, -v238
s_nop 0
v_mul_f32 v0, v0, v240
v_mul_f32 v1, v1, v240
v_mul_f32 v2, v2, v240
v_mul_f32 v3, v3, v240
v_mul_f32 v8, v8, v240
v_mul_f32 v9, v9, v240
v_mul_f32 v10, v10, v240
v_mul_f32 v11, v11, v240
v_mul_f32 v16, v16, v240
v_mul_f32 v17, v17, v240
v_mul_f32 v18, v18, v240
v_mul_f32 v19, v19, v240
v_mul_f32 v24, v24, v240
v_mul_f32 v25, v25, v240
v_mul_f32 v26, v26, v240
)"
R"(v_mul_f32 v27, v27, v240
v_mul_f32 v32, v32, v240
v_mul_f32 v33, v33, v240
v_mul_f32 v34, v34, v240
v_mul_f32 v35, v35, v240
v_mul_f32 v40, v40, v240
v_mul_f32 v41, v41, v240
v_mul_f32 v42, v42, v240
v_mul_f32 v43, v43, v240
v_mul_f32 v48, v48, v240
v_mul_f32 v49, v49, v240
v_mul_f32 v50, v50, v240
v_mul_f32 v51, v51, v240
v_mul_f32 v56, v56, v240
v_mul_f32 v57, v57, v240
v_mul_f32 v58, v58, v240
v_mul_f32 v59, v59, v240
v_mul_f32 v64, v64, v240
v_mul_f32 v65, v65, v240
v_mul_f32 v66, v66, v240
v_mul_f32 v67, v67, v240
v_mul_f32 v72, v72, v240
v_mul_f32 v73, v73, v240
v_mul_f32 v74, v74, v240
)"
R"(v_mul_f32 v75, v75, v240
v_mul_f32 v80, v80, v240
v_mul_f32 v81, v81, v240
v_mul_f32 v82, v82, v240
v_mul_f32 v83, v83, v240
v_mul_f32 v88, v88, v240
v_mul_f32 v89, v89, v240
v_mul_f32 v90, v90, v240
v_mul_f32 v91, v91, v240
v_accvgpr_read_b32 v239, a132
v_mul_f32 v239, v239, v240
v_accvgpr_write_b32 a132, v239
v_max3_f32 v238, v132, v133, v134
v_max3_f32 v238, v238, v135, v140
v_max3_f32 v238, v238, v141, v142
v_max3_f32 v238, v238, v143, v148
v_max3_f32 v238, v238, v149, v150
v_max3_f32 v238, v238, v151, v156
v_max3_f32 v238, v238, v157, v158
v_max_f32 v238, v238, v159
v_mov_b32 v239, v238
s_nop 1
v_permlane16_swap_b32 v238, v239
v_max_f32 v238, v238, v239
)"
R"(v_mov_b32 v239, v238
s_nop 1
v_permlane32_swap_b32 v238, v239
v_max_f32 v238, v238, v239
v_max_f32 v238, 0, v238
v_sub_f32 v180, v180, v238
v_sub_f32 v181, v181, v238
v_sub_f32 v182, v182, v238
v_sub_f32 v183, v183, v238
v_sub_f32 v132, v132, v238
v_sub_f32 v133, v133, v238
v_sub_f32 v134, v134, v238
v_sub_f32 v135, v135, v238
v_sub_f32 v140, v140, v238
v_sub_f32 v141, v141, v238
v_sub_f32 v142, v142, v238
v_sub_f32 v143, v143, v238
v_sub_f32 v148, v148, v238
v_sub_f32 v149, v149, v238
v_sub_f32 v150, v150, v238
v_sub_f32 v151, v151, v238
v_sub_f32 v156, v156, v238
v_sub_f32 v157, v157, v238
v_sub_f32 v158, v158, v238
)"
R"(v_sub_f32 v159, v159, v238
v_exp_f32 v240, -v238
s_nop 0
v_mul_f32 v4, v4, v240
v_mul_f32 v5, v5, v240
v_mul_f32 v6, v6, v240
v_mul_f32 v7, v7, v240
v_mul_f32 v12, v12, v240
v_mul_f32 v13, v13, v240
v_mul_f32 v14, v14, v240
v_mul_f32 v15, v15, v240
v_mul_f32 v20, v20, v240
v_mul_f32 v21, v21, v240
v_mul_f32 v22, v22, v240
v_mul_f32 v23, v23, v240
v_mul_f32 v28, v28, v240
v_mul_f32 v29, v29, v240
v_mul_f32 v30, v30, v240
v_mul_f32 v31, v31, v240
v_mul_f32 v36, v36, v240
v_mul_f32 v37, v37, v240
v_mul_f32 v38, v38, v240
v_mul_f32 v39, v39, v240
v_mul_f32 v44, v44, v240
)"
R"(v_mul_f32 v45, v45, v240
v_mul_f32 v46, v46, v240
v_mul_f32 v47, v47, v240
v_mul_f32 v52, v52, v240
v_mul_f32 v53, v53, v240
v_mul_f32 v54, v54, v240
v_mul_f32 v55, v55, v240
v_mul_f32 v60, v60, v240
v_mul_f32 v61, v61, v240
v_mul_f32 v62, v62, v240
v_mul_f32 v63, v63, v240
v_mul_f32 v68, v68, v240
v_mul_f32 v69, v69, v240
v_mul_f32 v70, v70, v240
v_mul_f32 v71, v71, v240
v_mul_f32 v76, v76, v240
v_mul_f32 v77, v77, v240
v_mul_f32 v78, v78, v240
v_mul_f32 v79, v79, v240
v_mul_f32 v84, v84, v240
v_mul_f32 v85, v85, v240
v_mul_f32 v86, v86, v240
v_mul_f32 v87, v87, v240
v_mul_f32 v92, v92, v240
)"
R"(v_mul_f32 v93, v93, v240
v_mul_f32 v94, v94, v240
v_mul_f32 v95, v95, v240
v_accvgpr_read_b32 v239, a136
v_mul_f32 v239, v239, v240
v_accvgpr_write_b32 a136, v239
s_nop 1
s_branch BACK12_%=
RARE13_%=:
s_nop 7
v_max3_f32 v238, v96, v97, v98
v_max3_f32 v238, v238, v99, v104
v_max3_f32 v238, v238, v105, v106
v_max3_f32 v238, v238, v107, v112
v_max3_f32 v238, v238, v113, v114
v_max3_f32 v238, v238, v115, v120
v_max3_f32 v238, v238, v121, v122
v_max_f32 v238, v238, v123
v_mov_b32 v239, v238
s_nop 1
v_permlane16_swap_b32 v238, v239
v_max_f32 v238, v238, v239
v_mov_b32 v239, v238
s_nop 1
)"
R"(v_permlane32_swap_b32 v238, v239
v_max_f32 v238, v238, v239
v_max_f32 v238, 0, v238
v_sub_f32 v176, v176, v238
v_sub_f32 v177, v177, v238
v_sub_f32 v178, v178, v238
v_sub_f32 v179, v179, v238
v_sub_f32 v96, v96, v238
v_sub_f32 v97, v97, v238
v_sub_f32 v98, v98, v238
v_sub_f32 v99, v99, v238
v_sub_f32 v104, v104, v238
v_sub_f32 v105, v105, v238
v_sub_f32 v106, v106, v238
v_sub_f32 v107, v107, v238
v_sub_f32 v112, v112, v238
v_sub_f32 v113, v113, v238
v_sub_f32 v114, v114, v238
v_sub_f32 v115, v115, v238
v_sub_f32 v120, v120, v238
v_sub_f32 v121, v121, v238
v_sub_f32 v122, v122, v238
v_sub_f32 v123, v123, v238
v_exp_f32 v240, -v238
)"
R"(s_nop 0
v_mul_f32 v0, v0, v240
v_mul_f32 v1, v1, v240
v_mul_f32 v2, v2, v240
v_mul_f32 v3, v3, v240
v_mul_f32 v8, v8, v240
v_mul_f32 v9, v9, v240
v_mul_f32 v10, v10, v240
v_mul_f32 v11, v11, v240
v_mul_f32 v16, v16, v240
v_mul_f32 v17, v17, v240
v_mul_f32 v18, v18, v240
v_mul_f32 v19, v19, v240
v_mul_f32 v24, v24, v240
v_mul_f32 v25, v25, v240
v_mul_f32 v26, v26, v240
v_mul_f32 v27, v27, v240
v_mul_f32 v32, v32, v240
v_mul_f32 v33, v33, v240
v_mul_f32 v34, v34, v240
v_mul_f32 v35, v35, v240
v_mul_f32 v40, v40, v240
v_mul_f32 v41, v41, v240
v_mul_f32 v42, v42, v240
)"
R"(v_mul_f32 v43, v43, v240
v_mul_f32 v48, v48, v240
v_mul_f32 v49, v49, v240
v_mul_f32 v50, v50, v240
v_mul_f32 v51, v51, v240
v_mul_f32 v56, v56, v240
v_mul_f32 v57, v57, v240
v_mul_f32 v58, v58, v240
v_mul_f32 v59, v59, v240
v_mul_f32 v64, v64, v240
v_mul_f32 v65, v65, v240
v_mul_f32 v66, v66, v240
v_mul_f32 v67, v67, v240
v_mul_f32 v72, v72, v240
v_mul_f32 v73, v73, v240
v_mul_f32 v74, v74, v240
v_mul_f32 v75, v75, v240
v_mul_f32 v80, v80, v240
v_mul_f32 v81, v81, v240
v_mul_f32 v82, v82, v240
v_mul_f32 v83, v83, v240
v_mul_f32 v88, v88, v240
v_mul_f32 v89, v89, v240
v_mul_f32 v90, v90, v240
)"
R"(v_mul_f32 v91, v91, v240
v_accvgpr_read_b32 v239, a132
v_mul_f32 v239, v239, v240
v_accvgpr_write_b32 a132, v239
v_max3_f32 v238, v100, v101, v102
v_max3_f32 v238, v238, v103, v108
v_max3_f32 v238, v238, v109, v110
v_max3_f32 v238, v238, v111, v116
v_max3_f32 v238, v238, v117, v118
v_max3_f32 v238, v238, v119, v124
v_max3_f32 v238, v238, v125, v126
v_max_f32 v238, v238, v127
v_mov_b32 v239, v238
s_nop 1
v_permlane16_swap_b32 v238, v239
v_max_f32 v238, v238, v239
v_mov_b32 v239, v238
s_nop 1
v_permlane32_swap_b32 v238, v239
v_max_f32 v238, v238, v239
v_max_f32 v238, 0, v238
v_sub_f32 v180, v180, v238
v_sub_f32 v181, v181, v238
v_sub_f32 v182, v182, v238
)"
R"(v_sub_f32 v183, v183, v238
v_sub_f32 v100, v100, v238
v_sub_f32 v101, v101, v238
v_sub_f32 v102, v102, v238
v_sub_f32 v103, v103, v238
v_sub_f32 v108, v108, v238
v_sub_f32 v109, v109, v238
v_sub_f32 v110, v110, v238
v_sub_f32 v111, v111, v238
v_sub_f32 v116, v116, v238
v_sub_f32 v117, v117, v238
v_sub_f32 v118, v118, v238
v_sub_f32 v119, v119, v238
v_sub_f32 v124, v124, v238
v_sub_f32 v125, v125, v238
v_sub_f32 v126, v126, v238
v_sub_f32 v127, v127, v238
v_exp_f32 v240, -v238
s_nop 0
v_mul_f32 v4, v4, v240
v_mul_f32 v5, v5, v240
v_mul_f32 v6, v6, v240
v_mul_f32 v7, v7, v240
v_mul_f32 v12, v12, v240
)"
R"(v_mul_f32 v13, v13, v240
v_mul_f32 v14, v14, v240
v_mul_f32 v15, v15, v240
v_mul_f32 v20, v20, v240
v_mul_f32 v21, v21, v240
v_mul_f32 v22, v22, v240
v_mul_f32 v23, v23, v240
v_mul_f32 v28, v28, v240
v_mul_f32 v29, v29, v240
v_mul_f32 v30, v30, v240
v_mul_f32 v31, v31, v240
v_mul_f32 v36, v36, v240
v_mul_f32 v37, v37, v240
v_mul_f32 v38, v38, v240
v_mul_f32 v39, v39, v240
v_mul_f32 v44, v44, v240
v_mul_f32 v45, v45, v240
v_mul_f32 v46, v46, v240
v_mul_f32 v47, v47, v240
v_mul_f32 v52, v52, v240
v_mul_f32 v53, v53, v240
v_mul_f32 v54, v54, v240
v_mul_f32 v55, v55, v240
v_mul_f32 v60, v60, v240
)"
R"(v_mul_f32 v61, v61, v240
v_mul_f32 v62, v62, v240
v_mul_f32 v63, v63, v240
v_mul_f32 v68, v68, v240
v_mul_f32 v69, v69, v240
v_mul_f32 v70, v70, v240
v_mul_f32 v71, v71, v240
v_mul_f32 v76, v76, v240
v_mul_f32 v77, v77, v240
v_mul_f32 v78, v78, v240
v_mul_f32 v79, v79, v240
v_mul_f32 v84, v84, v240
v_mul_f32 v85, v85, v240
v_mul_f32 v86, v86, v240
v_mul_f32 v87, v87, v240
v_mul_f32 v92, v92, v240
v_mul_f32 v93, v93, v240
v_mul_f32 v94, v94, v240
v_mul_f32 v95, v95, v240
v_accvgpr_read_b32 v239, a136
v_mul_f32 v239, v239, v240
v_accvgpr_write_b32 a136, v239
s_nop 1
s_branch BACK13_%=
)"
R"(RARE14_%=:
s_nop 7
v_max3_f32 v238, v128, v129, v130
v_max3_f32 v238, v238, v131, v136
v_max3_f32 v238, v238, v137, v138
v_max3_f32 v238, v238, v139, v144
v_max3_f32 v238, v238, v145, v146
v_max3_f32 v238, v238, v147, v152
v_max3_f32 v238, v238, v153, v154
v_max_f32 v238, v238, v155
v_mov_b32 v239, v238
s_nop 1
v_permlane16_swap_b32 v238, v239
v_max_f32 v238, v238, v239
v_mov_b32 v239, v238
s_nop 1
v_permlane32_swap_b32 v238, v239
v_max_f32 v238, v238, v239
v_max_f32 v238, 0, v238
v_sub_f32 v176, v176, v238
v_sub_f32 v177, v177, v238
v_sub_f32 v178, v178, v238
v_sub_f32 v179, v179, v238
v_sub_f32 v128, v128, v238
)"
R"(v_sub_f32 v129, v129, v238
v_sub_f32 v130, v130, v238
v_sub_f32 v131, v131, v238
v_sub_f32 v136, v136, v238
v_sub_f32 v137, v137, v238
v_sub_f32 v138, v138, v238
v_sub_f32 v139, v139, v238
v_sub_f32 v144, v144, v238
v_sub_f32 v145, v145, v238
v_sub_f32 v146, v146, v238
v_sub_f32 v147, v147, v238
v_sub_f32 v152, v152, v238
v_sub_f32 v153, v153, v238
v_sub_f32 v154, v154, v238
v_sub_f32 v155, v155, v238
v_exp_f32 v240, -v238
s_nop 0
v_mul_f32 v0, v0, v240
v_mul_f32 v1, v1, v240
v_mul_f32 v2, v2, v240
v_mul_f32 v3, v3, v240
v_mul_f32 v8, v8, v240
v_mul_f32 v9, v9, v240
v_mul_f32 v10, v10, v240
)"
R"(v_mul_f32 v11, v11, v240
v_mul_f32 v16, v16, v240
v_mul_f32 v17, v17, v240
v_mul_f32 v18, v18, v240
v_mul_f32 v19, v19, v240
v_mul_f32 v24, v24, v240
v_mul_f32 v25, v25, v240
v_mul_f32 v26, v26, v240
v_mul_f32 v27, v27, v240
v_mul_f32 v32, v32, v240
v_mul_f32 v33, v33, v240
v_mul_f32 v34, v34, v240
v_mul_f32 v35, v35, v240
v_mul_f32 v40, v40, v240
v_mul_f32 v41, v41, v240
v_mul_f32 v42, v42, v240
v_mul_f32 v43, v43, v240
v_mul_f32 v48, v48, v240
v_mul_f32 v49, v49, v240
v_mul_f32 v50, v50, v240
v_mul_f32 v51, v51, v240
v_mul_f32 v56, v56, v240
v_mul_f32 v57, v57, v240
v_mul_f32 v58, v58, v240
)"
R"(v_mul_f32 v59, v59, v240
v_mul_f32 v64, v64, v240
v_mul_f32 v65, v65, v240
v_mul_f32 v66, v66, v240
v_mul_f32 v67, v67, v240
v_mul_f32 v72, v72, v240
v_mul_f32 v73, v73, v240
v_mul_f32 v74, v74, v240
v_mul_f32 v75, v75, v240
v_mul_f32 v80, v80, v240
v_mul_f32 v81, v81, v240
v_mul_f32 v82, v82, v240
v_mul_f32 v83, v83, v240
v_mul_f32 v88, v88, v240
v_mul_f32 v89, v89, v240
v_mul_f32 v90, v90, v240
v_mul_f32 v91, v91, v240
v_accvgpr_read_b32 v239, a132
v_mul_f32 v239, v239, v240
v_accvgpr_write_b32 a132, v239
v_max3_f32 v238, v132, v133, v134
v_max3_f32 v238, v238, v135, v140
v_max3_f32 v238, v238, v141, v142
v_max3_f32 v238, v238, v143, v148
)"
R"(v_max3_f32 v238, v238, v149, v150
v_max3_f32 v238, v238, v151, v156
v_max3_f32 v238, v238, v157, v158
v_max_f32 v238, v238, v159
v_mov_b32 v239, v238
s_nop 1
v_permlane16_swap_b32 v238, v239
v_max_f32 v238, v238, v239
v_mov_b32 v239, v238
s_nop 1
v_permlane32_swap_b32 v238, v239
v_max_f32 v238, v238, v239
v_max_f32 v238, 0, v238
v_sub_f32 v180, v180, v238
v_sub_f32 v181, v181, v238
v_sub_f32 v182, v182, v238
v_sub_f32 v183, v183, v238
v_sub_f32 v132, v132, v238
v_sub_f32 v133, v133, v238
v_sub_f32 v134, v134, v238
v_sub_f32 v135, v135, v238
v_sub_f32 v140, v140, v238
v_sub_f32 v141, v141, v238
v_sub_f32 v142, v142, v238
)"
R"(v_sub_f32 v143, v143, v238
v_sub_f32 v148, v148, v238
v_sub_f32 v149, v149, v238
v_sub_f32 v150, v150, v238
v_sub_f32 v151, v151, v238
v_sub_f32 v156, v156, v238
v_sub_f32 v157, v157, v238
v_sub_f32 v158, v158, v238
v_sub_f32 v159, v159, v238
v_exp_f32 v240, -v238
s_nop 0
v_mul_f32 v4, v4, v240
v_mul_f32 v5, v5, v240
v_mul_f32 v6, v6, v240
v_mul_f32 v7, v7, v240
v_mul_f32 v12, v12, v240
v_mul_f32 v13, v13, v240
v_mul_f32 v14, v14, v240
v_mul_f32 v15, v15, v240
v_mul_f32 v20, v20, v240
v_mul_f32 v21, v21, v240
v_mul_f32 v22, v22, v240
v_mul_f32 v23, v23, v240
v_mul_f32 v28, v28, v240
)"
R"(v_mul_f32 v29, v29, v240
v_mul_f32 v30, v30, v240
v_mul_f32 v31, v31, v240
v_mul_f32 v36, v36, v240
v_mul_f32 v37, v37, v240
v_mul_f32 v38, v38, v240
v_mul_f32 v39, v39, v240
v_mul_f32 v44, v44, v240
v_mul_f32 v45, v45, v240
v_mul_f32 v46, v46, v240
v_mul_f32 v47, v47, v240
v_mul_f32 v52, v52, v240
v_mul_f32 v53, v53, v240
v_mul_f32 v54, v54, v240
v_mul_f32 v55, v55, v240
v_mul_f32 v60, v60, v240
v_mul_f32 v61, v61, v240
v_mul_f32 v62, v62, v240
v_mul_f32 v63, v63, v240
v_mul_f32 v68, v68, v240
v_mul_f32 v69, v69, v240
v_mul_f32 v70, v70, v240
v_mul_f32 v71, v71, v240
v_mul_f32 v76, v76, v240
)"
R"(v_mul_f32 v77, v77, v240
v_mul_f32 v78, v78, v240
v_mul_f32 v79, v79, v240
v_mul_f32 v84, v84, v240
v_mul_f32 v85, v85, v240
v_mul_f32 v86, v86, v240
v_mul_f32 v87, v87, v240
v_mul_f32 v92, v92, v240
v_mul_f32 v93, v93, v240
v_mul_f32 v94, v94, v240
v_mul_f32 v95, v95, v240
v_accvgpr_read_b32 v239, a136
v_mul_f32 v239, v239, v240
v_accvgpr_write_b32 a136, v239
s_nop 1
s_branch BACK14_%=
RARE15_%=:
s_nop 7
v_max3_f32 v238, v96, v97, v98
v_max3_f32 v238, v238, v99, v104
v_max3_f32 v238, v238, v105, v106
v_max3_f32 v238, v238, v107, v112
v_max3_f32 v238, v238, v113, v114
v_max3_f32 v238, v238, v115, v120
)"
R"(v_max3_f32 v238, v238, v121, v122
v_max_f32 v238, v238, v123
v_mov_b32 v239, v238
s_nop 1
v_permlane16_swap_b32 v238, v239
v_max_f32 v238, v238, v239
v_mov_b32 v239, v238
s_nop 1
v_permlane32_swap_b32 v238, v239
v_max_f32 v238, v238, v239
v_max_f32 v238, 0, v238
v_sub_f32 v176, v176, v238
v_sub_f32 v177, v177, v238
v_sub_f32 v178, v178, v238
v_sub_f32 v179, v179, v238
v_sub_f32 v96, v96, v238
v_sub_f32 v97, v97, v238
v_sub_f32 v98, v98, v238
v_sub_f32 v99, v99, v238
v_sub_f32 v104, v104, v238
v_sub_f32 v105, v105, v238
v_sub_f32 v106, v106, v238
v_sub_f32 v107, v107, v238
v_sub_f32 v112, v112, v238
)"
R"(v_sub_f32 v113, v113, v238
v_sub_f32 v114, v114, v238
v_sub_f32 v115, v115, v238
v_sub_f32 v120, v120, v238
v_sub_f32 v121, v121, v238
v_sub_f32 v122, v122, v238
v_sub_f32 v123, v123, v238
v_exp_f32 v240, -v238
s_nop 0
v_mul_f32 v0, v0, v240
v_mul_f32 v1, v1, v240
v_mul_f32 v2, v2, v240
v_mul_f32 v3, v3, v240
v_mul_f32 v8, v8, v240
v_mul_f32 v9, v9, v240
v_mul_f32 v10, v10, v240
v_mul_f32 v11, v11, v240
v_mul_f32 v16, v16, v240
v_mul_f32 v17, v17, v240
v_mul_f32 v18, v18, v240
v_mul_f32 v19, v19, v240
v_mul_f32 v24, v24, v240
v_mul_f32 v25, v25, v240
v_mul_f32 v26, v26, v240
)"
R"(v_mul_f32 v27, v27, v240
v_mul_f32 v32, v32, v240
v_mul_f32 v33, v33, v240
v_mul_f32 v34, v34, v240
v_mul_f32 v35, v35, v240
v_mul_f32 v40, v40, v240
v_mul_f32 v41, v41, v240
v_mul_f32 v42, v42, v240
v_mul_f32 v43, v43, v240
v_mul_f32 v48, v48, v240
v_mul_f32 v49, v49, v240
v_mul_f32 v50, v50, v240
v_mul_f32 v51, v51, v240
v_mul_f32 v56, v56, v240
v_mul_f32 v57, v57, v240
v_mul_f32 v58, v58, v240
v_mul_f32 v59, v59, v240
v_mul_f32 v64, v64, v240
v_mul_f32 v65, v65, v240
v_mul_f32 v66, v66, v240
v_mul_f32 v67, v67, v240
v_mul_f32 v72, v72, v240
v_mul_f32 v73, v73, v240
v_mul_f32 v74, v74, v240
)"
R"(v_mul_f32 v75, v75, v240
v_mul_f32 v80, v80, v240
v_mul_f32 v81, v81, v240
v_mul_f32 v82, v82, v240
v_mul_f32 v83, v83, v240
v_mul_f32 v88, v88, v240
v_mul_f32 v89, v89, v240
v_mul_f32 v90, v90, v240
v_mul_f32 v91, v91, v240
v_accvgpr_read_b32 v239, a132
v_mul_f32 v239, v239, v240
v_accvgpr_write_b32 a132, v239
v_max3_f32 v238, v100, v101, v102
v_max3_f32 v238, v238, v103, v108
v_max3_f32 v238, v238, v109, v110
v_max3_f32 v238, v238, v111, v116
v_max3_f32 v238, v238, v117, v118
v_max3_f32 v238, v238, v119, v124
v_max3_f32 v238, v238, v125, v126
v_max_f32 v238, v238, v127
v_mov_b32 v239, v238
s_nop 1
v_permlane16_swap_b32 v238, v239
v_max_f32 v238, v238, v239
)"
R"(v_mov_b32 v239, v238
s_nop 1
v_permlane32_swap_b32 v238, v239
v_max_f32 v238, v238, v239
v_max_f32 v238, 0, v238
v_sub_f32 v180, v180, v238
v_sub_f32 v181, v181, v238
v_sub_f32 v182, v182, v238
v_sub_f32 v183, v183, v238
v_sub_f32 v100, v100, v238
v_sub_f32 v101, v101, v238
v_sub_f32 v102, v102, v238
v_sub_f32 v103, v103, v238
v_sub_f32 v108, v108, v238
v_sub_f32 v109, v109, v238
v_sub_f32 v110, v110, v238
v_sub_f32 v111, v111, v238
v_sub_f32 v116, v116, v238
v_sub_f32 v117, v117, v238
v_sub_f32 v118, v118, v238
v_sub_f32 v119, v119, v238
v_sub_f32 v124, v124, v238
v_sub_f32 v125, v125, v238
v_sub_f32 v126, v126, v238
)"
R"(v_sub_f32 v127, v127, v238
v_exp_f32 v240, -v238
s_nop 0
v_mul_f32 v4, v4, v240
v_mul_f32 v5, v5, v240
v_mul_f32 v6, v6, v240
v_mul_f32 v7, v7, v240
v_mul_f32 v12, v12, v240
v_mul_f32 v13, v13, v240
v_mul_f32 v14, v14, v240
v_mul_f32 v15, v15, v240
v_mul_f32 v20, v20, v240
v_mul_f32 v21, v21, v240
v_mul_f32 v22, v22, v240
v_mul_f32 v23, v23, v240
v_mul_f32 v28, v28, v240
v_mul_f32 v29, v29, v240
v_mul_f32 v30, v30, v240
v_mul_f32 v31, v31, v240
v_mul_f32 v36, v36, v240
v_mul_f32 v37, v37, v240
v_mul_f32 v38, v38, v240
v_mul_f32 v39, v39, v240
v_mul_f32 v44, v44, v240
)"
R"(v_mul_f32 v45, v45, v240
v_mul_f32 v46, v46, v240
v_mul_f32 v47, v47, v240
v_mul_f32 v52, v52, v240
v_mul_f32 v53, v53, v240
v_mul_f32 v54, v54, v240
v_mul_f32 v55, v55, v240
v_mul_f32 v60, v60, v240
v_mul_f32 v61, v61, v240
v_mul_f32 v62, v62, v240
v_mul_f32 v63, v63, v240
v_mul_f32 v68, v68, v240
v_mul_f32 v69, v69, v240
v_mul_f32 v70, v70, v240
v_mul_f32 v71, v71, v240
v_mul_f32 v76, v76, v240
v_mul_f32 v77, v77, v240
v_mul_f32 v78, v78, v240
v_mul_f32 v79, v79, v240
v_mul_f32 v84, v84, v240
v_mul_f32 v85, v85, v240
v_mul_f32 v86, v86, v240
v_mul_f32 v87, v87, v240
v_mul_f32 v92, v92, v240
)"
R"(v_mul_f32 v93, v93, v240
v_mul_f32 v94, v94, v240
v_mul_f32 v95, v95, v240
v_accvgpr_read_b32 v239, a136
v_mul_f32 v239, v239, v240
v_accvgpr_write_b32 a136, v239
s_nop 1
s_branch BACK15_%=
RARE16_%=:
s_nop 7
v_max3_f32 v238, v128, v129, v130
v_max3_f32 v238, v238, v131, v136
v_max3_f32 v238, v238, v137, v138
v_max3_f32 v238, v238, v139, v144
v_max3_f32 v238, v238, v145, v146
v_max3_f32 v238, v238, v147, v152
v_max3_f32 v238, v238, v153, v154
v_max_f32 v238, v238, v155
v_mov_b32 v239, v238
s_nop 1
v_permlane16_swap_b32 v238, v239
v_max_f32 v238, v238, v239
v_mov_b32 v239, v238
s_nop 1
)"
R"(v_permlane32_swap_b32 v238, v239
v_max_f32 v238, v238, v239
v_max_f32 v238, 0, v238
v_sub_f32 v176, v176, v238
v_sub_f32 v177, v177, v238
v_sub_f32 v178, v178, v238
v_sub_f32 v179, v179, v238
v_sub_f32 v128, v128, v238
v_sub_f32 v129, v129, v238
v_sub_f32 v130, v130, v238
v_sub_f32 v131, v131, v238
v_sub_f32 v136, v136, v238
v_sub_f32 v137, v137, v238
v_sub_f32 v138, v138, v238
v_sub_f32 v139, v139, v238
v_sub_f32 v144, v144, v238
v_sub_f32 v145, v145, v238
v_sub_f32 v146, v146, v238
v_sub_f32 v147, v147, v238
v_sub_f32 v152, v152, v238
v_sub_f32 v153, v153, v238
v_sub_f32 v154, v154, v238
v_sub_f32 v155, v155, v238
v_exp_f32 v240, -v238
)"
R"(s_nop 0
v_mul_f32 v0, v0, v240
v_mul_f32 v1, v1, v240
v_mul_f32 v2, v2, v240
v_mul_f32 v3, v3, v240
v_mul_f32 v8, v8, v240
v_mul_f32 v9, v9, v240
v_mul_f32 v10, v10, v240
v_mul_f32 v11, v11, v240
v_mul_f32 v16, v16, v240
v_mul_f32 v17, v17, v240
v_mul_f32 v18, v18, v240
v_mul_f32 v19, v19, v240
v_mul_f32 v24, v24, v240
v_mul_f32 v25, v25, v240
v_mul_f32 v26, v26, v240
v_mul_f32 v27, v27, v240
v_mul_f32 v32, v32, v240
v_mul_f32 v33, v33, v240
v_mul_f32 v34, v34, v240
v_mul_f32 v35, v35, v240
v_mul_f32 v40, v40, v240
v_mul_f32 v41, v41, v240
v_mul_f32 v42, v42, v240
)"
R"(v_mul_f32 v43, v43, v240
v_mul_f32 v48, v48, v240
v_mul_f32 v49, v49, v240
v_mul_f32 v50, v50, v240
v_mul_f32 v51, v51, v240
v_mul_f32 v56, v56, v240
v_mul_f32 v57, v57, v240
v_mul_f32 v58, v58, v240
v_mul_f32 v59, v59, v240
v_mul_f32 v64, v64, v240
v_mul_f32 v65, v65, v240
v_mul_f32 v66, v66, v240
v_mul_f32 v67, v67, v240
v_mul_f32 v72, v72, v240
v_mul_f32 v73, v73, v240
v_mul_f32 v74, v74, v240
v_mul_f32 v75, v75, v240
v_mul_f32 v80, v80, v240
v_mul_f32 v81, v81, v240
v_mul_f32 v82, v82, v240
v_mul_f32 v83, v83, v240
v_mul_f32 v88, v88, v240
v_mul_f32 v89, v89, v240
v_mul_f32 v90, v90, v240
)"
R"(v_mul_f32 v91, v91, v240
v_accvgpr_read_b32 v239, a132
v_mul_f32 v239, v239, v240
v_accvgpr_write_b32 a132, v239
v_max3_f32 v238, v132, v133, v134
v_max3_f32 v238, v238, v135, v140
v_max3_f32 v238, v238, v141, v142
v_max3_f32 v238, v238, v143, v148
v_max3_f32 v238, v238, v149, v150
v_max3_f32 v238, v238, v151, v156
v_max3_f32 v238, v238, v157, v158
v_max_f32 v238, v238, v159
v_mov_b32 v239, v238
s_nop 1
v_permlane16_swap_b32 v238, v239
v_max_f32 v238, v238, v239
v_mov_b32 v239, v238
s_nop 1
v_permlane32_swap_b32 v238, v239
v_max_f32 v238, v238, v239
v_max_f32 v238, 0, v238
v_sub_f32 v180, v180, v238
v_sub_f32 v181, v181, v238
v_sub_f32 v182, v182, v238
)"
R"(v_sub_f32 v183, v183, v238
v_sub_f32 v132, v132, v238
v_sub_f32 v133, v133, v238
v_sub_f32 v134, v134, v238
v_sub_f32 v135, v135, v238
v_sub_f32 v140, v140, v238
v_sub_f32 v141, v141, v238
v_sub_f32 v142, v142, v238
v_sub_f32 v143, v143, v238
v_sub_f32 v148, v148, v238
v_sub_f32 v149, v149, v238
v_sub_f32 v150, v150, v238
v_sub_f32 v151, v151, v238
v_sub_f32 v156, v156, v238
v_sub_f32 v157, v157, v238
v_sub_f32 v158, v158, v238
v_sub_f32 v159, v159, v238
v_exp_f32 v240, -v238
s_nop 0
v_mul_f32 v4, v4, v240
v_mul_f32 v5, v5, v240
v_mul_f32 v6, v6, v240
v_mul_f32 v7, v7, v240
v_mul_f32 v12, v12, v240
)"
R"(v_mul_f32 v13, v13, v240
v_mul_f32 v14, v14, v240
v_mul_f32 v15, v15, v240
v_mul_f32 v20, v20, v240
v_mul_f32 v21, v21, v240
v_mul_f32 v22, v22, v240
v_mul_f32 v23, v23, v240
v_mul_f32 v28, v28, v240
v_mul_f32 v29, v29, v240
v_mul_f32 v30, v30, v240
v_mul_f32 v31, v31, v240
v_mul_f32 v36, v36, v240
v_mul_f32 v37, v37, v240
v_mul_f32 v38, v38, v240
v_mul_f32 v39, v39, v240
v_mul_f32 v44, v44, v240
v_mul_f32 v45, v45, v240
v_mul_f32 v46, v46, v240
v_mul_f32 v47, v47, v240
v_mul_f32 v52, v52, v240
v_mul_f32 v53, v53, v240
v_mul_f32 v54, v54, v240
v_mul_f32 v55, v55, v240
v_mul_f32 v60, v60, v240
)"
R"(v_mul_f32 v61, v61, v240
v_mul_f32 v62, v62, v240
v_mul_f32 v63, v63, v240
v_mul_f32 v68, v68, v240
v_mul_f32 v69, v69, v240
v_mul_f32 v70, v70, v240
v_mul_f32 v71, v71, v240
v_mul_f32 v76, v76, v240
v_mul_f32 v77, v77, v240
v_mul_f32 v78, v78, v240
v_mul_f32 v79, v79, v240
v_mul_f32 v84, v84, v240
v_mul_f32 v85, v85, v240
v_mul_f32 v86, v86, v240
v_mul_f32 v87, v87, v240
v_mul_f32 v92, v92, v240
v_mul_f32 v93, v93, v240
v_mul_f32 v94, v94, v240
v_mul_f32 v95, v95, v240
v_accvgpr_read_b32 v239, a136
v_mul_f32 v239, v239, v240
v_accvgpr_write_b32 a136, v239
s_nop 1
s_branch BACK16_%=
)"
R"(SPINM_%=:
s_cmp_eq_u32 s87, 0
s_cbranch_scc1 SPBM_%=
s_sub_u32 s87, s87, 1
buffer_load_dwordx2 v[190:191], v230, %[srdC], 0 offen offset:128 sc1
s_waitcnt vmcnt(0)
v_cmp_eq_u32 vcc, %[tag], v191
s_nop 1
s_cmp_eq_u64 vcc, -1
s_cbranch_scc1 SPBM_%=
s_sleep 1
s_branch SPINM_%=
SPINL0_%=:
s_cmp_eq_u32 s87, 0
s_cbranch_scc1 SPBL0_%=
s_sub_u32 s87, s87, 1
buffer_load_dword v239, v238, %[srdC], s70 offen sc1
s_waitcnt vmcnt(0)
v_cmp_eq_u32 vcc, v239, v224
s_nop 1
s_and_b32 s47, vcc_lo, s71
s_cmp_eq_u32 s47, s71
s_cbranch_scc1 SPBL0_%=
s_sleep 1
)"
R"(s_branch SPINL0_%=
SPINL1_%=:
s_cmp_eq_u32 s87, 0
s_cbranch_scc1 SPBL1_%=
s_sub_u32 s87, s87, 1
buffer_load_dword v239, v238, %[srdC], s70 offen sc1
s_waitcnt vmcnt(0)
v_cmp_eq_u32 vcc, v239, v224
s_nop 1
s_and_b32 s47, vcc_lo, s71
s_cmp_eq_u32 s47, s71
s_cbranch_scc1 SPBL1_%=
s_sleep 1
s_branch SPINL1_%=
SPINT5_%=:
s_cmp_eq_u32 s87, 0
s_cbranch_scc1 SPBT5_%=
s_sub_u32 s87, s87, 1
buffer_load_dword v239, v238, %[srdC], s70 offen sc1
s_waitcnt vmcnt(0)
v_cmp_eq_u32 vcc, v239, v224
s_nop 1
s_and_b32 s47, vcc_lo, 0xff20
s_cmp_eq_u32 s47, 0xff20
)"
R"(s_cbranch_scc1 SPBT5_%=
s_sleep 1
s_branch SPINT5_%=
SPINT6_%=:
s_cmp_eq_u32 s87, 0
s_cbranch_scc1 SPBT6_%=
s_sub_u32 s87, s87, 1
buffer_load_dword v239, v238, %[srdC], s70 offen sc1
s_waitcnt vmcnt(0)
v_cmp_eq_u32 vcc, v239, v224
s_nop 1
s_and_b32 s47, vcc_lo, 0x40
s_cmp_eq_u32 s47, 0x40
s_cbranch_scc1 SPBT6_%=
s_sleep 1
s_branch SPINT6_%=
END_%=:
)"
    : [vX0] "+v"(vX0), [vX1] "+v"(vX1), [vW] "+v"(vW), [pW] "+v"(pW)
    : [pK] "v"(pK), [pV] "v"(pV), [pX0] "v"(pX0), [pX1] "v"(pX1), [pWo] "v"(pWo), [vK] "v"(vK), [vV] "v"(vV),
      [vWo] "v"(vWo), [vP] "v"(vP),
      [srdX] "s"(srdX), [srdW] "s"(srdW), [srdK] "s"(srdK), [srdV] "s"(srdV), [srdWo] "s"(srdWo), [wid] "s"(wid),
      [srdG] "s"(srdG), [srdC] "s"(srdC), [srdO] "s"(srdO), [bq] "s"(bq), [cq] "s"(cq), [hgi] "s"(hg), [tag] "s"(tag), [pw2] "s"(pw2), [po2] "s"(po2)
    : "v0", "v1", "v2", "v3", "v4", "v5", "v6", "v7", "v8", "v9", "v10", "v11", "v12", "v13", "v14", "v15", "v16", "v17", "v18", "v19", "v20", "v21", "v22", "v23", "v24", "v25", "v26", "v27", "v28", "v29", "v30", "v31", "v32", "v33", "v34", "v35", "v36", "v37", "v38", "v39", "v40", "v41", "v42", "v43", "v44", "v45", "v46", "v47", "v48", "v49", "v50", "v51", "v52", "v53", "v54", "v55", "v56", "v57", "v58", "v59", "v60", "v61", "v62", "v63", "v64", "v65", "v66", "v67", "v68", "v69", "v70", "v71", "v72", "v73", "v74", "v75", "v76", "v77", "v78", "v79", "v80", "v81", "v82", "v83", "v84", "v85", "v86", "v87", "v88", "v89", "v90", "v91", "v92", "v93", "v94", "v95", "v96", "v97", "v98", "v99", "v100", "v101", "v102", "v103", "v104", "v105", "v106", "v107", "v108", "v109", "v110", "v111", "v112", "v113", "v114", "v115", "v116", "v117", "v118", "v119", "v120", "v121", "v122", "v123", "v124", "v125", "v126", "v127", "v128", "v129", "v130", "v131", "v132", "v133", "v134", "v135", "v136", "v137", "v138", "v139", "v140", "v141", "v142", "v143", "v144", "v145", "v146", "v147", "v148", "v149", "v150", "v151", "v152", "v153", "v154", "v155", "v156", "v157", "v158", "v159", "v160", "v161", "v162", "v163", "v164", "v165", "v166", "v167", "v168", "v169", "v170", "v171", "v172", "v173", "v174", "v175", "v176", "v177", "v178", "v179", "v180", "v181", "v182", "v183", "v184", "v185", "v186", "v187", "v188", "v189", "v190", "v191", "v192", "v193", "v194", "v195", "v196", "v197", "v198", "v199", "v200", "v201", "v202", "v203", "v204", "v205", "v206", "v207", "v208", "v209", "v210", "v211", "v212", "v213", "v214", "v215", "v216", "v217", "v218", "v219", "v220", "v221", "v222", "v223", "v224", "v225", "v226", "v227", "v228", "v229", "v230", "v231", "v232", "v233", "v234", "v235", "v236", "v237", "v238", "v239", "v240", "v241", "a0", "a1", "a2", "a3", "a4", "a5", "a6", "a7", "a8", "a9", "a10", "a11", "a12", "a13", "a14", "a15", "a16", "a17", "a18", "a19", "a20", "a21", "a22", "a23", "a24", "a25", "a26", "a27", "a28", "a29", "a30", "a31", "a32", "a33", "a34", "a35", "a36", "a37", "a38", "a39", "a40", "a41", "a42", "a43", "a44", "a45", "a46", "a47", "a48", "a49", "a50", "a51", "a52", "a53", "a54", "a55", "a56", "a57", "a58", "a59", "a60", "a61", "a62", "a63", "a64", "a65", "a66", "a67", "a68", "a69", "a70", "a71", "a72", "a73", "a74", "a75", "a76", "a77", "a78", "a79", "a80", "a81", "a82", "a83", "a84", "a85", "a86", "a87", "a88", "a89", "a90", "a91", "a92", "a93", "a94", "a95", "a96", "a97", "a98", "a99", "a100", "a101", "a102", "a103", "a104", "a105", "a106", "a107", "a108", "a109", "a110", "a111", "a112", "a113", "a114", "a115", "a116", "a117", "a118", "a119", "a120", "a121", "a122", "a123", "a124", "a125", "a126", "a127", "a128", "a129", "a130", "a131", "a132", "a133", "a134", "a135", "a136", "a137", "a138", "a139", "a140", "a141", "a142", "a143", "a144", "a145", "a146", "a147", "a148", "a149", "a150", "a151", "a152", "a153", "a154", "a155", "a156", "a157", "a158", "a159", "a160", "a161", "a162", "a163", "a164", "a165", "a166", "a167", "a168", "a169", "a170", "a171", "a172", "a173", "a174", "a175", "a176", "a177", "a178", "a179", "a180", "a181", "a182", "a183", "a184", "a185", "a186", "a187", "a188", "a189", "a190", "a191", "a192", "a193", "a194", "a195", "a196", "a197", "a198", "a199", "a200", "a201", "a202", "a203", "a204", "a205", "a206", "a207", "a208", "a209", "a210", "a211", "a212", "a213", "a214", "a215", "a216", "a217", "a218", "a219", "a220", "a221", "a222", "a223", "a224", "a225", "a226", "a227", "a228", "a229", "a230", "a231", "a232", "a233", "a234", "a235", "a236", "a237", "a238", "a239", "a240", "a241", "a242", "a243", "a244", "a245", "a246", "a247", "a248", "a249", "a250", "a251", "a252", "a253", "a254", "a255", "s45", "s46", "s47", "s48", "s49", "s50", "s51", "s52", "s53", "s54", "s55", "s56", "s57", "s58", "s59", "s60", "s61", "s62", "s63", "s64", "s65", "s66", "s67", "s68", "s69", "s70", "s71", "s72", "s73", "s74", "s75", "s76", "s77", "s78", "s79", "s80", "s81", "s82", "s83", "s84", "s85", "s86", "s87", "s88", "s89", "s90", "s91", "s92", "s93", "s94", "s95", "s96", "s97", "vcc", "scc", "memory");
}

extern "C" void kernel_launch(void* const* d_in, const int* in_sizes, int n_in, void* d_out, int out_size, void* d_ws, size_t ws_size, hipStream_t stream) {
  (void)hipFuncSetAttribute((const void*)fused_kernel, hipFuncAttributeMaxDynamicSharedMemorySize, LDS_BYTES);
  fused_kernel<<<256, 256, LDS_BYTES, stream>>>((const char*)d_in[0], (const char*)d_in[1], (const char*)d_in[2], (const char*)d_in[3], (const char*)d_in[4],
                                                (const char*)d_in[5], (const char*)d_in[6], (const char*)d_in[7], (const char*)d_in[8], (const char*)d_in[9], (char*)d_ws, (char*)d_out);
}
```
